```python
import torch

def _softmax(x, axis=-1):
    return torch.softmax(x, dim=axis)
def _gelu(x):
    return torch.nn.functional.gelu(x, approximate="tanh")
def _silu(x):
    return torch.nn.functional.silu(x)
def _sigmoid(x):
    return torch.sigmoid(x)
def _layernorm(x, axis=-1, eps=1e-5):
    m = x.mean(dim=axis, keepdim=True)
    v = x.var(dim=axis, keepdim=True, unbiased=False)
    return (x - m) / torch.sqrt(v + eps)
def _scatter_add(tgt, idx, src, axis=0):
    return tgt.clone().index_add_(axis, idx, src)
def _segment_sum(src, seg, n):
    out = torch.zeros((n,) + src.shape[1:], dtype=src.dtype)
    return out.index_add_(0, seg, src)
def _pad(x, pw):
    flat = tuple(v for pair in reversed(pw) for v in pair)
    return torch.nn.functional.pad(x, flat)

kernel_name = 'sequential_preLN__compressed_kv_cla__moe_arch_mod__prompt'

def setup_inputs():

    _w = (lambda a: (lambda t: t.to(torch.bfloat16) if t.is_floating_point() else t)(torch.as_tensor(a)))
    def _A_setup():
        B, H, Sq, Sk, D, M = 1, 32, 2048, 2048, 128, 640
        Dc = 4 * D
        return {
            "x": _w((lambda *s: torch.randn(*s, dtype=torch.bfloat16) * 0.05)(B, Sq, M)),
            "c_kv": _w((lambda *s: torch.randn(*s, dtype=torch.bfloat16) * 0.4)(B, Sk, Dc)),
            "w_q": _w((lambda *s: torch.randn(*s, dtype=torch.bfloat16) * 3.2)(M, H, D) * (M ** -0.5)),
            "w_uk": _w((lambda *s: torch.randn(*s, dtype=torch.bfloat16) * 3.2)(Dc, H, D) * (Dc ** -0.5)),
            "w_uv": _w((lambda *s: torch.randn(*s, dtype=torch.bfloat16) * 0.4)(Dc, H, D) * (Dc ** -0.5)),
            "w_o": _w((lambda *s: torch.randn(*s, dtype=torch.bfloat16) * 0.4)(H, D, M) * ((H * D) ** -0.5)),
        }
    _A_in = _A_setup()

    def _B_setup():
        B, S, D, F, C = 1, 2048, 640, 1728, 2
        return {
            "x": _w((lambda *s: torch.randn(*s, dtype=torch.bfloat16) * 0.05)(B, S, D)),
            "w_r": _w((lambda *s: torch.randn(*s, dtype=torch.bfloat16) * 0.05)(D)),
            "w_gate": _w((lambda *s: torch.randn(*s, dtype=torch.bfloat16) * 0.05)(D, F)),
            "w_up": _w((lambda *s: torch.randn(*s, dtype=torch.bfloat16) * 0.05)(D, F)),
            "w_down": _w((lambda *s: torch.randn(*s, dtype=torch.bfloat16) * 0.05)(F, D)),
        }
    _B_in = _B_setup()

    return {
        "A_x": _A_in["x"],
        "A_c_kv": _A_in["c_kv"],
        "A_w_q": _A_in["w_q"],
        "A_w_uk": _A_in["w_uk"],
        "A_w_uv": _A_in["w_uv"],
        "A_w_o": _A_in["w_o"],
        "B_x": _B_in["x"],
        "B_w_r": _B_in["w_r"],
        "B_w_gate": _B_in["w_gate"],
        "B_w_up": _B_in["w_up"],
        "B_w_down": _B_in["w_down"],
    }

def reference(A_x, A_c_kv, A_w_q, A_w_uk, A_w_uv, A_w_o, B_x, B_w_r, B_w_gate, B_w_up, B_w_down):
    def _A_ref(x, c_kv, w_q, w_uk, w_uv, w_o):
        B, Sq, M = x.shape[0], x.shape[1], x.shape[2]
        Sk = c_kv.shape[1]
        H, D = w_q.shape[1], w_q.shape[2]
        q = torch.einsum("bqm,mhd->bhqd", x, w_q)
        k = torch.einsum("bkc,chd->bhkd", c_kv, w_uk)
        v = torch.einsum("bkc,chd->bhkd", c_kv, w_uv)
        scores = torch.einsum("bhqd,bhkd->bhqk", q, k) * (D ** -0.5)
        off = Sk - Sq
        qi = torch.arange(Sq)[:, None]
        kj = torch.arange(Sk)[None, :]
        cmask = kj <= (qi + off)
        scores = torch.where(cmask[None, None], scores, -1e9)
        p = _softmax(scores, axis=-1)
        ctx = torch.einsum("bhqk,bhkd->bhqd", p, v)
        return torch.einsum("bhqd,hdm->bqm", ctx, w_o)

    def _B_ref(x, w_r, w_gate, w_up, w_down):
        B, S, D = x.shape
        C = 2
        r = x @ w_r
        idx = torch.argsort(-r, axis=-1)[:, :C]
        idx_e = (lambda x, n, axis: torch.repeat_interleave(x, n, dim=axis))(idx.reshape(B, C, 1), D, axis=2)
        x_sel = (lambda x, i, axis: torch.take_along_dim(x, i, dim=axis))(x, idx_e, axis=1)
        g = _silu(x_sel @ w_gate)
        u = x_sel @ w_up
        y = (g * u) @ w_down
        r_sel = _sigmoid((lambda x, i, axis: torch.take_along_dim(x, i, dim=axis))(r, idx, axis=1))
        y = y * r_sel.reshape(B, C, 1)
        b_off = (torch.arange(B) * S).reshape(B, 1)
        flat_idx = (idx + b_off).reshape(B * C)
        zero = (x * 0.0).reshape(B * S, D)
        upd = _scatter_add(zero, flat_idx, y.reshape(B * C, D), axis=0)
        return x + upd.reshape(B, S, D)

    x = A_x
    h = x + _A_ref(_layernorm(x, axis=-1), A_c_kv, A_w_q, A_w_uk, A_w_uv, A_w_o)
    h2 = h + _B_ref(_layernorm(h, axis=-1), B_w_r, B_w_gate, B_w_up, B_w_down)
    return h2
```

```cpp
#include <hip/hip_runtime.h>
#include <hip/hip_bf16.h>
#include <stdint.h>

typedef unsigned u32x4 __attribute__((ext_vector_type(4)));
typedef unsigned u32x2 __attribute__((ext_vector_type(2)));
typedef float f32x2 __attribute__((ext_vector_type(2)));
typedef float f32x4 __attribute__((ext_vector_type(4)));
typedef short s16x4 __attribute__((ext_vector_type(4)));
typedef short s16x8 __attribute__((ext_vector_type(8)));
typedef __bf16 bf16x2_t __attribute__((ext_vector_type(2)));
typedef __bf16 bf16x8_t __attribute__((ext_vector_type(8)));
typedef unsigned long long u64;
typedef const char* cptr_t;
typedef const __attribute__((address_space(4))) cptr_t* kargs_t;
#define LDSP __attribute__((address_space(3)))
#define GLB __attribute__((address_space(1)))

__device__ __forceinline__ int lane_id() { int l; asm volatile("v_mbcnt_lo_u32_b32 %0, -1, 0\n\tv_mbcnt_hi_u32_b32 %0, -1, %0" : "=v"(l)); return l; }
__device__ __forceinline__ float bf_lo(unsigned w) { return __uint_as_float(w << 16); }
__device__ __forceinline__ float bf_hi(unsigned w) { return __uint_as_float(w & 0xffff0000u); }
__device__ __forceinline__ unsigned pk_bf16(float lo, float hi) {
    f32x2 v = {lo, hi};
    return __builtin_bit_cast(unsigned, __builtin_convertvector(v, bf16x2_t));
}
__device__ __forceinline__ float bf_round(float v) { return bf_lo(pk_bf16(v, 0.f)); }
__device__ __forceinline__ float wave_sum(float v) {
#pragma unroll
    for (int o = 32; o >= 1; o >>= 1) v += __shfl_xor(v, o);
    return v;
}
__device__ __forceinline__ u64 ld_agent(const u64* p) { return __hip_atomic_load((const GLB u64*)p, __ATOMIC_RELAXED, __HIP_MEMORY_SCOPE_AGENT); }
__device__ __forceinline__ void st_agent(u64* p, u64 v) { __hip_atomic_store((GLB u64*)p, v, __ATOMIC_RELAXED, __HIP_MEMORY_SCOPE_AGENT); }
__device__ __forceinline__ unsigned ld_agent32(const unsigned* p) { return __hip_atomic_load((const GLB unsigned*)p, __ATOMIC_RELAXED, __HIP_MEMORY_SCOPE_AGENT); }
__device__ __forceinline__ float sum_hi3(float v) {
    v += __uint_as_float(__builtin_amdgcn_update_dpp(0u, __float_as_uint(v), 0x128, 0xf, 0xf, false));
    { auto r = __builtin_amdgcn_permlane16_swap(__float_as_uint(v), __float_as_uint(v), false, false); v = __uint_as_float(r[0]) + __uint_as_float(r[1]); }
    { auto r = __builtin_amdgcn_permlane32_swap(__float_as_uint(v), __float_as_uint(v), false, false); v = __uint_as_float(r[0]) + __uint_as_float(r[1]); }
    return v;
}
__device__ __forceinline__ unsigned wave_umax(unsigned v) {
    unsigned t;
    t = __builtin_amdgcn_update_dpp(0u, v, 0x121, 0xf, 0xf, false); v = t > v ? t : v;
    t = __builtin_amdgcn_update_dpp(0u, v, 0x122, 0xf, 0xf, false); v = t > v ? t : v;
    t = __builtin_amdgcn_update_dpp(0u, v, 0x124, 0xf, 0xf, false); v = t > v ? t : v;
    t = __builtin_amdgcn_update_dpp(0u, v, 0x128, 0xf, 0xf, false); v = t > v ? t : v;
    { auto r = __builtin_amdgcn_permlane16_swap(v, v, false, false); v = r[0] > r[1] ? r[0] : r[1]; }
    { auto r = __builtin_amdgcn_permlane32_swap(v, v, false, false); v = r[0] > r[1] ? r[0] : r[1]; }
    return v;
}
__device__ __forceinline__ unsigned fkey(unsigned b) { return (b & 0x80000000u) ? ~b : (b | 0x80000000u); }
__device__ __forceinline__ u64 wave_max64(u64 v) {
    const unsigned hi = (unsigned)(v >> 32), mh = wave_umax(hi);
    const unsigned ml = wave_umax(hi == mh ? (unsigned)v : 0u);
    return ((u64)mh << 32) | ml;
}
#define SPIN_CAP (1u << 20)
#define TST(k)
#define TSTID 0
#define TS 65536

__device__ __forceinline__ void lds_dma16(const int ldsoff, const int voff, const __amdgpu_buffer_rsrc_t rs, const int soff) {
    unsigned keep;
    asm volatile("s_mov_b32 %0, m0\n\ts_mov_b32 m0, %1\n\ts_nop 0\n\tbuffer_load_dwordx4 %2, %3, %4 offen lds\n\ts_mov_b32 m0, %0"
                 : "=&s"(keep) : "s"(ldsoff), "v"(voff), "s"(rs), "s"(soff) : "memory");
}
__device__ __forceinline__ void quad_xchg(unsigned& a0, unsigned& a1, unsigned& b0, unsigned& b1, const bool odd) {
    const unsigned s0 = odd ? a0 : b0, s1 = odd ? a1 : b1;
    const unsigned r0 = __builtin_amdgcn_update_dpp(0u, s0, 0xb1, 0xf, 0xf, false), r1 = __builtin_amdgcn_update_dpp(0u, s1, 0xb1, 0xf, 0xf, false);
    if (odd) { a0 = r0; a1 = r1; } else { b0 = r0; b1 = r1; }
}
__device__ __forceinline__ float wave_fsum(float v) {
    v += __uint_as_float(__builtin_amdgcn_update_dpp(0u, __float_as_uint(v), 0x121, 0xf, 0xf, false));
    v += __uint_as_float(__builtin_amdgcn_update_dpp(0u, __float_as_uint(v), 0x122, 0xf, 0xf, false));
    v += __uint_as_float(__builtin_amdgcn_update_dpp(0u, __float_as_uint(v), 0x124, 0xf, 0xf, false));
    return sum_hi3(v);
}

__device__ __forceinline__ float ld_bf16(const __amdgpu_buffer_rsrc_t rs, const int off) {
    return __uint_as_float((unsigned)__builtin_amdgcn_raw_buffer_load_b16(rs, off, 0, 16) << 16);
}
__device__ __forceinline__ unsigned exact_path_if_needed(char* lds_g, const int wid, const unsigned b, const u64 tag,
                                                         kargs_t ka) {
    LDSP char* lds = (LDSP char*)lds_g;
    const int lane = lane_id();
    asm volatile("s_waitcnt lgkmcnt(0)\n\ts_barrier" ::: "memory");
    const u32x2 fl = *(LDSP u32x2*)(lds + 0x27000 + (lane & 3) * 8);
    const unsigned badA = __any(fl[0] != 0u), badB = __any(fl[1] != 0u);
    if (__builtin_amdgcn_readfirstlane(badA | badB) == 0) return 0u;
    const unsigned h = (b & 7) * 4 + ((b >> 3) & 3), i = b >> 5;
    const char* wq = (const char*)ka[1];
    char* ws = (char*)ka[10];
    const int tid = wid * 64 + lane;
    asm volatile("s_waitcnt vmcnt(0) lgkmcnt(0)\n\ts_barrier" ::: "memory");
    const __amdgpu_buffer_rsrc_t rsX = __builtin_amdgcn_make_buffer_rsrc((void*)(ws + 0x40000), 0, 2048 * 1280, 0x20000);
    const __amdgpu_buffer_rsrc_t rsQ = __builtin_amdgcn_make_buffer_rsrc((void*)(wq + h * 256), 0, 640 * 8192 - h * 256, 0x20000);
    const __amdgpu_buffer_rsrc_t rsK = __builtin_amdgcn_make_buffer_rsrc((void*)(ws + 0x400000 + (size_t)h * 0x80000), 0, 0x80000, 0x20000);
    const __amdgpu_buffer_rsrc_t rsV = __builtin_amdgcn_make_buffer_rsrc((void*)(ws + 0x1400000 + (size_t)h * 0x80000), 0, 0x80000, 0x20000);
    const __amdgpu_buffer_rsrc_t rsC = __builtin_amdgcn_make_buffer_rsrc((void*)(ws + 0x2400000 + h * 4096), 0, 0x1000000 - h * 4096, 0x20000);
    LDSP float* ql = (LDSP float*)lds;
#pragma unroll 1
    for (int blk = 0; blk < 2; ++blk) {
        if (!(blk ? badB : badA)) continue;
        const int xb = blk ? (int)i : 15 - (int)i;
#pragma unroll 1
        for (int e = tid; e < 128 * 128; e += 512) {
            const int r = e >> 7, d = e & 127, row = xb * 128 + r;
            float acc = 0.f;
#pragma unroll 4
            for (int k = 0; k < 640; ++k)
                acc += ld_bf16(rsX, (row >> 4) * 20480 + (k >> 5) * 1024 + (((k >> 3) & 3) * 16 + (row & 15)) * 16 + (k & 7) * 2) * ld_bf16(rsQ, k * 8192 + d * 2);
            ql[r * 129 + d] = bf_round(acc);
        }
        __syncthreads();
        const int r = tid >> 2, p = tid & 3, row = xb * 128 + r;
        auto score = [&](const int key) {
            const int kb = (key >> 5) * 8192 + ((key >> 4) & 1) * 4096 + (key & 15) * 16;
            float s = 0.f;
#pragma unroll 4
            for (int d = 0; d < 128; ++d) s += ql[r * 129 + d] * ld_bf16(rsK, kb + (d >> 5) * 1024 + ((d & 15) >> 2) * 256 + (((d >> 4) & 1) * 4 + (d & 3)) * 2);
            return s;
        };
        float m = -__builtin_inff();
#pragma unroll 1
        for (int key = p; key <= row; key += 4) m = fmaxf(m, score(key));
        m = fmaxf(m, __shfl_xor(m, 1)); m = fmaxf(m, __shfl_xor(m, 2));
        float l = 0.f, o[32];
#pragma unroll
        for (int j = 0; j < 32; ++j) o[j] = 0.f;
#pragma unroll 1
        for (int key = 0; key <= row; ++key) {
            const float pr = exp2f(score(key) - m);
            l += pr;
            const int vb = (key >> 5) * 8192 + ((key & 15) >> 2) * 256 + (((key >> 4) & 1) * 4 + (key & 3)) * 2;
#pragma unroll
            for (int j = 0; j < 32; ++j) { const int d = 32 * p + j; o[j] += pr * ld_bf16(rsV, vb + (d >> 4) * 1024 + (d & 15) * 16); }
        }
        const float rl = 1.0f / l;
#pragma unroll
        for (int j = 0; j < 32; ++j)
            __builtin_amdgcn_raw_buffer_store_b16((unsigned short)(pk_bf16(o[j] * rl, 0.f) & 0xffffu), rsC,
                                                  (row >> 4) * 131072 + p * 1024 + (((j & 15) >> 2) * 16 + (row & 15)) * 16 + ((j >> 4) * 4 + (j & 3)) * 2, 0, 16);
        __syncthreads();
    }
    asm volatile("s_waitcnt vmcnt(0)\n\ts_barrier" ::: "memory");
    if (badA && tid == 0) st_agent((u64*)(ws + 0x10000) + (((h >> 2) * 8 + i) * 4 + (h & 3)) * 16, tag);
    return 1u;
}

__device__ __forceinline__ void row_reduce(const int row, const u64* flags, const u64 tag, const unsigned tag32, const int lane,
                                           const unsigned short* __restrict__ x, const unsigned short* __restrict__ wr,
                                           char* __restrict__ ws, unsigned short* __restrict__ out, const unsigned b, const int tid,
                                           LDSP volatile unsigned* relay, const bool poller) {
    const __amdgpu_buffer_rsrc_t rsOut = __builtin_amdgcn_make_buffer_rsrc((void*)out, 0, 2048 * 1280, 0x20000);
    const __amdgpu_buffer_rsrc_t rsHn = __builtin_amdgcn_make_buffer_rsrc((void*)(ws + 0x4800000), 0, 2048 * 1280, 0x20000);
    const __amdgpu_buffer_rsrc_t rsP = __builtin_amdgcn_make_buffer_rsrc((void*)(ws + 0x3400000), 0, 8 * 2048 * 1280, 0x20000);
    const __amdgpu_buffer_rsrc_t rsX = __builtin_amdgcn_make_buffer_rsrc((void*)x, 0, 2048 * 1280, 0x20000);
    const __amdgpu_buffer_rsrc_t rsW = __builtin_amdgcn_make_buffer_rsrc((void*)wr, 0, 1280, 0x20000);
    const bool odd = lane & 1, two = lane < 16;
    const int OOB = 0x7ff00000;
    const int lin = (lane >> 2) * 64 + (lane & 1) * 32 + (lane & 2) * 8;
    u32x4 xv[2], wv[2];
    xv[0] = __builtin_amdgcn_raw_buffer_load_b128(rsX, row * 1280 + lin, 0, 0);
    xv[1] = __builtin_amdgcn_raw_buffer_load_b128(rsX, two ? row * 1280 + 1024 + lin : OOB, 0, 0);
    wv[0] = __builtin_amdgcn_raw_buffer_load_b128(rsW, lin, 0, 0);
    wv[1] = __builtin_amdgcn_raw_buffer_load_b128(rsW, two ? 1024 + lin : OOB, 0, 0);
    {
        unsigned spins = 0;
        if (poller) {
            while (!__all(ld_agent(flags + (lane & 31) * 16) == tag) && ++spins < SPIN_CAP) __builtin_amdgcn_s_sleep(2);
            if (relay) *relay = tag32;
        } else {
            while (*relay != tag32 && ++spins < SPIN_CAP * 16) __builtin_amdgcn_s_sleep(1);
        }
        __builtin_amdgcn_fence(__ATOMIC_ACQUIRE, "wavefront");
    }
    TST(1)
    u32x4 pv[2][8];
#pragma unroll
    for (int j = 0; j < 8; ++j) {
        pv[0][j] = __builtin_amdgcn_raw_buffer_load_b128(rsP, row * 1280 + lane * 16, j * (2048 * 1280), 0x12);
        pv[1][j] = __builtin_amdgcn_raw_buffer_load_b128(rsP, two ? row * 1280 + 1024 + lane * 16 : OOB, j * (2048 * 1280), 0x12);
    }
    const float piv = bf_lo((unsigned)__builtin_amdgcn_readlane((int)xv[0][0], 0));
    float hv[2][8], s = 0.f, q = 0.f, d = 0.f, sw = 0.f;
#pragma unroll
    for (int k = 0; k < 2; ++k) {
        unsigned x0 = xv[k][0], x1 = xv[k][1], x2 = xv[k][2], x3 = xv[k][3], w0 = wv[k][0], w1 = wv[k][1], w2 = wv[k][2], w3 = wv[k][3];
        quad_xchg(x0, x1, x2, x3, odd); quad_xchg(w0, w1, w2, w3, odd);
        const unsigned xs[4] = {x0, x1, x2, x3}, wsv[4] = {w0, w1, w2, w3};
        const float m = (k == 0 || two) ? 1.f : 0.f;
#pragma unroll
        for (int e = 0; e < 4; ++e) {
            float lo = bf_lo(xs[e]), hi = bf_hi(xs[e]);
#pragma unroll
            for (int j = 0; j < 8; ++j) { lo += bf_lo(pv[k][j][e]); hi += bf_hi(pv[k][j][e]); }
            hv[k][2 * e] = lo; hv[k][2 * e + 1] = hi;
            const float a = (lo - piv) * m, c = (hi - piv) * m;
            s += a + c; q += a * a + c * c; d += a * bf_lo(wsv[e]) + c * bf_hi(wsv[e]); sw += bf_lo(wsv[e]) + bf_hi(wsv[e]);
        }
    }
    asm volatile("" : "+v"(s), "+v"(q));
    TST(2)
    s = wave_fsum(s); q = wave_fsum(q); d = wave_fsum(d); sw = wave_fsum(sw);
    const float ms = s * (1.0f / 640.0f);
    const float var = fmaxf(q * (1.0f / 640.0f) - ms * ms, 0.f);
    const float rstd = 1.0f / sqrtf(var + 1e-5f), mean = ms + piv;
#pragma unroll
    for (int k = 0; k < 2; ++k) {
        unsigned n[4], o[4];
#pragma unroll
        for (int e = 0; e < 4; ++e) {
            const float a = (hv[k][2 * e] - mean) * rstd, c = (hv[k][2 * e + 1] - mean) * rstd;
            n[e] = pk_bf16(a, c); o[e] = pk_bf16(hv[k][2 * e] + a, hv[k][2 * e + 1] + c);
        }
        quad_xchg(n[0], n[1], n[2], n[3], odd); quad_xchg(o[0], o[1], o[2], o[3], odd);
        const int off = (k == 0 || two) ? row * 1280 + k * 1024 + lin : OOB;
        __builtin_amdgcn_raw_buffer_store_b128((u32x4){n[0], n[1], n[2], n[3]}, rsHn, off, 0, 16);
        __builtin_amdgcn_raw_buffer_store_b128((u32x4){o[0], o[1], o[2], o[3]}, rsOut, off, 0, 16);
    }
    const float r = (d - ms * sw) * rstd;
    asm volatile("s_waitcnt vmcnt(0)" ::: "memory");
    if (lane == 0) st_agent((u64*)(ws + 0x30000) + row, ((u64)((fkey(__float_as_uint(r)) & 0xfffff800u) | (unsigned)(2047 - row)) << 32) | tag32);
}

__device__ __forceinline__ void block_tail(char* lds_g, const int wid, const unsigned b, const unsigned h, const unsigned i, const u64 tag,
                                           const unsigned short* __restrict__ x, const unsigned short* __restrict__ wr,
                                           const char* __restrict__ wg, const char* __restrict__ wu, const char* __restrict__ wd,
                                           char* __restrict__ ws, unsigned short* __restrict__ out) {
    LDSP char* lds = (LDSP char*)lds_g;
    const int lane = lane_id();
    const int tid = wid * 64 + lane;
    u64* cand = (u64*)(ws + 0x30000);
    u64* actg = (u64*)(ws + 0x4000);
    const unsigned tag32 = (unsigned)tag * 2u + 1u;
    const unsigned r_ = b >> 3;
    const __amdgpu_buffer_rsrc_t rsOut = __builtin_amdgcn_make_buffer_rsrc((void*)out, 0, 2048 * 640 * 2, 0x20000);
    const __amdgpu_buffer_rsrc_t rsHn = __builtin_amdgcn_make_buffer_rsrc((void*)(ws + 0x4800000), 0, 2048 * 640 * 2, 0x20000);
    if (wid >= 4) {
        row_reduce((15 - (int)i) * 128 + (int)h * 4 + (wid - 4), (const u64*)(ws + 0x20000) + i * 512, tag, tag32, lane, x, wr, ws, out, b, tid, nullptr, true);
        const int w4 = wid - 4;
        if (r_ < 27) {
            const int col = ((b & 7) * 27 + r_) * 16;
            const __amdgpu_buffer_rsrc_t rs = __builtin_amdgcn_make_buffer_rsrc((void*)((w4 & 2 ? wu : wg) + col), 0, 640 * 3456 - col, 0x20000);
#pragma unroll
            for (int p = 0; p < 5; ++p) {
                const int P = (w4 & 1) * 5 + p;
                lds_dma16(0x0 + (w4 >> 1) * 10240 + P * 1024, lane * 3456, rs, P * 64 * 3456);
            }
        } else {
            const int col = ((b & 7) * 5 + (r_ - 27)) * 32;
            const __amdgpu_buffer_rsrc_t rs = __builtin_amdgcn_make_buffer_rsrc((void*)(wd + col), 0, 1728 * 1280 - col, 0x20000);
#pragma unroll
            for (int p = 0; p < 14; ++p) {
                const int P = w4 * 14 + p;
                lds_dma16(0x0 + P * 1024, (lane >> 1) * 1280 + (lane & 1) * 16, rs, P * 32 * 1280);
            }
        }
    }
    if (wid < 4) {
        TST(0)
        unsigned old = 0;
        if (lane == 0) old = __hip_atomic_fetch_add((LDSP unsigned*)(lds + 0x27100), 1u, __ATOMIC_RELAXED, __HIP_MEMORY_SCOPE_WORKGROUP);
        if (lane == 0 && old == 3u) st_agent((u64*)(ws + 0x28000) + i * 512 + h * 16, tag);
        row_reduce((int)i * 128 + (int)h * 4 + wid, (const u64*)(ws + 0x28000) + i * 512, tag, tag32, lane, x, wr, ws, out, b, tid, (LDSP volatile unsigned*)(lds + 0x27100 + 4), wid == 0);
    }
    TST(3)
    u64* topw = (u64*)(ws + 0x2000);
    if (b == 255) {
        if (wid < 4) {
            u64 g[8]; unsigned spins = 0;
            for (;;) {
                bool ok = true;
#pragma unroll
                for (int k = 0; k < 8; ++k) { g[k] = ld_agent(cand + k * 256 + tid); ok &= (unsigned)g[k] == tag32; }
                if (__all(ok) || ++spins >= SPIN_CAP) break;
                __builtin_amdgcn_s_sleep(1);
            }
            u64 hi = g[0] > g[1] ? g[0] : g[1], lo = g[0] > g[1] ? g[1] : g[0];
#pragma unroll
            for (int k = 2; k < 8; ++k) { if (g[k] > hi) { lo = hi; hi = g[k]; } else if (g[k] > lo) lo = g[k]; }
            const u64 m0 = wave_max64(hi), m1 = wave_max64(hi == m0 ? lo : hi);
            if (lane < 2) *(LDSP u64*)(lds + TS + 2048 + (wid * 2 + lane) * 8) = lane ? m1 : m0;
        }
        __syncthreads();
        if (wid == 0) {
            const u64 k = lane < 8 ? *(LDSP u64*)(lds + TS + 2048 + lane * 8) : 0ull;
            const u64 m0 = wave_max64(k), m1 = wave_max64(k == m0 ? 0ull : k);
            if (lane < 16) st_agent(topw + (lane >> 1) * 16 + (lane & 1), (lane & 1) ? m1 : m0);
        }
    }
    if (wid == 0) {
        u64 g; unsigned spins = 0;
        do { g = ld_agent(topw + (b & 7) * 16 + (lane & 1)); if (__all((unsigned)g == tag32)) break; __builtin_amdgcn_s_sleep(2); } while (++spins < SPIN_CAP);
        TST(4)
        if (lane < 2) *(LDSP u64*)(lds + TS + 3072 + lane * 8) = g;
    }
    __syncthreads();
    const u64 t0 = *(LDSP u64*)(lds + TS + 3072), t1 = *(LDSP u64*)(lds + TS + 3080);
    const int i0 = 2047 - (int)((t0 >> 32) & 0x7ffull), i1 = 2047 - (int)((t1 >> 32) & 0x7ffull);
    TST(5)
    asm volatile("s_waitcnt vmcnt(0)" ::: "memory");
    LDSP char* wl = lds + 0x0;
    if (r_ < 27) {
        const int cgi = (b & 7) * 27 + r_;
        if (tid < 160) {
            const int t = tid >= 80, c = tid - 80 * t;
            const u32x4 v = __builtin_amdgcn_raw_buffer_load_b128(rsHn, ((t ? i1 : i0) * 640 + c * 8) * 2, 0, 16);
            *(LDSP u32x4*)(lds + TS + 4096 + t * 1280 + c * 16) = v;
        }
        __syncthreads();
        TST(6)
        if (wid < 4) {
            f32x4 acc = {0.f, 0.f, 0.f, 0.f};
            const int gq = lane >> 4, q = (lane & 15) >> 2, p = lane & 3;
#pragma unroll
            for (int k = 0; k < 5; ++k) {
                const int ks = wid * 5 + k;
                const s16x8 a = *(LDSP s16x8*)(lds + TS + 4096 + (lane & 1) * 1280 + (32 * ks + 8 * gq) * 2);
                LDSP char* wp = wl + (p >> 1) * 10240 + (32 * ks + 8 * gq + q) * 16 + (p & 1) * 8;
                const s16x4 b0 = __builtin_amdgcn_ds_read_tr16_b64_v4i16((LDSP s16x4*)wp), b1 = __builtin_amdgcn_ds_read_tr16_b64_v4i16((LDSP s16x4*)(wp + 64));
                const s16x8 bb = __builtin_shufflevector(b0, b1, 0, 1, 2, 3, 4, 5, 6, 7);
                acc = __builtin_amdgcn_mfma_f32_16x16x32_bf16(__builtin_bit_cast(bf16x8_t, a), __builtin_bit_cast(bf16x8_t, bb), acc, 0, 0, 0);
            }
            if (lane < 16) { f32x2 t = {acc[0], acc[1]}; *(LDSP f32x2*)(lds + TS + 8192 + (wid * 16 + lane) * 8) = t; }
        }
        __syncthreads();
        TST(7)
        if (tid < 8) {
            float gu[2][2];
#pragma unroll
            for (int m = 0; m < 2; ++m) { f32x2 t = {0.f, 0.f};
#pragma unroll
                for (int k = 0; k < 4; ++k) t += *(LDSP f32x2*)(lds + TS + 8192 + (k * 16 + m * 8 + tid) * 8);
                gu[m][0] = bf_round(t[0]); gu[m][1] = bf_round(t[1]); }
            float a[2];
#pragma unroll
            for (int t = 0; t < 2; ++t) { const float gg = gu[0][t]; a[t] = bf_round(gg / (1.0f + __expf(-gg))) * gu[1][t]; }
            st_agent(actg + cgi * 8 + tid, ((u64)pk_bf16(a[0], a[1]) << 32) | tag32);
        }
    } else {
        const int dd = (b & 7) * 5 + (r_ - 27);
        unsigned cur = 0;
        if (tid < 32) cur = __builtin_amdgcn_raw_buffer_load_b16(rsOut, ((tid >> 4 ? i1 : i0) * 640 + dd * 16 + (tid & 15)) * 2, 0, 16);
        {
            u64 a[4]; unsigned spins = 0;
            const int nk = wid < 3 ? 4 : 3;
            for (;;) {
                bool ok = true;
#pragma unroll
                for (int k = 0; k < 4; ++k) { a[k] = k < nk ? ld_agent(actg + tid + 512 * k) : (u64)tag32; ok &= (unsigned)a[k] == tag32; }
                if (__all(ok) || ++spins >= SPIN_CAP) break;
                __builtin_amdgcn_s_sleep(1);
            }
#pragma unroll
            for (int k = 0; k < 4; ++k) if (k < nk) {
                const int f = tid + 512 * k;
                *(LDSP unsigned short*)(lds + TS + 4096 + f * 2) = (unsigned short)(a[k] >> 32);
                *(LDSP unsigned short*)(lds + TS + 4096 + 3456 + f * 2) = (unsigned short)(a[k] >> 48);
            }
        }
        __syncthreads();
        TST(6)
        f32x4 acc = {0.f, 0.f, 0.f, 0.f};
        const int gq = lane >> 4, q = (lane & 15) >> 2, p = lane & 3;
#pragma unroll
        for (int k = 0; k < 7; ++k) {
            const int ks = wid + 8 * k;
            if (ks < 54) {
                const s16x8 a = *(LDSP s16x8*)(lds + TS + 4096 + (lane & 1) * 3456 + (32 * ks + 8 * gq) * 2);
                LDSP char* wp = wl + (32 * ks + 8 * gq + q) * 32 + p * 8;
                const s16x4 b0 = __builtin_amdgcn_ds_read_tr16_b64_v4i16((LDSP s16x4*)wp), b1 = __builtin_amdgcn_ds_read_tr16_b64_v4i16((LDSP s16x4*)(wp + 128));
                const s16x8 bb = __builtin_shufflevector(b0, b1, 0, 1, 2, 3, 4, 5, 6, 7);
                acc = __builtin_amdgcn_mfma_f32_16x16x32_bf16(__builtin_bit_cast(bf16x8_t, a), __builtin_bit_cast(bf16x8_t, bb), acc, 0, 0, 0);
            }
        }
        if (lane < 16) { f32x2 t = {acc[0], acc[1]}; *(LDSP f32x2*)(lds + TS + 12288 + (wid * 16 + lane) * 8) = t; }
        __syncthreads();
        TST(7)
        if (tid < 32) {
            const int t = tid >> 4, c = tid & 15;
            float y = 0.f;
#pragma unroll
            for (int k = 0; k < 8; ++k) y += *(LDSP float*)(lds + TS + 12288 + (k * 16 + c) * 8 + t * 4);
            const unsigned kb = (unsigned)((t ? t1 : t0) >> 32) & 0xfffff800u;
            const float r = bf_round(__uint_as_float((kb & 0x80000000u) ? (kb & 0x7fffffffu) : (~kb & 0xfffff800u)));
            const float sg = 1.0f / (1.0f + __expf(-r));
            out[(size_t)(t ? i1 : i0) * 640 + dd * 16 + c] = (unsigned short)(pk_bf16(bf_lo(cur) + y * sg, 0.f) & 0xffffu);
        }
    }
    if (b == 255 && tid == 0) st_agent((u64*)(ws + 0x0), tag);
    TST(8)
}

__global__ __launch_bounds__(512, 2) void k_block(const char* ckv, const char* wq, const char* wuk, const char* wuv, const char* wo,
                                                  const char* wg, const char* wu, const char* wd,
                                                  const unsigned short* x, const unsigned short* wr, char* ws, unsigned short* out) {
    __shared__ __attribute__((aligned(16))) char lds[163840];
    const unsigned b = blockIdx.x, xcd = b & 7, r = b >> 3, h = xcd * 4 + (r & 3), i = r >> 2;
    unsigned tid = threadIdx.x;
    int wid; unsigned taglo, taghi, wo2lo, wo2hi, ws2lo, ws2hi;
    asm volatile(
        "k_block_first_half:\n"
        "v_and_b32 v1, 63, v0\n"
        "v_lshrrev_b32 v2, 6, v0\n"
        "s_nop 0\n"
        "v_readfirstlane_b32 s36, v2\n"
        "v_lshlrev_b32 v0, 4, v1\n"
        "s_mov_b32 s37, %[h]\n"
        "s_mov_b32 s38, %[i]\n"
        "s_lshl_b32 s52, s38, 18\n"
        "s_add_u32 s64, %[ckvlo], s52\n"
        "s_addc_u32 s65, %[ckvhi], 0\n"
        "s_and_b32 s65, s65, 0xffff\n"
        "s_mov_b32 s66, 262144\n"
        "s_mov_b32 s67, 0x20000\n"
        "v_lshrrev_b32 v16, 4, v0\n"
        "v_lshrrev_b32 v17, 3, v16\n"
        "v_and_b32 v18, 7, v16\n"
        "s_lshl_b32 s54, s36, 5\n"
        "v_add_u32 v19, 0, v17\n"
        "v_bfe_u32 v20, v19, 1, 3\n"
        "v_xor_b32 v20, v18, v20\n"
        "v_add_u32 v19, s54, v19\n"
        "v_lshlrev_b32 v19, 10, v19\n"
        "v_lshl_add_u32 v19, v20, 4, v19\n"
        "v_mov_b32 v8, v19\n"
        "v_add_u32 v19, 8, v17\n"
        "v_bfe_u32 v20, v19, 1, 3\n"
        "v_xor_b32 v20, v18, v20\n"
        "v_add_u32 v19, s54, v19\n"
        "v_lshlrev_b32 v19, 10, v19\n"
        "v_lshl_add_u32 v19, v20, 4, v19\n"
        "v_subrev_u32 v9, 1024, v19\n"
        "v_add_u32 v19, 16, v17\n"
        "v_bfe_u32 v20, v19, 1, 3\n"
        "v_xor_b32 v20, v18, v20\n"
        "v_add_u32 v19, s54, v19\n"
        "v_lshlrev_b32 v19, 10, v19\n"
        "v_lshl_add_u32 v19, v20, 4, v19\n"
        "v_subrev_u32 v10, 2048, v19\n"
        "v_add_u32 v19, 24, v17\n"
        "v_bfe_u32 v20, v19, 1, 3\n"
        "v_xor_b32 v20, v18, v20\n"
        "v_add_u32 v19, s54, v19\n"
        "v_lshlrev_b32 v19, 10, v19\n"
        "v_lshl_add_u32 v19, v20, 4, v19\n"
        "v_subrev_u32 v11, 3072, v19\n"
        "s_lshl_b32 s72, s36, 12\n"
        "s_mov_b32 s74, 0\n"
        "s_mov_b32 m0, s72\n"
        "s_nop 0\n"
        "buffer_load_dwordx4 v8, s[64:67], s74 offen lds\n"
        "buffer_load_dwordx4 v9, s[64:67], s74 offen offset:1024 lds\n"
        "buffer_load_dwordx4 v10, s[64:67], s74 offen offset:2048 lds\n"
        "buffer_load_dwordx4 v11, s[64:67], s74 offen offset:3072 lds\n"
        "s_xor_b32 s72, s72, 32768\n"
        "s_add_u32 s74, s74, 128\n"
        "s_mov_b32 s86, %[wslo]\n"
        "s_mov_b32 s87, %[wshi]\n"
        "s_cmp_ge_u32 s36, 4\n"
        "s_cbranch_scc1 KA_NOX_%=\n"
        "s_lshl_b32 s52, %[blk], 3\n"
        "s_lshl_b32 s53, s36, 1\n"
        "s_add_u32 s52, s52, s53\n"
        "s_mul_i32 s53, s52, 1280\n"
        "s_add_u32 s54, s53, 1280\n"
        "s_mov_b32 s92, %[xlo]\n"
        "s_and_b32 s93, %[xhi], 0xffff\n"
        "s_mov_b32 s94, 2621440\n"
        "s_mov_b32 s95, 0x20000\n"
        "v_mov_b32 v112, 0x7ff00000\n"
        "v_add_u32 v113, 1024, v0\n"
        "v_cmp_gt_u32 vcc, 256, v0\n"
        "s_nop 1\n"
        "v_cndmask_b32 v113, v112, v113, vcc\n"
        "buffer_load_dwordx4 v[16:19], v0, s[92:95], s53 offen\n"
        "buffer_load_dwordx4 v[20:23], v113, s[92:95], s53 offen\n"
        "buffer_load_dwordx4 v[24:27], v0, s[92:95], s54 offen\n"
        "buffer_load_dwordx4 v[28:31], v113, s[92:95], s54 offen\n"
        "KA_NOX_%=:\n"
        "v_mov_b32 v124, 0\n"
        "global_load_dwordx2 v[126:127], v124, s[86:87] sc1\n"
        "s_lshl_b32 s52, s37, 8\n"
        "s_sub_u32 s53, 4194304, s52\n"
        "s_cmp_ge_u32 s36, 4\n"
        "s_cselect_b32 s54, %[wuvlo], %[wuklo]\n"
        "s_cselect_b32 s55, %[wuvhi], %[wukhi]\n"
        "s_add_u32 s68, s54, s52\n"
        "s_addc_u32 s69, s55, 0\n"
        "s_and_b32 s69, s69, 0xffff\n"
        "s_mov_b32 s70, s53\n"
        "s_mov_b32 s71, 0x20000\n"
        "v_lshrrev_b32 v32, 4, v0\n"
        "v_lshrrev_b32 v33, 4, v32\n"
        "v_and_b32 v34, 15, v32\n"
        "s_and_b32 s55, s36, 3\n"
        "s_lshl_b32 s54, s55, 4\n"
        "v_or_b32 v36, 0, v33\n"
        "v_lshlrev_b32 v36, 1, v36\n"
        "v_xor_b32 v36, v34, v36\n"
        "v_add_u32 v35, 0, v33\n"
        "v_add_u32 v35, s54, v35\n"
        "v_lshlrev_b32 v35, 13, v35\n"
        "v_lshl_add_u32 v35, v36, 4, v35\n"
        "v_mov_b32 v12, v35\n"
        "v_or_b32 v36, 0, v33\n"
        "v_lshlrev_b32 v36, 1, v36\n"
        "v_xor_b32 v36, v34, v36\n"
        "v_add_u32 v35, 4, v33\n"
        "v_add_u32 v35, s54, v35\n"
        "v_lshlrev_b32 v35, 13, v35\n"
        "v_lshl_add_u32 v35, v36, 4, v35\n"
        "v_subrev_u32 v13, 1024, v35\n"
        "v_or_b32 v36, 4, v33\n"
        "v_lshlrev_b32 v36, 1, v36\n"
        "v_xor_b32 v36, v34, v36\n"
        "v_add_u32 v35, 8, v33\n"
        "v_add_u32 v35, s54, v35\n"
        "v_lshlrev_b32 v35, 13, v35\n"
        "v_lshl_add_u32 v35, v36, 4, v35\n"
        "v_subrev_u32 v14, 2048, v35\n"
        "v_or_b32 v36, 4, v33\n"
        "v_lshlrev_b32 v36, 1, v36\n"
        "v_xor_b32 v36, v34, v36\n"
        "v_add_u32 v35, 12, v33\n"
        "v_add_u32 v35, s54, v35\n"
        "v_lshlrev_b32 v35, 13, v35\n"
        "v_lshl_add_u32 v35, v36, 4, v35\n"
        "v_subrev_u32 v15, 3072, v35\n"
        "s_lshl_b32 s73, s36, 12\n"
        "s_add_u32 s73, s73, 65536\n"
        "s_mov_b32 s75, 0\n"
        "s_mov_b32 s80, 0\n"
        "s_add_u32 m0, s73, 0\n"
        "s_nop 0\n"
        "buffer_load_dwordx4 v12, s[68:71], s75 offen lds\n"
        "buffer_load_dwordx4 v13, s[68:71], s75 offen offset:1024 lds\n"
        "buffer_load_dwordx4 v14, s[68:71], s75 offen offset:2048 lds\n"
        "buffer_load_dwordx4 v15, s[68:71], s75 offen offset:3072 lds\n"
        "s_add_u32 s75, s75, 0x80000\n"
        "s_mov_b32 m0, s72\n"
        "s_nop 0\n"
        "buffer_load_dwordx4 v8, s[64:67], s74 offen lds\n"
        "buffer_load_dwordx4 v9, s[64:67], s74 offen offset:1024 lds\n"
        "buffer_load_dwordx4 v10, s[64:67], s74 offen offset:2048 lds\n"
        "buffer_load_dwordx4 v11, s[64:67], s74 offen offset:3072 lds\n"
        "s_xor_b32 s72, s72, 32768\n"
        "s_add_u32 s74, s74, 128\n"
        "s_add_u32 m0, s73, 32768\n"
        "s_nop 0\n"
        "buffer_load_dwordx4 v12, s[68:71], s75 offen lds\n"
        "buffer_load_dwordx4 v13, s[68:71], s75 offen offset:1024 lds\n"
        "buffer_load_dwordx4 v14, s[68:71], s75 offen offset:2048 lds\n"
        "buffer_load_dwordx4 v15, s[68:71], s75 offen offset:3072 lds\n"
        "s_add_u32 s75, s75, 0x80000\n"
        "s_add_u32 m0, s73, 65536\n"
        "s_nop 0\n"
        "buffer_load_dwordx4 v12, s[68:71], s75 offen lds\n"
        "buffer_load_dwordx4 v13, s[68:71], s75 offen offset:1024 lds\n"
        "buffer_load_dwordx4 v14, s[68:71], s75 offen offset:2048 lds\n"
        "buffer_load_dwordx4 v15, s[68:71], s75 offen offset:3072 lds\n"
        "s_add_u32 s75, s75, 0x80000\n"
        "s_mov_b32 s82, 0\n"
        "s_lshl_b32 s52, s37, 19\n"
        "s_add_u32 s52, s52, 4194304\n"
        "s_add_u32 s40, s86, s52\n"
        "s_addc_u32 s41, s87, 0\n"
        "s_and_b32 s41, s41, 0xffff\n"
        "s_mov_b32 s42, 524288\n"
        "s_mov_b32 s43, 0x20000\n"
        "s_lshl_b32 s52, s37, 19\n"
        "s_add_u32 s52, s52, 20971520\n"
        "s_add_u32 s44, s86, s52\n"
        "s_addc_u32 s45, s87, 0\n"
        "s_and_b32 s45, s45, 0xffff\n"
        "s_mov_b32 s46, 524288\n"
        "s_mov_b32 s47, 0x20000\n"
        "v_lshrrev_b32 v32, 4, v0\n"
        "s_and_b32 s52, s36, 1\n"
        "s_lshr_b32 s53, s36, 1\n"
        "v_and_b32 v33, 15, v32\n"
        "v_lshrrev_b32 v34, 4, v32\n"
        "v_lshrrev_b32 v35, 1, v33\n"
        "v_xor_b32 v35, v34, v35\n"
        "v_lshlrev_b32 v35, 4, v35\n"
        "v_lshl_add_u32 v35, v33, 7, v35\n"
        "s_lshl_b32 s54, s52, 14\n"
        "v_add_u32 v1, s54, v35\n"
        "v_xor_b32 v2, 64, v1\n"
        "v_bfe_u32 v33, v32, 2, 2\n"
        "v_and_b32 v35, 1, v34\n"
        "v_lshl_or_b32 v35, v35, 2, v33\n"
        "v_lshlrev_b32 v35, 1, v35\n"
        "v_bfe_u32 v36, v32, 1, 1\n"
        "s_and_b32 s54, s53, 1\n"
        "s_lshl_b32 s54, s54, 3\n"
        "v_or_b32 v36, s54, v36\n"
        "v_xor_b32 v35, v35, v36\n"
        "v_lshlrev_b32 v35, 4, v35\n"
        "v_and_b32 v36, 1, v32\n"
        "v_lshl_add_u32 v35, v36, 3, v35\n"
        "v_lshl_add_u32 v33, v34, 3, v33\n"
        "v_lshl_add_u32 v35, v33, 8, v35\n"
        "s_lshr_b32 s54, s53, 1\n"
        "s_lshl_b32 s54, s54, 14\n"
        "s_add_u32 s54, s54, 65536\n"
        "v_add_u32 v3, s54, v35\n"
        "v_xor_b32 v4, 32, v3\n"
        "v_xor_b32 v5, 64, v3\n"
        "v_xor_b32 v6, 96, v3\n"
        "s_cmp_ge_u32 s36, 4\n"
        "s_cbranch_scc1 KA_V_%=\n"
        "s_lshl_b32 s52, s37, 12\n"
        "s_sub_u32 s53, 0x1000000, s52\n"
        "s_add_u32 s52, s52, 37748736\n"
        "s_add_u32 s48, s86, s52\n"
        "s_addc_u32 s49, s87, 0\n"
        "s_and_b32 s49, s49, 0xffff\n"
        "s_mov_b32 s50, s53\n"
        "s_mov_b32 s51, 0x20000\n"
        "s_lshr_b32 s52, %[blk], 3\n"
        "s_mul_i32 s52, s52, 20\n"
        "s_mul_i32 s53, s36, 5\n"
        "s_add_u32 s52, s52, s53\n"
        "v_lshrrev_b32 v116, 7, v0\n"
        "v_add_u32 v116, s52, v116\n"
        "v_lshlrev_b32 v116, 13, v116\n"
        "v_bfe_u32 v117, v0, 4, 3\n"
        "v_lshl_add_u32 v116, v117, 7, v116\n"
        "s_lshr_b32 s53, s37, 2\n"
        "s_lshl_b32 s53, s53, 10\n"
        "v_add_u32 v116, s53, v116\n"
        "v_cmp_gt_u32 vcc, 640, v0\n"
        "v_mov_b32 v117, 0x7ff00000\n"
        "s_nop 0\n"
        "v_cndmask_b32 v116, v117, v116, vcc\n"
        "s_mov_b32 s92, %[wqlo]\n"
        "s_and_b32 s93, %[wqhi], 0xffff\n"
        "s_mov_b32 s94, 5242880\n"
        "s_mov_b32 s95, 0x20000\n"
        "buffer_load_dword v7, v116, s[92:95], 0 offen\n"
        "s_getpc_b64 s[92:93]\n"
        "KA_PCREFK_%=:\n"
        "s_sub_u32 s92, s92, KA_PCREFK_%=-k_block_first_half\n"
        "s_subb_u32 s93, s93, 0\n"
        "s_and_b32 s93, s93, 0xffff\n"
        "s_mov_b32 s94, k_block_second_half-k_block_first_half\n"
        "s_mov_b32 s95, 0x20000\n"
        "s_lshl_b32 s52, s36, 13\n"
        "v_lshlrev_b32 v119, 3, v0\n"
        "buffer_load_dword v7, v119, s[92:95], s52 offen\n"
        "s_waitcnt vmcnt(14)\n"
        "s_barrier\n"
        "ds_read_b64_tr_b16 v[64:65], v3 offset:0\n"
        "ds_read_b64_tr_b16 v[66:67], v3 offset:1024\n"
        "ds_read_b64_tr_b16 v[68:69], v3 offset:8192\n"
        "ds_read_b64_tr_b16 v[70:71], v3 offset:9216\n"
        "ds_read_b64_tr_b16 v[72:73], v4 offset:0\n"
        "ds_read_b64_tr_b16 v[74:75], v4 offset:1024\n"
        "ds_read_b64_tr_b16 v[76:77], v4 offset:8192\n"
        "ds_read_b64_tr_b16 v[78:79], v4 offset:9216\n"
        "ds_read_b64_tr_b16 v[80:81], v5 offset:0\n"
        "ds_read_b64_tr_b16 v[82:83], v5 offset:1024\n"
        "ds_read_b64_tr_b16 v[84:85], v5 offset:8192\n"
        "ds_read_b64_tr_b16 v[86:87], v5 offset:9216\n"
        "ds_read_b64_tr_b16 v[88:89], v6 offset:0\n"
        "ds_read_b64_tr_b16 v[90:91], v6 offset:1024\n"
        "ds_read_b64_tr_b16 v[92:93], v6 offset:8192\n"
        "ds_read_b64_tr_b16 v[94:95], v6 offset:9216\n"
        "ds_read_b128 v[32:35], v1 offset:0\n"
        "ds_read_b128 v[36:39], v2 offset:0\n"
        "ds_read_b128 v[40:43], v1 offset:2048\n"
        "ds_read_b128 v[44:47], v2 offset:2048\n"
        "ds_read_b128 v[48:51], v1 offset:4096\n"
        "ds_read_b128 v[52:55], v2 offset:4096\n"
        "s_waitcnt lgkmcnt(0)\n"
        "s_setprio 0\n"
        "v_mfma_f32_16x16x32_bf16 a[0:3], v[64:67], v[32:35], 0\n"
        "ds_read_b128 v[56:59], v1 offset:6144\n"
        "ds_read_b128 v[60:63], v2 offset:6144\n"
        "v_readfirstlane_b32 s96, v126\n"
        "v_readfirstlane_b32 s97, v127\n"
        "v_mfma_f32_16x16x32_bf16 a[0:3], v[68:71], v[36:39], a[0:3]\n"
        "s_nop 1\n"
        "s_add_u32 s96, s96, 1\n"
        "s_addc_u32 s97, s97, 0\n"
        "v_lshlrev_b32 v96, 16, v16\n"
        "v_mfma_f32_16x16x32_bf16 a[4:7], v[72:75], v[32:35], 0\n"
        "v_and_b32 v97, 0xffff0000, v16\n"
        "v_lshlrev_b32 v98, 16, v17\n"
        "v_and_b32 v99, 0xffff0000, v17\n"
        "v_lshlrev_b32 v100, 16, v18\n"
        "v_mfma_f32_16x16x32_bf16 a[4:7], v[76:79], v[36:39], a[4:7]\n"
        "v_and_b32 v101, 0xffff0000, v18\n"
        "v_lshlrev_b32 v102, 16, v19\n"
        "v_and_b32 v103, 0xffff0000, v19\n"
        "v_lshlrev_b32 v104, 16, v20\n"
        "v_mfma_f32_16x16x32_bf16 a[8:11], v[80:83], v[32:35], 0\n"
        "v_and_b32 v105, 0xffff0000, v20\n"
        "v_lshlrev_b32 v106, 16, v21\n"
        "v_and_b32 v107, 0xffff0000, v21\n"
        "v_lshlrev_b32 v108, 16, v22\n"
        "v_mfma_f32_16x16x32_bf16 a[8:11], v[84:87], v[36:39], a[8:11]\n"
        "v_and_b32 v109, 0xffff0000, v22\n"
        "v_lshlrev_b32 v110, 16, v23\n"
        "v_and_b32 v111, 0xffff0000, v23\n"
        "v_add_f32 v112, v96, v97\n"
        "v_mfma_f32_16x16x32_bf16 a[12:15], v[88:91], v[32:35], 0\n"
        "v_add_f32 v112, v112, v98\n"
        "v_add_f32 v112, v112, v99\n"
        "v_add_f32 v112, v112, v100\n"
        "v_add_f32 v112, v112, v101\n"
        "v_mfma_f32_16x16x32_bf16 a[12:15], v[92:95], v[36:39], a[12:15]\n"
        "v_add_f32 v112, v112, v102\n"
        "v_add_f32 v112, v112, v103\n"
        "v_add_f32 v112, v112, v104\n"
        "v_add_f32 v112, v112, v105\n"
        "s_setprio 1\n"
        "v_mfma_f32_16x16x32_bf16 a[16:19], v[64:67], v[40:43], 0\n"
        "ds_read_b128 v[32:35], v1 offset:8192\n"
        "ds_read_b128 v[36:39], v2 offset:8192\n"
        "v_add_f32 v112, v112, v106\n"
        "v_add_f32 v112, v112, v107\n"
        "v_mfma_f32_16x16x32_bf16 a[16:19], v[68:71], v[44:47], a[16:19]\n"
        "v_add_f32 v112, v112, v108\n"
        "v_add_f32 v112, v112, v109\n"
        "v_add_f32 v112, v112, v110\n"
        "v_add_f32 v112, v112, v111\n"
        "v_mfma_f32_16x16x32_bf16 a[20:23], v[72:75], v[40:43], 0\n"
        "s_nop 0\n"
        "v_add_f32_dpp v112, v112, v112 row_ror:1 row_mask:0xf bank_mask:0xf\n"
        "s_nop 1\n"
        "v_add_f32_dpp v112, v112, v112 row_ror:2 row_mask:0xf bank_mask:0xf\n"
        "s_nop 1\n"
        "v_add_f32_dpp v112, v112, v112 row_ror:4 row_mask:0xf bank_mask:0xf\n"
        "s_nop 1\n"
        "v_add_f32_dpp v112, v112, v112 row_ror:8 row_mask:0xf bank_mask:0xf\n"
        "v_mfma_f32_16x16x32_bf16 a[20:23], v[76:79], v[44:47], a[20:23]\n"
        "v_mov_b32 v113, v112\n"
        "s_nop 1\n"
        "v_permlane16_swap_b32 v112, v113\n"
        "v_add_f32 v112, v112, v113\n"
        "v_mov_b32 v113, v112\n"
        "v_mfma_f32_16x16x32_bf16 a[24:27], v[80:83], v[40:43], 0\n"
        "s_nop 0\n"
        "v_permlane32_swap_b32 v112, v113\n"
        "v_add_f32 v112, v112, v113\n"
        "v_mul_f32 v112, 0x3acccccd, v112\n"
        "v_cmp_gt_u32 vcc, 256, v0\n"
        "v_mfma_f32_16x16x32_bf16 a[24:27], v[84:87], v[44:47], a[24:27]\n"
        "v_mov_b32 v114, 0\n"
        "v_mov_b32 v116, 1.0\n"
        "v_cndmask_b32 v114, v114, v116, vcc\n"
        "v_sub_f32 v96, v96, v112\n"
        "v_mfma_f32_16x16x32_bf16 a[28:31], v[88:91], v[40:43], 0\n"
        "v_sub_f32 v97, v97, v112\n"
        "v_sub_f32 v98, v98, v112\n"
        "v_sub_f32 v99, v99, v112\n"
        "v_sub_f32 v100, v100, v112\n"
        "v_mfma_f32_16x16x32_bf16 a[28:31], v[92:95], v[44:47], a[28:31]\n"
        "v_sub_f32 v101, v101, v112\n"
        "v_sub_f32 v102, v102, v112\n"
        "v_sub_f32 v103, v103, v112\n"
        "v_sub_f32 v104, v104, v112\n"
        "s_setprio 0\n"
        "v_mfma_f32_16x16x32_bf16 a[32:35], v[64:67], v[48:51], 0\n"
        "ds_read_b128 v[40:43], v1 offset:10240\n"
        "ds_read_b128 v[44:47], v2 offset:10240\n"
        "v_sub_f32 v105, v105, v112\n"
        "v_sub_f32 v106, v106, v112\n"
        "v_sub_f32 v107, v107, v112\n"
        "v_mfma_f32_16x16x32_bf16 a[32:35], v[68:71], v[52:55], a[32:35]\n"
        "v_sub_f32 v108, v108, v112\n"
        "v_sub_f32 v109, v109, v112\n"
        "v_sub_f32 v110, v110, v112\n"
        "v_sub_f32 v111, v111, v112\n"
        "v_mul_f32 v104, v104, v114\n"
        "v_mfma_f32_16x16x32_bf16 a[36:39], v[72:75], v[48:51], 0\n"
        "v_mul_f32 v105, v105, v114\n"
        "v_mul_f32 v106, v106, v114\n"
        "v_mul_f32 v107, v107, v114\n"
        "v_mul_f32 v108, v108, v114\n"
        "v_mul_f32 v109, v109, v114\n"
        "v_mfma_f32_16x16x32_bf16 a[36:39], v[76:79], v[52:55], a[36:39]\n"
        "v_mul_f32 v110, v110, v114\n"
        "v_mul_f32 v111, v111, v114\n"
        "v_mul_f32 v113, v96, v96\n"
        "v_fmac_f32 v113, v97, v97\n"
        "v_fmac_f32 v113, v98, v98\n"
        "v_mfma_f32_16x16x32_bf16 a[40:43], v[80:83], v[48:51], 0\n"
        "v_fmac_f32 v113, v99, v99\n"
        "v_fmac_f32 v113, v100, v100\n"
        "v_fmac_f32 v113, v101, v101\n"
        "v_fmac_f32 v113, v102, v102\n"
        "v_fmac_f32 v113, v103, v103\n"
        "v_mfma_f32_16x16x32_bf16 a[40:43], v[84:87], v[52:55], a[40:43]\n"
        "v_fmac_f32 v113, v104, v104\n"
        "v_fmac_f32 v113, v105, v105\n"
        "v_fmac_f32 v113, v106, v106\n"
        "v_fmac_f32 v113, v107, v107\n"
        "v_fmac_f32 v113, v108, v108\n"
        "v_mfma_f32_16x16x32_bf16 a[44:47], v[88:91], v[48:51], 0\n"
        "v_fmac_f32 v113, v109, v109\n"
        "v_fmac_f32 v113, v110, v110\n"
        "v_fmac_f32 v113, v111, v111\n"
        "v_mfma_f32_16x16x32_bf16 a[44:47], v[92:95], v[52:55], a[44:47]\n"
        "s_setprio 1\n"
        "s_waitcnt lgkmcnt(0)\n"
        "v_mfma_f32_16x16x32_bf16 a[48:51], v[64:67], v[56:59], 0\n"
        "ds_read_b128 v[48:51], v1 offset:12288\n"
        "ds_read_b128 v[52:55], v2 offset:12288\n"
        "v_add_f32_dpp v113, v113, v113 row_ror:1 row_mask:0xf bank_mask:0xf\n"
        "s_nop 1\n"
        "v_add_f32_dpp v113, v113, v113 row_ror:2 row_mask:0xf bank_mask:0xf\n"
        "v_mfma_f32_16x16x32_bf16 a[48:51], v[68:71], v[60:63], a[48:51]\n"
        "s_nop 0\n"
        "v_add_f32_dpp v113, v113, v113 row_ror:4 row_mask:0xf bank_mask:0xf\n"
        "s_nop 1\n"
        "v_add_f32_dpp v113, v113, v113 row_ror:8 row_mask:0xf bank_mask:0xf\n"
        "v_mov_b32 v115, v113\n"
        "s_nop 1\n"
        "v_permlane16_swap_b32 v113, v115\n"
        "v_mfma_f32_16x16x32_bf16 a[52:55], v[72:75], v[56:59], 0\n"
        "v_add_f32 v113, v113, v115\n"
        "v_mov_b32 v115, v113\n"
        "s_nop 1\n"
        "v_permlane32_swap_b32 v113, v115\n"
        "v_add_f32 v113, v113, v115\n"
        "v_mfma_f32_16x16x32_bf16 a[52:55], v[76:79], v[60:63], a[52:55]\n"
        "v_mov_b32 v115, 0x3727c5ac\n"
        "v_fmac_f32 v115, 0x3acccccd, v113\n"
        "v_rsq_f32 v115, v115\n"
        "v_mov_b32 v116, 0x7ff00000\n"
        "v_mfma_f32_16x16x32_bf16 a[56:59], v[80:83], v[56:59], 0\n"
        "v_mul_f32 v115, 0x3e0293ee, v115\n"
        "v_mul_f32 v96, v96, v115\n"
        "v_mul_f32 v97, v97, v115\n"
        "v_mul_f32 v98, v98, v115\n"
        "v_mfma_f32_16x16x32_bf16 a[56:59], v[84:87], v[60:63], a[56:59]\n"
        "v_mul_f32 v99, v99, v115\n"
        "v_mul_f32 v100, v100, v115\n"
        "v_mul_f32 v101, v101, v115\n"
        "v_mul_f32 v102, v102, v115\n"
        "v_mfma_f32_16x16x32_bf16 a[60:63], v[88:91], v[56:59], 0\n"
        "v_mul_f32 v103, v103, v115\n"
        "v_mul_f32 v104, v104, v115\n"
        "v_mul_f32 v105, v105, v115\n"
        "v_mul_f32 v106, v106, v115\n"
        "v_mfma_f32_16x16x32_bf16 a[60:63], v[92:95], v[60:63], a[60:63]\n"
        "v_mul_f32 v107, v107, v115\n"
        "v_mul_f32 v108, v108, v115\n"
        "v_mul_f32 v109, v109, v115\n"
        "v_mul_f32 v110, v110, v115\n"
        "s_setprio 0\n"
        "v_mfma_f32_16x16x32_bf16 a[64:67], v[64:67], v[32:35], 0\n"
        "ds_read_b128 v[56:59], v1 offset:14336\n"
        "ds_read_b128 v[60:63], v2 offset:14336\n"
        "v_mul_f32 v111, v111, v115\n"
        "v_cvt_pk_bf16_f32 v16, v96, v97\n"
        "v_cvt_pk_bf16_f32 v17, v98, v99\n"
        "v_mfma_f32_16x16x32_bf16 a[64:67], v[68:71], v[36:39], a[64:67]\n"
        "v_cvt_pk_bf16_f32 v18, v100, v101\n"
        "v_cvt_pk_bf16_f32 v19, v102, v103\n"
        "v_cvt_pk_bf16_f32 v20, v104, v105\n"
        "v_cvt_pk_bf16_f32 v21, v106, v107\n"
        "v_cvt_pk_bf16_f32 v22, v108, v109\n"
        "v_mfma_f32_16x16x32_bf16 a[68:71], v[72:75], v[32:35], 0\n"
        "v_cvt_pk_bf16_f32 v23, v110, v111\n"
        "s_lshl_b32 s52, %[blk], 3\n"
        "s_lshl_b32 s53, s36, 1\n"
        "s_add_u32 s52, s52, s53\n"
        "s_lshr_b32 s53, s52, 4\n"
        "v_mfma_f32_16x16x32_bf16 a[68:71], v[76:79], v[36:39], a[68:71]\n"
        "s_mul_i32 s53, s53, 20480\n"
        "s_and_b32 s52, s52, 15\n"
        "s_lshl_b32 s52, s52, 4\n"
        "s_add_u32 s53, s53, s52\n"
        "v_lshrrev_b32 v112, 6, v0\n"
        "v_mfma_f32_16x16x32_bf16 a[72:75], v[80:83], v[32:35], 0\n"
        "v_lshlrev_b32 v112, 10, v112\n"
        "v_bfe_u32 v113, v0, 4, 2\n"
        "v_lshl_add_u32 v112, v113, 8, v112\n"
        "v_add_u32 v113, 0x4000, v112\n"
        "v_cmp_gt_u32 vcc, 256, v0\n"
        "v_mfma_f32_16x16x32_bf16 a[72:75], v[84:87], v[36:39], a[72:75]\n"
        "s_add_u32 s92, s86, 262144\n"
        "s_addc_u32 s93, s87, 0\n"
        "v_cndmask_b32 v113, v116, v113, vcc\n"
        "s_and_b32 s93, s93, 0xffff\n"
        "s_mov_b32 s94, 2621440\n"
        "v_mfma_f32_16x16x32_bf16 a[76:79], v[88:91], v[32:35], 0\n"
        "s_mov_b32 s95, 0x20000\n"
        "buffer_store_dwordx4 v[16:19], v112, s[92:95], s53 offen sc1\n"
        "buffer_store_dwordx4 v[20:23], v113, s[92:95], s53 offen sc1\n"
        "v_mfma_f32_16x16x32_bf16 a[76:79], v[92:95], v[36:39], a[76:79]\n"
        "s_waitcnt vmcnt(8) lgkmcnt(0)\n"
        "s_barrier\n"
        "s_mov_b32 s84, s80\n"
        "s_add_u32 s80, s80, 32768\n"
        "s_cmp_eq_u32 s80, 98304\n"
        "s_cselect_b32 s80, 0, s80\n"
        "s_sub_u32 s85, s80, s84\n"
        "v_xor_b32 v1, 32768, v1\n"
        "v_xor_b32 v2, 32768, v2\n"
        "v_add_u32 v3, s85, v3\n"
        "v_add_u32 v4, s85, v4\n"
        "v_add_u32 v5, s85, v5\n"
        "v_add_u32 v6, s85, v6\n"
        "s_setprio 1\n"
        "v_mfma_f32_16x16x32_bf16 a[80:83], v[64:67], v[40:43], 0\n"
        "s_mov_b32 m0, s72\n"
        "s_nop 0\n"
        "buffer_load_dwordx4 v8, s[64:67], s74 offen lds\n"
        "v_mfma_f32_16x16x32_bf16 a[80:83], v[68:71], v[44:47], a[80:83]\n"
        "ds_read_b128 v[32:35], v1 offset:0\n"
        "v_mfma_f32_16x16x32_bf16 a[84:87], v[72:75], v[40:43], 0\n"
        "ds_read_b128 v[36:39], v2 offset:0\n"
        "v_mfma_f32_16x16x32_bf16 a[84:87], v[76:79], v[44:47], a[84:87]\n"
        "ds_read_b64_tr_b16 v[96:97], v3 offset:0\n"
        "v_mfma_f32_16x16x32_bf16 a[88:91], v[80:83], v[40:43], 0\n"
        "ds_read_b64_tr_b16 v[98:99], v3 offset:1024\n"
        "v_mfma_f32_16x16x32_bf16 a[88:91], v[84:87], v[44:47], a[88:91]\n"
        "ds_read_b64_tr_b16 v[100:101], v3 offset:8192\n"
        "v_mfma_f32_16x16x32_bf16 a[92:95], v[88:91], v[40:43], 0\n"
        "ds_read_b64_tr_b16 v[102:103], v3 offset:9216\n"
        "v_mfma_f32_16x16x32_bf16 a[92:95], v[92:95], v[44:47], a[92:95]\n"
        "ds_read_b64_tr_b16 v[104:105], v4 offset:0\n"
        "s_setprio 0\n"
        "v_mfma_f32_16x16x32_bf16 a[96:99], v[64:67], v[48:51], 0\n"
        "buffer_load_dwordx4 v9, s[64:67], s74 offen offset:1024 lds\n"
        "v_mfma_f32_16x16x32_bf16 a[96:99], v[68:71], v[52:55], a[96:99]\n"
        "ds_read_b128 v[40:43], v1 offset:2048\n"
        "v_mfma_f32_16x16x32_bf16 a[100:103], v[72:75], v[48:51], 0\n"
        "ds_read_b128 v[44:47], v2 offset:2048\n"
        "v_mfma_f32_16x16x32_bf16 a[100:103], v[76:79], v[52:55], a[100:103]\n"
        "ds_read_b64_tr_b16 v[106:107], v4 offset:1024\n"
        "v_mfma_f32_16x16x32_bf16 a[104:107], v[80:83], v[48:51], 0\n"
        "ds_read_b64_tr_b16 v[108:109], v4 offset:8192\n"
        "v_mfma_f32_16x16x32_bf16 a[104:107], v[84:87], v[52:55], a[104:107]\n"
        "ds_read_b64_tr_b16 v[110:111], v4 offset:9216\n"
        "v_mfma_f32_16x16x32_bf16 a[108:111], v[88:91], v[48:51], 0\n"
        "ds_read_b64_tr_b16 v[112:113], v5 offset:0\n"
        "v_mfma_f32_16x16x32_bf16 a[108:111], v[92:95], v[52:55], a[108:111]\n"
        "ds_read_b64_tr_b16 v[114:115], v5 offset:1024\n"
        "s_setprio 1\n"
        "v_mfma_f32_16x16x32_bf16 a[112:115], v[64:67], v[56:59], 0\n"
        "buffer_load_dwordx4 v10, s[64:67], s74 offen offset:2048 lds\n"
        "ds_read_b128 v[48:51], v1 offset:4096\n"
        "v_mfma_f32_16x16x32_bf16 a[112:115], v[68:71], v[60:63], a[112:115]\n"
        "ds_read_b128 v[52:55], v2 offset:4096\n"
        "ds_read_b64_tr_b16 v[116:117], v5 offset:8192\n"
        "v_mfma_f32_16x16x32_bf16 a[116:119], v[72:75], v[56:59], 0\n"
        "ds_read_b64_tr_b16 v[118:119], v5 offset:9216\n"
        "buffer_load_dwordx4 v11, s[64:67], s74 offen offset:3072 lds\n"
        "s_xor_b32 s72, s72, 32768\n"
        "s_add_u32 s74, s74, 128\n"
        "v_mfma_f32_16x16x32_bf16 a[116:119], v[76:79], v[60:63], a[116:119]\n"
        "ds_read_b64_tr_b16 v[120:121], v6 offset:0\n"
        "ds_read_b64_tr_b16 v[122:123], v6 offset:1024\n"
        "v_mfma_f32_16x16x32_bf16 a[120:123], v[80:83], v[56:59], 0\n"
        "ds_read_b64_tr_b16 v[124:125], v6 offset:8192\n"
        "ds_read_b64_tr_b16 v[126:127], v6 offset:9216\n"
        "v_mfma_f32_16x16x32_bf16 a[120:123], v[84:87], v[60:63], a[120:123]\n"
        "v_mfma_f32_16x16x32_bf16 a[124:127], v[88:91], v[56:59], 0\n"
        "v_mfma_f32_16x16x32_bf16 a[124:127], v[92:95], v[60:63], a[124:127]\n"
        "s_setprio 0\n"
        "s_waitcnt lgkmcnt(8)\n"
        "v_mfma_f32_16x16x32_bf16 a[0:3], v[96:99], v[32:35], a[0:3]\n"
        "s_add_u32 m0, s73, s84\n"
        "s_nop 0\n"
        "buffer_load_dwordx4 v12, s[68:71], s75 offen lds\n"
        "ds_read_b128 v[56:59], v1 offset:6144\n"
        "ds_read_b128 v[60:63], v2 offset:6144\n"
        "v_lshlrev_b32 v64, 16, v24\n"
        "v_mfma_f32_16x16x32_bf16 a[0:3], v[100:103], v[36:39], a[0:3]\n"
        "v_and_b32 v65, 0xffff0000, v24\n"
        "v_lshlrev_b32 v66, 16, v25\n"
        "v_and_b32 v67, 0xffff0000, v25\n"
        "v_lshlrev_b32 v68, 16, v26\n"
        "v_mfma_f32_16x16x32_bf16 a[4:7], v[104:107], v[32:35], a[4:7]\n"
        "v_and_b32 v69, 0xffff0000, v26\n"
        "v_lshlrev_b32 v70, 16, v27\n"
        "v_and_b32 v71, 0xffff0000, v27\n"
        "v_lshlrev_b32 v72, 16, v28\n"
        "v_mfma_f32_16x16x32_bf16 a[4:7], v[108:111], v[36:39], a[4:7]\n"
        "v_and_b32 v73, 0xffff0000, v28\n"
        "v_lshlrev_b32 v74, 16, v29\n"
        "v_and_b32 v75, 0xffff0000, v29\n"
        "v_lshlrev_b32 v76, 16, v30\n"
        "v_mfma_f32_16x16x32_bf16 a[8:11], v[112:115], v[32:35], a[8:11]\n"
        "v_and_b32 v77, 0xffff0000, v30\n"
        "v_lshlrev_b32 v78, 16, v31\n"
        "v_and_b32 v79, 0xffff0000, v31\n"
        "v_add_f32 v80, v64, v65\n"
        "s_waitcnt lgkmcnt(0)\n"
        "v_mfma_f32_16x16x32_bf16 a[8:11], v[116:119], v[36:39], a[8:11]\n"
        "v_add_f32 v80, v80, v66\n"
        "v_add_f32 v80, v80, v67\n"
        "v_add_f32 v80, v80, v68\n"
        "v_add_f32 v80, v80, v69\n"
        "v_mfma_f32_16x16x32_bf16 a[12:15], v[120:123], v[32:35], a[12:15]\n"
        "v_add_f32 v80, v80, v70\n"
        "v_add_f32 v80, v80, v71\n"
        "v_add_f32 v80, v80, v72\n"
        "v_add_f32 v80, v80, v73\n"
        "v_mfma_f32_16x16x32_bf16 a[12:15], v[124:127], v[36:39], a[12:15]\n"
        "v_add_f32 v80, v80, v74\n"
        "v_add_f32 v80, v80, v75\n"
        "v_add_f32 v80, v80, v76\n"
        "v_add_f32 v80, v80, v77\n"
        "s_setprio 1\n"
        "v_mfma_f32_16x16x32_bf16 a[16:19], v[96:99], v[40:43], a[16:19]\n"
        "buffer_load_dwordx4 v13, s[68:71], s75 offen offset:1024 lds\n"
        "ds_read_b128 v[32:35], v1 offset:8192\n"
        "ds_read_b128 v[36:39], v2 offset:8192\n"
        "v_add_f32 v80, v80, v78\n"
        "v_add_f32 v80, v80, v79\n"
        "v_mfma_f32_16x16x32_bf16 a[16:19], v[100:103], v[44:47], a[16:19]\n"
        "s_nop 0\n"
        "v_add_f32_dpp v80, v80, v80 row_ror:1 row_mask:0xf bank_mask:0xf\n"
        "s_nop 1\n"
        "v_add_f32_dpp v80, v80, v80 row_ror:2 row_mask:0xf bank_mask:0xf\n"
        "s_nop 1\n"
        "v_add_f32_dpp v80, v80, v80 row_ror:4 row_mask:0xf bank_mask:0xf\n"
        "s_nop 1\n"
        "v_add_f32_dpp v80, v80, v80 row_ror:8 row_mask:0xf bank_mask:0xf\n"
        "v_mov_b32 v81, v80\n"
        "v_mfma_f32_16x16x32_bf16 a[20:23], v[104:107], v[40:43], a[20:23]\n"
        "s_nop 0\n"
        "v_permlane16_swap_b32 v80, v81\n"
        "v_add_f32 v80, v80, v81\n"
        "v_mov_b32 v81, v80\n"
        "s_nop 1\n"
        "v_permlane32_swap_b32 v80, v81\n"
        "v_add_f32 v80, v80, v81\n"
        "v_mfma_f32_16x16x32_bf16 a[20:23], v[108:111], v[44:47], a[20:23]\n"
        "v_mul_f32 v80, 0x3acccccd, v80\n"
        "v_cmp_gt_u32 vcc, 256, v0\n"
        "v_mov_b32 v82, 0\n"
        "v_mov_b32 v84, 1.0\n"
        "v_cndmask_b32 v82, v82, v84, vcc\n"
        "v_mfma_f32_16x16x32_bf16 a[24:27], v[112:115], v[40:43], a[24:27]\n"
        "v_sub_f32 v64, v64, v80\n"
        "v_sub_f32 v65, v65, v80\n"
        "v_sub_f32 v66, v66, v80\n"
        "v_sub_f32 v67, v67, v80\n"
        "v_sub_f32 v68, v68, v80\n"
        "v_mfma_f32_16x16x32_bf16 a[24:27], v[116:119], v[44:47], a[24:27]\n"
        "v_sub_f32 v69, v69, v80\n"
        "v_sub_f32 v70, v70, v80\n"
        "v_sub_f32 v71, v71, v80\n"
        "v_sub_f32 v72, v72, v80\n"
        "v_sub_f32 v73, v73, v80\n"
        "v_mfma_f32_16x16x32_bf16 a[28:31], v[120:123], v[40:43], a[28:31]\n"
        "v_sub_f32 v74, v74, v80\n"
        "v_sub_f32 v75, v75, v80\n"
        "v_sub_f32 v76, v76, v80\n"
        "v_mfma_f32_16x16x32_bf16 a[28:31], v[124:127], v[44:47], a[28:31]\n"
        "s_setprio 0\n"
        "v_mfma_f32_16x16x32_bf16 a[32:35], v[96:99], v[48:51], a[32:35]\n"
        "buffer_load_dwordx4 v14, s[68:71], s75 offen offset:2048 lds\n"
        "ds_read_b128 v[40:43], v1 offset:10240\n"
        "ds_read_b128 v[44:47], v2 offset:10240\n"
        "v_sub_f32 v77, v77, v80\n"
        "v_mfma_f32_16x16x32_bf16 a[32:35], v[100:103], v[52:55], a[32:35]\n"
        "v_sub_f32 v78, v78, v80\n"
        "v_sub_f32 v79, v79, v80\n"
        "v_mul_f32 v72, v72, v82\n"
        "v_mul_f32 v73, v73, v82\n"
        "v_mfma_f32_16x16x32_bf16 a[36:39], v[104:107], v[48:51], a[36:39]\n"
        "v_mul_f32 v74, v74, v82\n"
        "v_mul_f32 v75, v75, v82\n"
        "v_mul_f32 v76, v76, v82\n"
        "v_mul_f32 v77, v77, v82\n"
        "v_mfma_f32_16x16x32_bf16 a[36:39], v[108:111], v[52:55], a[36:39]\n"
        "v_mul_f32 v78, v78, v82\n"
        "v_mul_f32 v79, v79, v82\n"
        "v_mul_f32 v81, v64, v64\n"
        "v_fmac_f32 v81, v65, v65\n"
        "v_mfma_f32_16x16x32_bf16 a[40:43], v[112:115], v[48:51], a[40:43]\n"
        "v_fmac_f32 v81, v66, v66\n"
        "v_fmac_f32 v81, v67, v67\n"
        "v_fmac_f32 v81, v68, v68\n"
        "v_fmac_f32 v81, v69, v69\n"
        "v_mfma_f32_16x16x32_bf16 a[40:43], v[116:119], v[52:55], a[40:43]\n"
        "v_fmac_f32 v81, v70, v70\n"
        "v_fmac_f32 v81, v71, v71\n"
        "v_fmac_f32 v81, v72, v72\n"
        "v_fmac_f32 v81, v73, v73\n"
        "v_mfma_f32_16x16x32_bf16 a[44:47], v[120:123], v[48:51], a[44:47]\n"
        "v_fmac_f32 v81, v74, v74\n"
        "v_fmac_f32 v81, v75, v75\n"
        "v_fmac_f32 v81, v76, v76\n"
        "v_fmac_f32 v81, v77, v77\n"
        "v_mfma_f32_16x16x32_bf16 a[44:47], v[124:127], v[52:55], a[44:47]\n"
        "v_fmac_f32 v81, v78, v78\n"
        "v_fmac_f32 v81, v79, v79\n"
        "s_nop 1\n"
        "v_add_f32_dpp v81, v81, v81 row_ror:1 row_mask:0xf bank_mask:0xf\n"
        "s_nop 1\n"
        "v_add_f32_dpp v81, v81, v81 row_ror:2 row_mask:0xf bank_mask:0xf\n"
        "s_setprio 1\n"
        "v_mfma_f32_16x16x32_bf16 a[48:51], v[96:99], v[56:59], a[48:51]\n"
        "buffer_load_dwordx4 v15, s[68:71], s75 offen offset:3072 lds\n"
        "s_add_u32 s75, s75, 0x80000\n"
        "ds_read_b128 v[48:51], v1 offset:12288\n"
        "ds_read_b128 v[52:55], v2 offset:12288\n"
        "v_add_f32_dpp v81, v81, v81 row_ror:4 row_mask:0xf bank_mask:0xf\n"
        "s_nop 1\n"
        "v_add_f32_dpp v81, v81, v81 row_ror:8 row_mask:0xf bank_mask:0xf\n"
        "v_mfma_f32_16x16x32_bf16 a[48:51], v[100:103], v[60:63], a[48:51]\n"
        "v_mov_b32 v83, v81\n"
        "s_nop 1\n"
        "v_permlane16_swap_b32 v81, v83\n"
        "v_add_f32 v81, v81, v83\n"
        "v_mov_b32 v83, v81\n"
        "s_nop 1\n"
        "v_permlane32_swap_b32 v81, v83\n"
        "v_mfma_f32_16x16x32_bf16 a[52:55], v[104:107], v[56:59], a[52:55]\n"
        "v_add_f32 v81, v81, v83\n"
        "v_mov_b32 v83, 0x3727c5ac\n"
        "v_fmac_f32 v83, 0x3acccccd, v81\n"
        "v_rsq_f32 v83, v83\n"
        "v_mov_b32 v84, 0x7ff00000\n"
        "v_mfma_f32_16x16x32_bf16 a[52:55], v[108:111], v[60:63], a[52:55]\n"
        "v_mul_f32 v83, 0x3e0293ee, v83\n"
        "v_mul_f32 v64, v64, v83\n"
        "v_mul_f32 v65, v65, v83\n"
        "v_mul_f32 v66, v66, v83\n"
        "v_mul_f32 v67, v67, v83\n"
        "v_mfma_f32_16x16x32_bf16 a[56:59], v[112:115], v[56:59], a[56:59]\n"
        "v_mul_f32 v68, v68, v83\n"
        "v_mul_f32 v69, v69, v83\n"
        "v_mul_f32 v70, v70, v83\n"
        "v_mul_f32 v71, v71, v83\n"
        "v_mul_f32 v72, v72, v83\n"
        "v_mfma_f32_16x16x32_bf16 a[56:59], v[116:119], v[60:63], a[56:59]\n"
        "v_mul_f32 v73, v73, v83\n"
        "v_mul_f32 v74, v74, v83\n"
        "v_mul_f32 v75, v75, v83\n"
        "v_mul_f32 v76, v76, v83\n"
        "v_mul_f32 v77, v77, v83\n"
        "v_mfma_f32_16x16x32_bf16 a[60:63], v[120:123], v[56:59], a[60:63]\n"
        "v_mul_f32 v78, v78, v83\n"
        "v_mul_f32 v79, v79, v83\n"
        "v_cvt_pk_bf16_f32 v24, v64, v65\n"
        "v_mfma_f32_16x16x32_bf16 a[60:63], v[124:127], v[60:63], a[60:63]\n"
        "s_setprio 0\n"
        "s_waitcnt lgkmcnt(0)\n"
        "v_mfma_f32_16x16x32_bf16 a[64:67], v[96:99], v[32:35], a[64:67]\n"
        "ds_read_b128 v[56:59], v1 offset:14336\n"
        "ds_read_b128 v[60:63], v2 offset:14336\n"
        "v_cvt_pk_bf16_f32 v25, v66, v67\n"
        "v_cvt_pk_bf16_f32 v26, v68, v69\n"
        "v_mfma_f32_16x16x32_bf16 a[64:67], v[100:103], v[36:39], a[64:67]\n"
        "v_cvt_pk_bf16_f32 v27, v70, v71\n"
        "v_cvt_pk_bf16_f32 v28, v72, v73\n"
        "v_cvt_pk_bf16_f32 v29, v74, v75\n"
        "v_cvt_pk_bf16_f32 v30, v76, v77\n"
        "v_mfma_f32_16x16x32_bf16 a[68:71], v[104:107], v[32:35], a[68:71]\n"
        "v_cvt_pk_bf16_f32 v31, v78, v79\n"
        "s_lshl_b32 s52, %[blk], 3\n"
        "s_lshl_b32 s53, s36, 1\n"
        "s_add_u32 s52, s52, s53\n"
        "v_mfma_f32_16x16x32_bf16 a[68:71], v[108:111], v[36:39], a[68:71]\n"
        "s_add_u32 s52, s52, 1\n"
        "s_lshr_b32 s53, s52, 4\n"
        "s_mul_i32 s53, s53, 20480\n"
        "s_and_b32 s52, s52, 15\n"
        "v_mfma_f32_16x16x32_bf16 a[72:75], v[112:115], v[32:35], a[72:75]\n"
        "s_lshl_b32 s52, s52, 4\n"
        "s_add_u32 s53, s53, s52\n"
        "v_lshrrev_b32 v80, 6, v0\n"
        "v_lshlrev_b32 v80, 10, v80\n"
        "v_mfma_f32_16x16x32_bf16 a[72:75], v[116:119], v[36:39], a[72:75]\n"
        "v_bfe_u32 v81, v0, 4, 2\n"
        "v_lshl_add_u32 v80, v81, 8, v80\n"
        "v_add_u32 v81, 0x4000, v80\n"
        "v_cmp_gt_u32 vcc, 256, v0\n"
        "v_mfma_f32_16x16x32_bf16 a[76:79], v[120:123], v[32:35], a[76:79]\n"
        "s_add_u32 s92, s86, 262144\n"
        "s_addc_u32 s93, s87, 0\n"
        "v_cndmask_b32 v81, v84, v81, vcc\n"
        "s_and_b32 s93, s93, 0xffff\n"
        "v_mfma_f32_16x16x32_bf16 a[76:79], v[124:127], v[36:39], a[76:79]\n"
        "s_mov_b32 s94, 2621440\n"
        "s_mov_b32 s95, 0x20000\n"
        "buffer_store_dwordx4 v[24:27], v80, s[92:95], s53 offen sc1\n"
        "buffer_store_dwordx4 v[28:31], v81, s[92:95], s53 offen sc1\n"
        "s_waitcnt vmcnt(6) lgkmcnt(0)\n"
        "s_barrier\n"
        "s_mov_b32 s84, s80\n"
        "s_add_u32 s80, s80, 32768\n"
        "s_cmp_eq_u32 s80, 98304\n"
        "s_cselect_b32 s80, 0, s80\n"
        "s_sub_u32 s85, s80, s84\n"
        "v_xor_b32 v1, 32768, v1\n"
        "v_xor_b32 v2, 32768, v2\n"
        "v_add_u32 v3, s85, v3\n"
        "v_add_u32 v4, s85, v4\n"
        "v_add_u32 v5, s85, v5\n"
        "v_add_u32 v6, s85, v6\n"
        "s_setprio 1\n"
        "v_mfma_f32_16x16x32_bf16 a[80:83], v[96:99], v[40:43], a[80:83]\n"
        "s_mov_b32 m0, s72\n"
        "s_nop 0\n"
        "buffer_load_dwordx4 v8, s[64:67], s74 offen lds\n"
        "v_mfma_f32_16x16x32_bf16 a[80:83], v[100:103], v[44:47], a[80:83]\n"
        "ds_read_b128 v[32:35], v1 offset:0\n"
        "v_mfma_f32_16x16x32_bf16 a[84:87], v[104:107], v[40:43], a[84:87]\n"
        "ds_read_b128 v[36:39], v2 offset:0\n"
        "v_mfma_f32_16x16x32_bf16 a[84:87], v[108:111], v[44:47], a[84:87]\n"
        "ds_read_b64_tr_b16 v[64:65], v3 offset:0\n"
        "v_mfma_f32_16x16x32_bf16 a[88:91], v[112:115], v[40:43], a[88:91]\n"
        "ds_read_b64_tr_b16 v[66:67], v3 offset:1024\n"
        "v_mfma_f32_16x16x32_bf16 a[88:91], v[116:119], v[44:47], a[88:91]\n"
        "ds_read_b64_tr_b16 v[68:69], v3 offset:8192\n"
        "v_mfma_f32_16x16x32_bf16 a[92:95], v[120:123], v[40:43], a[92:95]\n"
        "ds_read_b64_tr_b16 v[70:71], v3 offset:9216\n"
        "v_mfma_f32_16x16x32_bf16 a[92:95], v[124:127], v[44:47], a[92:95]\n"
        "ds_read_b64_tr_b16 v[72:73], v4 offset:0\n"
        "s_setprio 0\n"
        "v_mfma_f32_16x16x32_bf16 a[96:99], v[96:99], v[48:51], a[96:99]\n"
        "buffer_load_dwordx4 v9, s[64:67], s74 offen offset:1024 lds\n"
        "v_mfma_f32_16x16x32_bf16 a[96:99], v[100:103], v[52:55], a[96:99]\n"
        "ds_read_b128 v[40:43], v1 offset:2048\n"
        "v_mfma_f32_16x16x32_bf16 a[100:103], v[104:107], v[48:51], a[100:103]\n"
        "ds_read_b128 v[44:47], v2 offset:2048\n"
        "v_mfma_f32_16x16x32_bf16 a[100:103], v[108:111], v[52:55], a[100:103]\n"
        "ds_read_b64_tr_b16 v[74:75], v4 offset:1024\n"
        "v_mfma_f32_16x16x32_bf16 a[104:107], v[112:115], v[48:51], a[104:107]\n"
        "ds_read_b64_tr_b16 v[76:77], v4 offset:8192\n"
        "v_mfma_f32_16x16x32_bf16 a[104:107], v[116:119], v[52:55], a[104:107]\n"
        "ds_read_b64_tr_b16 v[78:79], v4 offset:9216\n"
        "v_mfma_f32_16x16x32_bf16 a[108:111], v[120:123], v[48:51], a[108:111]\n"
        "ds_read_b64_tr_b16 v[80:81], v5 offset:0\n"
        "v_mfma_f32_16x16x32_bf16 a[108:111], v[124:127], v[52:55], a[108:111]\n"
        "ds_read_b64_tr_b16 v[82:83], v5 offset:1024\n"
        "s_setprio 1\n"
        "v_mfma_f32_16x16x32_bf16 a[112:115], v[96:99], v[56:59], a[112:115]\n"
        "buffer_load_dwordx4 v10, s[64:67], s74 offen offset:2048 lds\n"
        "ds_read_b128 v[48:51], v1 offset:4096\n"
        "v_mfma_f32_16x16x32_bf16 a[112:115], v[100:103], v[60:63], a[112:115]\n"
        "ds_read_b128 v[52:55], v2 offset:4096\n"
        "ds_read_b64_tr_b16 v[84:85], v5 offset:8192\n"
        "v_mfma_f32_16x16x32_bf16 a[116:119], v[104:107], v[56:59], a[116:119]\n"
        "ds_read_b64_tr_b16 v[86:87], v5 offset:9216\n"
        "buffer_load_dwordx4 v11, s[64:67], s74 offen offset:3072 lds\n"
        "s_xor_b32 s72, s72, 32768\n"
        "s_add_u32 s74, s74, 128\n"
        "v_mfma_f32_16x16x32_bf16 a[116:119], v[108:111], v[60:63], a[116:119]\n"
        "ds_read_b64_tr_b16 v[88:89], v6 offset:0\n"
        "ds_read_b64_tr_b16 v[90:91], v6 offset:1024\n"
        "v_mfma_f32_16x16x32_bf16 a[120:123], v[112:115], v[56:59], a[120:123]\n"
        "ds_read_b64_tr_b16 v[92:93], v6 offset:8192\n"
        "ds_read_b64_tr_b16 v[94:95], v6 offset:9216\n"
        "v_mfma_f32_16x16x32_bf16 a[120:123], v[116:119], v[60:63], a[120:123]\n"
        "v_mfma_f32_16x16x32_bf16 a[124:127], v[120:123], v[56:59], a[124:127]\n"
        "v_mfma_f32_16x16x32_bf16 a[124:127], v[124:127], v[60:63], a[124:127]\n"
        "s_mov_b32 s39, 2\n"
        "P1K_LOOP_%=:\n"
        "s_setprio 0\n"
        "s_waitcnt lgkmcnt(8)\n"
        "v_mfma_f32_16x16x32_bf16 a[0:3], v[64:67], v[32:35], a[0:3]\n"
        "s_add_u32 m0, s73, s84\n"
        "s_nop 0\n"
        "buffer_load_dwordx4 v12, s[68:71], s75 offen lds\n"
        "v_mfma_f32_16x16x32_bf16 a[0:3], v[68:71], v[36:39], a[0:3]\n"
        "ds_read_b128 v[56:59], v1 offset:6144\n"
        "v_mfma_f32_16x16x32_bf16 a[4:7], v[72:75], v[32:35], a[4:7]\n"
        "ds_read_b128 v[60:63], v2 offset:6144\n"
        "v_mfma_f32_16x16x32_bf16 a[4:7], v[76:79], v[36:39], a[4:7]\n"
        "v_mfma_f32_16x16x32_bf16 a[8:11], v[80:83], v[32:35], a[8:11]\n"
        "s_waitcnt lgkmcnt(6)\n"
        "v_mfma_f32_16x16x32_bf16 a[8:11], v[84:87], v[36:39], a[8:11]\n"
        "s_waitcnt lgkmcnt(4)\n"
        "v_mfma_f32_16x16x32_bf16 a[12:15], v[88:91], v[32:35], a[12:15]\n"
        "s_waitcnt lgkmcnt(2)\n"
        "v_mfma_f32_16x16x32_bf16 a[12:15], v[92:95], v[36:39], a[12:15]\n"
        "s_setprio 1\n"
        "v_mfma_f32_16x16x32_bf16 a[16:19], v[64:67], v[40:43], a[16:19]\n"
        "buffer_load_dwordx4 v13, s[68:71], s75 offen offset:1024 lds\n"
        "v_mfma_f32_16x16x32_bf16 a[16:19], v[68:71], v[44:47], a[16:19]\n"
        "ds_read_b128 v[32:35], v1 offset:8192\n"
        "v_mfma_f32_16x16x32_bf16 a[20:23], v[72:75], v[40:43], a[20:23]\n"
        "ds_read_b128 v[36:39], v2 offset:8192\n"
        "v_mfma_f32_16x16x32_bf16 a[20:23], v[76:79], v[44:47], a[20:23]\n"
        "v_mfma_f32_16x16x32_bf16 a[24:27], v[80:83], v[40:43], a[24:27]\n"
        "v_mfma_f32_16x16x32_bf16 a[24:27], v[84:87], v[44:47], a[24:27]\n"
        "v_mfma_f32_16x16x32_bf16 a[28:31], v[88:91], v[40:43], a[28:31]\n"
        "v_mfma_f32_16x16x32_bf16 a[28:31], v[92:95], v[44:47], a[28:31]\n"
        "s_setprio 0\n"
        "v_mfma_f32_16x16x32_bf16 a[32:35], v[64:67], v[48:51], a[32:35]\n"
        "buffer_load_dwordx4 v14, s[68:71], s75 offen offset:2048 lds\n"
        "v_mfma_f32_16x16x32_bf16 a[32:35], v[68:71], v[52:55], a[32:35]\n"
        "ds_read_b128 v[40:43], v1 offset:10240\n"
        "v_mfma_f32_16x16x32_bf16 a[36:39], v[72:75], v[48:51], a[36:39]\n"
        "ds_read_b128 v[44:47], v2 offset:10240\n"
        "v_mfma_f32_16x16x32_bf16 a[36:39], v[76:79], v[52:55], a[36:39]\n"
        "v_mfma_f32_16x16x32_bf16 a[40:43], v[80:83], v[48:51], a[40:43]\n"
        "v_mfma_f32_16x16x32_bf16 a[40:43], v[84:87], v[52:55], a[40:43]\n"
        "v_mfma_f32_16x16x32_bf16 a[44:47], v[88:91], v[48:51], a[44:47]\n"
        "v_mfma_f32_16x16x32_bf16 a[44:47], v[92:95], v[52:55], a[44:47]\n"
        "s_setprio 1\n"
        "s_waitcnt lgkmcnt(3)\n"
        "v_mfma_f32_16x16x32_bf16 a[48:51], v[64:67], v[56:59], a[48:51]\n"
        "buffer_load_dwordx4 v15, s[68:71], s75 offen offset:3072 lds\n"
        "s_add_u32 s75, s75, 0x80000\n"
        "v_mfma_f32_16x16x32_bf16 a[48:51], v[68:71], v[60:63], a[48:51]\n"
        "ds_read_b128 v[48:51], v1 offset:12288\n"
        "v_mfma_f32_16x16x32_bf16 a[52:55], v[72:75], v[56:59], a[52:55]\n"
        "ds_read_b128 v[52:55], v2 offset:12288\n"
        "v_mfma_f32_16x16x32_bf16 a[52:55], v[76:79], v[60:63], a[52:55]\n"
        "v_mfma_f32_16x16x32_bf16 a[56:59], v[80:83], v[56:59], a[56:59]\n"
        "v_mfma_f32_16x16x32_bf16 a[56:59], v[84:87], v[60:63], a[56:59]\n"
        "v_mfma_f32_16x16x32_bf16 a[60:63], v[88:91], v[56:59], a[60:63]\n"
        "v_mfma_f32_16x16x32_bf16 a[60:63], v[92:95], v[60:63], a[60:63]\n"
        "s_setprio 0\n"
        "v_mfma_f32_16x16x32_bf16 a[64:67], v[64:67], v[32:35], a[64:67]\n"
        "ds_read_b128 v[56:59], v1 offset:14336\n"
        "s_waitcnt lgkmcnt(3)\n"
        "v_mfma_f32_16x16x32_bf16 a[64:67], v[68:71], v[36:39], a[64:67]\n"
        "ds_read_b128 v[60:63], v2 offset:14336\n"
        "v_mfma_f32_16x16x32_bf16 a[68:71], v[72:75], v[32:35], a[68:71]\n"
        "v_mfma_f32_16x16x32_bf16 a[68:71], v[76:79], v[36:39], a[68:71]\n"
        "v_mfma_f32_16x16x32_bf16 a[72:75], v[80:83], v[32:35], a[72:75]\n"
        "v_mfma_f32_16x16x32_bf16 a[72:75], v[84:87], v[36:39], a[72:75]\n"
        "v_mfma_f32_16x16x32_bf16 a[76:79], v[88:91], v[32:35], a[76:79]\n"
        "v_mfma_f32_16x16x32_bf16 a[76:79], v[92:95], v[36:39], a[76:79]\n"
        "s_waitcnt vmcnt(4) lgkmcnt(0)\n"
        "s_barrier\n"
        "s_mov_b32 s84, s80\n"
        "s_add_u32 s80, s80, 32768\n"
        "s_cmp_eq_u32 s80, 98304\n"
        "s_cselect_b32 s80, 0, s80\n"
        "s_sub_u32 s85, s80, s84\n"
        "v_xor_b32 v1, 32768, v1\n"
        "v_xor_b32 v2, 32768, v2\n"
        "v_add_u32 v3, s85, v3\n"
        "v_add_u32 v4, s85, v4\n"
        "v_add_u32 v5, s85, v5\n"
        "v_add_u32 v6, s85, v6\n"
        "s_setprio 1\n"
        "v_mfma_f32_16x16x32_bf16 a[80:83], v[64:67], v[40:43], a[80:83]\n"
        "s_mov_b32 m0, s72\n"
        "s_nop 0\n"
        "buffer_load_dwordx4 v8, s[64:67], s74 offen lds\n"
        "v_mfma_f32_16x16x32_bf16 a[80:83], v[68:71], v[44:47], a[80:83]\n"
        "ds_read_b128 v[32:35], v1 offset:0\n"
        "v_mfma_f32_16x16x32_bf16 a[84:87], v[72:75], v[40:43], a[84:87]\n"
        "ds_read_b128 v[36:39], v2 offset:0\n"
        "v_mfma_f32_16x16x32_bf16 a[84:87], v[76:79], v[44:47], a[84:87]\n"
        "ds_read_b64_tr_b16 v[96:97], v3 offset:0\n"
        "v_mfma_f32_16x16x32_bf16 a[88:91], v[80:83], v[40:43], a[88:91]\n"
        "ds_read_b64_tr_b16 v[98:99], v3 offset:1024\n"
        "v_mfma_f32_16x16x32_bf16 a[88:91], v[84:87], v[44:47], a[88:91]\n"
        "ds_read_b64_tr_b16 v[100:101], v3 offset:8192\n"
        "v_mfma_f32_16x16x32_bf16 a[92:95], v[88:91], v[40:43], a[92:95]\n"
        "ds_read_b64_tr_b16 v[102:103], v3 offset:9216\n"
        "v_mfma_f32_16x16x32_bf16 a[92:95], v[92:95], v[44:47], a[92:95]\n"
        "ds_read_b64_tr_b16 v[104:105], v4 offset:0\n"
        "s_setprio 0\n"
        "v_mfma_f32_16x16x32_bf16 a[96:99], v[64:67], v[48:51], a[96:99]\n"
        "buffer_load_dwordx4 v9, s[64:67], s74 offen offset:1024 lds\n"
        "v_mfma_f32_16x16x32_bf16 a[96:99], v[68:71], v[52:55], a[96:99]\n"
        "ds_read_b128 v[40:43], v1 offset:2048\n"
        "v_mfma_f32_16x16x32_bf16 a[100:103], v[72:75], v[48:51], a[100:103]\n"
        "ds_read_b128 v[44:47], v2 offset:2048\n"
        "v_mfma_f32_16x16x32_bf16 a[100:103], v[76:79], v[52:55], a[100:103]\n"
        "ds_read_b64_tr_b16 v[106:107], v4 offset:1024\n"
        "v_mfma_f32_16x16x32_bf16 a[104:107], v[80:83], v[48:51], a[104:107]\n"
        "ds_read_b64_tr_b16 v[108:109], v4 offset:8192\n"
        "v_mfma_f32_16x16x32_bf16 a[104:107], v[84:87], v[52:55], a[104:107]\n"
        "ds_read_b64_tr_b16 v[110:111], v4 offset:9216\n"
        "v_mfma_f32_16x16x32_bf16 a[108:111], v[88:91], v[48:51], a[108:111]\n"
        "ds_read_b64_tr_b16 v[112:113], v5 offset:0\n"
        "v_mfma_f32_16x16x32_bf16 a[108:111], v[92:95], v[52:55], a[108:111]\n"
        "ds_read_b64_tr_b16 v[114:115], v5 offset:1024\n"
        "s_setprio 1\n"
        "v_mfma_f32_16x16x32_bf16 a[112:115], v[64:67], v[56:59], a[112:115]\n"
        "buffer_load_dwordx4 v10, s[64:67], s74 offen offset:2048 lds\n"
        "ds_read_b128 v[48:51], v1 offset:4096\n"
        "v_mfma_f32_16x16x32_bf16 a[112:115], v[68:71], v[60:63], a[112:115]\n"
        "ds_read_b128 v[52:55], v2 offset:4096\n"
        "ds_read_b64_tr_b16 v[116:117], v5 offset:8192\n"
        "v_mfma_f32_16x16x32_bf16 a[116:119], v[72:75], v[56:59], a[116:119]\n"
        "ds_read_b64_tr_b16 v[118:119], v5 offset:9216\n"
        "buffer_load_dwordx4 v11, s[64:67], s74 offen offset:3072 lds\n"
        "s_xor_b32 s72, s72, 32768\n"
        "s_add_u32 s74, s74, 128\n"
        "v_mfma_f32_16x16x32_bf16 a[116:119], v[76:79], v[60:63], a[116:119]\n"
        "ds_read_b64_tr_b16 v[120:121], v6 offset:0\n"
        "ds_read_b64_tr_b16 v[122:123], v6 offset:1024\n"
        "v_mfma_f32_16x16x32_bf16 a[120:123], v[80:83], v[56:59], a[120:123]\n"
        "ds_read_b64_tr_b16 v[124:125], v6 offset:8192\n"
        "ds_read_b64_tr_b16 v[126:127], v6 offset:9216\n"
        "v_mfma_f32_16x16x32_bf16 a[120:123], v[84:87], v[60:63], a[120:123]\n"
        "v_mfma_f32_16x16x32_bf16 a[124:127], v[88:91], v[56:59], a[124:127]\n"
        "v_mfma_f32_16x16x32_bf16 a[124:127], v[92:95], v[60:63], a[124:127]\n"
        "s_setprio 0\n"
        "s_waitcnt lgkmcnt(8)\n"
        "v_mfma_f32_16x16x32_bf16 a[0:3], v[96:99], v[32:35], a[0:3]\n"
        "s_add_u32 m0, s73, s84\n"
        "s_nop 0\n"
        "buffer_load_dwordx4 v12, s[68:71], s75 offen lds\n"
        "v_mfma_f32_16x16x32_bf16 a[0:3], v[100:103], v[36:39], a[0:3]\n"
        "ds_read_b128 v[56:59], v1 offset:6144\n"
        "v_mfma_f32_16x16x32_bf16 a[4:7], v[104:107], v[32:35], a[4:7]\n"
        "ds_read_b128 v[60:63], v2 offset:6144\n"
        "v_mfma_f32_16x16x32_bf16 a[4:7], v[108:111], v[36:39], a[4:7]\n"
        "v_mfma_f32_16x16x32_bf16 a[8:11], v[112:115], v[32:35], a[8:11]\n"
        "s_waitcnt lgkmcnt(6)\n"
        "v_mfma_f32_16x16x32_bf16 a[8:11], v[116:119], v[36:39], a[8:11]\n"
        "s_waitcnt lgkmcnt(4)\n"
        "v_mfma_f32_16x16x32_bf16 a[12:15], v[120:123], v[32:35], a[12:15]\n"
        "s_waitcnt lgkmcnt(2)\n"
        "v_mfma_f32_16x16x32_bf16 a[12:15], v[124:127], v[36:39], a[12:15]\n"
        "s_setprio 1\n"
        "v_mfma_f32_16x16x32_bf16 a[16:19], v[96:99], v[40:43], a[16:19]\n"
        "buffer_load_dwordx4 v13, s[68:71], s75 offen offset:1024 lds\n"
        "v_mfma_f32_16x16x32_bf16 a[16:19], v[100:103], v[44:47], a[16:19]\n"
        "ds_read_b128 v[32:35], v1 offset:8192\n"
        "v_mfma_f32_16x16x32_bf16 a[20:23], v[104:107], v[40:43], a[20:23]\n"
        "ds_read_b128 v[36:39], v2 offset:8192\n"
        "v_mfma_f32_16x16x32_bf16 a[20:23], v[108:111], v[44:47], a[20:23]\n"
        "v_mfma_f32_16x16x32_bf16 a[24:27], v[112:115], v[40:43], a[24:27]\n"
        "v_mfma_f32_16x16x32_bf16 a[24:27], v[116:119], v[44:47], a[24:27]\n"
        "v_mfma_f32_16x16x32_bf16 a[28:31], v[120:123], v[40:43], a[28:31]\n"
        "v_mfma_f32_16x16x32_bf16 a[28:31], v[124:127], v[44:47], a[28:31]\n"
        "s_setprio 0\n"
        "v_mfma_f32_16x16x32_bf16 a[32:35], v[96:99], v[48:51], a[32:35]\n"
        "buffer_load_dwordx4 v14, s[68:71], s75 offen offset:2048 lds\n"
        "v_mfma_f32_16x16x32_bf16 a[32:35], v[100:103], v[52:55], a[32:35]\n"
        "ds_read_b128 v[40:43], v1 offset:10240\n"
        "v_mfma_f32_16x16x32_bf16 a[36:39], v[104:107], v[48:51], a[36:39]\n"
        "ds_read_b128 v[44:47], v2 offset:10240\n"
        "v_mfma_f32_16x16x32_bf16 a[36:39], v[108:111], v[52:55], a[36:39]\n"
        "v_mfma_f32_16x16x32_bf16 a[40:43], v[112:115], v[48:51], a[40:43]\n"
        "v_mfma_f32_16x16x32_bf16 a[40:43], v[116:119], v[52:55], a[40:43]\n"
        "v_mfma_f32_16x16x32_bf16 a[44:47], v[120:123], v[48:51], a[44:47]\n"
        "v_mfma_f32_16x16x32_bf16 a[44:47], v[124:127], v[52:55], a[44:47]\n"
        "s_setprio 1\n"
        "s_waitcnt lgkmcnt(3)\n"
        "v_mfma_f32_16x16x32_bf16 a[48:51], v[96:99], v[56:59], a[48:51]\n"
        "buffer_load_dwordx4 v15, s[68:71], s75 offen offset:3072 lds\n"
        "s_add_u32 s75, s75, 0x80000\n"
        "v_mfma_f32_16x16x32_bf16 a[48:51], v[100:103], v[60:63], a[48:51]\n"
        "ds_read_b128 v[48:51], v1 offset:12288\n"
        "v_mfma_f32_16x16x32_bf16 a[52:55], v[104:107], v[56:59], a[52:55]\n"
        "ds_read_b128 v[52:55], v2 offset:12288\n"
        "v_mfma_f32_16x16x32_bf16 a[52:55], v[108:111], v[60:63], a[52:55]\n"
        "v_mfma_f32_16x16x32_bf16 a[56:59], v[112:115], v[56:59], a[56:59]\n"
        "v_mfma_f32_16x16x32_bf16 a[56:59], v[116:119], v[60:63], a[56:59]\n"
        "v_mfma_f32_16x16x32_bf16 a[60:63], v[120:123], v[56:59], a[60:63]\n"
        "v_mfma_f32_16x16x32_bf16 a[60:63], v[124:127], v[60:63], a[60:63]\n"
        "s_setprio 0\n"
        "v_mfma_f32_16x16x32_bf16 a[64:67], v[96:99], v[32:35], a[64:67]\n"
        "ds_read_b128 v[56:59], v1 offset:14336\n"
        "s_waitcnt lgkmcnt(3)\n"
        "v_mfma_f32_16x16x32_bf16 a[64:67], v[100:103], v[36:39], a[64:67]\n"
        "ds_read_b128 v[60:63], v2 offset:14336\n"
        "v_mfma_f32_16x16x32_bf16 a[68:71], v[104:107], v[32:35], a[68:71]\n"
        "v_mfma_f32_16x16x32_bf16 a[68:71], v[108:111], v[36:39], a[68:71]\n"
        "v_mfma_f32_16x16x32_bf16 a[72:75], v[112:115], v[32:35], a[72:75]\n"
        "v_mfma_f32_16x16x32_bf16 a[72:75], v[116:119], v[36:39], a[72:75]\n"
        "v_mfma_f32_16x16x32_bf16 a[76:79], v[120:123], v[32:35], a[76:79]\n"
        "v_mfma_f32_16x16x32_bf16 a[76:79], v[124:127], v[36:39], a[76:79]\n"
        "s_waitcnt vmcnt(4) lgkmcnt(0)\n"
        "s_barrier\n"
        "s_mov_b32 s84, s80\n"
        "s_add_u32 s80, s80, 32768\n"
        "s_cmp_eq_u32 s80, 98304\n"
        "s_cselect_b32 s80, 0, s80\n"
        "s_sub_u32 s85, s80, s84\n"
        "v_xor_b32 v1, 32768, v1\n"
        "v_xor_b32 v2, 32768, v2\n"
        "v_add_u32 v3, s85, v3\n"
        "v_add_u32 v4, s85, v4\n"
        "v_add_u32 v5, s85, v5\n"
        "v_add_u32 v6, s85, v6\n"
        "s_setprio 1\n"
        "v_mfma_f32_16x16x32_bf16 a[80:83], v[96:99], v[40:43], a[80:83]\n"
        "s_mov_b32 m0, s72\n"
        "s_nop 0\n"
        "buffer_load_dwordx4 v8, s[64:67], s74 offen lds\n"
        "v_mfma_f32_16x16x32_bf16 a[80:83], v[100:103], v[44:47], a[80:83]\n"
        "ds_read_b128 v[32:35], v1 offset:0\n"
        "v_mfma_f32_16x16x32_bf16 a[84:87], v[104:107], v[40:43], a[84:87]\n"
        "ds_read_b128 v[36:39], v2 offset:0\n"
        "v_mfma_f32_16x16x32_bf16 a[84:87], v[108:111], v[44:47], a[84:87]\n"
        "ds_read_b64_tr_b16 v[64:65], v3 offset:0\n"
        "v_mfma_f32_16x16x32_bf16 a[88:91], v[112:115], v[40:43], a[88:91]\n"
        "ds_read_b64_tr_b16 v[66:67], v3 offset:1024\n"
        "v_mfma_f32_16x16x32_bf16 a[88:91], v[116:119], v[44:47], a[88:91]\n"
        "ds_read_b64_tr_b16 v[68:69], v3 offset:8192\n"
        "v_mfma_f32_16x16x32_bf16 a[92:95], v[120:123], v[40:43], a[92:95]\n"
        "ds_read_b64_tr_b16 v[70:71], v3 offset:9216\n"
        "v_mfma_f32_16x16x32_bf16 a[92:95], v[124:127], v[44:47], a[92:95]\n"
        "ds_read_b64_tr_b16 v[72:73], v4 offset:0\n"
        "s_setprio 0\n"
        "v_mfma_f32_16x16x32_bf16 a[96:99], v[96:99], v[48:51], a[96:99]\n"
        "buffer_load_dwordx4 v9, s[64:67], s74 offen offset:1024 lds\n"
        "v_mfma_f32_16x16x32_bf16 a[96:99], v[100:103], v[52:55], a[96:99]\n"
        "ds_read_b128 v[40:43], v1 offset:2048\n"
        "v_mfma_f32_16x16x32_bf16 a[100:103], v[104:107], v[48:51], a[100:103]\n"
        "ds_read_b128 v[44:47], v2 offset:2048\n"
        "v_mfma_f32_16x16x32_bf16 a[100:103], v[108:111], v[52:55], a[100:103]\n"
        "ds_read_b64_tr_b16 v[74:75], v4 offset:1024\n"
        "v_mfma_f32_16x16x32_bf16 a[104:107], v[112:115], v[48:51], a[104:107]\n"
        "ds_read_b64_tr_b16 v[76:77], v4 offset:8192\n"
        "v_mfma_f32_16x16x32_bf16 a[104:107], v[116:119], v[52:55], a[104:107]\n"
        "ds_read_b64_tr_b16 v[78:79], v4 offset:9216\n"
        "v_mfma_f32_16x16x32_bf16 a[108:111], v[120:123], v[48:51], a[108:111]\n"
        "ds_read_b64_tr_b16 v[80:81], v5 offset:0\n"
        "v_mfma_f32_16x16x32_bf16 a[108:111], v[124:127], v[52:55], a[108:111]\n"
        "ds_read_b64_tr_b16 v[82:83], v5 offset:1024\n"
        "s_setprio 1\n"
        "v_mfma_f32_16x16x32_bf16 a[112:115], v[96:99], v[56:59], a[112:115]\n"
        "buffer_load_dwordx4 v10, s[64:67], s74 offen offset:2048 lds\n"
        "ds_read_b128 v[48:51], v1 offset:4096\n"
        "v_mfma_f32_16x16x32_bf16 a[112:115], v[100:103], v[60:63], a[112:115]\n"
        "ds_read_b128 v[52:55], v2 offset:4096\n"
        "ds_read_b64_tr_b16 v[84:85], v5 offset:8192\n"
        "v_mfma_f32_16x16x32_bf16 a[116:119], v[104:107], v[56:59], a[116:119]\n"
        "ds_read_b64_tr_b16 v[86:87], v5 offset:9216\n"
        "buffer_load_dwordx4 v11, s[64:67], s74 offen offset:3072 lds\n"
        "s_xor_b32 s72, s72, 32768\n"
        "s_add_u32 s74, s74, 128\n"
        "v_mfma_f32_16x16x32_bf16 a[116:119], v[108:111], v[60:63], a[116:119]\n"
        "ds_read_b64_tr_b16 v[88:89], v6 offset:0\n"
        "ds_read_b64_tr_b16 v[90:91], v6 offset:1024\n"
        "v_mfma_f32_16x16x32_bf16 a[120:123], v[112:115], v[56:59], a[120:123]\n"
        "ds_read_b64_tr_b16 v[92:93], v6 offset:8192\n"
        "ds_read_b64_tr_b16 v[94:95], v6 offset:9216\n"
        "v_mfma_f32_16x16x32_bf16 a[120:123], v[116:119], v[60:63], a[120:123]\n"
        "v_mfma_f32_16x16x32_bf16 a[124:127], v[120:123], v[56:59], a[124:127]\n"
        "v_mfma_f32_16x16x32_bf16 a[124:127], v[124:127], v[60:63], a[124:127]\n"
        "s_sub_u32 s39, s39, 1\n"
        "s_cmp_lg_u32 s39, 0\n"
        "s_cbranch_scc1 P1K_LOOP_%=\n"
        "s_setprio 0\n"
        "s_waitcnt lgkmcnt(8)\n"
        "v_mfma_f32_16x16x32_bf16 a[0:3], v[64:67], v[32:35], a[0:3]\n"
        "ds_read_b128 v[56:59], v1 offset:6144\n"
        "v_mfma_f32_16x16x32_bf16 a[0:3], v[68:71], v[36:39], a[0:3]\n"
        "ds_read_b128 v[60:63], v2 offset:6144\n"
        "v_mfma_f32_16x16x32_bf16 a[4:7], v[72:75], v[32:35], a[4:7]\n"
        "v_mfma_f32_16x16x32_bf16 a[4:7], v[76:79], v[36:39], a[4:7]\n"
        "v_mfma_f32_16x16x32_bf16 a[8:11], v[80:83], v[32:35], a[8:11]\n"
        "s_waitcnt lgkmcnt(6)\n"
        "v_mfma_f32_16x16x32_bf16 a[8:11], v[84:87], v[36:39], a[8:11]\n"
        "s_waitcnt lgkmcnt(4)\n"
        "v_mfma_f32_16x16x32_bf16 a[12:15], v[88:91], v[32:35], a[12:15]\n"
        "s_waitcnt lgkmcnt(2)\n"
        "v_mfma_f32_16x16x32_bf16 a[12:15], v[92:95], v[36:39], a[12:15]\n"
        "s_setprio 1\n"
        "v_mfma_f32_16x16x32_bf16 a[16:19], v[64:67], v[40:43], a[16:19]\n"
        "ds_read_b128 v[32:35], v1 offset:8192\n"
        "v_mfma_f32_16x16x32_bf16 a[16:19], v[68:71], v[44:47], a[16:19]\n"
        "ds_read_b128 v[36:39], v2 offset:8192\n"
        "v_mfma_f32_16x16x32_bf16 a[20:23], v[72:75], v[40:43], a[20:23]\n"
        "v_mfma_f32_16x16x32_bf16 a[20:23], v[76:79], v[44:47], a[20:23]\n"
        "v_mfma_f32_16x16x32_bf16 a[24:27], v[80:83], v[40:43], a[24:27]\n"
        "v_mfma_f32_16x16x32_bf16 a[24:27], v[84:87], v[44:47], a[24:27]\n"
        "v_mfma_f32_16x16x32_bf16 a[28:31], v[88:91], v[40:43], a[28:31]\n"
        "v_mfma_f32_16x16x32_bf16 a[28:31], v[92:95], v[44:47], a[28:31]\n"
        "s_setprio 0\n"
        "v_mfma_f32_16x16x32_bf16 a[32:35], v[64:67], v[48:51], a[32:35]\n"
        "ds_read_b128 v[40:43], v1 offset:10240\n"
        "v_mfma_f32_16x16x32_bf16 a[32:35], v[68:71], v[52:55], a[32:35]\n"
        "ds_read_b128 v[44:47], v2 offset:10240\n"
        "v_mfma_f32_16x16x32_bf16 a[36:39], v[72:75], v[48:51], a[36:39]\n"
        "v_mfma_f32_16x16x32_bf16 a[36:39], v[76:79], v[52:55], a[36:39]\n"
        "v_mfma_f32_16x16x32_bf16 a[40:43], v[80:83], v[48:51], a[40:43]\n"
        "v_mfma_f32_16x16x32_bf16 a[40:43], v[84:87], v[52:55], a[40:43]\n"
        "v_mfma_f32_16x16x32_bf16 a[44:47], v[88:91], v[48:51], a[44:47]\n"
        "v_mfma_f32_16x16x32_bf16 a[44:47], v[92:95], v[52:55], a[44:47]\n"
        "s_setprio 1\n"
        "s_waitcnt lgkmcnt(3)\n"
        "v_mfma_f32_16x16x32_bf16 a[48:51], v[64:67], v[56:59], a[48:51]\n"
        "ds_read_b128 v[48:51], v1 offset:12288\n"
        "v_mfma_f32_16x16x32_bf16 a[48:51], v[68:71], v[60:63], a[48:51]\n"
        "ds_read_b128 v[52:55], v2 offset:12288\n"
        "v_mfma_f32_16x16x32_bf16 a[52:55], v[72:75], v[56:59], a[52:55]\n"
        "v_mfma_f32_16x16x32_bf16 a[52:55], v[76:79], v[60:63], a[52:55]\n"
        "v_mfma_f32_16x16x32_bf16 a[56:59], v[80:83], v[56:59], a[56:59]\n"
        "v_mfma_f32_16x16x32_bf16 a[56:59], v[84:87], v[60:63], a[56:59]\n"
        "v_mfma_f32_16x16x32_bf16 a[60:63], v[88:91], v[56:59], a[60:63]\n"
        "v_mfma_f32_16x16x32_bf16 a[60:63], v[92:95], v[60:63], a[60:63]\n"
        "s_setprio 0\n"
        "v_mfma_f32_16x16x32_bf16 a[64:67], v[64:67], v[32:35], a[64:67]\n"
        "ds_read_b128 v[56:59], v1 offset:14336\n"
        "s_waitcnt lgkmcnt(3)\n"
        "v_mfma_f32_16x16x32_bf16 a[64:67], v[68:71], v[36:39], a[64:67]\n"
        "ds_read_b128 v[60:63], v2 offset:14336\n"
        "v_mfma_f32_16x16x32_bf16 a[68:71], v[72:75], v[32:35], a[68:71]\n"
        "v_mfma_f32_16x16x32_bf16 a[68:71], v[76:79], v[36:39], a[68:71]\n"
        "v_mfma_f32_16x16x32_bf16 a[72:75], v[80:83], v[32:35], a[72:75]\n"
        "v_mfma_f32_16x16x32_bf16 a[72:75], v[84:87], v[36:39], a[72:75]\n"
        "v_mfma_f32_16x16x32_bf16 a[76:79], v[88:91], v[32:35], a[76:79]\n"
        "v_mfma_f32_16x16x32_bf16 a[76:79], v[92:95], v[36:39], a[76:79]\n"
        "s_waitcnt vmcnt(0) lgkmcnt(0)\n"
        "s_barrier\n"
        "s_nop 0\n"
        "s_cmp_lg_u32 s36, 0\n"
        "s_cbranch_scc1 KA_NOSIGX_%=\n"
        "s_lshl_b32 s88, %[blk], 3\n"
        "s_add_u32 s88, s88, 4096\n"
        "s_mov_b64 exec, 1\n"
        "v_mov_b32 v24, s88\n"
        "v_mov_b32 v26, s96\n"
        "v_mov_b32 v27, s97\n"
        "global_store_dwordx2 v24, v[26:27], s[86:87] sc1\n"
        "s_mov_b64 exec, -1\n"
        "KA_NOSIGX_%=:\n"
        "s_mov_b32 s84, s80\n"
        "s_add_u32 s80, s80, 32768\n"
        "s_cmp_eq_u32 s80, 98304\n"
        "s_cselect_b32 s80, 0, s80\n"
        "s_sub_u32 s85, s80, s84\n"
        "v_xor_b32 v1, 32768, v1\n"
        "v_xor_b32 v2, 32768, v2\n"
        "v_add_u32 v3, s85, v3\n"
        "v_add_u32 v4, s85, v4\n"
        "v_add_u32 v5, s85, v5\n"
        "v_add_u32 v6, s85, v6\n"
        "s_setprio 1\n"
        "v_mfma_f32_16x16x32_bf16 a[80:83], v[64:67], v[40:43], a[80:83]\n"
        "ds_read_b128 v[32:35], v1 offset:0\n"
        "v_mfma_f32_16x16x32_bf16 a[80:83], v[68:71], v[44:47], a[80:83]\n"
        "ds_read_b128 v[36:39], v2 offset:0\n"
        "v_mfma_f32_16x16x32_bf16 a[84:87], v[72:75], v[40:43], a[84:87]\n"
        "ds_read_b64_tr_b16 v[96:97], v3 offset:0\n"
        "v_mfma_f32_16x16x32_bf16 a[84:87], v[76:79], v[44:47], a[84:87]\n"
        "ds_read_b64_tr_b16 v[98:99], v3 offset:1024\n"
        "v_mfma_f32_16x16x32_bf16 a[88:91], v[80:83], v[40:43], a[88:91]\n"
        "ds_read_b64_tr_b16 v[100:101], v3 offset:8192\n"
        "v_mfma_f32_16x16x32_bf16 a[88:91], v[84:87], v[44:47], a[88:91]\n"
        "ds_read_b64_tr_b16 v[102:103], v3 offset:9216\n"
        "v_mfma_f32_16x16x32_bf16 a[92:95], v[88:91], v[40:43], a[92:95]\n"
        "ds_read_b64_tr_b16 v[104:105], v4 offset:0\n"
        "v_mfma_f32_16x16x32_bf16 a[92:95], v[92:95], v[44:47], a[92:95]\n"
        "s_setprio 0\n"
        "v_mfma_f32_16x16x32_bf16 a[96:99], v[64:67], v[48:51], a[96:99]\n"
        "ds_read_b128 v[40:43], v1 offset:2048\n"
        "v_mfma_f32_16x16x32_bf16 a[96:99], v[68:71], v[52:55], a[96:99]\n"
        "ds_read_b128 v[44:47], v2 offset:2048\n"
        "v_mfma_f32_16x16x32_bf16 a[100:103], v[72:75], v[48:51], a[100:103]\n"
        "ds_read_b64_tr_b16 v[106:107], v4 offset:1024\n"
        "v_mfma_f32_16x16x32_bf16 a[100:103], v[76:79], v[52:55], a[100:103]\n"
        "ds_read_b64_tr_b16 v[108:109], v4 offset:8192\n"
        "v_mfma_f32_16x16x32_bf16 a[104:107], v[80:83], v[48:51], a[104:107]\n"
        "ds_read_b64_tr_b16 v[110:111], v4 offset:9216\n"
        "v_mfma_f32_16x16x32_bf16 a[104:107], v[84:87], v[52:55], a[104:107]\n"
        "ds_read_b64_tr_b16 v[112:113], v5 offset:0\n"
        "v_mfma_f32_16x16x32_bf16 a[108:111], v[88:91], v[48:51], a[108:111]\n"
        "ds_read_b64_tr_b16 v[114:115], v5 offset:1024\n"
        "v_mfma_f32_16x16x32_bf16 a[108:111], v[92:95], v[52:55], a[108:111]\n"
        "s_setprio 1\n"
        "v_mfma_f32_16x16x32_bf16 a[112:115], v[64:67], v[56:59], a[112:115]\n"
        "ds_read_b128 v[48:51], v1 offset:4096\n"
        "v_mfma_f32_16x16x32_bf16 a[112:115], v[68:71], v[60:63], a[112:115]\n"
        "ds_read_b128 v[52:55], v2 offset:4096\n"
        "v_mfma_f32_16x16x32_bf16 a[116:119], v[72:75], v[56:59], a[116:119]\n"
        "ds_read_b64_tr_b16 v[116:117], v5 offset:8192\n"
        "v_mfma_f32_16x16x32_bf16 a[116:119], v[76:79], v[60:63], a[116:119]\n"
        "ds_read_b64_tr_b16 v[118:119], v5 offset:9216\n"
        "v_mfma_f32_16x16x32_bf16 a[120:123], v[80:83], v[56:59], a[120:123]\n"
        "ds_read_b64_tr_b16 v[120:121], v6 offset:0\n"
        "v_mfma_f32_16x16x32_bf16 a[120:123], v[84:87], v[60:63], a[120:123]\n"
        "ds_read_b64_tr_b16 v[122:123], v6 offset:1024\n"
        "v_mfma_f32_16x16x32_bf16 a[124:127], v[88:91], v[56:59], a[124:127]\n"
        "ds_read_b64_tr_b16 v[124:125], v6 offset:8192\n"
        "v_mfma_f32_16x16x32_bf16 a[124:127], v[92:95], v[60:63], a[124:127]\n"
        "ds_read_b64_tr_b16 v[126:127], v6 offset:9216\n"
        "s_setprio 0\n"
        "s_waitcnt lgkmcnt(9)\n"
        "v_mfma_f32_16x16x32_bf16 a[0:3], v[96:99], v[32:35], a[0:3]\n"
        "ds_read_b128 v[56:59], v1 offset:6144\n"
        "v_mfma_f32_16x16x32_bf16 a[0:3], v[100:103], v[36:39], a[0:3]\n"
        "ds_read_b128 v[60:63], v2 offset:6144\n"
        "v_mfma_f32_16x16x32_bf16 a[4:7], v[104:107], v[32:35], a[4:7]\n"
        "v_mfma_f32_16x16x32_bf16 a[4:7], v[108:111], v[36:39], a[4:7]\n"
        "s_waitcnt lgkmcnt(8)\n"
        "v_mfma_f32_16x16x32_bf16 a[8:11], v[112:115], v[32:35], a[8:11]\n"
        "s_waitcnt lgkmcnt(6)\n"
        "v_mfma_f32_16x16x32_bf16 a[8:11], v[116:119], v[36:39], a[8:11]\n"
        "s_waitcnt lgkmcnt(4)\n"
        "v_mfma_f32_16x16x32_bf16 a[12:15], v[120:123], v[32:35], a[12:15]\n"
        "s_waitcnt lgkmcnt(2)\n"
        "v_mfma_f32_16x16x32_bf16 a[12:15], v[124:127], v[36:39], a[12:15]\n"
        "s_setprio 1\n"
        "v_mfma_f32_16x16x32_bf16 a[16:19], v[96:99], v[40:43], a[16:19]\n"
        "ds_read_b128 v[32:35], v1 offset:8192\n"
        "v_mfma_f32_16x16x32_bf16 a[16:19], v[100:103], v[44:47], a[16:19]\n"
        "ds_read_b128 v[36:39], v2 offset:8192\n"
        "v_mfma_f32_16x16x32_bf16 a[20:23], v[104:107], v[40:43], a[20:23]\n"
        "v_mfma_f32_16x16x32_bf16 a[20:23], v[108:111], v[44:47], a[20:23]\n"
        "v_mfma_f32_16x16x32_bf16 a[24:27], v[112:115], v[40:43], a[24:27]\n"
        "v_mfma_f32_16x16x32_bf16 a[24:27], v[116:119], v[44:47], a[24:27]\n"
        "v_mfma_f32_16x16x32_bf16 a[28:31], v[120:123], v[40:43], a[28:31]\n"
        "v_mfma_f32_16x16x32_bf16 a[28:31], v[124:127], v[44:47], a[28:31]\n"
        "s_setprio 0\n"
        "v_mfma_f32_16x16x32_bf16 a[32:35], v[96:99], v[48:51], a[32:35]\n"
        "ds_read_b128 v[40:43], v1 offset:10240\n"
        "v_mfma_f32_16x16x32_bf16 a[32:35], v[100:103], v[52:55], a[32:35]\n"
        "ds_read_b128 v[44:47], v2 offset:10240\n"
        "v_mfma_f32_16x16x32_bf16 a[36:39], v[104:107], v[48:51], a[36:39]\n"
        "v_mfma_f32_16x16x32_bf16 a[36:39], v[108:111], v[52:55], a[36:39]\n"
        "v_mfma_f32_16x16x32_bf16 a[40:43], v[112:115], v[48:51], a[40:43]\n"
        "v_mfma_f32_16x16x32_bf16 a[40:43], v[116:119], v[52:55], a[40:43]\n"
        "v_mfma_f32_16x16x32_bf16 a[44:47], v[120:123], v[48:51], a[44:47]\n"
        "v_mfma_f32_16x16x32_bf16 a[44:47], v[124:127], v[52:55], a[44:47]\n"
        "s_setprio 1\n"
        "s_waitcnt lgkmcnt(3)\n"
        "v_mfma_f32_16x16x32_bf16 a[48:51], v[96:99], v[56:59], a[48:51]\n"
        "ds_read_b128 v[48:51], v1 offset:12288\n"
        "v_mfma_f32_16x16x32_bf16 a[48:51], v[100:103], v[60:63], a[48:51]\n"
        "ds_read_b128 v[52:55], v2 offset:12288\n"
        "v_mfma_f32_16x16x32_bf16 a[52:55], v[104:107], v[56:59], a[52:55]\n"
        "v_mfma_f32_16x16x32_bf16 a[52:55], v[108:111], v[60:63], a[52:55]\n"
        "v_mfma_f32_16x16x32_bf16 a[56:59], v[112:115], v[56:59], a[56:59]\n"
        "v_mfma_f32_16x16x32_bf16 a[56:59], v[116:119], v[60:63], a[56:59]\n"
        "v_mfma_f32_16x16x32_bf16 a[60:63], v[120:123], v[56:59], a[60:63]\n"
        "v_mfma_f32_16x16x32_bf16 a[60:63], v[124:127], v[60:63], a[60:63]\n"
        "s_setprio 0\n"
        "v_mfma_f32_16x16x32_bf16 a[64:67], v[96:99], v[32:35], a[64:67]\n"
        "ds_read_b128 v[56:59], v1 offset:14336\n"
        "s_waitcnt lgkmcnt(3)\n"
        "v_mfma_f32_16x16x32_bf16 a[64:67], v[100:103], v[36:39], a[64:67]\n"
        "ds_read_b128 v[60:63], v2 offset:14336\n"
        "v_mfma_f32_16x16x32_bf16 a[68:71], v[104:107], v[32:35], a[68:71]\n"
        "v_mfma_f32_16x16x32_bf16 a[68:71], v[108:111], v[36:39], a[68:71]\n"
        "v_mfma_f32_16x16x32_bf16 a[72:75], v[112:115], v[32:35], a[72:75]\n"
        "v_mfma_f32_16x16x32_bf16 a[72:75], v[116:119], v[36:39], a[72:75]\n"
        "v_mfma_f32_16x16x32_bf16 a[76:79], v[120:123], v[32:35], a[76:79]\n"
        "v_mfma_f32_16x16x32_bf16 a[76:79], v[124:127], v[36:39], a[76:79]\n"
        "s_waitcnt vmcnt(0) lgkmcnt(0)\n"
        "s_barrier\n"
        "s_setprio 1\n"
        "v_mfma_f32_16x16x32_bf16 a[80:83], v[96:99], v[40:43], a[80:83]\n"
        "v_mfma_f32_16x16x32_bf16 a[80:83], v[100:103], v[44:47], a[80:83]\n"
        "v_mfma_f32_16x16x32_bf16 a[84:87], v[104:107], v[40:43], a[84:87]\n"
        "v_mfma_f32_16x16x32_bf16 a[84:87], v[108:111], v[44:47], a[84:87]\n"
        "v_mfma_f32_16x16x32_bf16 a[88:91], v[112:115], v[40:43], a[88:91]\n"
        "v_mfma_f32_16x16x32_bf16 a[88:91], v[116:119], v[44:47], a[88:91]\n"
        "v_mfma_f32_16x16x32_bf16 a[92:95], v[120:123], v[40:43], a[92:95]\n"
        "v_mfma_f32_16x16x32_bf16 a[92:95], v[124:127], v[44:47], a[92:95]\n"
        "s_setprio 0\n"
        "v_mfma_f32_16x16x32_bf16 a[96:99], v[96:99], v[48:51], a[96:99]\n"
        "v_mfma_f32_16x16x32_bf16 a[96:99], v[100:103], v[52:55], a[96:99]\n"
        "v_mfma_f32_16x16x32_bf16 a[100:103], v[104:107], v[48:51], a[100:103]\n"
        "v_mfma_f32_16x16x32_bf16 a[100:103], v[108:111], v[52:55], a[100:103]\n"
        "v_mfma_f32_16x16x32_bf16 a[104:107], v[112:115], v[48:51], a[104:107]\n"
        "v_mfma_f32_16x16x32_bf16 a[104:107], v[116:119], v[52:55], a[104:107]\n"
        "v_mfma_f32_16x16x32_bf16 a[108:111], v[120:123], v[48:51], a[108:111]\n"
        "v_mfma_f32_16x16x32_bf16 a[108:111], v[124:127], v[52:55], a[108:111]\n"
        "s_setprio 1\n"
        "v_mfma_f32_16x16x32_bf16 a[112:115], v[96:99], v[56:59], a[112:115]\n"
        "v_mfma_f32_16x16x32_bf16 a[112:115], v[100:103], v[60:63], a[112:115]\n"
        "v_mfma_f32_16x16x32_bf16 a[116:119], v[104:107], v[56:59], a[116:119]\n"
        "v_mfma_f32_16x16x32_bf16 a[116:119], v[108:111], v[60:63], a[116:119]\n"
        "v_mfma_f32_16x16x32_bf16 a[120:123], v[112:115], v[56:59], a[120:123]\n"
        "v_mfma_f32_16x16x32_bf16 a[120:123], v[116:119], v[60:63], a[120:123]\n"
        "v_mfma_f32_16x16x32_bf16 a[124:127], v[120:123], v[56:59], a[124:127]\n"
        "v_mfma_f32_16x16x32_bf16 a[124:127], v[124:127], v[60:63], a[124:127]\n"
        "s_setprio 0\n"
        "s_cmp_ge_u32 s38, 2\n"
        "s_cbranch_scc1 KA_KHAND_%=\n"
        "s_nop 7\n"
        "s_and_b32 s52, s36, 1\n"
        "s_lshl_b32 s52, s52, 2\n"
        "s_lshl_b32 s53, s38, 3\n"
        "s_add_u32 s52, s52, s53\n"
        "s_lshl_b32 s52, s52, 13\n"
        "s_bfe_u32 s53, s36, 0x10001\n"
        "s_lshl_b32 s54, s53, 11\n"
        "s_add_u32 s84, s52, s54\n"
        "s_lshl_b32 s54, s53, 12\n"
        "s_add_u32 s85, s52, s54\n"
        "s_add_u32 s53, s84, 0\n"
        "v_accvgpr_read_b32 v16, a0\n"
        "v_accvgpr_read_b32 v17, a1\n"
        "v_accvgpr_read_b32 v18, a2\n"
        "v_accvgpr_read_b32 v19, a3\n"
        "v_accvgpr_read_b32 v20, a4\n"
        "v_accvgpr_read_b32 v21, a5\n"
        "v_accvgpr_read_b32 v22, a6\n"
        "v_accvgpr_read_b32 v23, a7\n"
        "v_cvt_pk_bf16_f32 v24, v16, v17\n"
        "v_cvt_pk_bf16_f32 v25, v18, v19\n"
        "v_cvt_pk_bf16_f32 v26, v20, v21\n"
        "v_cvt_pk_bf16_f32 v27, v22, v23\n"
        "buffer_store_dwordx4 v[24:27], v0, s[40:43], s53 offen offset:0 sc1\n"
        "v_accvgpr_read_b32 v28, a8\n"
        "v_accvgpr_read_b32 v29, a9\n"
        "v_accvgpr_read_b32 v30, a10\n"
        "v_accvgpr_read_b32 v31, a11\n"
        "v_accvgpr_read_b32 v32, a12\n"
        "v_accvgpr_read_b32 v33, a13\n"
        "v_accvgpr_read_b32 v34, a14\n"
        "v_accvgpr_read_b32 v35, a15\n"
        "v_cvt_pk_bf16_f32 v36, v28, v29\n"
        "v_cvt_pk_bf16_f32 v37, v30, v31\n"
        "v_cvt_pk_bf16_f32 v38, v32, v33\n"
        "v_cvt_pk_bf16_f32 v39, v34, v35\n"
        "buffer_store_dwordx4 v[36:39], v0, s[40:43], s53 offen offset:1024 sc1\n"
        "s_add_u32 s53, s84, 4096\n"
        "v_accvgpr_read_b32 v40, a16\n"
        "v_accvgpr_read_b32 v41, a17\n"
        "v_accvgpr_read_b32 v42, a18\n"
        "v_accvgpr_read_b32 v43, a19\n"
        "v_accvgpr_read_b32 v44, a20\n"
        "v_accvgpr_read_b32 v45, a21\n"
        "v_accvgpr_read_b32 v46, a22\n"
        "v_accvgpr_read_b32 v47, a23\n"
        "v_cvt_pk_bf16_f32 v48, v40, v41\n"
        "v_cvt_pk_bf16_f32 v49, v42, v43\n"
        "v_cvt_pk_bf16_f32 v50, v44, v45\n"
        "v_cvt_pk_bf16_f32 v51, v46, v47\n"
        "buffer_store_dwordx4 v[48:51], v0, s[40:43], s53 offen offset:0 sc1\n"
        "v_accvgpr_read_b32 v52, a24\n"
        "v_accvgpr_read_b32 v53, a25\n"
        "v_accvgpr_read_b32 v54, a26\n"
        "v_accvgpr_read_b32 v55, a27\n"
        "v_accvgpr_read_b32 v56, a28\n"
        "v_accvgpr_read_b32 v57, a29\n"
        "v_accvgpr_read_b32 v58, a30\n"
        "v_accvgpr_read_b32 v59, a31\n"
        "v_cvt_pk_bf16_f32 v60, v52, v53\n"
        "v_cvt_pk_bf16_f32 v61, v54, v55\n"
        "v_cvt_pk_bf16_f32 v62, v56, v57\n"
        "v_cvt_pk_bf16_f32 v63, v58, v59\n"
        "buffer_store_dwordx4 v[60:63], v0, s[40:43], s53 offen offset:1024 sc1\n"
        "s_add_u32 s53, s84, 8192\n"
        "v_accvgpr_read_b32 v16, a32\n"
        "v_accvgpr_read_b32 v17, a33\n"
        "v_accvgpr_read_b32 v18, a34\n"
        "v_accvgpr_read_b32 v19, a35\n"
        "v_accvgpr_read_b32 v20, a36\n"
        "v_accvgpr_read_b32 v21, a37\n"
        "v_accvgpr_read_b32 v22, a38\n"
        "v_accvgpr_read_b32 v23, a39\n"
        "v_cvt_pk_bf16_f32 v24, v16, v17\n"
        "v_cvt_pk_bf16_f32 v25, v18, v19\n"
        "v_cvt_pk_bf16_f32 v26, v20, v21\n"
        "v_cvt_pk_bf16_f32 v27, v22, v23\n"
        "buffer_store_dwordx4 v[24:27], v0, s[40:43], s53 offen offset:0 sc1\n"
        "v_accvgpr_read_b32 v28, a40\n"
        "v_accvgpr_read_b32 v29, a41\n"
        "v_accvgpr_read_b32 v30, a42\n"
        "v_accvgpr_read_b32 v31, a43\n"
        "v_accvgpr_read_b32 v32, a44\n"
        "v_accvgpr_read_b32 v33, a45\n"
        "v_accvgpr_read_b32 v34, a46\n"
        "v_accvgpr_read_b32 v35, a47\n"
        "v_cvt_pk_bf16_f32 v36, v28, v29\n"
        "v_cvt_pk_bf16_f32 v37, v30, v31\n"
        "v_cvt_pk_bf16_f32 v38, v32, v33\n"
        "v_cvt_pk_bf16_f32 v39, v34, v35\n"
        "buffer_store_dwordx4 v[36:39], v0, s[40:43], s53 offen offset:1024 sc1\n"
        "s_add_u32 s53, s84, 12288\n"
        "v_accvgpr_read_b32 v40, a48\n"
        "v_accvgpr_read_b32 v41, a49\n"
        "v_accvgpr_read_b32 v42, a50\n"
        "v_accvgpr_read_b32 v43, a51\n"
        "v_accvgpr_read_b32 v44, a52\n"
        "v_accvgpr_read_b32 v45, a53\n"
        "v_accvgpr_read_b32 v46, a54\n"
        "v_accvgpr_read_b32 v47, a55\n"
        "v_cvt_pk_bf16_f32 v48, v40, v41\n"
        "v_cvt_pk_bf16_f32 v49, v42, v43\n"
        "v_cvt_pk_bf16_f32 v50, v44, v45\n"
        "v_cvt_pk_bf16_f32 v51, v46, v47\n"
        "buffer_store_dwordx4 v[48:51], v0, s[40:43], s53 offen offset:0 sc1\n"
        "v_accvgpr_read_b32 v52, a56\n"
        "v_accvgpr_read_b32 v53, a57\n"
        "v_accvgpr_read_b32 v54, a58\n"
        "v_accvgpr_read_b32 v55, a59\n"
        "v_accvgpr_read_b32 v56, a60\n"
        "v_accvgpr_read_b32 v57, a61\n"
        "v_accvgpr_read_b32 v58, a62\n"
        "v_accvgpr_read_b32 v59, a63\n"
        "v_cvt_pk_bf16_f32 v60, v52, v53\n"
        "v_cvt_pk_bf16_f32 v61, v54, v55\n"
        "v_cvt_pk_bf16_f32 v62, v56, v57\n"
        "v_cvt_pk_bf16_f32 v63, v58, v59\n"
        "buffer_store_dwordx4 v[60:63], v0, s[40:43], s53 offen offset:1024 sc1\n"
        "s_add_u32 s53, s84, 16384\n"
        "v_accvgpr_read_b32 v16, a64\n"
        "v_accvgpr_read_b32 v17, a65\n"
        "v_accvgpr_read_b32 v18, a66\n"
        "v_accvgpr_read_b32 v19, a67\n"
        "v_accvgpr_read_b32 v20, a68\n"
        "v_accvgpr_read_b32 v21, a69\n"
        "v_accvgpr_read_b32 v22, a70\n"
        "v_accvgpr_read_b32 v23, a71\n"
        "v_cvt_pk_bf16_f32 v24, v16, v17\n"
        "v_cvt_pk_bf16_f32 v25, v18, v19\n"
        "v_cvt_pk_bf16_f32 v26, v20, v21\n"
        "v_cvt_pk_bf16_f32 v27, v22, v23\n"
        "buffer_store_dwordx4 v[24:27], v0, s[40:43], s53 offen offset:0 sc1\n"
        "v_accvgpr_read_b32 v28, a72\n"
        "v_accvgpr_read_b32 v29, a73\n"
        "v_accvgpr_read_b32 v30, a74\n"
        "v_accvgpr_read_b32 v31, a75\n"
        "v_accvgpr_read_b32 v32, a76\n"
        "v_accvgpr_read_b32 v33, a77\n"
        "v_accvgpr_read_b32 v34, a78\n"
        "v_accvgpr_read_b32 v35, a79\n"
        "v_cvt_pk_bf16_f32 v36, v28, v29\n"
        "v_cvt_pk_bf16_f32 v37, v30, v31\n"
        "v_cvt_pk_bf16_f32 v38, v32, v33\n"
        "v_cvt_pk_bf16_f32 v39, v34, v35\n"
        "buffer_store_dwordx4 v[36:39], v0, s[40:43], s53 offen offset:1024 sc1\n"
        "s_add_u32 s53, s84, 20480\n"
        "v_accvgpr_read_b32 v40, a80\n"
        "v_accvgpr_read_b32 v41, a81\n"
        "v_accvgpr_read_b32 v42, a82\n"
        "v_accvgpr_read_b32 v43, a83\n"
        "v_accvgpr_read_b32 v44, a84\n"
        "v_accvgpr_read_b32 v45, a85\n"
        "v_accvgpr_read_b32 v46, a86\n"
        "v_accvgpr_read_b32 v47, a87\n"
        "v_cvt_pk_bf16_f32 v48, v40, v41\n"
        "v_cvt_pk_bf16_f32 v49, v42, v43\n"
        "v_cvt_pk_bf16_f32 v50, v44, v45\n"
        "v_cvt_pk_bf16_f32 v51, v46, v47\n"
        "buffer_store_dwordx4 v[48:51], v0, s[40:43], s53 offen offset:0 sc1\n"
        "v_accvgpr_read_b32 v52, a88\n"
        "v_accvgpr_read_b32 v53, a89\n"
        "v_accvgpr_read_b32 v54, a90\n"
        "v_accvgpr_read_b32 v55, a91\n"
        "v_accvgpr_read_b32 v56, a92\n"
        "v_accvgpr_read_b32 v57, a93\n"
        "v_accvgpr_read_b32 v58, a94\n"
        "v_accvgpr_read_b32 v59, a95\n"
        "v_cvt_pk_bf16_f32 v60, v52, v53\n"
        "v_cvt_pk_bf16_f32 v61, v54, v55\n"
        "v_cvt_pk_bf16_f32 v62, v56, v57\n"
        "v_cvt_pk_bf16_f32 v63, v58, v59\n"
        "buffer_store_dwordx4 v[60:63], v0, s[40:43], s53 offen offset:1024 sc1\n"
        "s_add_u32 s53, s84, 24576\n"
        "v_accvgpr_read_b32 v16, a96\n"
        "v_accvgpr_read_b32 v17, a97\n"
        "v_accvgpr_read_b32 v18, a98\n"
        "v_accvgpr_read_b32 v19, a99\n"
        "v_accvgpr_read_b32 v20, a100\n"
        "v_accvgpr_read_b32 v21, a101\n"
        "v_accvgpr_read_b32 v22, a102\n"
        "v_accvgpr_read_b32 v23, a103\n"
        "v_cvt_pk_bf16_f32 v24, v16, v17\n"
        "v_cvt_pk_bf16_f32 v25, v18, v19\n"
        "v_cvt_pk_bf16_f32 v26, v20, v21\n"
        "v_cvt_pk_bf16_f32 v27, v22, v23\n"
        "buffer_store_dwordx4 v[24:27], v0, s[40:43], s53 offen offset:0 sc1\n"
        "v_accvgpr_read_b32 v28, a104\n"
        "v_accvgpr_read_b32 v29, a105\n"
        "v_accvgpr_read_b32 v30, a106\n"
        "v_accvgpr_read_b32 v31, a107\n"
        "v_accvgpr_read_b32 v32, a108\n"
        "v_accvgpr_read_b32 v33, a109\n"
        "v_accvgpr_read_b32 v34, a110\n"
        "v_accvgpr_read_b32 v35, a111\n"
        "v_cvt_pk_bf16_f32 v36, v28, v29\n"
        "v_cvt_pk_bf16_f32 v37, v30, v31\n"
        "v_cvt_pk_bf16_f32 v38, v32, v33\n"
        "v_cvt_pk_bf16_f32 v39, v34, v35\n"
        "buffer_store_dwordx4 v[36:39], v0, s[40:43], s53 offen offset:1024 sc1\n"
        "s_add_u32 s53, s84, 28672\n"
        "v_accvgpr_read_b32 v40, a112\n"
        "v_accvgpr_read_b32 v41, a113\n"
        "v_accvgpr_read_b32 v42, a114\n"
        "v_accvgpr_read_b32 v43, a115\n"
        "v_accvgpr_read_b32 v44, a116\n"
        "v_accvgpr_read_b32 v45, a117\n"
        "v_accvgpr_read_b32 v46, a118\n"
        "v_accvgpr_read_b32 v47, a119\n"
        "v_cvt_pk_bf16_f32 v48, v40, v41\n"
        "v_cvt_pk_bf16_f32 v49, v42, v43\n"
        "v_cvt_pk_bf16_f32 v50, v44, v45\n"
        "v_cvt_pk_bf16_f32 v51, v46, v47\n"
        "buffer_store_dwordx4 v[48:51], v0, s[40:43], s53 offen offset:0 sc1\n"
        "v_accvgpr_read_b32 v52, a120\n"
        "v_accvgpr_read_b32 v53, a121\n"
        "v_accvgpr_read_b32 v54, a122\n"
        "v_accvgpr_read_b32 v55, a123\n"
        "v_accvgpr_read_b32 v56, a124\n"
        "v_accvgpr_read_b32 v57, a125\n"
        "v_accvgpr_read_b32 v58, a126\n"
        "v_accvgpr_read_b32 v59, a127\n"
        "v_cvt_pk_bf16_f32 v60, v52, v53\n"
        "v_cvt_pk_bf16_f32 v61, v54, v55\n"
        "v_cvt_pk_bf16_f32 v62, v56, v57\n"
        "v_cvt_pk_bf16_f32 v63, v58, v59\n"
        "buffer_store_dwordx4 v[60:63], v0, s[40:43], s53 offen offset:1024 sc1\n"
        "s_branch KA_KDONE_%=\n"
        "KA_KHAND_%=:\n"
        "s_nop 7\n"
        "s_lshl_b32 s52, s36, 14\n"
        "s_add_u32 s52, s52, 98304\n"
        "v_add_u32 v1, s52, v0\n"
        "v_accvgpr_read_b32 v16, a0\n"
        "v_accvgpr_read_b32 v17, a1\n"
        "v_accvgpr_read_b32 v18, a2\n"
        "v_accvgpr_read_b32 v19, a3\n"
        "v_accvgpr_read_b32 v20, a4\n"
        "v_accvgpr_read_b32 v21, a5\n"
        "v_accvgpr_read_b32 v22, a6\n"
        "v_accvgpr_read_b32 v23, a7\n"
        "v_cvt_pk_bf16_f32 v24, v16, v17\n"
        "v_cvt_pk_bf16_f32 v25, v18, v19\n"
        "v_cvt_pk_bf16_f32 v26, v20, v21\n"
        "v_cvt_pk_bf16_f32 v27, v22, v23\n"
        "ds_write_b128 v1, v[24:27] offset:0\n"
        "v_accvgpr_read_b32 v28, a8\n"
        "v_accvgpr_read_b32 v29, a9\n"
        "v_accvgpr_read_b32 v30, a10\n"
        "v_accvgpr_read_b32 v31, a11\n"
        "v_accvgpr_read_b32 v32, a12\n"
        "v_accvgpr_read_b32 v33, a13\n"
        "v_accvgpr_read_b32 v34, a14\n"
        "v_accvgpr_read_b32 v35, a15\n"
        "v_cvt_pk_bf16_f32 v36, v28, v29\n"
        "v_cvt_pk_bf16_f32 v37, v30, v31\n"
        "v_cvt_pk_bf16_f32 v38, v32, v33\n"
        "v_cvt_pk_bf16_f32 v39, v34, v35\n"
        "ds_write_b128 v1, v[36:39] offset:1024\n"
        "v_accvgpr_read_b32 v40, a16\n"
        "v_accvgpr_read_b32 v41, a17\n"
        "v_accvgpr_read_b32 v42, a18\n"
        "v_accvgpr_read_b32 v43, a19\n"
        "v_accvgpr_read_b32 v44, a20\n"
        "v_accvgpr_read_b32 v45, a21\n"
        "v_accvgpr_read_b32 v46, a22\n"
        "v_accvgpr_read_b32 v47, a23\n"
        "v_cvt_pk_bf16_f32 v48, v40, v41\n"
        "v_cvt_pk_bf16_f32 v49, v42, v43\n"
        "v_cvt_pk_bf16_f32 v50, v44, v45\n"
        "v_cvt_pk_bf16_f32 v51, v46, v47\n"
        "ds_write_b128 v1, v[48:51] offset:2048\n"
        "v_accvgpr_read_b32 v52, a24\n"
        "v_accvgpr_read_b32 v53, a25\n"
        "v_accvgpr_read_b32 v54, a26\n"
        "v_accvgpr_read_b32 v55, a27\n"
        "v_accvgpr_read_b32 v56, a28\n"
        "v_accvgpr_read_b32 v57, a29\n"
        "v_accvgpr_read_b32 v58, a30\n"
        "v_accvgpr_read_b32 v59, a31\n"
        "v_cvt_pk_bf16_f32 v60, v52, v53\n"
        "v_cvt_pk_bf16_f32 v61, v54, v55\n"
        "v_cvt_pk_bf16_f32 v62, v56, v57\n"
        "v_cvt_pk_bf16_f32 v63, v58, v59\n"
        "ds_write_b128 v1, v[60:63] offset:3072\n"
        "v_accvgpr_read_b32 v16, a32\n"
        "v_accvgpr_read_b32 v17, a33\n"
        "v_accvgpr_read_b32 v18, a34\n"
        "v_accvgpr_read_b32 v19, a35\n"
        "v_accvgpr_read_b32 v20, a36\n"
        "v_accvgpr_read_b32 v21, a37\n"
        "v_accvgpr_read_b32 v22, a38\n"
        "v_accvgpr_read_b32 v23, a39\n"
        "v_cvt_pk_bf16_f32 v24, v16, v17\n"
        "v_cvt_pk_bf16_f32 v25, v18, v19\n"
        "v_cvt_pk_bf16_f32 v26, v20, v21\n"
        "v_cvt_pk_bf16_f32 v27, v22, v23\n"
        "ds_write_b128 v1, v[24:27] offset:4096\n"
        "v_accvgpr_read_b32 v28, a40\n"
        "v_accvgpr_read_b32 v29, a41\n"
        "v_accvgpr_read_b32 v30, a42\n"
        "v_accvgpr_read_b32 v31, a43\n"
        "v_accvgpr_read_b32 v32, a44\n"
        "v_accvgpr_read_b32 v33, a45\n"
        "v_accvgpr_read_b32 v34, a46\n"
        "v_accvgpr_read_b32 v35, a47\n"
        "v_cvt_pk_bf16_f32 v36, v28, v29\n"
        "v_cvt_pk_bf16_f32 v37, v30, v31\n"
        "v_cvt_pk_bf16_f32 v38, v32, v33\n"
        "v_cvt_pk_bf16_f32 v39, v34, v35\n"
        "ds_write_b128 v1, v[36:39] offset:5120\n"
        "v_accvgpr_read_b32 v40, a48\n"
        "v_accvgpr_read_b32 v41, a49\n"
        "v_accvgpr_read_b32 v42, a50\n"
        "v_accvgpr_read_b32 v43, a51\n"
        "v_accvgpr_read_b32 v44, a52\n"
        "v_accvgpr_read_b32 v45, a53\n"
        "v_accvgpr_read_b32 v46, a54\n"
        "v_accvgpr_read_b32 v47, a55\n"
        "v_cvt_pk_bf16_f32 v48, v40, v41\n"
        "v_cvt_pk_bf16_f32 v49, v42, v43\n"
        "v_cvt_pk_bf16_f32 v50, v44, v45\n"
        "v_cvt_pk_bf16_f32 v51, v46, v47\n"
        "ds_write_b128 v1, v[48:51] offset:6144\n"
        "v_accvgpr_read_b32 v52, a56\n"
        "v_accvgpr_read_b32 v53, a57\n"
        "v_accvgpr_read_b32 v54, a58\n"
        "v_accvgpr_read_b32 v55, a59\n"
        "v_accvgpr_read_b32 v56, a60\n"
        "v_accvgpr_read_b32 v57, a61\n"
        "v_accvgpr_read_b32 v58, a62\n"
        "v_accvgpr_read_b32 v59, a63\n"
        "v_cvt_pk_bf16_f32 v60, v52, v53\n"
        "v_cvt_pk_bf16_f32 v61, v54, v55\n"
        "v_cvt_pk_bf16_f32 v62, v56, v57\n"
        "v_cvt_pk_bf16_f32 v63, v58, v59\n"
        "ds_write_b128 v1, v[60:63] offset:7168\n"
        "v_accvgpr_read_b32 v16, a64\n"
        "v_accvgpr_read_b32 v17, a65\n"
        "v_accvgpr_read_b32 v18, a66\n"
        "v_accvgpr_read_b32 v19, a67\n"
        "v_accvgpr_read_b32 v20, a68\n"
        "v_accvgpr_read_b32 v21, a69\n"
        "v_accvgpr_read_b32 v22, a70\n"
        "v_accvgpr_read_b32 v23, a71\n"
        "v_cvt_pk_bf16_f32 v24, v16, v17\n"
        "v_cvt_pk_bf16_f32 v25, v18, v19\n"
        "v_cvt_pk_bf16_f32 v26, v20, v21\n"
        "v_cvt_pk_bf16_f32 v27, v22, v23\n"
        "ds_write_b128 v1, v[24:27] offset:8192\n"
        "v_accvgpr_read_b32 v28, a72\n"
        "v_accvgpr_read_b32 v29, a73\n"
        "v_accvgpr_read_b32 v30, a74\n"
        "v_accvgpr_read_b32 v31, a75\n"
        "v_accvgpr_read_b32 v32, a76\n"
        "v_accvgpr_read_b32 v33, a77\n"
        "v_accvgpr_read_b32 v34, a78\n"
        "v_accvgpr_read_b32 v35, a79\n"
        "v_cvt_pk_bf16_f32 v36, v28, v29\n"
        "v_cvt_pk_bf16_f32 v37, v30, v31\n"
        "v_cvt_pk_bf16_f32 v38, v32, v33\n"
        "v_cvt_pk_bf16_f32 v39, v34, v35\n"
        "ds_write_b128 v1, v[36:39] offset:9216\n"
        "v_accvgpr_read_b32 v40, a80\n"
        "v_accvgpr_read_b32 v41, a81\n"
        "v_accvgpr_read_b32 v42, a82\n"
        "v_accvgpr_read_b32 v43, a83\n"
        "v_accvgpr_read_b32 v44, a84\n"
        "v_accvgpr_read_b32 v45, a85\n"
        "v_accvgpr_read_b32 v46, a86\n"
        "v_accvgpr_read_b32 v47, a87\n"
        "v_cvt_pk_bf16_f32 v48, v40, v41\n"
        "v_cvt_pk_bf16_f32 v49, v42, v43\n"
        "v_cvt_pk_bf16_f32 v50, v44, v45\n"
        "v_cvt_pk_bf16_f32 v51, v46, v47\n"
        "ds_write_b128 v1, v[48:51] offset:10240\n"
        "v_accvgpr_read_b32 v52, a88\n"
        "v_accvgpr_read_b32 v53, a89\n"
        "v_accvgpr_read_b32 v54, a90\n"
        "v_accvgpr_read_b32 v55, a91\n"
        "v_accvgpr_read_b32 v56, a92\n"
        "v_accvgpr_read_b32 v57, a93\n"
        "v_accvgpr_read_b32 v58, a94\n"
        "v_accvgpr_read_b32 v59, a95\n"
        "v_cvt_pk_bf16_f32 v60, v52, v53\n"
        "v_cvt_pk_bf16_f32 v61, v54, v55\n"
        "v_cvt_pk_bf16_f32 v62, v56, v57\n"
        "v_cvt_pk_bf16_f32 v63, v58, v59\n"
        "ds_write_b128 v1, v[60:63] offset:11264\n"
        "v_accvgpr_read_b32 v16, a96\n"
        "v_accvgpr_read_b32 v17, a97\n"
        "v_accvgpr_read_b32 v18, a98\n"
        "v_accvgpr_read_b32 v19, a99\n"
        "v_accvgpr_read_b32 v20, a100\n"
        "v_accvgpr_read_b32 v21, a101\n"
        "v_accvgpr_read_b32 v22, a102\n"
        "v_accvgpr_read_b32 v23, a103\n"
        "v_cvt_pk_bf16_f32 v24, v16, v17\n"
        "v_cvt_pk_bf16_f32 v25, v18, v19\n"
        "v_cvt_pk_bf16_f32 v26, v20, v21\n"
        "v_cvt_pk_bf16_f32 v27, v22, v23\n"
        "ds_write_b128 v1, v[24:27] offset:12288\n"
        "v_accvgpr_read_b32 v28, a104\n"
        "v_accvgpr_read_b32 v29, a105\n"
        "v_accvgpr_read_b32 v30, a106\n"
        "v_accvgpr_read_b32 v31, a107\n"
        "v_accvgpr_read_b32 v32, a108\n"
        "v_accvgpr_read_b32 v33, a109\n"
        "v_accvgpr_read_b32 v34, a110\n"
        "v_accvgpr_read_b32 v35, a111\n"
        "v_cvt_pk_bf16_f32 v36, v28, v29\n"
        "v_cvt_pk_bf16_f32 v37, v30, v31\n"
        "v_cvt_pk_bf16_f32 v38, v32, v33\n"
        "v_cvt_pk_bf16_f32 v39, v34, v35\n"
        "ds_write_b128 v1, v[36:39] offset:13312\n"
        "v_accvgpr_read_b32 v40, a112\n"
        "v_accvgpr_read_b32 v41, a113\n"
        "v_accvgpr_read_b32 v42, a114\n"
        "v_accvgpr_read_b32 v43, a115\n"
        "v_accvgpr_read_b32 v44, a116\n"
        "v_accvgpr_read_b32 v45, a117\n"
        "v_accvgpr_read_b32 v46, a118\n"
        "v_accvgpr_read_b32 v47, a119\n"
        "v_cvt_pk_bf16_f32 v48, v40, v41\n"
        "v_cvt_pk_bf16_f32 v49, v42, v43\n"
        "v_cvt_pk_bf16_f32 v50, v44, v45\n"
        "v_cvt_pk_bf16_f32 v51, v46, v47\n"
        "ds_write_b128 v1, v[48:51] offset:14336\n"
        "v_accvgpr_read_b32 v52, a120\n"
        "v_accvgpr_read_b32 v53, a121\n"
        "v_accvgpr_read_b32 v54, a122\n"
        "v_accvgpr_read_b32 v55, a123\n"
        "v_accvgpr_read_b32 v56, a124\n"
        "v_accvgpr_read_b32 v57, a125\n"
        "v_accvgpr_read_b32 v58, a126\n"
        "v_accvgpr_read_b32 v59, a127\n"
        "v_cvt_pk_bf16_f32 v60, v52, v53\n"
        "v_cvt_pk_bf16_f32 v61, v54, v55\n"
        "v_cvt_pk_bf16_f32 v62, v56, v57\n"
        "v_cvt_pk_bf16_f32 v63, v58, v59\n"
        "ds_write_b128 v1, v[60:63] offset:15360\n"
        "s_waitcnt lgkmcnt(0)\n"
        "KA_KDONE_%=:\n"
        "s_barrier\n"
        "v_lshrrev_b32 v16, 4, v0\n"
        "v_lshrrev_b32 v18, 4, v16\n"
        "v_bfe_u32 v17, v16, 2, 2\n"
        "v_and_b32 v19, 1, v18\n"
        "v_lshl_or_b32 v19, v19, 2, v17\n"
        "v_lshlrev_b32 v19, 1, v19\n"
        "v_bfe_u32 v20, v16, 1, 1\n"
        "v_xor_b32 v19, v19, v20\n"
        "v_lshlrev_b32 v19, 4, v19\n"
        "v_and_b32 v20, 1, v16\n"
        "v_lshl_add_u32 v19, v20, 3, v19\n"
        "v_lshl_add_u32 v17, v18, 3, v17\n"
        "v_lshl_add_u32 v19, v17, 8, v19\n"
        "v_add_u32 v1, 0, v19\n"
        "v_xor_b32 v2, 32, v1\n"
        "v_xor_b32 v3, 64, v1\n"
        "v_xor_b32 v4, 96, v1\n"
        "v_xor_b32 v5, 128, v1\n"
        "v_xor_b32 v6, 160, v1\n"
        "v_xor_b32 v7, 192, v1\n"
        "v_xor_b32 v8, 224, v1\n"
        "s_lshl_b32 s52, s36, 11\n"
        "s_add_u32 s53, s52, 16384\n"
        "v_add_u32 v9, s53, v0\n"
        "s_sub_u32 s53, 30720, s52\n"
        "v_add_u32 v10, s53, v0\n"
        "s_barrier\n"
        "ds_read_b128 a[64:67], v9 offset:0\n"
        "ds_read_b128 a[68:71], v9 offset:1024\n"
        "ds_read_b128 a[72:75], v10 offset:0\n"
        "ds_read_b128 a[76:79], v10 offset:1024\n"
        "ds_read_b128 a[80:83], v9 offset:16384\n"
        "ds_read_b128 a[84:87], v9 offset:17408\n"
        "ds_read_b128 a[88:91], v10 offset:16384\n"
        "ds_read_b128 a[92:95], v10 offset:17408\n"
        "ds_read_b64_tr_b16 v[96:97], v1 offset:0\n"
        "ds_read_b64_tr_b16 v[98:99], v1 offset:1024\n"
        "ds_read_b64_tr_b16 v[100:101], v1 offset:8192\n"
        "ds_read_b64_tr_b16 v[102:103], v1 offset:9216\n"
        "ds_read_b64_tr_b16 v[104:105], v2 offset:0\n"
        "ds_read_b64_tr_b16 v[106:107], v2 offset:1024\n"
        "ds_read_b64_tr_b16 v[108:109], v2 offset:8192\n"
        "ds_read_b64_tr_b16 v[110:111], v2 offset:9216\n"
        "ds_read_b64_tr_b16 v[112:113], v3 offset:0\n"
        "ds_read_b64_tr_b16 v[114:115], v3 offset:1024\n"
        "ds_read_b64_tr_b16 v[116:117], v3 offset:8192\n"
        "ds_read_b64_tr_b16 v[118:119], v3 offset:9216\n"
        "s_waitcnt lgkmcnt(0)\n"
        "v_mfma_f32_16x16x32_bf16 v[32:35], v[96:99], a[64:67], 0\n"
        "ds_read_b64_tr_b16 v[120:121], v4 offset:0\n"
        "v_mfma_f32_16x16x32_bf16 v[32:35], v[100:103], a[68:71], v[32:35]\n"
        "ds_read_b64_tr_b16 v[122:123], v4 offset:1024\n"
        "v_mfma_f32_16x16x32_bf16 v[64:67], v[96:99], a[72:75], 0\n"
        "ds_read_b64_tr_b16 v[124:125], v4 offset:8192\n"
        "v_mfma_f32_16x16x32_bf16 v[64:67], v[100:103], a[76:79], v[64:67]\n"
        "ds_read_b64_tr_b16 v[126:127], v4 offset:9216\n"
        "v_mfma_f32_16x16x32_bf16 a[0:3], v[96:99], a[80:83], 0\n"
        "v_mfma_f32_16x16x32_bf16 a[0:3], v[100:103], a[84:87], a[0:3]\n"
        "v_mfma_f32_16x16x32_bf16 a[32:35], v[96:99], a[88:91], 0\n"
        "v_mfma_f32_16x16x32_bf16 a[32:35], v[100:103], a[92:95], a[32:35]\n"
        "v_mfma_f32_16x16x32_bf16 v[36:39], v[104:107], a[64:67], 0\n"
        "ds_read_b64_tr_b16 v[96:97], v5 offset:0\n"
        "v_mfma_f32_16x16x32_bf16 v[36:39], v[108:111], a[68:71], v[36:39]\n"
        "ds_read_b64_tr_b16 v[98:99], v5 offset:1024\n"
        "v_mfma_f32_16x16x32_bf16 v[68:71], v[104:107], a[72:75], 0\n"
        "ds_read_b64_tr_b16 v[100:101], v5 offset:8192\n"
        "v_mfma_f32_16x16x32_bf16 v[68:71], v[108:111], a[76:79], v[68:71]\n"
        "ds_read_b64_tr_b16 v[102:103], v5 offset:9216\n"
        "v_mfma_f32_16x16x32_bf16 a[4:7], v[104:107], a[80:83], 0\n"
        "v_mfma_f32_16x16x32_bf16 a[4:7], v[108:111], a[84:87], a[4:7]\n"
        "v_mfma_f32_16x16x32_bf16 a[36:39], v[104:107], a[88:91], 0\n"
        "v_mfma_f32_16x16x32_bf16 a[36:39], v[108:111], a[92:95], a[36:39]\n"
        "v_mfma_f32_16x16x32_bf16 v[40:43], v[112:115], a[64:67], 0\n"
        "ds_read_b64_tr_b16 v[104:105], v6 offset:0\n"
        "v_mfma_f32_16x16x32_bf16 v[40:43], v[116:119], a[68:71], v[40:43]\n"
        "ds_read_b64_tr_b16 v[106:107], v6 offset:1024\n"
        "v_mfma_f32_16x16x32_bf16 v[72:75], v[112:115], a[72:75], 0\n"
        "ds_read_b64_tr_b16 v[108:109], v6 offset:8192\n"
        "v_mfma_f32_16x16x32_bf16 v[72:75], v[116:119], a[76:79], v[72:75]\n"
        "ds_read_b64_tr_b16 v[110:111], v6 offset:9216\n"
        "v_mfma_f32_16x16x32_bf16 a[8:11], v[112:115], a[80:83], 0\n"
        "v_mfma_f32_16x16x32_bf16 a[8:11], v[116:119], a[84:87], a[8:11]\n"
        "v_mfma_f32_16x16x32_bf16 a[40:43], v[112:115], a[88:91], 0\n"
        "v_mfma_f32_16x16x32_bf16 a[40:43], v[116:119], a[92:95], a[40:43]\n"
        "s_waitcnt lgkmcnt(6)\n"
        "v_mfma_f32_16x16x32_bf16 v[44:47], v[120:123], a[64:67], 0\n"
        "ds_read_b64_tr_b16 v[112:113], v7 offset:0\n"
        "v_mfma_f32_16x16x32_bf16 v[44:47], v[124:127], a[68:71], v[44:47]\n"
        "ds_read_b64_tr_b16 v[114:115], v7 offset:1024\n"
        "v_mfma_f32_16x16x32_bf16 v[76:79], v[120:123], a[72:75], 0\n"
        "ds_read_b64_tr_b16 v[116:117], v7 offset:8192\n"
        "v_mfma_f32_16x16x32_bf16 v[76:79], v[124:127], a[76:79], v[76:79]\n"
        "ds_read_b64_tr_b16 v[118:119], v7 offset:9216\n"
        "v_mfma_f32_16x16x32_bf16 a[12:15], v[120:123], a[80:83], 0\n"
        "v_mfma_f32_16x16x32_bf16 a[12:15], v[124:127], a[84:87], a[12:15]\n"
        "v_mfma_f32_16x16x32_bf16 a[44:47], v[120:123], a[88:91], 0\n"
        "v_mfma_f32_16x16x32_bf16 a[44:47], v[124:127], a[92:95], a[44:47]\n"
        "v_mfma_f32_16x16x32_bf16 v[48:51], v[96:99], a[64:67], 0\n"
        "ds_read_b64_tr_b16 v[120:121], v8 offset:0\n"
        "s_waitcnt lgkmcnt(6)\n"
        "v_mfma_f32_16x16x32_bf16 v[48:51], v[100:103], a[68:71], v[48:51]\n"
        "ds_read_b64_tr_b16 v[122:123], v8 offset:1024\n"
        "v_mfma_f32_16x16x32_bf16 v[80:83], v[96:99], a[72:75], 0\n"
        "ds_read_b64_tr_b16 v[124:125], v8 offset:8192\n"
        "v_mfma_f32_16x16x32_bf16 v[80:83], v[100:103], a[76:79], v[80:83]\n"
        "ds_read_b64_tr_b16 v[126:127], v8 offset:9216\n"
        "v_mfma_f32_16x16x32_bf16 a[16:19], v[96:99], a[80:83], 0\n"
        "v_mfma_f32_16x16x32_bf16 a[16:19], v[100:103], a[84:87], a[16:19]\n"
        "v_mfma_f32_16x16x32_bf16 a[48:51], v[96:99], a[88:91], 0\n"
        "v_mfma_f32_16x16x32_bf16 a[48:51], v[100:103], a[92:95], a[48:51]\n"
        "s_waitcnt lgkmcnt(0)\n"
        "s_barrier\n"
        "v_add_u32 v1, 49152, v1\n"
        "v_add_u32 v2, 49152, v2\n"
        "v_add_u32 v3, 49152, v3\n"
        "v_add_u32 v4, 49152, v4\n"
        "v_add_u32 v5, 49152, v5\n"
        "v_add_u32 v6, 49152, v6\n"
        "v_add_u32 v7, 49152, v7\n"
        "v_add_u32 v8, 49152, v8\n"
        "v_add_u32 v9, 49152, v9\n"
        "v_add_u32 v10, 49152, v10\n"
        "v_mfma_f32_16x16x32_bf16 v[52:55], v[104:107], a[64:67], 0\n"
        "ds_read_b64_tr_b16 v[96:97], v1 offset:0\n"
        "v_mfma_f32_16x16x32_bf16 v[52:55], v[108:111], a[68:71], v[52:55]\n"
        "ds_read_b64_tr_b16 v[98:99], v1 offset:1024\n"
        "v_mfma_f32_16x16x32_bf16 v[84:87], v[104:107], a[72:75], 0\n"
        "ds_read_b64_tr_b16 v[100:101], v1 offset:8192\n"
        "v_mfma_f32_16x16x32_bf16 v[84:87], v[108:111], a[76:79], v[84:87]\n"
        "ds_read_b64_tr_b16 v[102:103], v1 offset:9216\n"
        "v_mfma_f32_16x16x32_bf16 a[20:23], v[104:107], a[80:83], 0\n"
        "ds_read_b128 a[96:99], v9 offset:0\n"
        "v_mfma_f32_16x16x32_bf16 a[20:23], v[108:111], a[84:87], a[20:23]\n"
        "ds_read_b128 a[100:103], v9 offset:1024\n"
        "v_mfma_f32_16x16x32_bf16 a[52:55], v[104:107], a[88:91], 0\n"
        "ds_read_b128 a[104:107], v10 offset:0\n"
        "v_mfma_f32_16x16x32_bf16 a[52:55], v[108:111], a[92:95], a[52:55]\n"
        "v_mfma_f32_16x16x32_bf16 v[56:59], v[112:115], a[64:67], 0\n"
        "ds_read_b64_tr_b16 v[104:105], v2 offset:0\n"
        "v_mfma_f32_16x16x32_bf16 v[56:59], v[116:119], a[68:71], v[56:59]\n"
        "ds_read_b64_tr_b16 v[106:107], v2 offset:1024\n"
        "v_mfma_f32_16x16x32_bf16 v[88:91], v[112:115], a[72:75], 0\n"
        "ds_read_b64_tr_b16 v[108:109], v2 offset:8192\n"
        "v_mfma_f32_16x16x32_bf16 v[88:91], v[116:119], a[76:79], v[88:91]\n"
        "ds_read_b64_tr_b16 v[110:111], v2 offset:9216\n"
        "v_mfma_f32_16x16x32_bf16 a[24:27], v[112:115], a[80:83], 0\n"
        "ds_read_b128 a[108:111], v10 offset:1024\n"
        "v_mfma_f32_16x16x32_bf16 a[24:27], v[116:119], a[84:87], a[24:27]\n"
        "ds_read_b128 a[112:115], v9 offset:16384\n"
        "v_mfma_f32_16x16x32_bf16 a[56:59], v[112:115], a[88:91], 0\n"
        "ds_read_b128 a[116:119], v9 offset:17408\n"
        "v_mfma_f32_16x16x32_bf16 a[56:59], v[116:119], a[92:95], a[56:59]\n"
        "v_mfma_f32_16x16x32_bf16 v[60:63], v[120:123], a[64:67], 0\n"
        "ds_read_b64_tr_b16 v[112:113], v3 offset:0\n"
        "v_mfma_f32_16x16x32_bf16 v[60:63], v[124:127], a[68:71], v[60:63]\n"
        "ds_read_b64_tr_b16 v[114:115], v3 offset:1024\n"
        "v_mfma_f32_16x16x32_bf16 v[92:95], v[120:123], a[72:75], 0\n"
        "ds_read_b64_tr_b16 v[116:117], v3 offset:8192\n"
        "v_mfma_f32_16x16x32_bf16 v[92:95], v[124:127], a[76:79], v[92:95]\n"
        "ds_read_b64_tr_b16 v[118:119], v3 offset:9216\n"
        "v_mfma_f32_16x16x32_bf16 a[28:31], v[120:123], a[80:83], 0\n"
        "ds_read_b128 a[120:123], v10 offset:16384\n"
        "v_mfma_f32_16x16x32_bf16 a[28:31], v[124:127], a[84:87], a[28:31]\n"
        "ds_read_b128 a[124:127], v10 offset:17408\n"
        "v_mfma_f32_16x16x32_bf16 a[60:63], v[120:123], a[88:91], 0\n"
        "v_mfma_f32_16x16x32_bf16 a[60:63], v[124:127], a[92:95], a[60:63]\n"
        "s_waitcnt lgkmcnt(9)\n"
        "v_mfma_f32_16x16x32_bf16 v[32:35], v[96:99], a[96:99], v[32:35]\n"
        "ds_read_b64_tr_b16 v[120:121], v4 offset:0\n"
        "v_mfma_f32_16x16x32_bf16 v[32:35], v[100:103], a[100:103], v[32:35]\n"
        "ds_read_b64_tr_b16 v[122:123], v4 offset:1024\n"
        "v_mfma_f32_16x16x32_bf16 v[64:67], v[96:99], a[104:107], v[64:67]\n"
        "ds_read_b64_tr_b16 v[124:125], v4 offset:8192\n"
        "s_waitcnt lgkmcnt(9)\n"
        "v_mfma_f32_16x16x32_bf16 v[64:67], v[100:103], a[108:111], v[64:67]\n"
        "ds_read_b64_tr_b16 v[126:127], v4 offset:9216\n"
        "v_mfma_f32_16x16x32_bf16 a[0:3], v[96:99], a[112:115], a[0:3]\n"
        "v_mfma_f32_16x16x32_bf16 a[0:3], v[100:103], a[116:119], a[0:3]\n"
        "s_waitcnt lgkmcnt(5)\n"
        "v_mfma_f32_16x16x32_bf16 a[32:35], v[96:99], a[120:123], a[32:35]\n"
        "s_waitcnt lgkmcnt(4)\n"
        "v_mfma_f32_16x16x32_bf16 a[32:35], v[100:103], a[124:127], a[32:35]\n"
        "v_mfma_f32_16x16x32_bf16 v[36:39], v[104:107], a[96:99], v[36:39]\n"
        "ds_read_b64_tr_b16 v[96:97], v5 offset:0\n"
        "v_mfma_f32_16x16x32_bf16 v[36:39], v[108:111], a[100:103], v[36:39]\n"
        "ds_read_b64_tr_b16 v[98:99], v5 offset:1024\n"
        "v_mfma_f32_16x16x32_bf16 v[68:71], v[104:107], a[104:107], v[68:71]\n"
        "ds_read_b64_tr_b16 v[100:101], v5 offset:8192\n"
        "v_mfma_f32_16x16x32_bf16 v[68:71], v[108:111], a[108:111], v[68:71]\n"
        "ds_read_b64_tr_b16 v[102:103], v5 offset:9216\n"
        "v_mfma_f32_16x16x32_bf16 a[4:7], v[104:107], a[112:115], a[4:7]\n"
        "v_mfma_f32_16x16x32_bf16 a[4:7], v[108:111], a[116:119], a[4:7]\n"
        "v_mfma_f32_16x16x32_bf16 a[36:39], v[104:107], a[120:123], a[36:39]\n"
        "v_mfma_f32_16x16x32_bf16 a[36:39], v[108:111], a[124:127], a[36:39]\n"
        "v_mfma_f32_16x16x32_bf16 v[40:43], v[112:115], a[96:99], v[40:43]\n"
        "ds_read_b64_tr_b16 v[104:105], v6 offset:0\n"
        "v_mfma_f32_16x16x32_bf16 v[40:43], v[116:119], a[100:103], v[40:43]\n"
        "ds_read_b64_tr_b16 v[106:107], v6 offset:1024\n"
        "v_mfma_f32_16x16x32_bf16 v[72:75], v[112:115], a[104:107], v[72:75]\n"
        "ds_read_b64_tr_b16 v[108:109], v6 offset:8192\n"
        "v_mfma_f32_16x16x32_bf16 v[72:75], v[116:119], a[108:111], v[72:75]\n"
        "ds_read_b64_tr_b16 v[110:111], v6 offset:9216\n"
        "v_mfma_f32_16x16x32_bf16 a[8:11], v[112:115], a[112:115], a[8:11]\n"
        "v_mfma_f32_16x16x32_bf16 a[8:11], v[116:119], a[116:119], a[8:11]\n"
        "v_mfma_f32_16x16x32_bf16 a[40:43], v[112:115], a[120:123], a[40:43]\n"
        "v_mfma_f32_16x16x32_bf16 a[40:43], v[116:119], a[124:127], a[40:43]\n"
        "s_waitcnt lgkmcnt(6)\n"
        "v_mfma_f32_16x16x32_bf16 v[44:47], v[120:123], a[96:99], v[44:47]\n"
        "ds_read_b64_tr_b16 v[112:113], v7 offset:0\n"
        "v_mfma_f32_16x16x32_bf16 v[44:47], v[124:127], a[100:103], v[44:47]\n"
        "ds_read_b64_tr_b16 v[114:115], v7 offset:1024\n"
        "v_mfma_f32_16x16x32_bf16 v[76:79], v[120:123], a[104:107], v[76:79]\n"
        "ds_read_b64_tr_b16 v[116:117], v7 offset:8192\n"
        "v_mfma_f32_16x16x32_bf16 v[76:79], v[124:127], a[108:111], v[76:79]\n"
        "ds_read_b64_tr_b16 v[118:119], v7 offset:9216\n"
        "v_mfma_f32_16x16x32_bf16 a[12:15], v[120:123], a[112:115], a[12:15]\n"
        "v_mfma_f32_16x16x32_bf16 a[12:15], v[124:127], a[116:119], a[12:15]\n"
        "v_mfma_f32_16x16x32_bf16 a[44:47], v[120:123], a[120:123], a[44:47]\n"
        "v_mfma_f32_16x16x32_bf16 a[44:47], v[124:127], a[124:127], a[44:47]\n"
        "v_mfma_f32_16x16x32_bf16 v[48:51], v[96:99], a[96:99], v[48:51]\n"
        "ds_read_b64_tr_b16 v[120:121], v8 offset:0\n"
        "s_waitcnt lgkmcnt(6)\n"
        "v_mfma_f32_16x16x32_bf16 v[48:51], v[100:103], a[100:103], v[48:51]\n"
        "ds_read_b64_tr_b16 v[122:123], v8 offset:1024\n"
        "v_mfma_f32_16x16x32_bf16 v[80:83], v[96:99], a[104:107], v[80:83]\n"
        "ds_read_b64_tr_b16 v[124:125], v8 offset:8192\n"
        "v_mfma_f32_16x16x32_bf16 v[80:83], v[100:103], a[108:111], v[80:83]\n"
        "ds_read_b64_tr_b16 v[126:127], v8 offset:9216\n"
        "v_mfma_f32_16x16x32_bf16 a[16:19], v[96:99], a[112:115], a[16:19]\n"
        "v_mfma_f32_16x16x32_bf16 a[16:19], v[100:103], a[116:119], a[16:19]\n"
        "v_mfma_f32_16x16x32_bf16 a[48:51], v[96:99], a[120:123], a[48:51]\n"
        "v_mfma_f32_16x16x32_bf16 a[48:51], v[100:103], a[124:127], a[48:51]\n"
        "s_waitcnt vmcnt(0) lgkmcnt(0)\n"
        "s_barrier\n"
        "v_add_u32 v1, 49152, v1\n"
        "v_add_u32 v2, 49152, v2\n"
        "v_add_u32 v3, 49152, v3\n"
        "v_add_u32 v4, 49152, v4\n"
        "v_add_u32 v5, 49152, v5\n"
        "v_add_u32 v6, 49152, v6\n"
        "v_add_u32 v7, 49152, v7\n"
        "v_add_u32 v8, 49152, v8\n"
        "v_add_u32 v9, 49152, v9\n"
        "v_add_u32 v10, 49152, v10\n"
        "v_mfma_f32_16x16x32_bf16 v[52:55], v[104:107], a[96:99], v[52:55]\n"
        "ds_read_b64_tr_b16 v[96:97], v1 offset:0\n"
        "v_mfma_f32_16x16x32_bf16 v[52:55], v[108:111], a[100:103], v[52:55]\n"
        "ds_read_b64_tr_b16 v[98:99], v1 offset:1024\n"
        "v_mfma_f32_16x16x32_bf16 v[84:87], v[104:107], a[104:107], v[84:87]\n"
        "ds_read_b64_tr_b16 v[100:101], v1 offset:8192\n"
        "v_mfma_f32_16x16x32_bf16 v[84:87], v[108:111], a[108:111], v[84:87]\n"
        "ds_read_b64_tr_b16 v[102:103], v1 offset:9216\n"
        "v_mfma_f32_16x16x32_bf16 a[20:23], v[104:107], a[112:115], a[20:23]\n"
        "ds_read_b128 a[64:67], v9 offset:0\n"
        "v_mfma_f32_16x16x32_bf16 a[20:23], v[108:111], a[116:119], a[20:23]\n"
        "ds_read_b128 a[68:71], v9 offset:1024\n"
        "v_mfma_f32_16x16x32_bf16 a[52:55], v[104:107], a[120:123], a[52:55]\n"
        "ds_read_b128 a[72:75], v10 offset:0\n"
        "v_mfma_f32_16x16x32_bf16 a[52:55], v[108:111], a[124:127], a[52:55]\n"
        "v_mfma_f32_16x16x32_bf16 v[56:59], v[112:115], a[96:99], v[56:59]\n"
        "ds_read_b64_tr_b16 v[104:105], v2 offset:0\n"
        "v_mfma_f32_16x16x32_bf16 v[56:59], v[116:119], a[100:103], v[56:59]\n"
        "ds_read_b64_tr_b16 v[106:107], v2 offset:1024\n"
        "v_mfma_f32_16x16x32_bf16 v[88:91], v[112:115], a[104:107], v[88:91]\n"
        "ds_read_b64_tr_b16 v[108:109], v2 offset:8192\n"
        "v_mfma_f32_16x16x32_bf16 v[88:91], v[116:119], a[108:111], v[88:91]\n"
        "ds_read_b64_tr_b16 v[110:111], v2 offset:9216\n"
        "v_mfma_f32_16x16x32_bf16 a[24:27], v[112:115], a[112:115], a[24:27]\n"
        "ds_read_b128 a[76:79], v10 offset:1024\n"
        "v_mfma_f32_16x16x32_bf16 a[24:27], v[116:119], a[116:119], a[24:27]\n"
        "ds_read_b128 a[80:83], v9 offset:16384\n"
        "v_mfma_f32_16x16x32_bf16 a[56:59], v[112:115], a[120:123], a[56:59]\n"
        "ds_read_b128 a[84:87], v9 offset:17408\n"
        "v_mfma_f32_16x16x32_bf16 a[56:59], v[116:119], a[124:127], a[56:59]\n"
        "v_mfma_f32_16x16x32_bf16 v[60:63], v[120:123], a[96:99], v[60:63]\n"
        "ds_read_b64_tr_b16 v[112:113], v3 offset:0\n"
        "v_mfma_f32_16x16x32_bf16 v[60:63], v[124:127], a[100:103], v[60:63]\n"
        "ds_read_b64_tr_b16 v[114:115], v3 offset:1024\n"
        "v_mfma_f32_16x16x32_bf16 v[92:95], v[120:123], a[104:107], v[92:95]\n"
        "ds_read_b64_tr_b16 v[116:117], v3 offset:8192\n"
        "v_mfma_f32_16x16x32_bf16 v[92:95], v[124:127], a[108:111], v[92:95]\n"
        "ds_read_b64_tr_b16 v[118:119], v3 offset:9216\n"
        "v_mfma_f32_16x16x32_bf16 a[28:31], v[120:123], a[112:115], a[28:31]\n"
        "ds_read_b128 a[88:91], v10 offset:16384\n"
        "v_mfma_f32_16x16x32_bf16 a[28:31], v[124:127], a[116:119], a[28:31]\n"
        "ds_read_b128 a[92:95], v10 offset:17408\n"
        "v_mfma_f32_16x16x32_bf16 a[60:63], v[120:123], a[120:123], a[60:63]\n"
        "v_mfma_f32_16x16x32_bf16 a[60:63], v[124:127], a[124:127], a[60:63]\n"
        "s_waitcnt lgkmcnt(9)\n"
        "v_mfma_f32_16x16x32_bf16 v[32:35], v[96:99], a[64:67], v[32:35]\n"
        "ds_read_b64_tr_b16 v[120:121], v4 offset:0\n"
        "v_mfma_f32_16x16x32_bf16 v[32:35], v[100:103], a[68:71], v[32:35]\n"
        "ds_read_b64_tr_b16 v[122:123], v4 offset:1024\n"
        "v_mfma_f32_16x16x32_bf16 v[64:67], v[96:99], a[72:75], v[64:67]\n"
        "ds_read_b64_tr_b16 v[124:125], v4 offset:8192\n"
        "s_waitcnt lgkmcnt(9)\n"
        "v_mfma_f32_16x16x32_bf16 v[64:67], v[100:103], a[76:79], v[64:67]\n"
        "ds_read_b64_tr_b16 v[126:127], v4 offset:9216\n"
        "v_mfma_f32_16x16x32_bf16 a[0:3], v[96:99], a[80:83], a[0:3]\n"
        "v_mfma_f32_16x16x32_bf16 a[0:3], v[100:103], a[84:87], a[0:3]\n"
        "s_waitcnt lgkmcnt(5)\n"
        "v_mfma_f32_16x16x32_bf16 a[32:35], v[96:99], a[88:91], a[32:35]\n"
        "s_waitcnt lgkmcnt(4)\n"
        "v_mfma_f32_16x16x32_bf16 a[32:35], v[100:103], a[92:95], a[32:35]\n"
        "v_mfma_f32_16x16x32_bf16 v[36:39], v[104:107], a[64:67], v[36:39]\n"
        "ds_read_b64_tr_b16 v[96:97], v5 offset:0\n"
        "v_mfma_f32_16x16x32_bf16 v[36:39], v[108:111], a[68:71], v[36:39]\n"
        "ds_read_b64_tr_b16 v[98:99], v5 offset:1024\n"
        "v_mfma_f32_16x16x32_bf16 v[68:71], v[104:107], a[72:75], v[68:71]\n"
        "ds_read_b64_tr_b16 v[100:101], v5 offset:8192\n"
        "v_mfma_f32_16x16x32_bf16 v[68:71], v[108:111], a[76:79], v[68:71]\n"
        "ds_read_b64_tr_b16 v[102:103], v5 offset:9216\n"
        "v_mfma_f32_16x16x32_bf16 a[4:7], v[104:107], a[80:83], a[4:7]\n"
        "v_mfma_f32_16x16x32_bf16 a[4:7], v[108:111], a[84:87], a[4:7]\n"
        "v_mfma_f32_16x16x32_bf16 a[36:39], v[104:107], a[88:91], a[36:39]\n"
        "v_mfma_f32_16x16x32_bf16 a[36:39], v[108:111], a[92:95], a[36:39]\n"
        "v_mfma_f32_16x16x32_bf16 v[40:43], v[112:115], a[64:67], v[40:43]\n"
        "ds_read_b64_tr_b16 v[104:105], v6 offset:0\n"
        "v_mfma_f32_16x16x32_bf16 v[40:43], v[116:119], a[68:71], v[40:43]\n"
        "ds_read_b64_tr_b16 v[106:107], v6 offset:1024\n"
        "v_mfma_f32_16x16x32_bf16 v[72:75], v[112:115], a[72:75], v[72:75]\n"
        "ds_read_b64_tr_b16 v[108:109], v6 offset:8192\n"
        "v_mfma_f32_16x16x32_bf16 v[72:75], v[116:119], a[76:79], v[72:75]\n"
        "ds_read_b64_tr_b16 v[110:111], v6 offset:9216\n"
        "v_mfma_f32_16x16x32_bf16 a[8:11], v[112:115], a[80:83], a[8:11]\n"
        "v_mfma_f32_16x16x32_bf16 a[8:11], v[116:119], a[84:87], a[8:11]\n"
        "v_mfma_f32_16x16x32_bf16 a[40:43], v[112:115], a[88:91], a[40:43]\n"
        "v_mfma_f32_16x16x32_bf16 a[40:43], v[116:119], a[92:95], a[40:43]\n"
        "s_waitcnt lgkmcnt(6)\n"
        "v_mfma_f32_16x16x32_bf16 v[44:47], v[120:123], a[64:67], v[44:47]\n"
        "ds_read_b64_tr_b16 v[112:113], v7 offset:0\n"
        "v_mfma_f32_16x16x32_bf16 v[44:47], v[124:127], a[68:71], v[44:47]\n"
        "ds_read_b64_tr_b16 v[114:115], v7 offset:1024\n"
        "v_mfma_f32_16x16x32_bf16 v[76:79], v[120:123], a[72:75], v[76:79]\n"
        "ds_read_b64_tr_b16 v[116:117], v7 offset:8192\n"
        "v_mfma_f32_16x16x32_bf16 v[76:79], v[124:127], a[76:79], v[76:79]\n"
        "ds_read_b64_tr_b16 v[118:119], v7 offset:9216\n"
        "v_mfma_f32_16x16x32_bf16 a[12:15], v[120:123], a[80:83], a[12:15]\n"
        "v_mfma_f32_16x16x32_bf16 a[12:15], v[124:127], a[84:87], a[12:15]\n"
        "v_mfma_f32_16x16x32_bf16 a[44:47], v[120:123], a[88:91], a[44:47]\n"
        "v_mfma_f32_16x16x32_bf16 a[44:47], v[124:127], a[92:95], a[44:47]\n"
        "v_mfma_f32_16x16x32_bf16 v[48:51], v[96:99], a[64:67], v[48:51]\n"
        "ds_read_b64_tr_b16 v[120:121], v8 offset:0\n"
        "s_waitcnt lgkmcnt(6)\n"
        "v_mfma_f32_16x16x32_bf16 v[48:51], v[100:103], a[68:71], v[48:51]\n"
        "ds_read_b64_tr_b16 v[122:123], v8 offset:1024\n"
        "v_mfma_f32_16x16x32_bf16 v[80:83], v[96:99], a[72:75], v[80:83]\n"
        "ds_read_b64_tr_b16 v[124:125], v8 offset:8192\n"
        "v_mfma_f32_16x16x32_bf16 v[80:83], v[100:103], a[76:79], v[80:83]\n"
        "ds_read_b64_tr_b16 v[126:127], v8 offset:9216\n"
        "v_mfma_f32_16x16x32_bf16 a[16:19], v[96:99], a[80:83], a[16:19]\n"
        "v_mfma_f32_16x16x32_bf16 a[16:19], v[100:103], a[84:87], a[16:19]\n"
        "v_mfma_f32_16x16x32_bf16 a[48:51], v[96:99], a[88:91], a[48:51]\n"
        "v_mfma_f32_16x16x32_bf16 a[48:51], v[100:103], a[92:95], a[48:51]\n"
        "s_waitcnt lgkmcnt(0)\n"
        "s_barrier\n"
        "v_subrev_u32 v1, 98304, v1\n"
        "v_subrev_u32 v2, 98304, v2\n"
        "v_subrev_u32 v3, 98304, v3\n"
        "v_subrev_u32 v4, 98304, v4\n"
        "v_subrev_u32 v5, 98304, v5\n"
        "v_subrev_u32 v6, 98304, v6\n"
        "v_subrev_u32 v7, 98304, v7\n"
        "v_subrev_u32 v8, 98304, v8\n"
        "v_subrev_u32 v9, 98304, v9\n"
        "v_subrev_u32 v10, 98304, v10\n"
        "v_mfma_f32_16x16x32_bf16 v[52:55], v[104:107], a[64:67], v[52:55]\n"
        "ds_read_b64_tr_b16 v[96:97], v1 offset:0\n"
        "v_mfma_f32_16x16x32_bf16 v[52:55], v[108:111], a[68:71], v[52:55]\n"
        "ds_read_b64_tr_b16 v[98:99], v1 offset:1024\n"
        "v_mfma_f32_16x16x32_bf16 v[84:87], v[104:107], a[72:75], v[84:87]\n"
        "ds_read_b64_tr_b16 v[100:101], v1 offset:8192\n"
        "v_mfma_f32_16x16x32_bf16 v[84:87], v[108:111], a[76:79], v[84:87]\n"
        "ds_read_b64_tr_b16 v[102:103], v1 offset:9216\n"
        "v_mfma_f32_16x16x32_bf16 a[20:23], v[104:107], a[80:83], a[20:23]\n"
        "ds_read_b128 a[96:99], v9 offset:0\n"
        "v_mfma_f32_16x16x32_bf16 a[20:23], v[108:111], a[84:87], a[20:23]\n"
        "ds_read_b128 a[100:103], v9 offset:1024\n"
        "v_mfma_f32_16x16x32_bf16 a[52:55], v[104:107], a[88:91], a[52:55]\n"
        "ds_read_b128 a[104:107], v10 offset:0\n"
        "v_mfma_f32_16x16x32_bf16 a[52:55], v[108:111], a[92:95], a[52:55]\n"
        "v_mfma_f32_16x16x32_bf16 v[56:59], v[112:115], a[64:67], v[56:59]\n"
        "ds_read_b64_tr_b16 v[104:105], v2 offset:0\n"
        "v_mfma_f32_16x16x32_bf16 v[56:59], v[116:119], a[68:71], v[56:59]\n"
        "ds_read_b64_tr_b16 v[106:107], v2 offset:1024\n"
        "v_mfma_f32_16x16x32_bf16 v[88:91], v[112:115], a[72:75], v[88:91]\n"
        "ds_read_b64_tr_b16 v[108:109], v2 offset:8192\n"
        "v_mfma_f32_16x16x32_bf16 v[88:91], v[116:119], a[76:79], v[88:91]\n"
        "ds_read_b64_tr_b16 v[110:111], v2 offset:9216\n"
        "v_mfma_f32_16x16x32_bf16 a[24:27], v[112:115], a[80:83], a[24:27]\n"
        "ds_read_b128 a[108:111], v10 offset:1024\n"
        "v_mfma_f32_16x16x32_bf16 a[24:27], v[116:119], a[84:87], a[24:27]\n"
        "ds_read_b128 a[112:115], v9 offset:16384\n"
        "v_mfma_f32_16x16x32_bf16 a[56:59], v[112:115], a[88:91], a[56:59]\n"
        "ds_read_b128 a[116:119], v9 offset:17408\n"
        "v_mfma_f32_16x16x32_bf16 a[56:59], v[116:119], a[92:95], a[56:59]\n"
        "v_mfma_f32_16x16x32_bf16 v[60:63], v[120:123], a[64:67], v[60:63]\n"
        "ds_read_b64_tr_b16 v[112:113], v3 offset:0\n"
        "v_mfma_f32_16x16x32_bf16 v[60:63], v[124:127], a[68:71], v[60:63]\n"
        "ds_read_b64_tr_b16 v[114:115], v3 offset:1024\n"
        "v_mfma_f32_16x16x32_bf16 v[92:95], v[120:123], a[72:75], v[92:95]\n"
        "ds_read_b64_tr_b16 v[116:117], v3 offset:8192\n"
        "v_mfma_f32_16x16x32_bf16 v[92:95], v[124:127], a[76:79], v[92:95]\n"
        "ds_read_b64_tr_b16 v[118:119], v3 offset:9216\n"
        "v_mfma_f32_16x16x32_bf16 a[28:31], v[120:123], a[80:83], a[28:31]\n"
        "ds_read_b128 a[120:123], v10 offset:16384\n"
        "v_mfma_f32_16x16x32_bf16 a[28:31], v[124:127], a[84:87], a[28:31]\n"
        "ds_read_b128 a[124:127], v10 offset:17408\n"
        "v_mfma_f32_16x16x32_bf16 a[60:63], v[120:123], a[88:91], a[60:63]\n"
        "v_mfma_f32_16x16x32_bf16 a[60:63], v[124:127], a[92:95], a[60:63]\n"
        "s_waitcnt lgkmcnt(9)\n"
        "v_mfma_f32_16x16x32_bf16 v[32:35], v[96:99], a[96:99], v[32:35]\n"
        "ds_read_b64_tr_b16 v[120:121], v4 offset:0\n"
        "v_mfma_f32_16x16x32_bf16 v[32:35], v[100:103], a[100:103], v[32:35]\n"
        "ds_read_b64_tr_b16 v[122:123], v4 offset:1024\n"
        "v_mfma_f32_16x16x32_bf16 v[64:67], v[96:99], a[104:107], v[64:67]\n"
        "ds_read_b64_tr_b16 v[124:125], v4 offset:8192\n"
        "s_waitcnt lgkmcnt(9)\n"
        "v_mfma_f32_16x16x32_bf16 v[64:67], v[100:103], a[108:111], v[64:67]\n"
        "ds_read_b64_tr_b16 v[126:127], v4 offset:9216\n"
        "v_mfma_f32_16x16x32_bf16 a[0:3], v[96:99], a[112:115], a[0:3]\n"
        "v_mfma_f32_16x16x32_bf16 a[0:3], v[100:103], a[116:119], a[0:3]\n"
        "s_waitcnt lgkmcnt(5)\n"
        "v_mfma_f32_16x16x32_bf16 a[32:35], v[96:99], a[120:123], a[32:35]\n"
        "s_waitcnt lgkmcnt(4)\n"
        "v_mfma_f32_16x16x32_bf16 a[32:35], v[100:103], a[124:127], a[32:35]\n"
        "v_mfma_f32_16x16x32_bf16 v[36:39], v[104:107], a[96:99], v[36:39]\n"
        "ds_read_b64_tr_b16 v[96:97], v5 offset:0\n"
        "v_mfma_f32_16x16x32_bf16 v[36:39], v[108:111], a[100:103], v[36:39]\n"
        "ds_read_b64_tr_b16 v[98:99], v5 offset:1024\n"
        "v_mfma_f32_16x16x32_bf16 v[68:71], v[104:107], a[104:107], v[68:71]\n"
        "ds_read_b64_tr_b16 v[100:101], v5 offset:8192\n"
        "v_mfma_f32_16x16x32_bf16 v[68:71], v[108:111], a[108:111], v[68:71]\n"
        "ds_read_b64_tr_b16 v[102:103], v5 offset:9216\n"
        "v_mfma_f32_16x16x32_bf16 a[4:7], v[104:107], a[112:115], a[4:7]\n"
        "v_mfma_f32_16x16x32_bf16 a[4:7], v[108:111], a[116:119], a[4:7]\n"
        "v_mfma_f32_16x16x32_bf16 a[36:39], v[104:107], a[120:123], a[36:39]\n"
        "v_mfma_f32_16x16x32_bf16 a[36:39], v[108:111], a[124:127], a[36:39]\n"
        "v_mfma_f32_16x16x32_bf16 v[40:43], v[112:115], a[96:99], v[40:43]\n"
        "ds_read_b64_tr_b16 v[104:105], v6 offset:0\n"
        "v_mfma_f32_16x16x32_bf16 v[40:43], v[116:119], a[100:103], v[40:43]\n"
        "ds_read_b64_tr_b16 v[106:107], v6 offset:1024\n"
        "v_mfma_f32_16x16x32_bf16 v[72:75], v[112:115], a[104:107], v[72:75]\n"
        "ds_read_b64_tr_b16 v[108:109], v6 offset:8192\n"
        "v_mfma_f32_16x16x32_bf16 v[72:75], v[116:119], a[108:111], v[72:75]\n"
        "ds_read_b64_tr_b16 v[110:111], v6 offset:9216\n"
        "v_mfma_f32_16x16x32_bf16 a[8:11], v[112:115], a[112:115], a[8:11]\n"
        "v_mfma_f32_16x16x32_bf16 a[8:11], v[116:119], a[116:119], a[8:11]\n"
        "v_mfma_f32_16x16x32_bf16 a[40:43], v[112:115], a[120:123], a[40:43]\n"
        "v_mfma_f32_16x16x32_bf16 a[40:43], v[116:119], a[124:127], a[40:43]\n"
        "s_waitcnt lgkmcnt(6)\n"
        "v_mfma_f32_16x16x32_bf16 v[44:47], v[120:123], a[96:99], v[44:47]\n"
        "ds_read_b64_tr_b16 v[112:113], v7 offset:0\n"
        "v_mfma_f32_16x16x32_bf16 v[44:47], v[124:127], a[100:103], v[44:47]\n"
        "ds_read_b64_tr_b16 v[114:115], v7 offset:1024\n"
        "v_mfma_f32_16x16x32_bf16 v[76:79], v[120:123], a[104:107], v[76:79]\n"
        "ds_read_b64_tr_b16 v[116:117], v7 offset:8192\n"
        "v_mfma_f32_16x16x32_bf16 v[76:79], v[124:127], a[108:111], v[76:79]\n"
        "ds_read_b64_tr_b16 v[118:119], v7 offset:9216\n"
        "v_mfma_f32_16x16x32_bf16 a[12:15], v[120:123], a[112:115], a[12:15]\n"
        "v_mfma_f32_16x16x32_bf16 a[12:15], v[124:127], a[116:119], a[12:15]\n"
        "v_mfma_f32_16x16x32_bf16 a[44:47], v[120:123], a[120:123], a[44:47]\n"
        "v_mfma_f32_16x16x32_bf16 a[44:47], v[124:127], a[124:127], a[44:47]\n"
        "v_mfma_f32_16x16x32_bf16 v[48:51], v[96:99], a[96:99], v[48:51]\n"
        "ds_read_b64_tr_b16 v[120:121], v8 offset:0\n"
        "s_waitcnt lgkmcnt(6)\n"
        "v_mfma_f32_16x16x32_bf16 v[48:51], v[100:103], a[100:103], v[48:51]\n"
        "ds_read_b64_tr_b16 v[122:123], v8 offset:1024\n"
        "v_mfma_f32_16x16x32_bf16 v[80:83], v[96:99], a[104:107], v[80:83]\n"
        "ds_read_b64_tr_b16 v[124:125], v8 offset:8192\n"
        "v_mfma_f32_16x16x32_bf16 v[80:83], v[100:103], a[108:111], v[80:83]\n"
        "ds_read_b64_tr_b16 v[126:127], v8 offset:9216\n"
        "v_mfma_f32_16x16x32_bf16 a[16:19], v[96:99], a[112:115], a[16:19]\n"
        "v_mfma_f32_16x16x32_bf16 a[16:19], v[100:103], a[116:119], a[16:19]\n"
        "v_mfma_f32_16x16x32_bf16 a[48:51], v[96:99], a[120:123], a[48:51]\n"
        "v_mfma_f32_16x16x32_bf16 a[48:51], v[100:103], a[124:127], a[48:51]\n"
        "s_waitcnt lgkmcnt(0)\n"
        "s_barrier\n"
        "v_add_u32 v1, 49152, v1\n"
        "v_add_u32 v2, 49152, v2\n"
        "v_add_u32 v3, 49152, v3\n"
        "v_add_u32 v4, 49152, v4\n"
        "v_add_u32 v5, 49152, v5\n"
        "v_add_u32 v6, 49152, v6\n"
        "v_add_u32 v7, 49152, v7\n"
        "v_add_u32 v8, 49152, v8\n"
        "v_add_u32 v9, 49152, v9\n"
        "v_add_u32 v10, 49152, v10\n"
        "v_mfma_f32_16x16x32_bf16 v[52:55], v[104:107], a[96:99], v[52:55]\n"
        "ds_read_b64_tr_b16 v[96:97], v1 offset:0\n"
        "v_mfma_f32_16x16x32_bf16 v[52:55], v[108:111], a[100:103], v[52:55]\n"
        "ds_read_b64_tr_b16 v[98:99], v1 offset:1024\n"
        "v_mfma_f32_16x16x32_bf16 v[84:87], v[104:107], a[104:107], v[84:87]\n"
        "ds_read_b64_tr_b16 v[100:101], v1 offset:8192\n"
        "v_mfma_f32_16x16x32_bf16 v[84:87], v[108:111], a[108:111], v[84:87]\n"
        "ds_read_b64_tr_b16 v[102:103], v1 offset:9216\n"
        "v_mfma_f32_16x16x32_bf16 a[20:23], v[104:107], a[112:115], a[20:23]\n"
        "ds_read_b128 a[64:67], v9 offset:0\n"
        "v_mfma_f32_16x16x32_bf16 a[20:23], v[108:111], a[116:119], a[20:23]\n"
        "ds_read_b128 a[68:71], v9 offset:1024\n"
        "v_mfma_f32_16x16x32_bf16 a[52:55], v[104:107], a[120:123], a[52:55]\n"
        "ds_read_b128 a[72:75], v10 offset:0\n"
        "v_mfma_f32_16x16x32_bf16 a[52:55], v[108:111], a[124:127], a[52:55]\n"
        "v_mfma_f32_16x16x32_bf16 v[56:59], v[112:115], a[96:99], v[56:59]\n"
        "ds_read_b64_tr_b16 v[104:105], v2 offset:0\n"
        "v_mfma_f32_16x16x32_bf16 v[56:59], v[116:119], a[100:103], v[56:59]\n"
        "ds_read_b64_tr_b16 v[106:107], v2 offset:1024\n"
        "v_mfma_f32_16x16x32_bf16 v[88:91], v[112:115], a[104:107], v[88:91]\n"
        "ds_read_b64_tr_b16 v[108:109], v2 offset:8192\n"
        "v_mfma_f32_16x16x32_bf16 v[88:91], v[116:119], a[108:111], v[88:91]\n"
        "ds_read_b64_tr_b16 v[110:111], v2 offset:9216\n"
        "v_mfma_f32_16x16x32_bf16 a[24:27], v[112:115], a[112:115], a[24:27]\n"
        "ds_read_b128 a[76:79], v10 offset:1024\n"
        "v_mfma_f32_16x16x32_bf16 a[24:27], v[116:119], a[116:119], a[24:27]\n"
        "ds_read_b128 a[80:83], v9 offset:16384\n"
        "v_mfma_f32_16x16x32_bf16 a[56:59], v[112:115], a[120:123], a[56:59]\n"
        "ds_read_b128 a[84:87], v9 offset:17408\n"
        "v_mfma_f32_16x16x32_bf16 a[56:59], v[116:119], a[124:127], a[56:59]\n"
        "v_mfma_f32_16x16x32_bf16 v[60:63], v[120:123], a[96:99], v[60:63]\n"
        "ds_read_b64_tr_b16 v[112:113], v3 offset:0\n"
        "v_mfma_f32_16x16x32_bf16 v[60:63], v[124:127], a[100:103], v[60:63]\n"
        "ds_read_b64_tr_b16 v[114:115], v3 offset:1024\n"
        "v_mfma_f32_16x16x32_bf16 v[92:95], v[120:123], a[104:107], v[92:95]\n"
        "ds_read_b64_tr_b16 v[116:117], v3 offset:8192\n"
        "v_mfma_f32_16x16x32_bf16 v[92:95], v[124:127], a[108:111], v[92:95]\n"
        "ds_read_b64_tr_b16 v[118:119], v3 offset:9216\n"
        "v_mfma_f32_16x16x32_bf16 a[28:31], v[120:123], a[112:115], a[28:31]\n"
        "ds_read_b128 a[88:91], v10 offset:16384\n"
        "v_mfma_f32_16x16x32_bf16 a[28:31], v[124:127], a[116:119], a[28:31]\n"
        "ds_read_b128 a[92:95], v10 offset:17408\n"
        "v_mfma_f32_16x16x32_bf16 a[60:63], v[120:123], a[120:123], a[60:63]\n"
        "v_mfma_f32_16x16x32_bf16 a[60:63], v[124:127], a[124:127], a[60:63]\n"
        "s_waitcnt lgkmcnt(9)\n"
        "v_mfma_f32_16x16x32_bf16 v[32:35], v[96:99], a[64:67], v[32:35]\n"
        "ds_read_b64_tr_b16 v[120:121], v4 offset:0\n"
        "v_mfma_f32_16x16x32_bf16 v[32:35], v[100:103], a[68:71], v[32:35]\n"
        "ds_read_b64_tr_b16 v[122:123], v4 offset:1024\n"
        "v_mfma_f32_16x16x32_bf16 v[64:67], v[96:99], a[72:75], v[64:67]\n"
        "ds_read_b64_tr_b16 v[124:125], v4 offset:8192\n"
        "s_waitcnt lgkmcnt(9)\n"
        "v_mfma_f32_16x16x32_bf16 v[64:67], v[100:103], a[76:79], v[64:67]\n"
        "ds_read_b64_tr_b16 v[126:127], v4 offset:9216\n"
        "v_mfma_f32_16x16x32_bf16 a[0:3], v[96:99], a[80:83], a[0:3]\n"
        "v_mfma_f32_16x16x32_bf16 a[0:3], v[100:103], a[84:87], a[0:3]\n"
        "s_waitcnt lgkmcnt(5)\n"
        "v_mfma_f32_16x16x32_bf16 a[32:35], v[96:99], a[88:91], a[32:35]\n"
        "s_waitcnt lgkmcnt(4)\n"
        "v_mfma_f32_16x16x32_bf16 a[32:35], v[100:103], a[92:95], a[32:35]\n"
        "v_mfma_f32_16x16x32_bf16 v[36:39], v[104:107], a[64:67], v[36:39]\n"
        "ds_read_b64_tr_b16 v[96:97], v5 offset:0\n"
        "v_mfma_f32_16x16x32_bf16 v[36:39], v[108:111], a[68:71], v[36:39]\n"
        "ds_read_b64_tr_b16 v[98:99], v5 offset:1024\n"
        "v_mfma_f32_16x16x32_bf16 v[68:71], v[104:107], a[72:75], v[68:71]\n"
        "ds_read_b64_tr_b16 v[100:101], v5 offset:8192\n"
        "v_mfma_f32_16x16x32_bf16 v[68:71], v[108:111], a[76:79], v[68:71]\n"
        "ds_read_b64_tr_b16 v[102:103], v5 offset:9216\n"
        "v_mfma_f32_16x16x32_bf16 a[4:7], v[104:107], a[80:83], a[4:7]\n"
        "v_mfma_f32_16x16x32_bf16 a[4:7], v[108:111], a[84:87], a[4:7]\n"
        "v_mfma_f32_16x16x32_bf16 a[36:39], v[104:107], a[88:91], a[36:39]\n"
        "v_mfma_f32_16x16x32_bf16 a[36:39], v[108:111], a[92:95], a[36:39]\n"
        "v_mfma_f32_16x16x32_bf16 v[40:43], v[112:115], a[64:67], v[40:43]\n"
        "ds_read_b64_tr_b16 v[104:105], v6 offset:0\n"
        "v_mfma_f32_16x16x32_bf16 v[40:43], v[116:119], a[68:71], v[40:43]\n"
        "ds_read_b64_tr_b16 v[106:107], v6 offset:1024\n"
        "v_mfma_f32_16x16x32_bf16 v[72:75], v[112:115], a[72:75], v[72:75]\n"
        "ds_read_b64_tr_b16 v[108:109], v6 offset:8192\n"
        "v_mfma_f32_16x16x32_bf16 v[72:75], v[116:119], a[76:79], v[72:75]\n"
        "ds_read_b64_tr_b16 v[110:111], v6 offset:9216\n"
        "v_mfma_f32_16x16x32_bf16 a[8:11], v[112:115], a[80:83], a[8:11]\n"
        "v_mfma_f32_16x16x32_bf16 a[8:11], v[116:119], a[84:87], a[8:11]\n"
        "v_mfma_f32_16x16x32_bf16 a[40:43], v[112:115], a[88:91], a[40:43]\n"
        "v_mfma_f32_16x16x32_bf16 a[40:43], v[116:119], a[92:95], a[40:43]\n"
        "s_waitcnt lgkmcnt(6)\n"
        "v_mfma_f32_16x16x32_bf16 v[44:47], v[120:123], a[64:67], v[44:47]\n"
        "ds_read_b64_tr_b16 v[112:113], v7 offset:0\n"
        "v_mfma_f32_16x16x32_bf16 v[44:47], v[124:127], a[68:71], v[44:47]\n"
        "ds_read_b64_tr_b16 v[114:115], v7 offset:1024\n"
        "v_mfma_f32_16x16x32_bf16 v[76:79], v[120:123], a[72:75], v[76:79]\n"
        "ds_read_b64_tr_b16 v[116:117], v7 offset:8192\n"
        "v_mfma_f32_16x16x32_bf16 v[76:79], v[124:127], a[76:79], v[76:79]\n"
        "ds_read_b64_tr_b16 v[118:119], v7 offset:9216\n"
        "v_mfma_f32_16x16x32_bf16 a[12:15], v[120:123], a[80:83], a[12:15]\n"
        "v_mfma_f32_16x16x32_bf16 a[12:15], v[124:127], a[84:87], a[12:15]\n"
        "v_mfma_f32_16x16x32_bf16 a[44:47], v[120:123], a[88:91], a[44:47]\n"
        "v_mfma_f32_16x16x32_bf16 a[44:47], v[124:127], a[92:95], a[44:47]\n"
        "v_mfma_f32_16x16x32_bf16 v[48:51], v[96:99], a[64:67], v[48:51]\n"
        "ds_read_b64_tr_b16 v[120:121], v8 offset:0\n"
        "s_waitcnt lgkmcnt(6)\n"
        "v_mfma_f32_16x16x32_bf16 v[48:51], v[100:103], a[68:71], v[48:51]\n"
        "ds_read_b64_tr_b16 v[122:123], v8 offset:1024\n"
        "v_mfma_f32_16x16x32_bf16 v[80:83], v[96:99], a[72:75], v[80:83]\n"
        "ds_read_b64_tr_b16 v[124:125], v8 offset:8192\n"
        "v_mfma_f32_16x16x32_bf16 v[80:83], v[100:103], a[76:79], v[80:83]\n"
        "ds_read_b64_tr_b16 v[126:127], v8 offset:9216\n"
        "v_mfma_f32_16x16x32_bf16 a[16:19], v[96:99], a[80:83], a[16:19]\n"
        "v_mfma_f32_16x16x32_bf16 a[16:19], v[100:103], a[84:87], a[16:19]\n"
        "v_mfma_f32_16x16x32_bf16 a[48:51], v[96:99], a[88:91], a[48:51]\n"
        "v_mfma_f32_16x16x32_bf16 a[48:51], v[100:103], a[92:95], a[48:51]\n"
        "s_waitcnt lgkmcnt(0)\n"
        "s_barrier\n"
        "v_add_u32 v1, 49152, v1\n"
        "v_add_u32 v2, 49152, v2\n"
        "v_add_u32 v3, 49152, v3\n"
        "v_add_u32 v4, 49152, v4\n"
        "v_add_u32 v5, 49152, v5\n"
        "v_add_u32 v6, 49152, v6\n"
        "v_add_u32 v7, 49152, v7\n"
        "v_add_u32 v8, 49152, v8\n"
        "v_add_u32 v9, 49152, v9\n"
        "v_add_u32 v10, 49152, v10\n"
        "v_mfma_f32_16x16x32_bf16 v[52:55], v[104:107], a[64:67], v[52:55]\n"
        "ds_read_b64_tr_b16 v[96:97], v1 offset:0\n"
        "v_mfma_f32_16x16x32_bf16 v[52:55], v[108:111], a[68:71], v[52:55]\n"
        "ds_read_b64_tr_b16 v[98:99], v1 offset:1024\n"
        "v_mfma_f32_16x16x32_bf16 v[84:87], v[104:107], a[72:75], v[84:87]\n"
        "ds_read_b64_tr_b16 v[100:101], v1 offset:8192\n"
        "v_mfma_f32_16x16x32_bf16 v[84:87], v[108:111], a[76:79], v[84:87]\n"
        "ds_read_b64_tr_b16 v[102:103], v1 offset:9216\n"
        "v_mfma_f32_16x16x32_bf16 a[20:23], v[104:107], a[80:83], a[20:23]\n"
        "ds_read_b128 a[96:99], v9 offset:0\n"
        "v_mfma_f32_16x16x32_bf16 a[20:23], v[108:111], a[84:87], a[20:23]\n"
        "ds_read_b128 a[100:103], v9 offset:1024\n"
        "v_mfma_f32_16x16x32_bf16 a[52:55], v[104:107], a[88:91], a[52:55]\n"
        "ds_read_b128 a[104:107], v10 offset:0\n"
        "v_mfma_f32_16x16x32_bf16 a[52:55], v[108:111], a[92:95], a[52:55]\n"
        "v_mfma_f32_16x16x32_bf16 v[56:59], v[112:115], a[64:67], v[56:59]\n"
        "ds_read_b64_tr_b16 v[104:105], v2 offset:0\n"
        "v_mfma_f32_16x16x32_bf16 v[56:59], v[116:119], a[68:71], v[56:59]\n"
        "ds_read_b64_tr_b16 v[106:107], v2 offset:1024\n"
        "v_mfma_f32_16x16x32_bf16 v[88:91], v[112:115], a[72:75], v[88:91]\n"
        "ds_read_b64_tr_b16 v[108:109], v2 offset:8192\n"
        "v_mfma_f32_16x16x32_bf16 v[88:91], v[116:119], a[76:79], v[88:91]\n"
        "ds_read_b64_tr_b16 v[110:111], v2 offset:9216\n"
        "v_mfma_f32_16x16x32_bf16 a[24:27], v[112:115], a[80:83], a[24:27]\n"
        "ds_read_b128 a[108:111], v10 offset:1024\n"
        "v_mfma_f32_16x16x32_bf16 a[24:27], v[116:119], a[84:87], a[24:27]\n"
        "ds_read_b128 a[112:115], v9 offset:16384\n"
        "v_mfma_f32_16x16x32_bf16 a[56:59], v[112:115], a[88:91], a[56:59]\n"
        "ds_read_b128 a[116:119], v9 offset:17408\n"
        "v_mfma_f32_16x16x32_bf16 a[56:59], v[116:119], a[92:95], a[56:59]\n"
        "v_mfma_f32_16x16x32_bf16 v[60:63], v[120:123], a[64:67], v[60:63]\n"
        "ds_read_b64_tr_b16 v[112:113], v3 offset:0\n"
        "v_mfma_f32_16x16x32_bf16 v[60:63], v[124:127], a[68:71], v[60:63]\n"
        "ds_read_b64_tr_b16 v[114:115], v3 offset:1024\n"
        "v_mfma_f32_16x16x32_bf16 v[92:95], v[120:123], a[72:75], v[92:95]\n"
        "ds_read_b64_tr_b16 v[116:117], v3 offset:8192\n"
        "v_mfma_f32_16x16x32_bf16 v[92:95], v[124:127], a[76:79], v[92:95]\n"
        "ds_read_b64_tr_b16 v[118:119], v3 offset:9216\n"
        "v_mfma_f32_16x16x32_bf16 a[28:31], v[120:123], a[80:83], a[28:31]\n"
        "ds_read_b128 a[120:123], v10 offset:16384\n"
        "v_mfma_f32_16x16x32_bf16 a[28:31], v[124:127], a[84:87], a[28:31]\n"
        "ds_read_b128 a[124:127], v10 offset:17408\n"
        "v_mfma_f32_16x16x32_bf16 a[60:63], v[120:123], a[88:91], a[60:63]\n"
        "v_mfma_f32_16x16x32_bf16 a[60:63], v[124:127], a[92:95], a[60:63]\n"
        "s_waitcnt lgkmcnt(9)\n"
        "v_mfma_f32_16x16x32_bf16 v[32:35], v[96:99], a[96:99], v[32:35]\n"
        "ds_read_b64_tr_b16 v[120:121], v4 offset:0\n"
        "v_mfma_f32_16x16x32_bf16 v[32:35], v[100:103], a[100:103], v[32:35]\n"
        "ds_read_b64_tr_b16 v[122:123], v4 offset:1024\n"
        "v_mfma_f32_16x16x32_bf16 v[64:67], v[96:99], a[104:107], v[64:67]\n"
        "ds_read_b64_tr_b16 v[124:125], v4 offset:8192\n"
        "s_waitcnt lgkmcnt(9)\n"
        "v_mfma_f32_16x16x32_bf16 v[64:67], v[100:103], a[108:111], v[64:67]\n"
        "ds_read_b64_tr_b16 v[126:127], v4 offset:9216\n"
        "v_mfma_f32_16x16x32_bf16 a[0:3], v[96:99], a[112:115], a[0:3]\n"
        "v_mfma_f32_16x16x32_bf16 a[0:3], v[100:103], a[116:119], a[0:3]\n"
        "s_waitcnt lgkmcnt(5)\n"
        "v_mfma_f32_16x16x32_bf16 a[32:35], v[96:99], a[120:123], a[32:35]\n"
        "s_waitcnt lgkmcnt(4)\n"
        "v_mfma_f32_16x16x32_bf16 a[32:35], v[100:103], a[124:127], a[32:35]\n"
        "v_mfma_f32_16x16x32_bf16 v[36:39], v[104:107], a[96:99], v[36:39]\n"
        "ds_read_b64_tr_b16 v[96:97], v5 offset:0\n"
        "v_mfma_f32_16x16x32_bf16 v[36:39], v[108:111], a[100:103], v[36:39]\n"
        "ds_read_b64_tr_b16 v[98:99], v5 offset:1024\n"
        "v_mfma_f32_16x16x32_bf16 v[68:71], v[104:107], a[104:107], v[68:71]\n"
        "ds_read_b64_tr_b16 v[100:101], v5 offset:8192\n"
        "v_mfma_f32_16x16x32_bf16 v[68:71], v[108:111], a[108:111], v[68:71]\n"
        "ds_read_b64_tr_b16 v[102:103], v5 offset:9216\n"
        "v_mfma_f32_16x16x32_bf16 a[4:7], v[104:107], a[112:115], a[4:7]\n"
        "v_mfma_f32_16x16x32_bf16 a[4:7], v[108:111], a[116:119], a[4:7]\n"
        "v_mfma_f32_16x16x32_bf16 a[36:39], v[104:107], a[120:123], a[36:39]\n"
        "v_mfma_f32_16x16x32_bf16 a[36:39], v[108:111], a[124:127], a[36:39]\n"
        "v_mfma_f32_16x16x32_bf16 v[40:43], v[112:115], a[96:99], v[40:43]\n"
        "ds_read_b64_tr_b16 v[104:105], v6 offset:0\n"
        "v_mfma_f32_16x16x32_bf16 v[40:43], v[116:119], a[100:103], v[40:43]\n"
        "ds_read_b64_tr_b16 v[106:107], v6 offset:1024\n"
        "v_mfma_f32_16x16x32_bf16 v[72:75], v[112:115], a[104:107], v[72:75]\n"
        "ds_read_b64_tr_b16 v[108:109], v6 offset:8192\n"
        "v_mfma_f32_16x16x32_bf16 v[72:75], v[116:119], a[108:111], v[72:75]\n"
        "ds_read_b64_tr_b16 v[110:111], v6 offset:9216\n"
        "v_mfma_f32_16x16x32_bf16 a[8:11], v[112:115], a[112:115], a[8:11]\n"
        "v_mfma_f32_16x16x32_bf16 a[8:11], v[116:119], a[116:119], a[8:11]\n"
        "v_mfma_f32_16x16x32_bf16 a[40:43], v[112:115], a[120:123], a[40:43]\n"
        "v_mfma_f32_16x16x32_bf16 a[40:43], v[116:119], a[124:127], a[40:43]\n"
        "s_waitcnt lgkmcnt(6)\n"
        "v_mfma_f32_16x16x32_bf16 v[44:47], v[120:123], a[96:99], v[44:47]\n"
        "ds_read_b64_tr_b16 v[112:113], v7 offset:0\n"
        "v_mfma_f32_16x16x32_bf16 v[44:47], v[124:127], a[100:103], v[44:47]\n"
        "ds_read_b64_tr_b16 v[114:115], v7 offset:1024\n"
        "v_mfma_f32_16x16x32_bf16 v[76:79], v[120:123], a[104:107], v[76:79]\n"
        "ds_read_b64_tr_b16 v[116:117], v7 offset:8192\n"
        "v_mfma_f32_16x16x32_bf16 v[76:79], v[124:127], a[108:111], v[76:79]\n"
        "ds_read_b64_tr_b16 v[118:119], v7 offset:9216\n"
        "v_mfma_f32_16x16x32_bf16 a[12:15], v[120:123], a[112:115], a[12:15]\n"
        "v_mfma_f32_16x16x32_bf16 a[12:15], v[124:127], a[116:119], a[12:15]\n"
        "v_mfma_f32_16x16x32_bf16 a[44:47], v[120:123], a[120:123], a[44:47]\n"
        "v_mfma_f32_16x16x32_bf16 a[44:47], v[124:127], a[124:127], a[44:47]\n"
        "v_mfma_f32_16x16x32_bf16 v[48:51], v[96:99], a[96:99], v[48:51]\n"
        "ds_read_b64_tr_b16 v[120:121], v8 offset:0\n"
        "s_waitcnt lgkmcnt(6)\n"
        "v_mfma_f32_16x16x32_bf16 v[48:51], v[100:103], a[100:103], v[48:51]\n"
        "ds_read_b64_tr_b16 v[122:123], v8 offset:1024\n"
        "v_mfma_f32_16x16x32_bf16 v[80:83], v[96:99], a[104:107], v[80:83]\n"
        "ds_read_b64_tr_b16 v[124:125], v8 offset:8192\n"
        "v_mfma_f32_16x16x32_bf16 v[80:83], v[100:103], a[108:111], v[80:83]\n"
        "ds_read_b64_tr_b16 v[126:127], v8 offset:9216\n"
        "v_mfma_f32_16x16x32_bf16 a[16:19], v[96:99], a[112:115], a[16:19]\n"
        "v_mfma_f32_16x16x32_bf16 a[16:19], v[100:103], a[116:119], a[16:19]\n"
        "v_mfma_f32_16x16x32_bf16 a[48:51], v[96:99], a[120:123], a[48:51]\n"
        "v_mfma_f32_16x16x32_bf16 a[48:51], v[100:103], a[124:127], a[48:51]\n"
        "s_waitcnt lgkmcnt(0)\n"
        "s_barrier\n"
        "v_subrev_u32 v1, 98304, v1\n"
        "v_subrev_u32 v2, 98304, v2\n"
        "v_subrev_u32 v3, 98304, v3\n"
        "v_subrev_u32 v4, 98304, v4\n"
        "v_subrev_u32 v5, 98304, v5\n"
        "v_subrev_u32 v6, 98304, v6\n"
        "v_subrev_u32 v7, 98304, v7\n"
        "v_subrev_u32 v8, 98304, v8\n"
        "v_subrev_u32 v9, 98304, v9\n"
        "v_subrev_u32 v10, 98304, v10\n"
        "v_mfma_f32_16x16x32_bf16 v[52:55], v[104:107], a[96:99], v[52:55]\n"
        "ds_read_b64_tr_b16 v[96:97], v1 offset:0\n"
        "v_mfma_f32_16x16x32_bf16 v[52:55], v[108:111], a[100:103], v[52:55]\n"
        "ds_read_b64_tr_b16 v[98:99], v1 offset:1024\n"
        "v_mfma_f32_16x16x32_bf16 v[84:87], v[104:107], a[104:107], v[84:87]\n"
        "ds_read_b64_tr_b16 v[100:101], v1 offset:8192\n"
        "v_mfma_f32_16x16x32_bf16 v[84:87], v[108:111], a[108:111], v[84:87]\n"
        "ds_read_b64_tr_b16 v[102:103], v1 offset:9216\n"
        "v_mfma_f32_16x16x32_bf16 a[20:23], v[104:107], a[112:115], a[20:23]\n"
        "ds_read_b128 a[64:67], v9 offset:0\n"
        "v_mfma_f32_16x16x32_bf16 a[20:23], v[108:111], a[116:119], a[20:23]\n"
        "ds_read_b128 a[68:71], v9 offset:1024\n"
        "v_mfma_f32_16x16x32_bf16 a[52:55], v[104:107], a[120:123], a[52:55]\n"
        "ds_read_b128 a[72:75], v10 offset:0\n"
        "v_mfma_f32_16x16x32_bf16 a[52:55], v[108:111], a[124:127], a[52:55]\n"
        "v_mfma_f32_16x16x32_bf16 v[56:59], v[112:115], a[96:99], v[56:59]\n"
        "ds_read_b64_tr_b16 v[104:105], v2 offset:0\n"
        "v_mfma_f32_16x16x32_bf16 v[56:59], v[116:119], a[100:103], v[56:59]\n"
        "ds_read_b64_tr_b16 v[106:107], v2 offset:1024\n"
        "v_mfma_f32_16x16x32_bf16 v[88:91], v[112:115], a[104:107], v[88:91]\n"
        "ds_read_b64_tr_b16 v[108:109], v2 offset:8192\n"
        "v_mfma_f32_16x16x32_bf16 v[88:91], v[116:119], a[108:111], v[88:91]\n"
        "ds_read_b64_tr_b16 v[110:111], v2 offset:9216\n"
        "v_mfma_f32_16x16x32_bf16 a[24:27], v[112:115], a[112:115], a[24:27]\n"
        "ds_read_b128 a[76:79], v10 offset:1024\n"
        "v_mfma_f32_16x16x32_bf16 a[24:27], v[116:119], a[116:119], a[24:27]\n"
        "ds_read_b128 a[80:83], v9 offset:16384\n"
        "v_mfma_f32_16x16x32_bf16 a[56:59], v[112:115], a[120:123], a[56:59]\n"
        "ds_read_b128 a[84:87], v9 offset:17408\n"
        "v_mfma_f32_16x16x32_bf16 a[56:59], v[116:119], a[124:127], a[56:59]\n"
        "v_mfma_f32_16x16x32_bf16 v[60:63], v[120:123], a[96:99], v[60:63]\n"
        "ds_read_b64_tr_b16 v[112:113], v3 offset:0\n"
        "v_mfma_f32_16x16x32_bf16 v[60:63], v[124:127], a[100:103], v[60:63]\n"
        "ds_read_b64_tr_b16 v[114:115], v3 offset:1024\n"
        "v_mfma_f32_16x16x32_bf16 v[92:95], v[120:123], a[104:107], v[92:95]\n"
        "ds_read_b64_tr_b16 v[116:117], v3 offset:8192\n"
        "v_mfma_f32_16x16x32_bf16 v[92:95], v[124:127], a[108:111], v[92:95]\n"
        "ds_read_b64_tr_b16 v[118:119], v3 offset:9216\n"
        "v_mfma_f32_16x16x32_bf16 a[28:31], v[120:123], a[112:115], a[28:31]\n"
        "ds_read_b128 a[88:91], v10 offset:16384\n"
        "v_mfma_f32_16x16x32_bf16 a[28:31], v[124:127], a[116:119], a[28:31]\n"
        "ds_read_b128 a[92:95], v10 offset:17408\n"
        "v_mfma_f32_16x16x32_bf16 a[60:63], v[120:123], a[120:123], a[60:63]\n"
        "v_mfma_f32_16x16x32_bf16 a[60:63], v[124:127], a[124:127], a[60:63]\n"
        "s_waitcnt lgkmcnt(9)\n"
        "v_mfma_f32_16x16x32_bf16 v[32:35], v[96:99], a[64:67], v[32:35]\n"
        "ds_read_b64_tr_b16 v[120:121], v4 offset:0\n"
        "v_mfma_f32_16x16x32_bf16 v[32:35], v[100:103], a[68:71], v[32:35]\n"
        "ds_read_b64_tr_b16 v[122:123], v4 offset:1024\n"
        "v_mfma_f32_16x16x32_bf16 v[64:67], v[96:99], a[72:75], v[64:67]\n"
        "ds_read_b64_tr_b16 v[124:125], v4 offset:8192\n"
        "s_waitcnt lgkmcnt(9)\n"
        "v_mfma_f32_16x16x32_bf16 v[64:67], v[100:103], a[76:79], v[64:67]\n"
        "ds_read_b64_tr_b16 v[126:127], v4 offset:9216\n"
        "v_mfma_f32_16x16x32_bf16 a[0:3], v[96:99], a[80:83], a[0:3]\n"
        "v_mfma_f32_16x16x32_bf16 a[0:3], v[100:103], a[84:87], a[0:3]\n"
        "s_waitcnt lgkmcnt(5)\n"
        "v_mfma_f32_16x16x32_bf16 a[32:35], v[96:99], a[88:91], a[32:35]\n"
        "s_waitcnt lgkmcnt(4)\n"
        "v_mfma_f32_16x16x32_bf16 a[32:35], v[100:103], a[92:95], a[32:35]\n"
        "v_mfma_f32_16x16x32_bf16 v[36:39], v[104:107], a[64:67], v[36:39]\n"
        "ds_read_b64_tr_b16 v[96:97], v5 offset:0\n"
        "v_mfma_f32_16x16x32_bf16 v[36:39], v[108:111], a[68:71], v[36:39]\n"
        "ds_read_b64_tr_b16 v[98:99], v5 offset:1024\n"
        "v_mfma_f32_16x16x32_bf16 v[68:71], v[104:107], a[72:75], v[68:71]\n"
        "ds_read_b64_tr_b16 v[100:101], v5 offset:8192\n"
        "v_mfma_f32_16x16x32_bf16 v[68:71], v[108:111], a[76:79], v[68:71]\n"
        "ds_read_b64_tr_b16 v[102:103], v5 offset:9216\n"
        "v_mfma_f32_16x16x32_bf16 a[4:7], v[104:107], a[80:83], a[4:7]\n"
        "v_mfma_f32_16x16x32_bf16 a[4:7], v[108:111], a[84:87], a[4:7]\n"
        "v_mfma_f32_16x16x32_bf16 a[36:39], v[104:107], a[88:91], a[36:39]\n"
        "v_mfma_f32_16x16x32_bf16 a[36:39], v[108:111], a[92:95], a[36:39]\n"
        "v_mfma_f32_16x16x32_bf16 v[40:43], v[112:115], a[64:67], v[40:43]\n"
        "ds_read_b64_tr_b16 v[104:105], v6 offset:0\n"
        "v_mfma_f32_16x16x32_bf16 v[40:43], v[116:119], a[68:71], v[40:43]\n"
        "ds_read_b64_tr_b16 v[106:107], v6 offset:1024\n"
        "v_mfma_f32_16x16x32_bf16 v[72:75], v[112:115], a[72:75], v[72:75]\n"
        "ds_read_b64_tr_b16 v[108:109], v6 offset:8192\n"
        "v_mfma_f32_16x16x32_bf16 v[72:75], v[116:119], a[76:79], v[72:75]\n"
        "ds_read_b64_tr_b16 v[110:111], v6 offset:9216\n"
        "v_mfma_f32_16x16x32_bf16 a[8:11], v[112:115], a[80:83], a[8:11]\n"
        "v_mfma_f32_16x16x32_bf16 a[8:11], v[116:119], a[84:87], a[8:11]\n"
        "v_mfma_f32_16x16x32_bf16 a[40:43], v[112:115], a[88:91], a[40:43]\n"
        "v_mfma_f32_16x16x32_bf16 a[40:43], v[116:119], a[92:95], a[40:43]\n"
        "s_waitcnt lgkmcnt(6)\n"
        "v_mfma_f32_16x16x32_bf16 v[44:47], v[120:123], a[64:67], v[44:47]\n"
        "ds_read_b64_tr_b16 v[112:113], v7 offset:0\n"
        "v_mfma_f32_16x16x32_bf16 v[44:47], v[124:127], a[68:71], v[44:47]\n"
        "ds_read_b64_tr_b16 v[114:115], v7 offset:1024\n"
        "v_mfma_f32_16x16x32_bf16 v[76:79], v[120:123], a[72:75], v[76:79]\n"
        "ds_read_b64_tr_b16 v[116:117], v7 offset:8192\n"
        "v_mfma_f32_16x16x32_bf16 v[76:79], v[124:127], a[76:79], v[76:79]\n"
        "ds_read_b64_tr_b16 v[118:119], v7 offset:9216\n"
        "v_mfma_f32_16x16x32_bf16 a[12:15], v[120:123], a[80:83], a[12:15]\n"
        "v_mfma_f32_16x16x32_bf16 a[12:15], v[124:127], a[84:87], a[12:15]\n"
        "v_mfma_f32_16x16x32_bf16 a[44:47], v[120:123], a[88:91], a[44:47]\n"
        "v_mfma_f32_16x16x32_bf16 a[44:47], v[124:127], a[92:95], a[44:47]\n"
        "v_mfma_f32_16x16x32_bf16 v[48:51], v[96:99], a[64:67], v[48:51]\n"
        "ds_read_b64_tr_b16 v[120:121], v8 offset:0\n"
        "s_waitcnt lgkmcnt(6)\n"
        "v_mfma_f32_16x16x32_bf16 v[48:51], v[100:103], a[68:71], v[48:51]\n"
        "ds_read_b64_tr_b16 v[122:123], v8 offset:1024\n"
        "v_mfma_f32_16x16x32_bf16 v[80:83], v[96:99], a[72:75], v[80:83]\n"
        "ds_read_b64_tr_b16 v[124:125], v8 offset:8192\n"
        "v_mfma_f32_16x16x32_bf16 v[80:83], v[100:103], a[76:79], v[80:83]\n"
        "ds_read_b64_tr_b16 v[126:127], v8 offset:9216\n"
        "v_mfma_f32_16x16x32_bf16 a[16:19], v[96:99], a[80:83], a[16:19]\n"
        "v_mfma_f32_16x16x32_bf16 a[16:19], v[100:103], a[84:87], a[16:19]\n"
        "v_mfma_f32_16x16x32_bf16 a[48:51], v[96:99], a[88:91], a[48:51]\n"
        "v_mfma_f32_16x16x32_bf16 a[48:51], v[100:103], a[92:95], a[48:51]\n"
        "s_waitcnt lgkmcnt(0)\n"
        "s_barrier\n"
        "v_add_u32 v1, 49152, v1\n"
        "v_add_u32 v2, 49152, v2\n"
        "v_add_u32 v3, 49152, v3\n"
        "v_add_u32 v4, 49152, v4\n"
        "v_add_u32 v5, 49152, v5\n"
        "v_add_u32 v6, 49152, v6\n"
        "v_add_u32 v7, 49152, v7\n"
        "v_add_u32 v8, 49152, v8\n"
        "v_add_u32 v9, 49152, v9\n"
        "v_add_u32 v10, 49152, v10\n"
        "v_mfma_f32_16x16x32_bf16 v[52:55], v[104:107], a[64:67], v[52:55]\n"
        "ds_read_b64_tr_b16 v[96:97], v1 offset:0\n"
        "v_mfma_f32_16x16x32_bf16 v[52:55], v[108:111], a[68:71], v[52:55]\n"
        "ds_read_b64_tr_b16 v[98:99], v1 offset:1024\n"
        "v_mfma_f32_16x16x32_bf16 v[84:87], v[104:107], a[72:75], v[84:87]\n"
        "ds_read_b64_tr_b16 v[100:101], v1 offset:8192\n"
        "v_mfma_f32_16x16x32_bf16 v[84:87], v[108:111], a[76:79], v[84:87]\n"
        "ds_read_b64_tr_b16 v[102:103], v1 offset:9216\n"
        "v_mfma_f32_16x16x32_bf16 a[20:23], v[104:107], a[80:83], a[20:23]\n"
        "ds_read_b128 a[96:99], v9 offset:0\n"
        "v_mfma_f32_16x16x32_bf16 a[20:23], v[108:111], a[84:87], a[20:23]\n"
        "ds_read_b128 a[100:103], v9 offset:1024\n"
        "v_mfma_f32_16x16x32_bf16 a[52:55], v[104:107], a[88:91], a[52:55]\n"
        "ds_read_b128 a[104:107], v10 offset:0\n"
        "v_mfma_f32_16x16x32_bf16 a[52:55], v[108:111], a[92:95], a[52:55]\n"
        "v_mfma_f32_16x16x32_bf16 v[56:59], v[112:115], a[64:67], v[56:59]\n"
        "ds_read_b64_tr_b16 v[104:105], v2 offset:0\n"
        "v_mfma_f32_16x16x32_bf16 v[56:59], v[116:119], a[68:71], v[56:59]\n"
        "ds_read_b64_tr_b16 v[106:107], v2 offset:1024\n"
        "v_mfma_f32_16x16x32_bf16 v[88:91], v[112:115], a[72:75], v[88:91]\n"
        "ds_read_b64_tr_b16 v[108:109], v2 offset:8192\n"
        "v_mfma_f32_16x16x32_bf16 v[88:91], v[116:119], a[76:79], v[88:91]\n"
        "ds_read_b64_tr_b16 v[110:111], v2 offset:9216\n"
        "v_mfma_f32_16x16x32_bf16 a[24:27], v[112:115], a[80:83], a[24:27]\n"
        "ds_read_b128 a[108:111], v10 offset:1024\n"
        "v_mfma_f32_16x16x32_bf16 a[24:27], v[116:119], a[84:87], a[24:27]\n"
        "ds_read_b128 a[112:115], v9 offset:16384\n"
        "v_mfma_f32_16x16x32_bf16 a[56:59], v[112:115], a[88:91], a[56:59]\n"
        "ds_read_b128 a[116:119], v9 offset:17408\n"
        "v_mfma_f32_16x16x32_bf16 a[56:59], v[116:119], a[92:95], a[56:59]\n"
        "v_mfma_f32_16x16x32_bf16 v[60:63], v[120:123], a[64:67], v[60:63]\n"
        "ds_read_b64_tr_b16 v[112:113], v3 offset:0\n"
        "v_mfma_f32_16x16x32_bf16 v[60:63], v[124:127], a[68:71], v[60:63]\n"
        "ds_read_b64_tr_b16 v[114:115], v3 offset:1024\n"
        "v_mfma_f32_16x16x32_bf16 v[92:95], v[120:123], a[72:75], v[92:95]\n"
        "ds_read_b64_tr_b16 v[116:117], v3 offset:8192\n"
        "v_mfma_f32_16x16x32_bf16 v[92:95], v[124:127], a[76:79], v[92:95]\n"
        "ds_read_b64_tr_b16 v[118:119], v3 offset:9216\n"
        "v_mfma_f32_16x16x32_bf16 a[28:31], v[120:123], a[80:83], a[28:31]\n"
        "ds_read_b128 a[120:123], v10 offset:16384\n"
        "v_mfma_f32_16x16x32_bf16 a[28:31], v[124:127], a[84:87], a[28:31]\n"
        "ds_read_b128 a[124:127], v10 offset:17408\n"
        "v_mfma_f32_16x16x32_bf16 a[60:63], v[120:123], a[88:91], a[60:63]\n"
        "v_mfma_f32_16x16x32_bf16 a[60:63], v[124:127], a[92:95], a[60:63]\n"
        "s_waitcnt lgkmcnt(9)\n"
        "v_mfma_f32_16x16x32_bf16 v[32:35], v[96:99], a[96:99], v[32:35]\n"
        "ds_read_b64_tr_b16 v[120:121], v4 offset:0\n"
        "v_mfma_f32_16x16x32_bf16 v[32:35], v[100:103], a[100:103], v[32:35]\n"
        "ds_read_b64_tr_b16 v[122:123], v4 offset:1024\n"
        "v_mfma_f32_16x16x32_bf16 v[64:67], v[96:99], a[104:107], v[64:67]\n"
        "ds_read_b64_tr_b16 v[124:125], v4 offset:8192\n"
        "s_waitcnt lgkmcnt(9)\n"
        "v_mfma_f32_16x16x32_bf16 v[64:67], v[100:103], a[108:111], v[64:67]\n"
        "ds_read_b64_tr_b16 v[126:127], v4 offset:9216\n"
        "v_mfma_f32_16x16x32_bf16 a[0:3], v[96:99], a[112:115], a[0:3]\n"
        "v_mfma_f32_16x16x32_bf16 a[0:3], v[100:103], a[116:119], a[0:3]\n"
        "s_waitcnt lgkmcnt(5)\n"
        "v_mfma_f32_16x16x32_bf16 a[32:35], v[96:99], a[120:123], a[32:35]\n"
        "s_waitcnt lgkmcnt(4)\n"
        "v_mfma_f32_16x16x32_bf16 a[32:35], v[100:103], a[124:127], a[32:35]\n"
        "v_mfma_f32_16x16x32_bf16 v[36:39], v[104:107], a[96:99], v[36:39]\n"
        "ds_read_b64_tr_b16 v[96:97], v5 offset:0\n"
        "v_mfma_f32_16x16x32_bf16 v[36:39], v[108:111], a[100:103], v[36:39]\n"
        "ds_read_b64_tr_b16 v[98:99], v5 offset:1024\n"
        "v_mfma_f32_16x16x32_bf16 v[68:71], v[104:107], a[104:107], v[68:71]\n"
        "ds_read_b64_tr_b16 v[100:101], v5 offset:8192\n"
        "v_mfma_f32_16x16x32_bf16 v[68:71], v[108:111], a[108:111], v[68:71]\n"
        "ds_read_b64_tr_b16 v[102:103], v5 offset:9216\n"
        "v_mfma_f32_16x16x32_bf16 a[4:7], v[104:107], a[112:115], a[4:7]\n"
        "v_mfma_f32_16x16x32_bf16 a[4:7], v[108:111], a[116:119], a[4:7]\n"
        "v_mfma_f32_16x16x32_bf16 a[36:39], v[104:107], a[120:123], a[36:39]\n"
        "v_mfma_f32_16x16x32_bf16 a[36:39], v[108:111], a[124:127], a[36:39]\n"
        "v_mfma_f32_16x16x32_bf16 v[40:43], v[112:115], a[96:99], v[40:43]\n"
        "ds_read_b64_tr_b16 v[104:105], v6 offset:0\n"
        "v_mfma_f32_16x16x32_bf16 v[40:43], v[116:119], a[100:103], v[40:43]\n"
        "ds_read_b64_tr_b16 v[106:107], v6 offset:1024\n"
        "v_mfma_f32_16x16x32_bf16 v[72:75], v[112:115], a[104:107], v[72:75]\n"
        "ds_read_b64_tr_b16 v[108:109], v6 offset:8192\n"
        "v_mfma_f32_16x16x32_bf16 v[72:75], v[116:119], a[108:111], v[72:75]\n"
        "ds_read_b64_tr_b16 v[110:111], v6 offset:9216\n"
        "v_mfma_f32_16x16x32_bf16 a[8:11], v[112:115], a[112:115], a[8:11]\n"
        "v_mfma_f32_16x16x32_bf16 a[8:11], v[116:119], a[116:119], a[8:11]\n"
        "v_mfma_f32_16x16x32_bf16 a[40:43], v[112:115], a[120:123], a[40:43]\n"
        "v_mfma_f32_16x16x32_bf16 a[40:43], v[116:119], a[124:127], a[40:43]\n"
        "s_waitcnt lgkmcnt(6)\n"
        "v_mfma_f32_16x16x32_bf16 v[44:47], v[120:123], a[96:99], v[44:47]\n"
        "ds_read_b64_tr_b16 v[112:113], v7 offset:0\n"
        "v_mfma_f32_16x16x32_bf16 v[44:47], v[124:127], a[100:103], v[44:47]\n"
        "ds_read_b64_tr_b16 v[114:115], v7 offset:1024\n"
        "v_mfma_f32_16x16x32_bf16 v[76:79], v[120:123], a[104:107], v[76:79]\n"
        "ds_read_b64_tr_b16 v[116:117], v7 offset:8192\n"
        "v_mfma_f32_16x16x32_bf16 v[76:79], v[124:127], a[108:111], v[76:79]\n"
        "ds_read_b64_tr_b16 v[118:119], v7 offset:9216\n"
        "v_mfma_f32_16x16x32_bf16 a[12:15], v[120:123], a[112:115], a[12:15]\n"
        "v_mfma_f32_16x16x32_bf16 a[12:15], v[124:127], a[116:119], a[12:15]\n"
        "v_mfma_f32_16x16x32_bf16 a[44:47], v[120:123], a[120:123], a[44:47]\n"
        "v_mfma_f32_16x16x32_bf16 a[44:47], v[124:127], a[124:127], a[44:47]\n"
        "v_mfma_f32_16x16x32_bf16 v[48:51], v[96:99], a[96:99], v[48:51]\n"
        "ds_read_b64_tr_b16 v[120:121], v8 offset:0\n"
        "s_waitcnt lgkmcnt(6)\n"
        "v_mfma_f32_16x16x32_bf16 v[48:51], v[100:103], a[100:103], v[48:51]\n"
        "ds_read_b64_tr_b16 v[122:123], v8 offset:1024\n"
        "v_mfma_f32_16x16x32_bf16 v[80:83], v[96:99], a[104:107], v[80:83]\n"
        "ds_read_b64_tr_b16 v[124:125], v8 offset:8192\n"
        "v_mfma_f32_16x16x32_bf16 v[80:83], v[100:103], a[108:111], v[80:83]\n"
        "ds_read_b64_tr_b16 v[126:127], v8 offset:9216\n"
        "v_mfma_f32_16x16x32_bf16 a[16:19], v[96:99], a[112:115], a[16:19]\n"
        "v_mfma_f32_16x16x32_bf16 a[16:19], v[100:103], a[116:119], a[16:19]\n"
        "v_mfma_f32_16x16x32_bf16 a[48:51], v[96:99], a[120:123], a[48:51]\n"
        "v_mfma_f32_16x16x32_bf16 a[48:51], v[100:103], a[124:127], a[48:51]\n"
        "s_waitcnt lgkmcnt(0)\n"
        "s_barrier\n"
        "v_add_u32 v1, 49152, v1\n"
        "v_add_u32 v2, 49152, v2\n"
        "v_add_u32 v3, 49152, v3\n"
        "v_add_u32 v4, 49152, v4\n"
        "v_add_u32 v5, 49152, v5\n"
        "v_add_u32 v6, 49152, v6\n"
        "v_add_u32 v7, 49152, v7\n"
        "v_add_u32 v8, 49152, v8\n"
        "v_add_u32 v9, 49152, v9\n"
        "v_add_u32 v10, 49152, v10\n"
        "v_mfma_f32_16x16x32_bf16 v[52:55], v[104:107], a[96:99], v[52:55]\n"
        "ds_read_b64_tr_b16 v[96:97], v1 offset:0\n"
        "v_mfma_f32_16x16x32_bf16 v[52:55], v[108:111], a[100:103], v[52:55]\n"
        "ds_read_b64_tr_b16 v[98:99], v1 offset:1024\n"
        "v_mfma_f32_16x16x32_bf16 v[84:87], v[104:107], a[104:107], v[84:87]\n"
        "ds_read_b64_tr_b16 v[100:101], v1 offset:8192\n"
        "v_mfma_f32_16x16x32_bf16 v[84:87], v[108:111], a[108:111], v[84:87]\n"
        "ds_read_b64_tr_b16 v[102:103], v1 offset:9216\n"
        "v_mfma_f32_16x16x32_bf16 a[20:23], v[104:107], a[112:115], a[20:23]\n"
        "ds_read_b128 a[64:67], v9 offset:0\n"
        "v_mfma_f32_16x16x32_bf16 a[20:23], v[108:111], a[116:119], a[20:23]\n"
        "ds_read_b128 a[68:71], v9 offset:1024\n"
        "v_mfma_f32_16x16x32_bf16 a[52:55], v[104:107], a[120:123], a[52:55]\n"
        "ds_read_b128 a[72:75], v10 offset:0\n"
        "v_mfma_f32_16x16x32_bf16 a[52:55], v[108:111], a[124:127], a[52:55]\n"
        "v_mfma_f32_16x16x32_bf16 v[56:59], v[112:115], a[96:99], v[56:59]\n"
        "ds_read_b64_tr_b16 v[104:105], v2 offset:0\n"
        "v_mfma_f32_16x16x32_bf16 v[56:59], v[116:119], a[100:103], v[56:59]\n"
        "ds_read_b64_tr_b16 v[106:107], v2 offset:1024\n"
        "v_mfma_f32_16x16x32_bf16 v[88:91], v[112:115], a[104:107], v[88:91]\n"
        "ds_read_b64_tr_b16 v[108:109], v2 offset:8192\n"
        "v_mfma_f32_16x16x32_bf16 v[88:91], v[116:119], a[108:111], v[88:91]\n"
        "ds_read_b64_tr_b16 v[110:111], v2 offset:9216\n"
        "v_mfma_f32_16x16x32_bf16 a[24:27], v[112:115], a[112:115], a[24:27]\n"
        "ds_read_b128 a[76:79], v10 offset:1024\n"
        "v_mfma_f32_16x16x32_bf16 a[24:27], v[116:119], a[116:119], a[24:27]\n"
        "ds_read_b128 a[80:83], v9 offset:16384\n"
        "v_mfma_f32_16x16x32_bf16 a[56:59], v[112:115], a[120:123], a[56:59]\n"
        "ds_read_b128 a[84:87], v9 offset:17408\n"
        "v_mfma_f32_16x16x32_bf16 a[56:59], v[116:119], a[124:127], a[56:59]\n"
        "v_mfma_f32_16x16x32_bf16 v[60:63], v[120:123], a[96:99], v[60:63]\n"
        "ds_read_b64_tr_b16 v[112:113], v3 offset:0\n"
        "v_mfma_f32_16x16x32_bf16 v[60:63], v[124:127], a[100:103], v[60:63]\n"
        "ds_read_b64_tr_b16 v[114:115], v3 offset:1024\n"
        "v_mfma_f32_16x16x32_bf16 v[92:95], v[120:123], a[104:107], v[92:95]\n"
        "ds_read_b64_tr_b16 v[116:117], v3 offset:8192\n"
        "v_mfma_f32_16x16x32_bf16 v[92:95], v[124:127], a[108:111], v[92:95]\n"
        "ds_read_b64_tr_b16 v[118:119], v3 offset:9216\n"
        "v_mfma_f32_16x16x32_bf16 a[28:31], v[120:123], a[112:115], a[28:31]\n"
        "ds_read_b128 a[88:91], v10 offset:16384\n"
        "v_mfma_f32_16x16x32_bf16 a[28:31], v[124:127], a[116:119], a[28:31]\n"
        "ds_read_b128 a[92:95], v10 offset:17408\n"
        "v_mfma_f32_16x16x32_bf16 a[60:63], v[120:123], a[120:123], a[60:63]\n"
        "v_mfma_f32_16x16x32_bf16 a[60:63], v[124:127], a[124:127], a[60:63]\n"
        "s_waitcnt lgkmcnt(9)\n"
        "v_mfma_f32_16x16x32_bf16 v[32:35], v[96:99], a[64:67], v[32:35]\n"
        "ds_read_b64_tr_b16 v[120:121], v4 offset:0\n"
        "v_mfma_f32_16x16x32_bf16 v[32:35], v[100:103], a[68:71], v[32:35]\n"
        "ds_read_b64_tr_b16 v[122:123], v4 offset:1024\n"
        "v_mfma_f32_16x16x32_bf16 v[64:67], v[96:99], a[72:75], v[64:67]\n"
        "ds_read_b64_tr_b16 v[124:125], v4 offset:8192\n"
        "s_waitcnt lgkmcnt(9)\n"
        "v_mfma_f32_16x16x32_bf16 v[64:67], v[100:103], a[76:79], v[64:67]\n"
        "ds_read_b64_tr_b16 v[126:127], v4 offset:9216\n"
        "v_mfma_f32_16x16x32_bf16 a[0:3], v[96:99], a[80:83], a[0:3]\n"
        "v_mfma_f32_16x16x32_bf16 a[0:3], v[100:103], a[84:87], a[0:3]\n"
        "s_waitcnt lgkmcnt(5)\n"
        "v_mfma_f32_16x16x32_bf16 a[32:35], v[96:99], a[88:91], a[32:35]\n"
        "s_waitcnt lgkmcnt(4)\n"
        "v_mfma_f32_16x16x32_bf16 a[32:35], v[100:103], a[92:95], a[32:35]\n"
        "v_mfma_f32_16x16x32_bf16 v[36:39], v[104:107], a[64:67], v[36:39]\n"
        "ds_read_b64_tr_b16 v[96:97], v5 offset:0\n"
        "v_mfma_f32_16x16x32_bf16 v[36:39], v[108:111], a[68:71], v[36:39]\n"
        "ds_read_b64_tr_b16 v[98:99], v5 offset:1024\n"
        "v_mfma_f32_16x16x32_bf16 v[68:71], v[104:107], a[72:75], v[68:71]\n"
        "ds_read_b64_tr_b16 v[100:101], v5 offset:8192\n"
        "v_mfma_f32_16x16x32_bf16 v[68:71], v[108:111], a[76:79], v[68:71]\n"
        "ds_read_b64_tr_b16 v[102:103], v5 offset:9216\n"
        "v_mfma_f32_16x16x32_bf16 a[4:7], v[104:107], a[80:83], a[4:7]\n"
        "v_mfma_f32_16x16x32_bf16 a[4:7], v[108:111], a[84:87], a[4:7]\n"
        "v_mfma_f32_16x16x32_bf16 a[36:39], v[104:107], a[88:91], a[36:39]\n"
        "v_mfma_f32_16x16x32_bf16 a[36:39], v[108:111], a[92:95], a[36:39]\n"
        "v_mfma_f32_16x16x32_bf16 v[40:43], v[112:115], a[64:67], v[40:43]\n"
        "ds_read_b64_tr_b16 v[104:105], v6 offset:0\n"
        "v_mfma_f32_16x16x32_bf16 v[40:43], v[116:119], a[68:71], v[40:43]\n"
        "ds_read_b64_tr_b16 v[106:107], v6 offset:1024\n"
        "v_mfma_f32_16x16x32_bf16 v[72:75], v[112:115], a[72:75], v[72:75]\n"
        "ds_read_b64_tr_b16 v[108:109], v6 offset:8192\n"
        "v_mfma_f32_16x16x32_bf16 v[72:75], v[116:119], a[76:79], v[72:75]\n"
        "ds_read_b64_tr_b16 v[110:111], v6 offset:9216\n"
        "v_mfma_f32_16x16x32_bf16 a[8:11], v[112:115], a[80:83], a[8:11]\n"
        "v_mfma_f32_16x16x32_bf16 a[8:11], v[116:119], a[84:87], a[8:11]\n"
        "v_mfma_f32_16x16x32_bf16 a[40:43], v[112:115], a[88:91], a[40:43]\n"
        "v_mfma_f32_16x16x32_bf16 a[40:43], v[116:119], a[92:95], a[40:43]\n"
        "s_waitcnt lgkmcnt(6)\n"
        "v_mfma_f32_16x16x32_bf16 v[44:47], v[120:123], a[64:67], v[44:47]\n"
        "ds_read_b64_tr_b16 v[112:113], v7 offset:0\n"
        "v_mfma_f32_16x16x32_bf16 v[44:47], v[124:127], a[68:71], v[44:47]\n"
        "ds_read_b64_tr_b16 v[114:115], v7 offset:1024\n"
        "v_mfma_f32_16x16x32_bf16 v[76:79], v[120:123], a[72:75], v[76:79]\n"
        "ds_read_b64_tr_b16 v[116:117], v7 offset:8192\n"
        "v_mfma_f32_16x16x32_bf16 v[76:79], v[124:127], a[76:79], v[76:79]\n"
        "ds_read_b64_tr_b16 v[118:119], v7 offset:9216\n"
        "v_mfma_f32_16x16x32_bf16 a[12:15], v[120:123], a[80:83], a[12:15]\n"
        "v_mfma_f32_16x16x32_bf16 a[12:15], v[124:127], a[84:87], a[12:15]\n"
        "v_mfma_f32_16x16x32_bf16 a[44:47], v[120:123], a[88:91], a[44:47]\n"
        "v_mfma_f32_16x16x32_bf16 a[44:47], v[124:127], a[92:95], a[44:47]\n"
        "v_mfma_f32_16x16x32_bf16 v[48:51], v[96:99], a[64:67], v[48:51]\n"
        "ds_read_b64_tr_b16 v[120:121], v8 offset:0\n"
        "s_waitcnt lgkmcnt(6)\n"
        "v_mfma_f32_16x16x32_bf16 v[48:51], v[100:103], a[68:71], v[48:51]\n"
        "ds_read_b64_tr_b16 v[122:123], v8 offset:1024\n"
        "v_mfma_f32_16x16x32_bf16 v[80:83], v[96:99], a[72:75], v[80:83]\n"
        "ds_read_b64_tr_b16 v[124:125], v8 offset:8192\n"
        "v_mfma_f32_16x16x32_bf16 v[80:83], v[100:103], a[76:79], v[80:83]\n"
        "ds_read_b64_tr_b16 v[126:127], v8 offset:9216\n"
        "v_mfma_f32_16x16x32_bf16 a[16:19], v[96:99], a[80:83], a[16:19]\n"
        "v_mfma_f32_16x16x32_bf16 a[16:19], v[100:103], a[84:87], a[16:19]\n"
        "v_mfma_f32_16x16x32_bf16 a[48:51], v[96:99], a[88:91], a[48:51]\n"
        "v_mfma_f32_16x16x32_bf16 a[48:51], v[100:103], a[92:95], a[48:51]\n"
        "s_waitcnt lgkmcnt(0)\n"
        "s_barrier\n"
        "v_subrev_u32 v1, 98304, v1\n"
        "v_subrev_u32 v2, 98304, v2\n"
        "v_subrev_u32 v3, 98304, v3\n"
        "v_subrev_u32 v4, 98304, v4\n"
        "v_subrev_u32 v5, 98304, v5\n"
        "v_subrev_u32 v6, 98304, v6\n"
        "v_subrev_u32 v7, 98304, v7\n"
        "v_subrev_u32 v8, 98304, v8\n"
        "v_subrev_u32 v9, 98304, v9\n"
        "v_subrev_u32 v10, 98304, v10\n"
        "v_mfma_f32_16x16x32_bf16 v[52:55], v[104:107], a[64:67], v[52:55]\n"
        "ds_read_b64_tr_b16 v[96:97], v1 offset:0\n"
        "v_mfma_f32_16x16x32_bf16 v[52:55], v[108:111], a[68:71], v[52:55]\n"
        "ds_read_b64_tr_b16 v[98:99], v1 offset:1024\n"
        "v_mfma_f32_16x16x32_bf16 v[84:87], v[104:107], a[72:75], v[84:87]\n"
        "ds_read_b64_tr_b16 v[100:101], v1 offset:8192\n"
        "v_mfma_f32_16x16x32_bf16 v[84:87], v[108:111], a[76:79], v[84:87]\n"
        "ds_read_b64_tr_b16 v[102:103], v1 offset:9216\n"
        "v_mfma_f32_16x16x32_bf16 a[20:23], v[104:107], a[80:83], a[20:23]\n"
        "ds_read_b128 a[96:99], v9 offset:0\n"
        "v_mfma_f32_16x16x32_bf16 a[20:23], v[108:111], a[84:87], a[20:23]\n"
        "ds_read_b128 a[100:103], v9 offset:1024\n"
        "v_mfma_f32_16x16x32_bf16 a[52:55], v[104:107], a[88:91], a[52:55]\n"
        "ds_read_b128 a[104:107], v10 offset:0\n"
        "v_mfma_f32_16x16x32_bf16 a[52:55], v[108:111], a[92:95], a[52:55]\n"
        "v_mfma_f32_16x16x32_bf16 v[56:59], v[112:115], a[64:67], v[56:59]\n"
        "ds_read_b64_tr_b16 v[104:105], v2 offset:0\n"
        "v_mfma_f32_16x16x32_bf16 v[56:59], v[116:119], a[68:71], v[56:59]\n"
        "ds_read_b64_tr_b16 v[106:107], v2 offset:1024\n"
        "v_mfma_f32_16x16x32_bf16 v[88:91], v[112:115], a[72:75], v[88:91]\n"
        "ds_read_b64_tr_b16 v[108:109], v2 offset:8192\n"
        "v_mfma_f32_16x16x32_bf16 v[88:91], v[116:119], a[76:79], v[88:91]\n"
        "ds_read_b64_tr_b16 v[110:111], v2 offset:9216\n"
        "v_mfma_f32_16x16x32_bf16 a[24:27], v[112:115], a[80:83], a[24:27]\n"
        "ds_read_b128 a[108:111], v10 offset:1024\n"
        "v_mfma_f32_16x16x32_bf16 a[24:27], v[116:119], a[84:87], a[24:27]\n"
        "ds_read_b128 a[112:115], v9 offset:16384\n"
        "v_mfma_f32_16x16x32_bf16 a[56:59], v[112:115], a[88:91], a[56:59]\n"
        "ds_read_b128 a[116:119], v9 offset:17408\n"
        "v_mfma_f32_16x16x32_bf16 a[56:59], v[116:119], a[92:95], a[56:59]\n"
        "v_mfma_f32_16x16x32_bf16 v[60:63], v[120:123], a[64:67], v[60:63]\n"
        "ds_read_b64_tr_b16 v[112:113], v3 offset:0\n"
        "v_mfma_f32_16x16x32_bf16 v[60:63], v[124:127], a[68:71], v[60:63]\n"
        "ds_read_b64_tr_b16 v[114:115], v3 offset:1024\n"
        "v_mfma_f32_16x16x32_bf16 v[92:95], v[120:123], a[72:75], v[92:95]\n"
        "ds_read_b64_tr_b16 v[116:117], v3 offset:8192\n"
        "v_mfma_f32_16x16x32_bf16 v[92:95], v[124:127], a[76:79], v[92:95]\n"
        "ds_read_b64_tr_b16 v[118:119], v3 offset:9216\n"
        "v_mfma_f32_16x16x32_bf16 a[28:31], v[120:123], a[80:83], a[28:31]\n"
        "ds_read_b128 a[120:123], v10 offset:16384\n"
        "v_mfma_f32_16x16x32_bf16 a[28:31], v[124:127], a[84:87], a[28:31]\n"
        "ds_read_b128 a[124:127], v10 offset:17408\n"
        "v_mfma_f32_16x16x32_bf16 a[60:63], v[120:123], a[88:91], a[60:63]\n"
        "v_mfma_f32_16x16x32_bf16 a[60:63], v[124:127], a[92:95], a[60:63]\n"
        "s_waitcnt lgkmcnt(9)\n"
        "v_mfma_f32_16x16x32_bf16 v[32:35], v[96:99], a[96:99], v[32:35]\n"
        "ds_read_b64_tr_b16 v[120:121], v4 offset:0\n"
        "v_mfma_f32_16x16x32_bf16 v[32:35], v[100:103], a[100:103], v[32:35]\n"
        "ds_read_b64_tr_b16 v[122:123], v4 offset:1024\n"
        "v_mfma_f32_16x16x32_bf16 v[64:67], v[96:99], a[104:107], v[64:67]\n"
        "ds_read_b64_tr_b16 v[124:125], v4 offset:8192\n"
        "s_waitcnt lgkmcnt(9)\n"
        "v_mfma_f32_16x16x32_bf16 v[64:67], v[100:103], a[108:111], v[64:67]\n"
        "ds_read_b64_tr_b16 v[126:127], v4 offset:9216\n"
        "v_mfma_f32_16x16x32_bf16 a[0:3], v[96:99], a[112:115], a[0:3]\n"
        "v_mfma_f32_16x16x32_bf16 a[0:3], v[100:103], a[116:119], a[0:3]\n"
        "s_waitcnt lgkmcnt(5)\n"
        "v_mfma_f32_16x16x32_bf16 a[32:35], v[96:99], a[120:123], a[32:35]\n"
        "s_waitcnt lgkmcnt(4)\n"
        "v_mfma_f32_16x16x32_bf16 a[32:35], v[100:103], a[124:127], a[32:35]\n"
        "v_mfma_f32_16x16x32_bf16 v[36:39], v[104:107], a[96:99], v[36:39]\n"
        "ds_read_b64_tr_b16 v[96:97], v5 offset:0\n"
        "v_mfma_f32_16x16x32_bf16 v[36:39], v[108:111], a[100:103], v[36:39]\n"
        "ds_read_b64_tr_b16 v[98:99], v5 offset:1024\n"
        "v_mfma_f32_16x16x32_bf16 v[68:71], v[104:107], a[104:107], v[68:71]\n"
        "ds_read_b64_tr_b16 v[100:101], v5 offset:8192\n"
        "v_mfma_f32_16x16x32_bf16 v[68:71], v[108:111], a[108:111], v[68:71]\n"
        "ds_read_b64_tr_b16 v[102:103], v5 offset:9216\n"
        "v_mfma_f32_16x16x32_bf16 a[4:7], v[104:107], a[112:115], a[4:7]\n"
        "v_mfma_f32_16x16x32_bf16 a[4:7], v[108:111], a[116:119], a[4:7]\n"
        "v_mfma_f32_16x16x32_bf16 a[36:39], v[104:107], a[120:123], a[36:39]\n"
        "v_mfma_f32_16x16x32_bf16 a[36:39], v[108:111], a[124:127], a[36:39]\n"
        "v_mfma_f32_16x16x32_bf16 v[40:43], v[112:115], a[96:99], v[40:43]\n"
        "ds_read_b64_tr_b16 v[104:105], v6 offset:0\n"
        "v_mfma_f32_16x16x32_bf16 v[40:43], v[116:119], a[100:103], v[40:43]\n"
        "ds_read_b64_tr_b16 v[106:107], v6 offset:1024\n"
        "v_mfma_f32_16x16x32_bf16 v[72:75], v[112:115], a[104:107], v[72:75]\n"
        "ds_read_b64_tr_b16 v[108:109], v6 offset:8192\n"
        "v_mfma_f32_16x16x32_bf16 v[72:75], v[116:119], a[108:111], v[72:75]\n"
        "ds_read_b64_tr_b16 v[110:111], v6 offset:9216\n"
        "v_mfma_f32_16x16x32_bf16 a[8:11], v[112:115], a[112:115], a[8:11]\n"
        "v_mfma_f32_16x16x32_bf16 a[8:11], v[116:119], a[116:119], a[8:11]\n"
        "v_mfma_f32_16x16x32_bf16 a[40:43], v[112:115], a[120:123], a[40:43]\n"
        "v_mfma_f32_16x16x32_bf16 a[40:43], v[116:119], a[124:127], a[40:43]\n"
        "s_waitcnt lgkmcnt(6)\n"
        "v_mfma_f32_16x16x32_bf16 v[44:47], v[120:123], a[96:99], v[44:47]\n"
        "ds_read_b64_tr_b16 v[112:113], v7 offset:0\n"
        "v_mfma_f32_16x16x32_bf16 v[44:47], v[124:127], a[100:103], v[44:47]\n"
        "ds_read_b64_tr_b16 v[114:115], v7 offset:1024\n"
        "v_mfma_f32_16x16x32_bf16 v[76:79], v[120:123], a[104:107], v[76:79]\n"
        "ds_read_b64_tr_b16 v[116:117], v7 offset:8192\n"
        "v_mfma_f32_16x16x32_bf16 v[76:79], v[124:127], a[108:111], v[76:79]\n"
        "ds_read_b64_tr_b16 v[118:119], v7 offset:9216\n"
        "v_mfma_f32_16x16x32_bf16 a[12:15], v[120:123], a[112:115], a[12:15]\n"
        "v_mfma_f32_16x16x32_bf16 a[12:15], v[124:127], a[116:119], a[12:15]\n"
        "v_mfma_f32_16x16x32_bf16 a[44:47], v[120:123], a[120:123], a[44:47]\n"
        "v_mfma_f32_16x16x32_bf16 a[44:47], v[124:127], a[124:127], a[44:47]\n"
        "v_mfma_f32_16x16x32_bf16 v[48:51], v[96:99], a[96:99], v[48:51]\n"
        "ds_read_b64_tr_b16 v[120:121], v8 offset:0\n"
        "s_waitcnt lgkmcnt(6)\n"
        "v_mfma_f32_16x16x32_bf16 v[48:51], v[100:103], a[100:103], v[48:51]\n"
        "ds_read_b64_tr_b16 v[122:123], v8 offset:1024\n"
        "v_mfma_f32_16x16x32_bf16 v[80:83], v[96:99], a[104:107], v[80:83]\n"
        "ds_read_b64_tr_b16 v[124:125], v8 offset:8192\n"
        "v_mfma_f32_16x16x32_bf16 v[80:83], v[100:103], a[108:111], v[80:83]\n"
        "ds_read_b64_tr_b16 v[126:127], v8 offset:9216\n"
        "v_mfma_f32_16x16x32_bf16 a[16:19], v[96:99], a[112:115], a[16:19]\n"
        "v_mfma_f32_16x16x32_bf16 a[16:19], v[100:103], a[116:119], a[16:19]\n"
        "v_mfma_f32_16x16x32_bf16 a[48:51], v[96:99], a[120:123], a[48:51]\n"
        "v_mfma_f32_16x16x32_bf16 a[48:51], v[100:103], a[124:127], a[48:51]\n"
        "v_mfma_f32_16x16x32_bf16 v[52:55], v[104:107], a[96:99], v[52:55]\n"
        "s_waitcnt lgkmcnt(5)\n"
        "v_mfma_f32_16x16x32_bf16 v[52:55], v[108:111], a[100:103], v[52:55]\n"
        "v_mfma_f32_16x16x32_bf16 v[84:87], v[104:107], a[104:107], v[84:87]\n"
        "v_mfma_f32_16x16x32_bf16 v[84:87], v[108:111], a[108:111], v[84:87]\n"
        "v_mfma_f32_16x16x32_bf16 a[20:23], v[104:107], a[112:115], a[20:23]\n"
        "v_mfma_f32_16x16x32_bf16 a[20:23], v[108:111], a[116:119], a[20:23]\n"
        "v_mfma_f32_16x16x32_bf16 a[52:55], v[104:107], a[120:123], a[52:55]\n"
        "v_mfma_f32_16x16x32_bf16 a[52:55], v[108:111], a[124:127], a[52:55]\n"
        "v_mfma_f32_16x16x32_bf16 v[56:59], v[112:115], a[96:99], v[56:59]\n"
        "s_waitcnt lgkmcnt(3)\n"
        "v_mfma_f32_16x16x32_bf16 v[56:59], v[116:119], a[100:103], v[56:59]\n"
        "v_mfma_f32_16x16x32_bf16 v[88:91], v[112:115], a[104:107], v[88:91]\n"
        "v_mfma_f32_16x16x32_bf16 v[88:91], v[116:119], a[108:111], v[88:91]\n"
        "v_mfma_f32_16x16x32_bf16 a[24:27], v[112:115], a[112:115], a[24:27]\n"
        "v_mfma_f32_16x16x32_bf16 a[24:27], v[116:119], a[116:119], a[24:27]\n"
        "v_mfma_f32_16x16x32_bf16 a[56:59], v[112:115], a[120:123], a[56:59]\n"
        "v_mfma_f32_16x16x32_bf16 a[56:59], v[116:119], a[124:127], a[56:59]\n"
        "s_waitcnt lgkmcnt(0)\n"
        "v_mfma_f32_16x16x32_bf16 v[60:63], v[120:123], a[96:99], v[60:63]\n"
        "v_mfma_f32_16x16x32_bf16 v[60:63], v[124:127], a[100:103], v[60:63]\n"
        "v_mfma_f32_16x16x32_bf16 v[92:95], v[120:123], a[104:107], v[92:95]\n"
        "v_mfma_f32_16x16x32_bf16 v[92:95], v[124:127], a[108:111], v[92:95]\n"
        "v_mfma_f32_16x16x32_bf16 a[28:31], v[120:123], a[112:115], a[28:31]\n"
        "v_mfma_f32_16x16x32_bf16 a[28:31], v[124:127], a[116:119], a[28:31]\n"
        "v_mfma_f32_16x16x32_bf16 a[60:63], v[120:123], a[120:123], a[60:63]\n"
        "v_mfma_f32_16x16x32_bf16 a[60:63], v[124:127], a[124:127], a[60:63]\n"
        "s_nop 7\n"
        "v_cvt_pk_bf16_f32 v96, v32, v33\n"
        "v_cvt_pk_bf16_f32 v97, v34, v35\n"
        "v_cvt_pk_bf16_f32 v98, v36, v37\n"
        "v_cvt_pk_bf16_f32 v99, v38, v39\n"
        "v_cvt_pk_bf16_f32 v100, v40, v41\n"
        "v_cvt_pk_bf16_f32 v101, v42, v43\n"
        "v_cvt_pk_bf16_f32 v102, v44, v45\n"
        "v_cvt_pk_bf16_f32 v103, v46, v47\n"
        "v_cvt_pk_bf16_f32 v104, v48, v49\n"
        "v_cvt_pk_bf16_f32 v105, v50, v51\n"
        "v_cvt_pk_bf16_f32 v106, v52, v53\n"
        "v_cvt_pk_bf16_f32 v107, v54, v55\n"
        "v_cvt_pk_bf16_f32 v108, v56, v57\n"
        "v_cvt_pk_bf16_f32 v109, v58, v59\n"
        "v_cvt_pk_bf16_f32 v110, v60, v61\n"
        "v_cvt_pk_bf16_f32 v111, v62, v63\n"
        "v_cvt_pk_bf16_f32 v112, v64, v65\n"
        "v_cvt_pk_bf16_f32 v113, v66, v67\n"
        "v_cvt_pk_bf16_f32 v114, v68, v69\n"
        "v_cvt_pk_bf16_f32 v115, v70, v71\n"
        "v_cvt_pk_bf16_f32 v116, v72, v73\n"
        "v_cvt_pk_bf16_f32 v117, v74, v75\n"
        "v_cvt_pk_bf16_f32 v118, v76, v77\n"
        "v_cvt_pk_bf16_f32 v119, v78, v79\n"
        "v_cvt_pk_bf16_f32 v120, v80, v81\n"
        "v_cvt_pk_bf16_f32 v121, v82, v83\n"
        "v_cvt_pk_bf16_f32 v122, v84, v85\n"
        "v_cvt_pk_bf16_f32 v123, v86, v87\n"
        "v_cvt_pk_bf16_f32 v124, v88, v89\n"
        "v_cvt_pk_bf16_f32 v125, v90, v91\n"
        "v_cvt_pk_bf16_f32 v126, v92, v93\n"
        "v_cvt_pk_bf16_f32 v127, v94, v95\n"
        "s_barrier\n"
        "s_lshl_b32 s52, s36, 13\n"
        "s_add_u32 s52, s52, 131072\n"
        "v_add_u32 v11, s52, v0\n"
        "v_accvgpr_read_b32 v32, a0\n"
        "v_accvgpr_read_b32 v33, a1\n"
        "v_accvgpr_read_b32 v34, a2\n"
        "v_accvgpr_read_b32 v35, a3\n"
        "v_accvgpr_read_b32 v36, a4\n"
        "v_accvgpr_read_b32 v37, a5\n"
        "v_accvgpr_read_b32 v38, a6\n"
        "v_accvgpr_read_b32 v39, a7\n"
        "v_cvt_pk_bf16_f32 v40, v32, v33\n"
        "v_cvt_pk_bf16_f32 v41, v34, v35\n"
        "v_cvt_pk_bf16_f32 v42, v36, v37\n"
        "v_cvt_pk_bf16_f32 v43, v38, v39\n"
        "ds_write_b128 v11, v[40:43] offset:0\n"
        "v_accvgpr_read_b32 v44, a8\n"
        "v_accvgpr_read_b32 v45, a9\n"
        "v_accvgpr_read_b32 v46, a10\n"
        "v_accvgpr_read_b32 v47, a11\n"
        "v_accvgpr_read_b32 v48, a12\n"
        "v_accvgpr_read_b32 v49, a13\n"
        "v_accvgpr_read_b32 v50, a14\n"
        "v_accvgpr_read_b32 v51, a15\n"
        "v_cvt_pk_bf16_f32 v52, v44, v45\n"
        "v_cvt_pk_bf16_f32 v53, v46, v47\n"
        "v_cvt_pk_bf16_f32 v54, v48, v49\n"
        "v_cvt_pk_bf16_f32 v55, v50, v51\n"
        "ds_write_b128 v11, v[52:55] offset:1024\n"
        "v_accvgpr_read_b32 v56, a16\n"
        "v_accvgpr_read_b32 v57, a17\n"
        "v_accvgpr_read_b32 v58, a18\n"
        "v_accvgpr_read_b32 v59, a19\n"
        "v_accvgpr_read_b32 v60, a20\n"
        "v_accvgpr_read_b32 v61, a21\n"
        "v_accvgpr_read_b32 v62, a22\n"
        "v_accvgpr_read_b32 v63, a23\n"
        "v_cvt_pk_bf16_f32 v64, v56, v57\n"
        "v_cvt_pk_bf16_f32 v65, v58, v59\n"
        "v_cvt_pk_bf16_f32 v66, v60, v61\n"
        "v_cvt_pk_bf16_f32 v67, v62, v63\n"
        "ds_write_b128 v11, v[64:67] offset:2048\n"
        "v_accvgpr_read_b32 v68, a24\n"
        "v_accvgpr_read_b32 v69, a25\n"
        "v_accvgpr_read_b32 v70, a26\n"
        "v_accvgpr_read_b32 v71, a27\n"
        "v_accvgpr_read_b32 v72, a28\n"
        "v_accvgpr_read_b32 v73, a29\n"
        "v_accvgpr_read_b32 v74, a30\n"
        "v_accvgpr_read_b32 v75, a31\n"
        "v_cvt_pk_bf16_f32 v76, v68, v69\n"
        "v_cvt_pk_bf16_f32 v77, v70, v71\n"
        "v_cvt_pk_bf16_f32 v78, v72, v73\n"
        "v_cvt_pk_bf16_f32 v79, v74, v75\n"
        "ds_write_b128 v11, v[76:79] offset:3072\n"
        "v_accvgpr_read_b32 v32, a32\n"
        "v_accvgpr_read_b32 v33, a33\n"
        "v_accvgpr_read_b32 v34, a34\n"
        "v_accvgpr_read_b32 v35, a35\n"
        "v_accvgpr_read_b32 v36, a36\n"
        "v_accvgpr_read_b32 v37, a37\n"
        "v_accvgpr_read_b32 v38, a38\n"
        "v_accvgpr_read_b32 v39, a39\n"
        "v_cvt_pk_bf16_f32 v40, v32, v33\n"
        "v_cvt_pk_bf16_f32 v41, v34, v35\n"
        "v_cvt_pk_bf16_f32 v42, v36, v37\n"
        "v_cvt_pk_bf16_f32 v43, v38, v39\n"
        "ds_write_b128 v11, v[40:43] offset:4096\n"
        "v_accvgpr_read_b32 v44, a40\n"
        "v_accvgpr_read_b32 v45, a41\n"
        "v_accvgpr_read_b32 v46, a42\n"
        "v_accvgpr_read_b32 v47, a43\n"
        "v_accvgpr_read_b32 v48, a44\n"
        "v_accvgpr_read_b32 v49, a45\n"
        "v_accvgpr_read_b32 v50, a46\n"
        "v_accvgpr_read_b32 v51, a47\n"
        "v_cvt_pk_bf16_f32 v52, v44, v45\n"
        "v_cvt_pk_bf16_f32 v53, v46, v47\n"
        "v_cvt_pk_bf16_f32 v54, v48, v49\n"
        "v_cvt_pk_bf16_f32 v55, v50, v51\n"
        "ds_write_b128 v11, v[52:55] offset:5120\n"
        "v_accvgpr_read_b32 v56, a48\n"
        "v_accvgpr_read_b32 v57, a49\n"
        "v_accvgpr_read_b32 v58, a50\n"
        "v_accvgpr_read_b32 v59, a51\n"
        "v_accvgpr_read_b32 v60, a52\n"
        "v_accvgpr_read_b32 v61, a53\n"
        "v_accvgpr_read_b32 v62, a54\n"
        "v_accvgpr_read_b32 v63, a55\n"
        "v_cvt_pk_bf16_f32 v64, v56, v57\n"
        "v_cvt_pk_bf16_f32 v65, v58, v59\n"
        "v_cvt_pk_bf16_f32 v66, v60, v61\n"
        "v_cvt_pk_bf16_f32 v67, v62, v63\n"
        "ds_write_b128 v11, v[64:67] offset:6144\n"
        "v_accvgpr_read_b32 v68, a56\n"
        "v_accvgpr_read_b32 v69, a57\n"
        "v_accvgpr_read_b32 v70, a58\n"
        "v_accvgpr_read_b32 v71, a59\n"
        "v_accvgpr_read_b32 v72, a60\n"
        "v_accvgpr_read_b32 v73, a61\n"
        "v_accvgpr_read_b32 v74, a62\n"
        "v_accvgpr_read_b32 v75, a63\n"
        "v_cvt_pk_bf16_f32 v76, v68, v69\n"
        "v_cvt_pk_bf16_f32 v77, v70, v71\n"
        "v_cvt_pk_bf16_f32 v78, v72, v73\n"
        "v_cvt_pk_bf16_f32 v79, v74, v75\n"
        "ds_write_b128 v11, v[76:79] offset:7168\n"
        "s_waitcnt lgkmcnt(0)\n"
        "s_mov_b32 s64, 0xfff0ffff\n"
        "s_mov_b32 s65, 0xf000ff00\n"
        "s_mov_b32 s66, 0xffe0fffe\n"
        "s_mov_b32 s67, 0xe000fe00\n"
        "s_mov_b32 s68, 0xffc0fffc\n"
        "s_mov_b32 s69, 0xc000fc00\n"
        "s_mov_b32 s70, 0xff80fff8\n"
        "s_mov_b32 s71, 0x8000f800\n"
        "v_add_u32 v1, 65536, v0\n"
        "v_xor_b32 v2, 0x8000, v1\n"
        "v_add_u32 v3, 0, v0\n"
        "v_mov_b32 v7, 0xff800000\n"
        "v_mov_b32 v4, 0x3f803f80\n"
        "v_accvgpr_write_b32 a104, v4\n"
        "v_accvgpr_write_b32 a105, v4\n"
        "v_accvgpr_write_b32 a106, v4\n"
        "v_accvgpr_write_b32 a107, v4\n"
        "s_mov_b64 s[80:81], 0\n"
        "s_mov_b32 s60, 0\n"
        "s_sub_u32 s83, 15, s38\n"
        "s_lshl_b32 s52, s83, 3\n"
        "s_add_u32 s53, s52, s36\n"
        "s_lshl_b32 s61, s53, 17\n"
        "s_add_u32 s53, s52, 7\n"
        "s_sub_u32 s53, s53, s36\n"
        "s_lshl_b32 s62, s53, 17\n"
        "s_mov_b32 s63, s83\n"
        "s_or_b32 s52, s83, s36\n"
        "s_cmp_eq_u32 s52, 0\n"
        "s_cselect_b64 s[72:73], s[64:65], -1\n"
        "s_cselect_b64 s[74:75], s[66:67], -1\n"
        "s_cselect_b64 s[76:77], s[68:69], -1\n"
        "s_cselect_b64 s[78:79], s[70:71], -1\n"
        "s_barrier\n"
        "ds_read_b128 v[64:67], v1 offset:0\n"
        "ds_read_b128 v[68:71], v1 offset:1024\n"
        "ds_read_b128 v[72:75], v1 offset:2048\n"
        "ds_read_b128 v[76:79], v1 offset:3072\n"
        "ds_read_b128 v[80:83], v1 offset:4096\n"
        "ds_read_b128 v[84:87], v1 offset:5120\n"
        "ds_read_b128 v[88:91], v1 offset:6144\n"
        "ds_read_b128 v[92:95], v1 offset:7168\n"
        "s_waitcnt lgkmcnt(0)\n"
        "s_barrier\n"
        "P3_BLOCK_%=:\n"
        "v_mfma_f32_16x16x32_bf16 v[16:19], v[64:67], v[96:99], 0\n"
        "v_mfma_f32_16x16x32_bf16 v[20:23], v[64:67], v[112:115], 0\n"
        "ds_read_b128 v[64:67], v1 offset:8192\n"
        "v_mfma_f32_16x16x32_bf16 v[16:19], v[68:71], v[100:103], v[16:19]\n"
        "v_mfma_f32_16x16x32_bf16 v[20:23], v[68:71], v[116:119], v[20:23]\n"
        "ds_read_b128 v[68:71], v1 offset:9216\n"
        "v_mfma_f32_16x16x32_bf16 v[16:19], v[72:75], v[104:107], v[16:19]\n"
        "v_mfma_f32_16x16x32_bf16 v[20:23], v[72:75], v[120:123], v[20:23]\n"
        "ds_read_b128 v[72:75], v1 offset:10240\n"
        "v_mfma_f32_16x16x32_bf16 v[16:19], v[76:79], v[108:111], v[16:19]\n"
        "v_mfma_f32_16x16x32_bf16 v[20:23], v[76:79], v[124:127], v[20:23]\n"
        "ds_read_b128 v[76:79], v1 offset:11264\n"
        "v_mfma_f32_16x16x32_bf16 v[24:27], v[80:83], v[96:99], 0\n"
        "v_mfma_f32_16x16x32_bf16 v[28:31], v[80:83], v[112:115], 0\n"
        "ds_read_b128 v[80:83], v1 offset:12288\n"
        "v_mfma_f32_16x16x32_bf16 v[24:27], v[84:87], v[100:103], v[24:27]\n"
        "v_mfma_f32_16x16x32_bf16 v[28:31], v[84:87], v[116:119], v[28:31]\n"
        "ds_read_b128 v[84:87], v1 offset:13312\n"
        "v_mfma_f32_16x16x32_bf16 v[24:27], v[88:91], v[104:107], v[24:27]\n"
        "v_mfma_f32_16x16x32_bf16 v[28:31], v[88:91], v[120:123], v[28:31]\n"
        "ds_read_b128 v[88:91], v1 offset:14336\n"
        "v_mfma_f32_16x16x32_bf16 v[24:27], v[92:95], v[108:111], v[24:27]\n"
        "v_mfma_f32_16x16x32_bf16 v[28:31], v[92:95], v[124:127], v[28:31]\n"
        "ds_read_b128 v[92:95], v1 offset:15360\n"
        "s_cmp_eq_u32 s82, 0\n"
        "s_cbranch_scc0 P3_GIVEN_M_%=\n"
        "s_nop 7\n"
        "v_cndmask_b32 v8, v7, v16, s[72:73]\n"
        "v_cndmask_b32 v9, v7, v17, s[74:75]\n"
        "v_cndmask_b32 v10, v7, v18, s[76:77]\n"
        "v_cndmask_b32 v11, v7, v19, s[78:79]\n"
        "v_max3_f32 v4, v8, v9, v10\n"
        "v_max_f32 v4, v4, v11\n"
        "v_mov_b32 v5, v4\n"
        "s_nop 1\n"
        "v_permlane32_swap_b32 v4, v5\n"
        "v_max_f32 v4, v4, v5\n"
        "v_mov_b32 v5, v4\n"
        "s_nop 1\n"
        "v_permlane16_swap_b32 v4, v5\n"
        "v_max_f32 v4, v4, v5\n"
        "v_sub_f32 v8, 0, v4\n"
        "v_sub_f32 v9, 0, v4\n"
        "v_sub_f32 v10, 0, v4\n"
        "v_sub_f32 v11, 0, v4\n"
        "v_max3_f32 v4, v20, v21, v22\n"
        "v_max_f32 v4, v4, v23\n"
        "v_mov_b32 v5, v4\n"
        "s_nop 1\n"
        "v_permlane32_swap_b32 v4, v5\n"
        "v_max_f32 v4, v4, v5\n"
        "v_mov_b32 v5, v4\n"
        "s_nop 1\n"
        "v_permlane16_swap_b32 v4, v5\n"
        "v_max_f32 v4, v4, v5\n"
        "v_sub_f32 v12, 0, v4\n"
        "v_sub_f32 v13, 0, v4\n"
        "v_sub_f32 v14, 0, v4\n"
        "v_sub_f32 v15, 0, v4\n"
        "s_branch P3_HAVE_M_%=\n"
        "P3_GIVEN_M_%=:\n"
        "s_cmp_eq_u32 s60, 0\n"
        "s_cbranch_scc0 P3_GIVEN_B_%=\n"
        "v_accvgpr_read_b32 v4, a108\n"
        "s_nop 0\n"
        "v_sub_f32 v8, 0, v4\n"
        "v_sub_f32 v9, 0, v4\n"
        "v_sub_f32 v10, 0, v4\n"
        "v_sub_f32 v11, 0, v4\n"
        "v_accvgpr_read_b32 v4, a109\n"
        "s_nop 0\n"
        "v_sub_f32 v12, 0, v4\n"
        "v_sub_f32 v13, 0, v4\n"
        "v_sub_f32 v14, 0, v4\n"
        "v_sub_f32 v15, 0, v4\n"
        "s_branch P3_HAVE_M_%=\n"
        "P3_GIVEN_B_%=:\n"
        "v_accvgpr_read_b32 v4, a110\n"
        "s_nop 0\n"
        "v_sub_f32 v8, 0, v4\n"
        "v_sub_f32 v9, 0, v4\n"
        "v_sub_f32 v10, 0, v4\n"
        "v_sub_f32 v11, 0, v4\n"
        "v_accvgpr_read_b32 v4, a111\n"
        "s_nop 0\n"
        "v_sub_f32 v12, 0, v4\n"
        "v_sub_f32 v13, 0, v4\n"
        "v_sub_f32 v14, 0, v4\n"
        "v_sub_f32 v15, 0, v4\n"
        "P3_HAVE_M_%=:\n"
        "s_nop 7\n"
        "v_add_f32 v16, v16, v8\n"
        "v_add_f32 v17, v17, v8\n"
        "v_add_f32 v18, v18, v8\n"
        "v_add_f32 v19, v19, v8\n"
        "v_add_f32 v20, v20, v12\n"
        "v_add_f32 v21, v21, v12\n"
        "v_add_f32 v22, v22, v12\n"
        "v_add_f32 v23, v23, v12\n"
        "v_add_f32 v24, v24, v8\n"
        "v_add_f32 v25, v25, v8\n"
        "v_add_f32 v26, v26, v8\n"
        "v_add_f32 v27, v27, v8\n"
        "v_add_f32 v28, v28, v12\n"
        "v_add_f32 v29, v29, v12\n"
        "v_add_f32 v30, v30, v12\n"
        "v_add_f32 v31, v31, v12\n"
        "s_cmp_eq_u32 s63, 0\n"
        "s_cbranch_scc1 P3_DIAG_FIRST_%=\n"
        "s_waitcnt lgkmcnt(0)\n"
        "v_mfma_f32_16x16x32_bf16 v[32:35], v[64:67], v[96:99], v[8:11]\n"
        "v_mfma_f32_16x16x32_bf16 v[36:39], v[64:67], v[112:115], v[12:15]\n"
        "ds_read_b128 v[64:67], v1 offset:16384\n"
        "ds_read_b128 a[72:75], v3 offset:0\n"
        "v_mfma_f32_16x16x32_bf16 v[32:35], v[68:71], v[100:103], v[32:35]\n"
        "v_exp_f32 v16, v16\n"
        "v_exp_f32 v17, v17\n"
        "v_mfma_f32_16x16x32_bf16 v[36:39], v[68:71], v[116:119], v[36:39]\n"
        "ds_read_b128 v[68:71], v1 offset:17408\n"
        "ds_read_b128 a[76:79], v3 offset:1024\n"
        "v_exp_f32 v18, v18\n"
        "v_exp_f32 v19, v19\n"
        "v_mfma_f32_16x16x32_bf16 v[32:35], v[72:75], v[104:107], v[32:35]\n"
        "v_cvt_pk_bf16_f32 v48, v16, v17\n"
        "v_exp_f32 v20, v20\n"
        "v_mfma_f32_16x16x32_bf16 v[36:39], v[72:75], v[120:123], v[36:39]\n"
        "ds_read_b128 v[72:75], v1 offset:18432\n"
        "ds_read_b128 a[80:83], v3 offset:2048\n"
        "v_exp_f32 v21, v21\n"
        "v_mfma_f32_16x16x32_bf16 v[32:35], v[76:79], v[108:111], v[32:35]\n"
        "v_cvt_pk_bf16_f32 v49, v18, v19\n"
        "v_exp_f32 v22, v22\n"
        "v_mfma_f32_16x16x32_bf16 v[36:39], v[76:79], v[124:127], v[36:39]\n"
        "ds_read_b128 v[76:79], v1 offset:19456\n"
        "ds_read_b128 a[84:87], v3 offset:3072\n"
        "v_exp_f32 v23, v23\n"
        "v_cvt_pk_bf16_f32 v52, v20, v21\n"
        "v_mfma_f32_16x16x32_bf16 v[40:43], v[80:83], v[96:99], v[8:11]\n"
        "v_exp_f32 v24, v24\n"
        "v_mfma_f32_16x16x32_bf16 v[44:47], v[80:83], v[112:115], v[12:15]\n"
        "ds_read_b128 v[80:83], v1 offset:20480\n"
        "ds_read_b128 a[88:91], v3 offset:4096\n"
        "v_exp_f32 v25, v25\n"
        "v_cvt_pk_bf16_f32 v53, v22, v23\n"
        "v_mfma_f32_16x16x32_bf16 v[40:43], v[84:87], v[100:103], v[40:43]\n"
        "v_exp_f32 v26, v26\n"
        "v_exp_f32 v27, v27\n"
        "v_mfma_f32_16x16x32_bf16 v[44:47], v[84:87], v[116:119], v[44:47]\n"
        "ds_read_b128 v[84:87], v1 offset:21504\n"
        "ds_read_b128 a[92:95], v3 offset:5120\n"
        "v_cvt_pk_bf16_f32 v50, v24, v25\n"
        "v_exp_f32 v28, v28\n"
        "v_mfma_f32_16x16x32_bf16 v[40:43], v[88:91], v[104:107], v[40:43]\n"
        "v_exp_f32 v29, v29\n"
        "v_mfma_f32_16x16x32_bf16 v[44:47], v[88:91], v[120:123], v[44:47]\n"
        "ds_read_b128 v[88:91], v1 offset:22528\n"
        "ds_read_b128 a[96:99], v3 offset:6144\n"
        "v_cvt_pk_bf16_f32 v51, v26, v27\n"
        "v_exp_f32 v30, v30\n"
        "v_mfma_f32_16x16x32_bf16 v[40:43], v[92:95], v[108:111], v[40:43]\n"
        "v_exp_f32 v31, v31\n"
        "v_cvt_pk_bf16_f32 v54, v28, v29\n"
        "v_mfma_f32_16x16x32_bf16 v[44:47], v[92:95], v[124:127], v[44:47]\n"
        "ds_read_b128 v[92:95], v1 offset:23552\n"
        "ds_read_b128 a[100:103], v3 offset:7168\n"
        "v_cvt_pk_bf16_f32 v55, v30, v31\n"
        "s_waitcnt lgkmcnt(6)\n"
        "v_mfma_f32_16x16x32_bf16 v[16:19], v[64:67], v[96:99], v[8:11]\n"
        "v_mfma_f32_16x16x32_bf16 v[20:23], v[64:67], v[112:115], v[12:15]\n"
        "ds_read_b128 v[64:67], v1 offset:24576\n"
        "v_mfma_f32_16x16x32_bf16 a[0:3], a[72:75], v[48:51], 0\n"
        "v_exp_f32 v32, v32\n"
        "v_mfma_f32_16x16x32_bf16 a[4:7], a[72:75], v[52:55], 0\n"
        "ds_read_b128 a[72:75], v3 offset:8192\n"
        "v_exp_f32 v33, v33\n"
        "v_mfma_f32_16x16x32_bf16 v[16:19], v[68:71], v[100:103], v[16:19]\n"
        "v_exp_f32 v34, v34\n"
        "v_mfma_f32_16x16x32_bf16 v[20:23], v[68:71], v[116:119], v[20:23]\n"
        "ds_read_b128 v[68:71], v1 offset:25600\n"
        "v_mfma_f32_16x16x32_bf16 a[8:11], a[76:79], v[48:51], 0\n"
        "v_exp_f32 v35, v35\n"
        "v_mfma_f32_16x16x32_bf16 a[12:15], a[76:79], v[52:55], 0\n"
        "ds_read_b128 a[76:79], v3 offset:9216\n"
        "v_cvt_pk_bf16_f32 v56, v32, v33\n"
        "v_mfma_f32_16x16x32_bf16 v[16:19], v[72:75], v[104:107], v[16:19]\n"
        "v_exp_f32 v36, v36\n"
        "v_mfma_f32_16x16x32_bf16 v[20:23], v[72:75], v[120:123], v[20:23]\n"
        "ds_read_b128 v[72:75], v1 offset:26624\n"
        "v_mfma_f32_16x16x32_bf16 a[16:19], a[80:83], v[48:51], 0\n"
        "v_exp_f32 v37, v37\n"
        "v_mfma_f32_16x16x32_bf16 a[20:23], a[80:83], v[52:55], 0\n"
        "ds_read_b128 a[80:83], v3 offset:10240\n"
        "v_cvt_pk_bf16_f32 v57, v34, v35\n"
        "v_mfma_f32_16x16x32_bf16 v[16:19], v[76:79], v[108:111], v[16:19]\n"
        "v_exp_f32 v38, v38\n"
        "v_mfma_f32_16x16x32_bf16 v[20:23], v[76:79], v[124:127], v[20:23]\n"
        "ds_read_b128 v[76:79], v1 offset:27648\n"
        "v_mfma_f32_16x16x32_bf16 a[24:27], a[84:87], v[48:51], 0\n"
        "v_exp_f32 v39, v39\n"
        "v_mfma_f32_16x16x32_bf16 a[28:31], a[84:87], v[52:55], 0\n"
        "ds_read_b128 a[84:87], v3 offset:11264\n"
        "v_cvt_pk_bf16_f32 v60, v36, v37\n"
        "s_waitcnt lgkmcnt(8)\n"
        "s_barrier\n"
        "v_bfe_u32 v5, v0, 4, 2\n"
        "v_lshlrev_b32 v5, 3, v5\n"
        "v_add_u32 v5, 159744, v5\n"
        "ds_read_b32 v4, v5\n"
        "v_mfma_f32_16x16x32_bf16 v[24:27], v[80:83], v[96:99], v[8:11]\n"
        "v_exp_f32 v40, v40\n"
        "v_mfma_f32_16x16x32_bf16 v[28:31], v[80:83], v[112:115], v[12:15]\n"
        "ds_read_b128 v[80:83], v1 offset:28672\n"
        "v_mfma_f32_16x16x32_bf16 a[32:35], a[88:91], v[48:51], 0\n"
        "v_exp_f32 v41, v41\n"
        "v_mfma_f32_16x16x32_bf16 a[36:39], a[88:91], v[52:55], 0\n"
        "ds_read_b128 a[88:91], v3 offset:12288\n"
        "v_cvt_pk_bf16_f32 v61, v38, v39\n"
        "v_mfma_f32_16x16x32_bf16 v[24:27], v[84:87], v[100:103], v[24:27]\n"
        "v_exp_f32 v42, v42\n"
        "v_mfma_f32_16x16x32_bf16 v[28:31], v[84:87], v[116:119], v[28:31]\n"
        "ds_read_b128 v[84:87], v1 offset:29696\n"
        "v_mfma_f32_16x16x32_bf16 a[40:43], a[92:95], v[48:51], 0\n"
        "v_exp_f32 v43, v43\n"
        "v_mfma_f32_16x16x32_bf16 a[44:47], a[92:95], v[52:55], 0\n"
        "ds_read_b128 a[92:95], v3 offset:13312\n"
        "v_cvt_pk_bf16_f32 v58, v40, v41\n"
        "v_mfma_f32_16x16x32_bf16 v[24:27], v[88:91], v[104:107], v[24:27]\n"
        "v_exp_f32 v44, v44\n"
        "v_mfma_f32_16x16x32_bf16 v[28:31], v[88:91], v[120:123], v[28:31]\n"
        "ds_read_b128 v[88:91], v1 offset:30720\n"
        "v_mfma_f32_16x16x32_bf16 a[48:51], a[96:99], v[48:51], 0\n"
        "v_exp_f32 v45, v45\n"
        "v_mfma_f32_16x16x32_bf16 a[52:55], a[96:99], v[52:55], 0\n"
        "ds_read_b128 a[96:99], v3 offset:14336\n"
        "v_cvt_pk_bf16_f32 v59, v42, v43\n"
        "v_mfma_f32_16x16x32_bf16 v[24:27], v[92:95], v[108:111], v[24:27]\n"
        "v_exp_f32 v46, v46\n"
        "v_mfma_f32_16x16x32_bf16 v[28:31], v[92:95], v[124:127], v[28:31]\n"
        "ds_read_b128 v[92:95], v1 offset:31744\n"
        "v_mfma_f32_16x16x32_bf16 a[56:59], a[100:103], v[48:51], 0\n"
        "v_exp_f32 v47, v47\n"
        "v_mfma_f32_16x16x32_bf16 a[60:63], a[100:103], v[52:55], 0\n"
        "ds_read_b128 a[100:103], v3 offset:15360\n"
        "v_cvt_pk_bf16_f32 v62, v44, v45\n"
        "v_mfma_f32_16x16x32_bf16 a[64:67], a[104:107], v[48:51], 0\n"
        "v_cvt_pk_bf16_f32 v63, v46, v47\n"
        "v_mfma_f32_16x16x32_bf16 a[68:71], a[104:107], v[52:55], 0\n"
        "s_waitcnt lgkmcnt(4)\n"
        "v_mfma_f32_16x16x32_bf16 v[32:35], v[64:67], v[96:99], v[8:11]\n"
        "v_mfma_f32_16x16x32_bf16 v[36:39], v[64:67], v[112:115], v[12:15]\n"
        "ds_read_b128 v[64:67], v2 offset:0\n"
        "v_mfma_f32_16x16x32_bf16 a[0:3], a[72:75], v[56:59], a[0:3]\n"
        "v_exp_f32 v16, v16\n"
        "v_mfma_f32_16x16x32_bf16 a[4:7], a[72:75], v[60:63], a[4:7]\n"
        "ds_read_b128 a[72:75], v3 offset:16384\n"
        "v_exp_f32 v17, v17\n"
        "v_mfma_f32_16x16x32_bf16 v[32:35], v[68:71], v[100:103], v[32:35]\n"
        "v_exp_f32 v18, v18\n"
        "v_mfma_f32_16x16x32_bf16 v[36:39], v[68:71], v[116:119], v[36:39]\n"
        "ds_read_b128 v[68:71], v2 offset:1024\n"
        "v_mfma_f32_16x16x32_bf16 a[8:11], a[76:79], v[56:59], a[8:11]\n"
        "v_exp_f32 v19, v19\n"
        "v_mfma_f32_16x16x32_bf16 a[12:15], a[76:79], v[60:63], a[12:15]\n"
        "ds_read_b128 a[76:79], v3 offset:17408\n"
        "v_cvt_pk_bf16_f32 v48, v16, v17\n"
        "v_mfma_f32_16x16x32_bf16 v[32:35], v[72:75], v[104:107], v[32:35]\n"
        "v_exp_f32 v20, v20\n"
        "v_mfma_f32_16x16x32_bf16 v[36:39], v[72:75], v[120:123], v[36:39]\n"
        "ds_read_b128 v[72:75], v2 offset:2048\n"
        "v_mfma_f32_16x16x32_bf16 a[16:19], a[80:83], v[56:59], a[16:19]\n"
        "v_exp_f32 v21, v21\n"
        "v_mfma_f32_16x16x32_bf16 a[20:23], a[80:83], v[60:63], a[20:23]\n"
        "ds_read_b128 a[80:83], v3 offset:18432\n"
        "v_cvt_pk_bf16_f32 v49, v18, v19\n"
        "v_mfma_f32_16x16x32_bf16 v[32:35], v[76:79], v[108:111], v[32:35]\n"
        "v_exp_f32 v22, v22\n"
        "v_mfma_f32_16x16x32_bf16 v[36:39], v[76:79], v[124:127], v[36:39]\n"
        "ds_read_b128 v[76:79], v2 offset:3072\n"
        "v_mfma_f32_16x16x32_bf16 a[24:27], a[84:87], v[56:59], a[24:27]\n"
        "v_exp_f32 v23, v23\n"
        "v_mfma_f32_16x16x32_bf16 a[28:31], a[84:87], v[60:63], a[28:31]\n"
        "ds_read_b128 a[84:87], v3 offset:19456\n"
        "v_cvt_pk_bf16_f32 v52, v20, v21\n"
        "v_mfma_f32_16x16x32_bf16 v[40:43], v[80:83], v[96:99], v[8:11]\n"
        "v_exp_f32 v24, v24\n"
        "v_mfma_f32_16x16x32_bf16 v[44:47], v[80:83], v[112:115], v[12:15]\n"
        "ds_read_b128 v[80:83], v2 offset:4096\n"
        "v_mfma_f32_16x16x32_bf16 a[32:35], a[88:91], v[56:59], a[32:35]\n"
        "v_exp_f32 v25, v25\n"
        "v_mfma_f32_16x16x32_bf16 a[36:39], a[88:91], v[60:63], a[36:39]\n"
        "ds_read_b128 a[88:91], v3 offset:20480\n"
        "v_cvt_pk_bf16_f32 v53, v22, v23\n"
        "v_mfma_f32_16x16x32_bf16 v[40:43], v[84:87], v[100:103], v[40:43]\n"
        "v_exp_f32 v26, v26\n"
        "v_mfma_f32_16x16x32_bf16 v[44:47], v[84:87], v[116:119], v[44:47]\n"
        "ds_read_b128 v[84:87], v2 offset:5120\n"
        "v_mfma_f32_16x16x32_bf16 a[40:43], a[92:95], v[56:59], a[40:43]\n"
        "v_exp_f32 v27, v27\n"
        "v_mfma_f32_16x16x32_bf16 a[44:47], a[92:95], v[60:63], a[44:47]\n"
        "ds_read_b128 a[92:95], v3 offset:21504\n"
        "v_cvt_pk_bf16_f32 v50, v24, v25\n"
        "s_waitcnt lgkmcnt(5)\n"
        "v_mfma_f32_16x16x32_bf16 v[40:43], v[88:91], v[104:107], v[40:43]\n"
        "v_exp_f32 v28, v28\n"
        "v_mfma_f32_16x16x32_bf16 v[44:47], v[88:91], v[120:123], v[44:47]\n"
        "ds_read_b128 v[88:91], v2 offset:6144\n"
        "v_mfma_f32_16x16x32_bf16 a[48:51], a[96:99], v[56:59], a[48:51]\n"
        "v_exp_f32 v29, v29\n"
        "v_mfma_f32_16x16x32_bf16 a[52:55], a[96:99], v[60:63], a[52:55]\n"
        "ds_read_b128 a[96:99], v3 offset:22528\n"
        "v_cvt_pk_bf16_f32 v51, v26, v27\n"
        "v_mfma_f32_16x16x32_bf16 v[40:43], v[92:95], v[108:111], v[40:43]\n"
        "v_exp_f32 v30, v30\n"
        "v_mfma_f32_16x16x32_bf16 v[44:47], v[92:95], v[124:127], v[44:47]\n"
        "ds_read_b128 v[92:95], v2 offset:7168\n"
        "v_mfma_f32_16x16x32_bf16 a[56:59], a[100:103], v[56:59], a[56:59]\n"
        "v_exp_f32 v31, v31\n"
        "v_mfma_f32_16x16x32_bf16 a[60:63], a[100:103], v[60:63], a[60:63]\n"
        "ds_read_b128 a[100:103], v3 offset:23552\n"
        "v_cvt_pk_bf16_f32 v54, v28, v29\n"
        "v_mfma_f32_16x16x32_bf16 a[64:67], a[104:107], v[56:59], a[64:67]\n"
        "v_cvt_pk_bf16_f32 v55, v30, v31\n"
        "v_mfma_f32_16x16x32_bf16 a[68:71], a[104:107], v[60:63], a[68:71]\n"
        "v_mfma_f32_16x16x32_bf16 v[16:19], v[64:67], v[96:99], v[8:11]\n"
        "v_mfma_f32_16x16x32_bf16 v[20:23], v[64:67], v[112:115], v[12:15]\n"
        "ds_read_b128 v[64:67], v2 offset:8192\n"
        "v_mfma_f32_16x16x32_bf16 a[0:3], a[72:75], v[48:51], a[0:3]\n"
        "v_exp_f32 v32, v32\n"
        "v_mfma_f32_16x16x32_bf16 a[4:7], a[72:75], v[52:55], a[4:7]\n"
        "ds_read_b128 a[72:75], v3 offset:24576\n"
        "v_exp_f32 v33, v33\n"
        "v_mfma_f32_16x16x32_bf16 v[16:19], v[68:71], v[100:103], v[16:19]\n"
        "v_exp_f32 v34, v34\n"
        "v_mfma_f32_16x16x32_bf16 v[20:23], v[68:71], v[116:119], v[20:23]\n"
        "ds_read_b128 v[68:71], v2 offset:9216\n"
        "v_mfma_f32_16x16x32_bf16 a[8:11], a[76:79], v[48:51], a[8:11]\n"
        "v_exp_f32 v35, v35\n"
        "v_mfma_f32_16x16x32_bf16 a[12:15], a[76:79], v[52:55], a[12:15]\n"
        "ds_read_b128 a[76:79], v3 offset:25600\n"
        "v_cvt_pk_bf16_f32 v56, v32, v33\n"
        "v_mfma_f32_16x16x32_bf16 v[16:19], v[72:75], v[104:107], v[16:19]\n"
        "v_exp_f32 v36, v36\n"
        "v_mfma_f32_16x16x32_bf16 v[20:23], v[72:75], v[120:123], v[20:23]\n"
        "ds_read_b128 v[72:75], v2 offset:10240\n"
        "v_mfma_f32_16x16x32_bf16 a[16:19], a[80:83], v[48:51], a[16:19]\n"
        "v_exp_f32 v37, v37\n"
        "v_mfma_f32_16x16x32_bf16 a[20:23], a[80:83], v[52:55], a[20:23]\n"
        "ds_read_b128 a[80:83], v3 offset:26624\n"
        "v_cvt_pk_bf16_f32 v57, v34, v35\n"
        "v_mfma_f32_16x16x32_bf16 v[16:19], v[76:79], v[108:111], v[16:19]\n"
        "v_exp_f32 v38, v38\n"
        "v_mfma_f32_16x16x32_bf16 v[20:23], v[76:79], v[124:127], v[20:23]\n"
        "ds_read_b128 v[76:79], v2 offset:11264\n"
        "s_waitcnt lgkmcnt(5)\n"
        "v_mfma_f32_16x16x32_bf16 a[24:27], a[84:87], v[48:51], a[24:27]\n"
        "v_exp_f32 v39, v39\n"
        "v_mfma_f32_16x16x32_bf16 a[28:31], a[84:87], v[52:55], a[28:31]\n"
        "ds_read_b128 a[84:87], v3 offset:27648\n"
        "v_cvt_pk_bf16_f32 v60, v36, v37\n"
        "s_waitcnt vmcnt(0)\n"
        "s_waitcnt lgkmcnt(8)\n"
        "s_barrier\n"
        "s_cmp_lg_u32 s60, 1\n"
        "s_cbranch_scc1 KA_NCAS_%=\n"
        "s_cmp_lg_u32 s36, 0\n"
        "s_cbranch_scc1 KA_NCAS_%=\n"
        "v_cmp_ne_u32 vcc, 0, v4\n"
        "s_nop 4\n"
        "s_cbranch_vccnz KA_NCAS_%=\n"
        "s_lshr_b32 s88, s37, 2\n"
        "s_lshl_b32 s88, s88, 3\n"
        "s_add_u32 s88, s88, s38\n"
        "s_lshl_b32 s88, s88, 2\n"
        "s_and_b32 s52, s37, 3\n"
        "s_add_u32 s88, s88, s52\n"
        "s_lshl_b32 s88, s88, 7\n"
        "s_add_u32 s88, s88, 65536\n"
        "s_mov_b64 exec, 1\n"
        "v_mov_b32 v6, s88\n"
        "v_mov_b32 v4, s96\n"
        "v_mov_b32 v5, s97\n"
        "global_store_dwordx2 v6, v[4:5], s[86:87] sc1\n"
        "s_mov_b64 exec, -1\n"
        "KA_NCAS_%=:\n"
        "s_nop 3\n"
        "v_mfma_f32_16x16x32_bf16 v[24:27], v[80:83], v[96:99], v[8:11]\n"
        "v_exp_f32 v40, v40\n"
        "v_mfma_f32_16x16x32_bf16 v[28:31], v[80:83], v[112:115], v[12:15]\n"
        "ds_read_b128 v[80:83], v2 offset:12288\n"
        "v_mfma_f32_16x16x32_bf16 a[32:35], a[88:91], v[48:51], a[32:35]\n"
        "v_exp_f32 v41, v41\n"
        "v_mfma_f32_16x16x32_bf16 a[36:39], a[88:91], v[52:55], a[36:39]\n"
        "ds_read_b128 a[88:91], v3 offset:28672\n"
        "v_cvt_pk_bf16_f32 v61, v38, v39\n"
        "v_mfma_f32_16x16x32_bf16 v[24:27], v[84:87], v[100:103], v[24:27]\n"
        "v_exp_f32 v42, v42\n"
        "v_mfma_f32_16x16x32_bf16 v[28:31], v[84:87], v[116:119], v[28:31]\n"
        "ds_read_b128 v[84:87], v2 offset:13312\n"
        "v_mfma_f32_16x16x32_bf16 a[40:43], a[92:95], v[48:51], a[40:43]\n"
        "v_exp_f32 v43, v43\n"
        "v_mfma_f32_16x16x32_bf16 a[44:47], a[92:95], v[52:55], a[44:47]\n"
        "ds_read_b128 a[92:95], v3 offset:29696\n"
        "v_cvt_pk_bf16_f32 v58, v40, v41\n"
        "v_mfma_f32_16x16x32_bf16 v[24:27], v[88:91], v[104:107], v[24:27]\n"
        "v_exp_f32 v44, v44\n"
        "v_mfma_f32_16x16x32_bf16 v[28:31], v[88:91], v[120:123], v[28:31]\n"
        "ds_read_b128 v[88:91], v2 offset:14336\n"
        "v_mfma_f32_16x16x32_bf16 a[48:51], a[96:99], v[48:51], a[48:51]\n"
        "v_exp_f32 v45, v45\n"
        "v_mfma_f32_16x16x32_bf16 a[52:55], a[96:99], v[52:55], a[52:55]\n"
        "ds_read_b128 a[96:99], v3 offset:30720\n"
        "v_cvt_pk_bf16_f32 v59, v42, v43\n"
        "v_mfma_f32_16x16x32_bf16 v[24:27], v[92:95], v[108:111], v[24:27]\n"
        "v_exp_f32 v46, v46\n"
        "v_mfma_f32_16x16x32_bf16 v[28:31], v[92:95], v[124:127], v[28:31]\n"
        "ds_read_b128 v[92:95], v2 offset:15360\n"
        "v_mfma_f32_16x16x32_bf16 a[56:59], a[100:103], v[48:51], a[56:59]\n"
        "v_exp_f32 v47, v47\n"
        "v_mfma_f32_16x16x32_bf16 a[60:63], a[100:103], v[52:55], a[60:63]\n"
        "ds_read_b128 a[100:103], v3 offset:31744\n"
        "v_cvt_pk_bf16_f32 v62, v44, v45\n"
        "v_mfma_f32_16x16x32_bf16 a[64:67], a[104:107], v[48:51], a[64:67]\n"
        "v_cvt_pk_bf16_f32 v63, v46, v47\n"
        "v_mfma_f32_16x16x32_bf16 a[68:71], a[104:107], v[52:55], a[68:71]\n"
        "v_xor_b32 v1, 0x8000, v1\n"
        "v_xor_b32 v2, 0x8000, v2\n"
        "v_xor_b32 v3, 0x8000, v3\n"
        "s_sub_u32 s63, s63, 1\n"
        "s_cmp_eq_u32 s63, 0\n"
        "s_cbranch_scc1 P3_DIAG_MID_%=\n"
        "P3_LOOP_%=:\n"
        "v_mfma_f32_16x16x32_bf16 v[32:35], v[64:67], v[96:99], v[8:11]\n"
        "v_mfma_f32_16x16x32_bf16 v[36:39], v[64:67], v[112:115], v[12:15]\n"
        "ds_read_b128 v[64:67], v1 offset:16384\n"
        "v_mfma_f32_16x16x32_bf16 a[0:3], a[72:75], v[56:59], a[0:3]\n"
        "v_exp_f32 v16, v16\n"
        "v_mfma_f32_16x16x32_bf16 a[4:7], a[72:75], v[60:63], a[4:7]\n"
        "ds_read_b128 a[72:75], v3 offset:0\n"
        "v_exp_f32 v17, v17\n"
        "s_waitcnt lgkmcnt(4)\n"
        "v_mfma_f32_16x16x32_bf16 v[32:35], v[68:71], v[100:103], v[32:35]\n"
        "v_exp_f32 v18, v18\n"
        "v_mfma_f32_16x16x32_bf16 v[36:39], v[68:71], v[116:119], v[36:39]\n"
        "ds_read_b128 v[68:71], v1 offset:17408\n"
        "v_mfma_f32_16x16x32_bf16 a[8:11], a[76:79], v[56:59], a[8:11]\n"
        "v_exp_f32 v19, v19\n"
        "v_mfma_f32_16x16x32_bf16 a[12:15], a[76:79], v[60:63], a[12:15]\n"
        "ds_read_b128 a[76:79], v3 offset:1024\n"
        "v_cvt_pk_bf16_f32 v48, v16, v17\n"
        "v_mfma_f32_16x16x32_bf16 v[32:35], v[72:75], v[104:107], v[32:35]\n"
        "v_exp_f32 v20, v20\n"
        "v_mfma_f32_16x16x32_bf16 v[36:39], v[72:75], v[120:123], v[36:39]\n"
        "ds_read_b128 v[72:75], v1 offset:18432\n"
        "v_mfma_f32_16x16x32_bf16 a[16:19], a[80:83], v[56:59], a[16:19]\n"
        "v_exp_f32 v21, v21\n"
        "v_mfma_f32_16x16x32_bf16 a[20:23], a[80:83], v[60:63], a[20:23]\n"
        "ds_read_b128 a[80:83], v3 offset:2048\n"
        "v_cvt_pk_bf16_f32 v49, v18, v19\n"
        "v_mfma_f32_16x16x32_bf16 v[32:35], v[76:79], v[108:111], v[32:35]\n"
        "v_exp_f32 v22, v22\n"
        "v_mfma_f32_16x16x32_bf16 v[36:39], v[76:79], v[124:127], v[36:39]\n"
        "ds_read_b128 v[76:79], v1 offset:19456\n"
        "v_mfma_f32_16x16x32_bf16 a[24:27], a[84:87], v[56:59], a[24:27]\n"
        "v_exp_f32 v23, v23\n"
        "v_mfma_f32_16x16x32_bf16 a[28:31], a[84:87], v[60:63], a[28:31]\n"
        "ds_read_b128 a[84:87], v3 offset:3072\n"
        "v_cvt_pk_bf16_f32 v52, v20, v21\n"
        "v_mfma_f32_16x16x32_bf16 v[40:43], v[80:83], v[96:99], v[8:11]\n"
        "v_exp_f32 v24, v24\n"
        "v_mfma_f32_16x16x32_bf16 v[44:47], v[80:83], v[112:115], v[12:15]\n"
        "ds_read_b128 v[80:83], v1 offset:20480\n"
        "v_mfma_f32_16x16x32_bf16 a[32:35], a[88:91], v[56:59], a[32:35]\n"
        "v_exp_f32 v25, v25\n"
        "v_mfma_f32_16x16x32_bf16 a[36:39], a[88:91], v[60:63], a[36:39]\n"
        "ds_read_b128 a[88:91], v3 offset:4096\n"
        "v_cvt_pk_bf16_f32 v53, v22, v23\n"
        "v_mfma_f32_16x16x32_bf16 v[40:43], v[84:87], v[100:103], v[40:43]\n"
        "v_exp_f32 v26, v26\n"
        "v_mfma_f32_16x16x32_bf16 v[44:47], v[84:87], v[116:119], v[44:47]\n"
        "ds_read_b128 v[84:87], v1 offset:21504\n"
        "v_mfma_f32_16x16x32_bf16 a[40:43], a[92:95], v[56:59], a[40:43]\n"
        "v_exp_f32 v27, v27\n"
        "v_mfma_f32_16x16x32_bf16 a[44:47], a[92:95], v[60:63], a[44:47]\n"
        "ds_read_b128 a[92:95], v3 offset:5120\n"
        "v_cvt_pk_bf16_f32 v50, v24, v25\n"
        "v_mfma_f32_16x16x32_bf16 v[40:43], v[88:91], v[104:107], v[40:43]\n"
        "v_exp_f32 v28, v28\n"
        "v_mfma_f32_16x16x32_bf16 v[44:47], v[88:91], v[120:123], v[44:47]\n"
        "ds_read_b128 v[88:91], v1 offset:22528\n"
        "v_mfma_f32_16x16x32_bf16 a[48:51], a[96:99], v[56:59], a[48:51]\n"
        "v_exp_f32 v29, v29\n"
        "v_mfma_f32_16x16x32_bf16 a[52:55], a[96:99], v[60:63], a[52:55]\n"
        "ds_read_b128 a[96:99], v3 offset:6144\n"
        "v_cvt_pk_bf16_f32 v51, v26, v27\n"
        "s_waitcnt lgkmcnt(5)\n"
        "v_mfma_f32_16x16x32_bf16 v[40:43], v[92:95], v[108:111], v[40:43]\n"
        "v_exp_f32 v30, v30\n"
        "v_mfma_f32_16x16x32_bf16 v[44:47], v[92:95], v[124:127], v[44:47]\n"
        "ds_read_b128 v[92:95], v1 offset:23552\n"
        "v_mfma_f32_16x16x32_bf16 a[56:59], a[100:103], v[56:59], a[56:59]\n"
        "v_exp_f32 v31, v31\n"
        "v_mfma_f32_16x16x32_bf16 a[60:63], a[100:103], v[60:63], a[60:63]\n"
        "ds_read_b128 a[100:103], v3 offset:7168\n"
        "v_cvt_pk_bf16_f32 v54, v28, v29\n"
        "v_mfma_f32_16x16x32_bf16 a[64:67], a[104:107], v[56:59], a[64:67]\n"
        "v_cvt_pk_bf16_f32 v55, v30, v31\n"
        "v_mfma_f32_16x16x32_bf16 a[68:71], a[104:107], v[60:63], a[68:71]\n"
        "v_mfma_f32_16x16x32_bf16 v[16:19], v[64:67], v[96:99], v[8:11]\n"
        "v_mfma_f32_16x16x32_bf16 v[20:23], v[64:67], v[112:115], v[12:15]\n"
        "ds_read_b128 v[64:67], v1 offset:24576\n"
        "v_mfma_f32_16x16x32_bf16 a[0:3], a[72:75], v[48:51], a[0:3]\n"
        "v_exp_f32 v32, v32\n"
        "v_mfma_f32_16x16x32_bf16 a[4:7], a[72:75], v[52:55], a[4:7]\n"
        "ds_read_b128 a[72:75], v3 offset:8192\n"
        "v_exp_f32 v33, v33\n"
        "v_mfma_f32_16x16x32_bf16 v[16:19], v[68:71], v[100:103], v[16:19]\n"
        "v_exp_f32 v34, v34\n"
        "v_mfma_f32_16x16x32_bf16 v[20:23], v[68:71], v[116:119], v[20:23]\n"
        "ds_read_b128 v[68:71], v1 offset:25600\n"
        "v_mfma_f32_16x16x32_bf16 a[8:11], a[76:79], v[48:51], a[8:11]\n"
        "v_exp_f32 v35, v35\n"
        "v_mfma_f32_16x16x32_bf16 a[12:15], a[76:79], v[52:55], a[12:15]\n"
        "ds_read_b128 a[76:79], v3 offset:9216\n"
        "v_cvt_pk_bf16_f32 v56, v32, v33\n"
        "v_mfma_f32_16x16x32_bf16 v[16:19], v[72:75], v[104:107], v[16:19]\n"
        "v_exp_f32 v36, v36\n"
        "v_mfma_f32_16x16x32_bf16 v[20:23], v[72:75], v[120:123], v[20:23]\n"
        "ds_read_b128 v[72:75], v1 offset:26624\n"
        "v_mfma_f32_16x16x32_bf16 a[16:19], a[80:83], v[48:51], a[16:19]\n"
        "v_exp_f32 v37, v37\n"
        "v_mfma_f32_16x16x32_bf16 a[20:23], a[80:83], v[52:55], a[20:23]\n"
        "ds_read_b128 a[80:83], v3 offset:10240\n"
        "v_cvt_pk_bf16_f32 v57, v34, v35\n"
        "v_mfma_f32_16x16x32_bf16 v[16:19], v[76:79], v[108:111], v[16:19]\n"
        "v_exp_f32 v38, v38\n"
        "v_mfma_f32_16x16x32_bf16 v[20:23], v[76:79], v[124:127], v[20:23]\n"
        "ds_read_b128 v[76:79], v1 offset:27648\n"
        "v_mfma_f32_16x16x32_bf16 a[24:27], a[84:87], v[48:51], a[24:27]\n"
        "v_exp_f32 v39, v39\n"
        "v_mfma_f32_16x16x32_bf16 a[28:31], a[84:87], v[52:55], a[28:31]\n"
        "ds_read_b128 a[84:87], v3 offset:11264\n"
        "v_cvt_pk_bf16_f32 v60, v36, v37\n"
        "s_waitcnt lgkmcnt(8)\n"
        "s_barrier\n"
        "v_mfma_f32_16x16x32_bf16 v[24:27], v[80:83], v[96:99], v[8:11]\n"
        "v_exp_f32 v40, v40\n"
        "v_mfma_f32_16x16x32_bf16 v[28:31], v[80:83], v[112:115], v[12:15]\n"
        "ds_read_b128 v[80:83], v1 offset:28672\n"
        "v_mfma_f32_16x16x32_bf16 a[32:35], a[88:91], v[48:51], a[32:35]\n"
        "v_exp_f32 v41, v41\n"
        "v_mfma_f32_16x16x32_bf16 a[36:39], a[88:91], v[52:55], a[36:39]\n"
        "ds_read_b128 a[88:91], v3 offset:12288\n"
        "v_cvt_pk_bf16_f32 v61, v38, v39\n"
        "v_mfma_f32_16x16x32_bf16 v[24:27], v[84:87], v[100:103], v[24:27]\n"
        "v_exp_f32 v42, v42\n"
        "v_mfma_f32_16x16x32_bf16 v[28:31], v[84:87], v[116:119], v[28:31]\n"
        "ds_read_b128 v[84:87], v1 offset:29696\n"
        "v_mfma_f32_16x16x32_bf16 a[40:43], a[92:95], v[48:51], a[40:43]\n"
        "v_exp_f32 v43, v43\n"
        "v_mfma_f32_16x16x32_bf16 a[44:47], a[92:95], v[52:55], a[44:47]\n"
        "ds_read_b128 a[92:95], v3 offset:13312\n"
        "v_cvt_pk_bf16_f32 v58, v40, v41\n"
        "v_mfma_f32_16x16x32_bf16 v[24:27], v[88:91], v[104:107], v[24:27]\n"
        "v_exp_f32 v44, v44\n"
        "v_mfma_f32_16x16x32_bf16 v[28:31], v[88:91], v[120:123], v[28:31]\n"
        "ds_read_b128 v[88:91], v1 offset:30720\n"
        "v_mfma_f32_16x16x32_bf16 a[48:51], a[96:99], v[48:51], a[48:51]\n"
        "v_exp_f32 v45, v45\n"
        "v_mfma_f32_16x16x32_bf16 a[52:55], a[96:99], v[52:55], a[52:55]\n"
        "ds_read_b128 a[96:99], v3 offset:14336\n"
        "v_cvt_pk_bf16_f32 v59, v42, v43\n"
        "v_mfma_f32_16x16x32_bf16 v[24:27], v[92:95], v[108:111], v[24:27]\n"
        "v_exp_f32 v46, v46\n"
        "v_mfma_f32_16x16x32_bf16 v[28:31], v[92:95], v[124:127], v[28:31]\n"
        "ds_read_b128 v[92:95], v1 offset:31744\n"
        "v_mfma_f32_16x16x32_bf16 a[56:59], a[100:103], v[48:51], a[56:59]\n"
        "v_exp_f32 v47, v47\n"
        "v_mfma_f32_16x16x32_bf16 a[60:63], a[100:103], v[52:55], a[60:63]\n"
        "ds_read_b128 a[100:103], v3 offset:15360\n"
        "v_cvt_pk_bf16_f32 v62, v44, v45\n"
        "v_mfma_f32_16x16x32_bf16 a[64:67], a[104:107], v[48:51], a[64:67]\n"
        "v_cvt_pk_bf16_f32 v63, v46, v47\n"
        "v_mfma_f32_16x16x32_bf16 a[68:71], a[104:107], v[52:55], a[68:71]\n"
        "s_waitcnt lgkmcnt(4)\n"
        "v_mfma_f32_16x16x32_bf16 v[32:35], v[64:67], v[96:99], v[8:11]\n"
        "v_mfma_f32_16x16x32_bf16 v[36:39], v[64:67], v[112:115], v[12:15]\n"
        "ds_read_b128 v[64:67], v2 offset:0\n"
        "v_mfma_f32_16x16x32_bf16 a[0:3], a[72:75], v[56:59], a[0:3]\n"
        "v_exp_f32 v16, v16\n"
        "v_mfma_f32_16x16x32_bf16 a[4:7], a[72:75], v[60:63], a[4:7]\n"
        "ds_read_b128 a[72:75], v3 offset:16384\n"
        "v_exp_f32 v17, v17\n"
        "v_mfma_f32_16x16x32_bf16 v[32:35], v[68:71], v[100:103], v[32:35]\n"
        "v_exp_f32 v18, v18\n"
        "v_mfma_f32_16x16x32_bf16 v[36:39], v[68:71], v[116:119], v[36:39]\n"
        "ds_read_b128 v[68:71], v2 offset:1024\n"
        "v_mfma_f32_16x16x32_bf16 a[8:11], a[76:79], v[56:59], a[8:11]\n"
        "v_exp_f32 v19, v19\n"
        "v_mfma_f32_16x16x32_bf16 a[12:15], a[76:79], v[60:63], a[12:15]\n"
        "ds_read_b128 a[76:79], v3 offset:17408\n"
        "v_cvt_pk_bf16_f32 v48, v16, v17\n"
        "v_mfma_f32_16x16x32_bf16 v[32:35], v[72:75], v[104:107], v[32:35]\n"
        "v_exp_f32 v20, v20\n"
        "v_mfma_f32_16x16x32_bf16 v[36:39], v[72:75], v[120:123], v[36:39]\n"
        "ds_read_b128 v[72:75], v2 offset:2048\n"
        "v_mfma_f32_16x16x32_bf16 a[16:19], a[80:83], v[56:59], a[16:19]\n"
        "v_exp_f32 v21, v21\n"
        "v_mfma_f32_16x16x32_bf16 a[20:23], a[80:83], v[60:63], a[20:23]\n"
        "ds_read_b128 a[80:83], v3 offset:18432\n"
        "v_cvt_pk_bf16_f32 v49, v18, v19\n"
        "v_mfma_f32_16x16x32_bf16 v[32:35], v[76:79], v[108:111], v[32:35]\n"
        "v_exp_f32 v22, v22\n"
        "v_mfma_f32_16x16x32_bf16 v[36:39], v[76:79], v[124:127], v[36:39]\n"
        "ds_read_b128 v[76:79], v2 offset:3072\n"
        "v_mfma_f32_16x16x32_bf16 a[24:27], a[84:87], v[56:59], a[24:27]\n"
        "v_exp_f32 v23, v23\n"
        "v_mfma_f32_16x16x32_bf16 a[28:31], a[84:87], v[60:63], a[28:31]\n"
        "ds_read_b128 a[84:87], v3 offset:19456\n"
        "v_cvt_pk_bf16_f32 v52, v20, v21\n"
        "v_mfma_f32_16x16x32_bf16 v[40:43], v[80:83], v[96:99], v[8:11]\n"
        "v_exp_f32 v24, v24\n"
        "v_mfma_f32_16x16x32_bf16 v[44:47], v[80:83], v[112:115], v[12:15]\n"
        "ds_read_b128 v[80:83], v2 offset:4096\n"
        "v_mfma_f32_16x16x32_bf16 a[32:35], a[88:91], v[56:59], a[32:35]\n"
        "v_exp_f32 v25, v25\n"
        "v_mfma_f32_16x16x32_bf16 a[36:39], a[88:91], v[60:63], a[36:39]\n"
        "ds_read_b128 a[88:91], v3 offset:20480\n"
        "v_cvt_pk_bf16_f32 v53, v22, v23\n"
        "v_mfma_f32_16x16x32_bf16 v[40:43], v[84:87], v[100:103], v[40:43]\n"
        "v_exp_f32 v26, v26\n"
        "v_mfma_f32_16x16x32_bf16 v[44:47], v[84:87], v[116:119], v[44:47]\n"
        "ds_read_b128 v[84:87], v2 offset:5120\n"
        "v_mfma_f32_16x16x32_bf16 a[40:43], a[92:95], v[56:59], a[40:43]\n"
        "v_exp_f32 v27, v27\n"
        "v_mfma_f32_16x16x32_bf16 a[44:47], a[92:95], v[60:63], a[44:47]\n"
        "ds_read_b128 a[92:95], v3 offset:21504\n"
        "v_cvt_pk_bf16_f32 v50, v24, v25\n"
        "s_waitcnt lgkmcnt(5)\n"
        "v_mfma_f32_16x16x32_bf16 v[40:43], v[88:91], v[104:107], v[40:43]\n"
        "v_exp_f32 v28, v28\n"
        "v_mfma_f32_16x16x32_bf16 v[44:47], v[88:91], v[120:123], v[44:47]\n"
        "ds_read_b128 v[88:91], v2 offset:6144\n"
        "v_mfma_f32_16x16x32_bf16 a[48:51], a[96:99], v[56:59], a[48:51]\n"
        "v_exp_f32 v29, v29\n"
        "v_mfma_f32_16x16x32_bf16 a[52:55], a[96:99], v[60:63], a[52:55]\n"
        "ds_read_b128 a[96:99], v3 offset:22528\n"
        "v_cvt_pk_bf16_f32 v51, v26, v27\n"
        "v_mfma_f32_16x16x32_bf16 v[40:43], v[92:95], v[108:111], v[40:43]\n"
        "v_exp_f32 v30, v30\n"
        "v_mfma_f32_16x16x32_bf16 v[44:47], v[92:95], v[124:127], v[44:47]\n"
        "ds_read_b128 v[92:95], v2 offset:7168\n"
        "v_mfma_f32_16x16x32_bf16 a[56:59], a[100:103], v[56:59], a[56:59]\n"
        "v_exp_f32 v31, v31\n"
        "v_mfma_f32_16x16x32_bf16 a[60:63], a[100:103], v[60:63], a[60:63]\n"
        "ds_read_b128 a[100:103], v3 offset:23552\n"
        "v_cvt_pk_bf16_f32 v54, v28, v29\n"
        "v_mfma_f32_16x16x32_bf16 a[64:67], a[104:107], v[56:59], a[64:67]\n"
        "v_cvt_pk_bf16_f32 v55, v30, v31\n"
        "v_mfma_f32_16x16x32_bf16 a[68:71], a[104:107], v[60:63], a[68:71]\n"
        "v_mfma_f32_16x16x32_bf16 v[16:19], v[64:67], v[96:99], v[8:11]\n"
        "v_mfma_f32_16x16x32_bf16 v[20:23], v[64:67], v[112:115], v[12:15]\n"
        "ds_read_b128 v[64:67], v2 offset:8192\n"
        "v_mfma_f32_16x16x32_bf16 a[0:3], a[72:75], v[48:51], a[0:3]\n"
        "v_exp_f32 v32, v32\n"
        "v_mfma_f32_16x16x32_bf16 a[4:7], a[72:75], v[52:55], a[4:7]\n"
        "ds_read_b128 a[72:75], v3 offset:24576\n"
        "v_exp_f32 v33, v33\n"
        "v_mfma_f32_16x16x32_bf16 v[16:19], v[68:71], v[100:103], v[16:19]\n"
        "v_exp_f32 v34, v34\n"
        "v_mfma_f32_16x16x32_bf16 v[20:23], v[68:71], v[116:119], v[20:23]\n"
        "ds_read_b128 v[68:71], v2 offset:9216\n"
        "v_mfma_f32_16x16x32_bf16 a[8:11], a[76:79], v[48:51], a[8:11]\n"
        "v_exp_f32 v35, v35\n"
        "v_mfma_f32_16x16x32_bf16 a[12:15], a[76:79], v[52:55], a[12:15]\n"
        "ds_read_b128 a[76:79], v3 offset:25600\n"
        "v_cvt_pk_bf16_f32 v56, v32, v33\n"
        "v_mfma_f32_16x16x32_bf16 v[16:19], v[72:75], v[104:107], v[16:19]\n"
        "v_exp_f32 v36, v36\n"
        "v_mfma_f32_16x16x32_bf16 v[20:23], v[72:75], v[120:123], v[20:23]\n"
        "ds_read_b128 v[72:75], v2 offset:10240\n"
        "v_mfma_f32_16x16x32_bf16 a[16:19], a[80:83], v[48:51], a[16:19]\n"
        "v_exp_f32 v37, v37\n"
        "v_mfma_f32_16x16x32_bf16 a[20:23], a[80:83], v[52:55], a[20:23]\n"
        "ds_read_b128 a[80:83], v3 offset:26624\n"
        "v_cvt_pk_bf16_f32 v57, v34, v35\n"
        "v_mfma_f32_16x16x32_bf16 v[16:19], v[76:79], v[108:111], v[16:19]\n"
        "v_exp_f32 v38, v38\n"
        "v_mfma_f32_16x16x32_bf16 v[20:23], v[76:79], v[124:127], v[20:23]\n"
        "ds_read_b128 v[76:79], v2 offset:11264\n"
        "s_waitcnt lgkmcnt(5)\n"
        "v_mfma_f32_16x16x32_bf16 a[24:27], a[84:87], v[48:51], a[24:27]\n"
        "v_exp_f32 v39, v39\n"
        "v_mfma_f32_16x16x32_bf16 a[28:31], a[84:87], v[52:55], a[28:31]\n"
        "ds_read_b128 a[84:87], v3 offset:27648\n"
        "v_cvt_pk_bf16_f32 v60, v36, v37\n"
        "s_waitcnt lgkmcnt(8)\n"
        "s_barrier\n"
        "v_mfma_f32_16x16x32_bf16 v[24:27], v[80:83], v[96:99], v[8:11]\n"
        "v_exp_f32 v40, v40\n"
        "v_mfma_f32_16x16x32_bf16 v[28:31], v[80:83], v[112:115], v[12:15]\n"
        "ds_read_b128 v[80:83], v2 offset:12288\n"
        "v_mfma_f32_16x16x32_bf16 a[32:35], a[88:91], v[48:51], a[32:35]\n"
        "v_exp_f32 v41, v41\n"
        "v_mfma_f32_16x16x32_bf16 a[36:39], a[88:91], v[52:55], a[36:39]\n"
        "ds_read_b128 a[88:91], v3 offset:28672\n"
        "v_cvt_pk_bf16_f32 v61, v38, v39\n"
        "v_mfma_f32_16x16x32_bf16 v[24:27], v[84:87], v[100:103], v[24:27]\n"
        "v_exp_f32 v42, v42\n"
        "v_mfma_f32_16x16x32_bf16 v[28:31], v[84:87], v[116:119], v[28:31]\n"
        "ds_read_b128 v[84:87], v2 offset:13312\n"
        "v_mfma_f32_16x16x32_bf16 a[40:43], a[92:95], v[48:51], a[40:43]\n"
        "v_exp_f32 v43, v43\n"
        "v_mfma_f32_16x16x32_bf16 a[44:47], a[92:95], v[52:55], a[44:47]\n"
        "ds_read_b128 a[92:95], v3 offset:29696\n"
        "v_cvt_pk_bf16_f32 v58, v40, v41\n"
        "v_mfma_f32_16x16x32_bf16 v[24:27], v[88:91], v[104:107], v[24:27]\n"
        "v_exp_f32 v44, v44\n"
        "v_mfma_f32_16x16x32_bf16 v[28:31], v[88:91], v[120:123], v[28:31]\n"
        "ds_read_b128 v[88:91], v2 offset:14336\n"
        "v_mfma_f32_16x16x32_bf16 a[48:51], a[96:99], v[48:51], a[48:51]\n"
        "v_exp_f32 v45, v45\n"
        "v_mfma_f32_16x16x32_bf16 a[52:55], a[96:99], v[52:55], a[52:55]\n"
        "ds_read_b128 a[96:99], v3 offset:30720\n"
        "v_cvt_pk_bf16_f32 v59, v42, v43\n"
        "v_mfma_f32_16x16x32_bf16 v[24:27], v[92:95], v[108:111], v[24:27]\n"
        "v_exp_f32 v46, v46\n"
        "v_mfma_f32_16x16x32_bf16 v[28:31], v[92:95], v[124:127], v[28:31]\n"
        "ds_read_b128 v[92:95], v2 offset:15360\n"
        "v_mfma_f32_16x16x32_bf16 a[56:59], a[100:103], v[48:51], a[56:59]\n"
        "v_exp_f32 v47, v47\n"
        "v_mfma_f32_16x16x32_bf16 a[60:63], a[100:103], v[52:55], a[60:63]\n"
        "ds_read_b128 a[100:103], v3 offset:31744\n"
        "v_cvt_pk_bf16_f32 v62, v44, v45\n"
        "v_mfma_f32_16x16x32_bf16 a[64:67], a[104:107], v[48:51], a[64:67]\n"
        "v_cvt_pk_bf16_f32 v63, v46, v47\n"
        "v_mfma_f32_16x16x32_bf16 a[68:71], a[104:107], v[52:55], a[68:71]\n"
        "v_xor_b32 v1, 0x8000, v1\n"
        "v_xor_b32 v2, 0x8000, v2\n"
        "v_xor_b32 v3, 0x8000, v3\n"
        "s_sub_u32 s63, s63, 1\n"
        "s_cmp_lg_u32 s63, 0\n"
        "s_cbranch_scc1 P3_LOOP_%=\n"
        "P3_DIAG_MID_%=:\n"
        "s_cmp_eq_u32 s36, 0\n"
        "s_cbranch_scc1 P3_DM0_%=\n"
        "s_cmp_eq_u32 s36, 1\n"
        "s_cbranch_scc1 P3_DM1_%=\n"
        "s_cmp_eq_u32 s36, 2\n"
        "s_cbranch_scc1 P3_DM2_%=\n"
        "s_branch P3_DM3_%=\n"
        "P3_DIAG_FIRST_%=:\n"
        "s_cmp_eq_u32 s36, 0\n"
        "s_cbranch_scc1 P3_DF0_%=\n"
        "s_cmp_eq_u32 s36, 1\n"
        "s_cbranch_scc1 P3_DF1_%=\n"
        "s_cmp_eq_u32 s36, 2\n"
        "s_cbranch_scc1 P3_DF2_%=\n"
        "s_branch P3_DF3_%=\n"
        "P3_DM0_%=:\n"
        "v_mfma_f32_16x16x32_bf16 v[36:39], v[64:67], v[112:115], v[12:15]\n"
        "ds_read_b128 v[64:67], v1 offset:16384\n"
        "v_mfma_f32_16x16x32_bf16 a[0:3], a[72:75], v[56:59], a[0:3]\n"
        "v_mfma_f32_16x16x32_bf16 a[4:7], a[72:75], v[60:63], a[4:7]\n"
        "ds_read_b128 a[72:75], v3 offset:0\n"
        "v_cndmask_b32 v16, v7, v16, s[64:65]\n"
        "s_waitcnt lgkmcnt(4)\n"
        "v_mfma_f32_16x16x32_bf16 v[36:39], v[68:71], v[116:119], v[36:39]\n"
        "ds_read_b128 v[68:71], v1 offset:17408\n"
        "v_cndmask_b32 v17, v7, v17, s[66:67]\n"
        "v_mfma_f32_16x16x32_bf16 a[8:11], a[76:79], v[56:59], a[8:11]\n"
        "v_exp_f32 v16, v16\n"
        "v_mfma_f32_16x16x32_bf16 a[12:15], a[76:79], v[60:63], a[12:15]\n"
        "ds_read_b128 a[76:79], v3 offset:1024\n"
        "v_exp_f32 v17, v17\n"
        "v_mfma_f32_16x16x32_bf16 v[36:39], v[72:75], v[120:123], v[36:39]\n"
        "ds_read_b128 v[72:75], v1 offset:18432\n"
        "v_cndmask_b32 v18, v7, v18, s[68:69]\n"
        "v_mfma_f32_16x16x32_bf16 a[16:19], a[80:83], v[56:59], a[16:19]\n"
        "v_cndmask_b32 v19, v7, v19, s[70:71]\n"
        "v_mfma_f32_16x16x32_bf16 a[20:23], a[80:83], v[60:63], a[20:23]\n"
        "ds_read_b128 a[80:83], v3 offset:2048\n"
        "v_exp_f32 v18, v18\n"
        "v_mfma_f32_16x16x32_bf16 v[36:39], v[76:79], v[124:127], v[36:39]\n"
        "ds_read_b128 v[76:79], v1 offset:19456\n"
        "v_exp_f32 v19, v19\n"
        "v_mfma_f32_16x16x32_bf16 a[24:27], a[84:87], v[56:59], a[24:27]\n"
        "v_cvt_pk_bf16_f32 v48, v16, v17\n"
        "v_mfma_f32_16x16x32_bf16 a[28:31], a[84:87], v[60:63], a[28:31]\n"
        "ds_read_b128 a[84:87], v3 offset:3072\n"
        "v_exp_f32 v20, v20\n"
        "v_mfma_f32_16x16x32_bf16 v[44:47], v[80:83], v[112:115], v[12:15]\n"
        "ds_read_b128 v[80:83], v1 offset:20480\n"
        "v_exp_f32 v21, v21\n"
        "v_mfma_f32_16x16x32_bf16 a[32:35], a[88:91], v[56:59], a[32:35]\n"
        "v_cvt_pk_bf16_f32 v49, v18, v19\n"
        "v_mfma_f32_16x16x32_bf16 a[36:39], a[88:91], v[60:63], a[36:39]\n"
        "ds_read_b128 a[88:91], v3 offset:4096\n"
        "v_exp_f32 v22, v22\n"
        "v_mfma_f32_16x16x32_bf16 v[44:47], v[84:87], v[116:119], v[44:47]\n"
        "ds_read_b128 v[84:87], v1 offset:21504\n"
        "v_exp_f32 v23, v23\n"
        "v_mfma_f32_16x16x32_bf16 a[40:43], a[92:95], v[56:59], a[40:43]\n"
        "v_cvt_pk_bf16_f32 v52, v20, v21\n"
        "v_mfma_f32_16x16x32_bf16 a[44:47], a[92:95], v[60:63], a[44:47]\n"
        "ds_read_b128 a[92:95], v3 offset:5120\n"
        "v_mov_b32 v50, 0\n"
        "v_mfma_f32_16x16x32_bf16 v[44:47], v[88:91], v[120:123], v[44:47]\n"
        "ds_read_b128 v[88:91], v1 offset:22528\n"
        "v_mov_b32 v51, 0\n"
        "v_mfma_f32_16x16x32_bf16 a[48:51], a[96:99], v[56:59], a[48:51]\n"
        "v_exp_f32 v28, v28\n"
        "v_mfma_f32_16x16x32_bf16 a[52:55], a[96:99], v[60:63], a[52:55]\n"
        "ds_read_b128 a[96:99], v3 offset:6144\n"
        "v_exp_f32 v29, v29\n"
        "s_waitcnt lgkmcnt(5)\n"
        "v_mfma_f32_16x16x32_bf16 v[44:47], v[92:95], v[124:127], v[44:47]\n"
        "ds_read_b128 v[92:95], v1 offset:23552\n"
        "v_cvt_pk_bf16_f32 v53, v22, v23\n"
        "v_mfma_f32_16x16x32_bf16 a[56:59], a[100:103], v[56:59], a[56:59]\n"
        "v_exp_f32 v30, v30\n"
        "v_mfma_f32_16x16x32_bf16 a[60:63], a[100:103], v[60:63], a[60:63]\n"
        "ds_read_b128 a[100:103], v3 offset:7168\n"
        "v_exp_f32 v31, v31\n"
        "v_mfma_f32_16x16x32_bf16 a[64:67], a[104:107], v[56:59], a[64:67]\n"
        "v_cvt_pk_bf16_f32 v54, v28, v29\n"
        "v_mfma_f32_16x16x32_bf16 a[68:71], a[104:107], v[60:63], a[68:71]\n"
        "v_cvt_pk_bf16_f32 v55, v30, v31\n"
        "v_mfma_f32_16x16x32_bf16 v[20:23], v[64:67], v[112:115], v[12:15]\n"
        "ds_read_b128 v[64:67], v1 offset:24576\n"
        "v_mfma_f32_16x16x32_bf16 a[0:3], a[72:75], v[48:51], a[0:3]\n"
        "v_mfma_f32_16x16x32_bf16 a[4:7], a[72:75], v[52:55], a[4:7]\n"
        "ds_read_b128 a[72:75], v3 offset:8192\n"
        "v_exp_f32 v36, v36\n"
        "v_mfma_f32_16x16x32_bf16 v[20:23], v[68:71], v[116:119], v[20:23]\n"
        "ds_read_b128 v[68:71], v1 offset:25600\n"
        "v_mfma_f32_16x16x32_bf16 a[8:11], a[76:79], v[48:51], a[8:11]\n"
        "v_exp_f32 v37, v37\n"
        "v_mfma_f32_16x16x32_bf16 a[12:15], a[76:79], v[52:55], a[12:15]\n"
        "ds_read_b128 a[76:79], v3 offset:9216\n"
        "v_mfma_f32_16x16x32_bf16 v[20:23], v[72:75], v[120:123], v[20:23]\n"
        "ds_read_b128 v[72:75], v1 offset:26624\n"
        "v_exp_f32 v38, v38\n"
        "v_mfma_f32_16x16x32_bf16 a[16:19], a[80:83], v[48:51], a[16:19]\n"
        "v_mfma_f32_16x16x32_bf16 a[20:23], a[80:83], v[52:55], a[20:23]\n"
        "ds_read_b128 a[80:83], v3 offset:10240\n"
        "v_exp_f32 v39, v39\n"
        "v_mfma_f32_16x16x32_bf16 v[20:23], v[76:79], v[124:127], v[20:23]\n"
        "ds_read_b128 v[76:79], v1 offset:27648\n"
        "v_mfma_f32_16x16x32_bf16 a[24:27], a[84:87], v[48:51], a[24:27]\n"
        "v_cvt_pk_bf16_f32 v60, v36, v37\n"
        "v_mfma_f32_16x16x32_bf16 a[28:31], a[84:87], v[52:55], a[28:31]\n"
        "ds_read_b128 a[84:87], v3 offset:11264\n"
        "v_mfma_f32_16x16x32_bf16 v[28:31], v[80:83], v[112:115], v[12:15]\n"
        "ds_read_b128 v[80:83], v1 offset:28672\n"
        "v_exp_f32 v44, v44\n"
        "s_waitcnt lgkmcnt(9)\n"
        "s_barrier\n"
        "v_mfma_f32_16x16x32_bf16 a[32:35], a[88:91], v[48:51], a[32:35]\n"
        "v_mfma_f32_16x16x32_bf16 a[36:39], a[88:91], v[52:55], a[36:39]\n"
        "ds_read_b128 a[88:91], v3 offset:12288\n"
        "v_exp_f32 v45, v45\n"
        "v_mfma_f32_16x16x32_bf16 v[28:31], v[84:87], v[116:119], v[28:31]\n"
        "ds_read_b128 v[84:87], v1 offset:29696\n"
        "v_mfma_f32_16x16x32_bf16 a[40:43], a[92:95], v[48:51], a[40:43]\n"
        "v_cvt_pk_bf16_f32 v61, v38, v39\n"
        "v_mfma_f32_16x16x32_bf16 a[44:47], a[92:95], v[52:55], a[44:47]\n"
        "ds_read_b128 a[92:95], v3 offset:13312\n"
        "v_mfma_f32_16x16x32_bf16 v[28:31], v[88:91], v[120:123], v[28:31]\n"
        "ds_read_b128 v[88:91], v1 offset:30720\n"
        "v_exp_f32 v46, v46\n"
        "v_mfma_f32_16x16x32_bf16 a[48:51], a[96:99], v[48:51], a[48:51]\n"
        "v_mfma_f32_16x16x32_bf16 a[52:55], a[96:99], v[52:55], a[52:55]\n"
        "ds_read_b128 a[96:99], v3 offset:14336\n"
        "v_exp_f32 v47, v47\n"
        "v_mfma_f32_16x16x32_bf16 v[28:31], v[92:95], v[124:127], v[28:31]\n"
        "ds_read_b128 v[92:95], v1 offset:31744\n"
        "v_mfma_f32_16x16x32_bf16 a[56:59], a[100:103], v[48:51], a[56:59]\n"
        "v_cvt_pk_bf16_f32 v62, v44, v45\n"
        "v_mfma_f32_16x16x32_bf16 a[60:63], a[100:103], v[52:55], a[60:63]\n"
        "ds_read_b128 a[100:103], v3 offset:15360\n"
        "v_mfma_f32_16x16x32_bf16 a[64:67], a[104:107], v[48:51], a[64:67]\n"
        "v_cvt_pk_bf16_f32 v63, v46, v47\n"
        "v_mfma_f32_16x16x32_bf16 a[68:71], a[104:107], v[52:55], a[68:71]\n"
        "s_waitcnt lgkmcnt(5)\n"
        "v_mfma_f32_16x16x32_bf16 v[36:39], v[64:67], v[112:115], v[12:15]\n"
        "ds_read_b128 v[64:67], v2 offset:0\n"
        "v_mfma_f32_16x16x32_bf16 a[4:7], a[72:75], v[60:63], a[4:7]\n"
        "ds_read_b128 a[72:75], v3 offset:16384\n"
        "v_mfma_f32_16x16x32_bf16 v[36:39], v[68:71], v[116:119], v[36:39]\n"
        "ds_read_b128 v[68:71], v2 offset:1024\n"
        "v_exp_f32 v20, v20\n"
        "v_mfma_f32_16x16x32_bf16 a[12:15], a[76:79], v[60:63], a[12:15]\n"
        "ds_read_b128 a[76:79], v3 offset:17408\n"
        "v_exp_f32 v21, v21\n"
        "v_mfma_f32_16x16x32_bf16 v[36:39], v[72:75], v[120:123], v[36:39]\n"
        "ds_read_b128 v[72:75], v2 offset:2048\n"
        "v_exp_f32 v22, v22\n"
        "v_mfma_f32_16x16x32_bf16 a[20:23], a[80:83], v[60:63], a[20:23]\n"
        "ds_read_b128 a[80:83], v3 offset:18432\n"
        "v_exp_f32 v23, v23\n"
        "v_mfma_f32_16x16x32_bf16 v[36:39], v[76:79], v[124:127], v[36:39]\n"
        "ds_read_b128 v[76:79], v2 offset:3072\n"
        "v_mfma_f32_16x16x32_bf16 a[28:31], a[84:87], v[60:63], a[28:31]\n"
        "ds_read_b128 a[84:87], v3 offset:19456\n"
        "v_cvt_pk_bf16_f32 v52, v20, v21\n"
        "v_mfma_f32_16x16x32_bf16 v[44:47], v[80:83], v[112:115], v[12:15]\n"
        "ds_read_b128 v[80:83], v2 offset:4096\n"
        "v_exp_f32 v28, v28\n"
        "v_mfma_f32_16x16x32_bf16 a[36:39], a[88:91], v[60:63], a[36:39]\n"
        "ds_read_b128 a[88:91], v3 offset:20480\n"
        "v_exp_f32 v29, v29\n"
        "v_mfma_f32_16x16x32_bf16 v[44:47], v[84:87], v[116:119], v[44:47]\n"
        "ds_read_b128 v[84:87], v2 offset:5120\n"
        "v_cvt_pk_bf16_f32 v53, v22, v23\n"
        "s_waitcnt lgkmcnt(7)\n"
        "v_mfma_f32_16x16x32_bf16 a[44:47], a[92:95], v[60:63], a[44:47]\n"
        "ds_read_b128 a[92:95], v3 offset:21504\n"
        "v_mfma_f32_16x16x32_bf16 v[44:47], v[88:91], v[120:123], v[44:47]\n"
        "ds_read_b128 v[88:91], v2 offset:6144\n"
        "v_exp_f32 v30, v30\n"
        "v_mfma_f32_16x16x32_bf16 a[52:55], a[96:99], v[60:63], a[52:55]\n"
        "ds_read_b128 a[96:99], v3 offset:22528\n"
        "v_exp_f32 v31, v31\n"
        "v_mfma_f32_16x16x32_bf16 v[44:47], v[92:95], v[124:127], v[44:47]\n"
        "ds_read_b128 v[92:95], v2 offset:7168\n"
        "v_cvt_pk_bf16_f32 v54, v28, v29\n"
        "v_mfma_f32_16x16x32_bf16 a[60:63], a[100:103], v[60:63], a[60:63]\n"
        "ds_read_b128 a[100:103], v3 offset:23552\n"
        "v_cvt_pk_bf16_f32 v55, v30, v31\n"
        "v_mfma_f32_16x16x32_bf16 a[68:71], a[104:107], v[60:63], a[68:71]\n"
        "s_lshl_b32 s52, s36, 13\n"
        "s_add_u32 s52, s52, 131072\n"
        "v_add_u32 v6, s52, v0\n"
        "ds_read_b128 v[96:99], v6 offset:0\n"
        "ds_read_b128 v[100:103], v6 offset:1024\n"
        "ds_read_b128 v[104:107], v6 offset:2048\n"
        "ds_read_b128 v[108:111], v6 offset:3072\n"
        "ds_read_b128 v[112:115], v6 offset:4096\n"
        "ds_read_b128 v[116:119], v6 offset:5120\n"
        "ds_read_b128 v[120:123], v6 offset:6144\n"
        "ds_read_b128 v[124:127], v6 offset:7168\n"
        "v_mfma_f32_16x16x32_bf16 a[4:7], a[72:75], v[52:55], a[4:7]\n"
        "ds_read_b128 a[72:75], v3 offset:24576\n"
        "v_mfma_f32_16x16x32_bf16 a[12:15], a[76:79], v[52:55], a[12:15]\n"
        "ds_read_b128 a[76:79], v3 offset:25600\n"
        "s_waitcnt lgkmcnt(11)\n"
        "v_mfma_f32_16x16x32_bf16 a[20:23], a[80:83], v[52:55], a[20:23]\n"
        "ds_read_b128 a[80:83], v3 offset:26624\n"
        "v_exp_f32 v36, v36\n"
        "v_exp_f32 v37, v37\n"
        "v_exp_f32 v38, v38\n"
        "v_mfma_f32_16x16x32_bf16 a[28:31], a[84:87], v[52:55], a[28:31]\n"
        "ds_read_b128 a[84:87], v3 offset:27648\n"
        "v_exp_f32 v39, v39\n"
        "v_cvt_pk_bf16_f32 v60, v36, v37\n"
        "s_waitcnt lgkmcnt(4)\n"
        "s_barrier\n"
        "v_mfma_f32_16x16x32_bf16 a[36:39], a[88:91], v[52:55], a[36:39]\n"
        "ds_read_b128 a[88:91], v3 offset:28672\n"
        "v_cndmask_b32 v44, v7, v44, s[64:65]\n"
        "v_cndmask_b32 v45, v7, v45, s[66:67]\n"
        "v_mfma_f32_16x16x32_bf16 a[44:47], a[92:95], v[52:55], a[44:47]\n"
        "ds_read_b128 a[92:95], v3 offset:29696\n"
        "v_exp_f32 v44, v44\n"
        "v_exp_f32 v45, v45\n"
        "v_cvt_pk_bf16_f32 v61, v38, v39\n"
        "v_mfma_f32_16x16x32_bf16 a[52:55], a[96:99], v[52:55], a[52:55]\n"
        "ds_read_b128 a[96:99], v3 offset:30720\n"
        "v_cndmask_b32 v46, v7, v46, s[68:69]\n"
        "v_cndmask_b32 v47, v7, v47, s[70:71]\n"
        "v_mfma_f32_16x16x32_bf16 a[60:63], a[100:103], v[52:55], a[60:63]\n"
        "ds_read_b128 a[100:103], v3 offset:31744\n"
        "v_exp_f32 v46, v46\n"
        "v_exp_f32 v47, v47\n"
        "v_mfma_f32_16x16x32_bf16 a[68:71], a[104:107], v[52:55], a[68:71]\n"
        "v_cvt_pk_bf16_f32 v62, v44, v45\n"
        "v_cvt_pk_bf16_f32 v63, v46, v47\n"
        "v_xor_b32 v1, 0x8000, v1\n"
        "v_xor_b32 v2, 0x8000, v2\n"
        "v_xor_b32 v3, 0x8000, v3\n"
        "s_waitcnt lgkmcnt(3)\n"
        "v_mfma_f32_16x16x32_bf16 a[4:7], a[72:75], v[60:63], a[4:7]\n"
        "v_mfma_f32_16x16x32_bf16 a[12:15], a[76:79], v[60:63], a[12:15]\n"
        "v_mfma_f32_16x16x32_bf16 a[20:23], a[80:83], v[60:63], a[20:23]\n"
        "v_mfma_f32_16x16x32_bf16 a[28:31], a[84:87], v[60:63], a[28:31]\n"
        "v_mfma_f32_16x16x32_bf16 a[36:39], a[88:91], v[60:63], a[36:39]\n"
        "s_waitcnt lgkmcnt(2)\n"
        "v_mfma_f32_16x16x32_bf16 a[44:47], a[92:95], v[60:63], a[44:47]\n"
        "s_waitcnt lgkmcnt(1)\n"
        "v_mfma_f32_16x16x32_bf16 a[52:55], a[96:99], v[60:63], a[52:55]\n"
        "s_waitcnt lgkmcnt(0)\n"
        "v_mfma_f32_16x16x32_bf16 a[60:63], a[100:103], v[60:63], a[60:63]\n"
        "v_mfma_f32_16x16x32_bf16 a[68:71], a[104:107], v[60:63], a[68:71]\n"
        "s_branch P3_EPI_%=\n"
        "P3_DM1_%=:\n"
        "v_mfma_f32_16x16x32_bf16 v[36:39], v[64:67], v[112:115], v[12:15]\n"
        "ds_read_b128 v[64:67], v1 offset:16384\n"
        "v_mfma_f32_16x16x32_bf16 a[0:3], a[72:75], v[56:59], a[0:3]\n"
        "v_mfma_f32_16x16x32_bf16 a[4:7], a[72:75], v[60:63], a[4:7]\n"
        "ds_read_b128 a[72:75], v3 offset:0\n"
        "v_exp_f32 v16, v16\n"
        "v_exp_f32 v17, v17\n"
        "s_waitcnt lgkmcnt(4)\n"
        "v_mfma_f32_16x16x32_bf16 v[36:39], v[68:71], v[116:119], v[36:39]\n"
        "ds_read_b128 v[68:71], v1 offset:17408\n"
        "v_exp_f32 v18, v18\n"
        "v_mfma_f32_16x16x32_bf16 a[8:11], a[76:79], v[56:59], a[8:11]\n"
        "v_exp_f32 v19, v19\n"
        "v_mfma_f32_16x16x32_bf16 a[12:15], a[76:79], v[60:63], a[12:15]\n"
        "ds_read_b128 a[76:79], v3 offset:1024\n"
        "v_cvt_pk_bf16_f32 v48, v16, v17\n"
        "v_mfma_f32_16x16x32_bf16 v[36:39], v[72:75], v[120:123], v[36:39]\n"
        "ds_read_b128 v[72:75], v1 offset:18432\n"
        "v_exp_f32 v20, v20\n"
        "v_mfma_f32_16x16x32_bf16 a[16:19], a[80:83], v[56:59], a[16:19]\n"
        "v_exp_f32 v21, v21\n"
        "v_mfma_f32_16x16x32_bf16 a[20:23], a[80:83], v[60:63], a[20:23]\n"
        "ds_read_b128 a[80:83], v3 offset:2048\n"
        "v_cvt_pk_bf16_f32 v49, v18, v19\n"
        "v_exp_f32 v22, v22\n"
        "v_mfma_f32_16x16x32_bf16 v[36:39], v[76:79], v[124:127], v[36:39]\n"
        "ds_read_b128 v[76:79], v1 offset:19456\n"
        "v_exp_f32 v23, v23\n"
        "v_mfma_f32_16x16x32_bf16 a[24:27], a[84:87], v[56:59], a[24:27]\n"
        "v_cvt_pk_bf16_f32 v52, v20, v21\n"
        "v_mfma_f32_16x16x32_bf16 a[28:31], a[84:87], v[60:63], a[28:31]\n"
        "ds_read_b128 a[84:87], v3 offset:3072\n"
        "v_cndmask_b32 v24, v7, v24, s[64:65]\n"
        "v_mfma_f32_16x16x32_bf16 v[44:47], v[80:83], v[112:115], v[12:15]\n"
        "ds_read_b128 v[80:83], v1 offset:20480\n"
        "v_cndmask_b32 v25, v7, v25, s[66:67]\n"
        "v_mfma_f32_16x16x32_bf16 a[32:35], a[88:91], v[56:59], a[32:35]\n"
        "v_exp_f32 v24, v24\n"
        "v_mfma_f32_16x16x32_bf16 a[36:39], a[88:91], v[60:63], a[36:39]\n"
        "ds_read_b128 a[88:91], v3 offset:4096\n"
        "v_exp_f32 v25, v25\n"
        "v_cvt_pk_bf16_f32 v53, v22, v23\n"
        "v_mfma_f32_16x16x32_bf16 v[44:47], v[84:87], v[116:119], v[44:47]\n"
        "ds_read_b128 v[84:87], v1 offset:21504\n"
        "v_cndmask_b32 v26, v7, v26, s[68:69]\n"
        "v_mfma_f32_16x16x32_bf16 a[40:43], a[92:95], v[56:59], a[40:43]\n"
        "v_cndmask_b32 v27, v7, v27, s[70:71]\n"
        "v_mfma_f32_16x16x32_bf16 a[44:47], a[92:95], v[60:63], a[44:47]\n"
        "ds_read_b128 a[92:95], v3 offset:5120\n"
        "v_exp_f32 v26, v26\n"
        "v_mfma_f32_16x16x32_bf16 v[44:47], v[88:91], v[120:123], v[44:47]\n"
        "ds_read_b128 v[88:91], v1 offset:22528\n"
        "v_exp_f32 v27, v27\n"
        "v_mfma_f32_16x16x32_bf16 a[48:51], a[96:99], v[56:59], a[48:51]\n"
        "v_cvt_pk_bf16_f32 v50, v24, v25\n"
        "v_mfma_f32_16x16x32_bf16 a[52:55], a[96:99], v[60:63], a[52:55]\n"
        "ds_read_b128 a[96:99], v3 offset:6144\n"
        "v_exp_f32 v28, v28\n"
        "v_exp_f32 v29, v29\n"
        "s_waitcnt lgkmcnt(5)\n"
        "v_mfma_f32_16x16x32_bf16 v[44:47], v[92:95], v[124:127], v[44:47]\n"
        "ds_read_b128 v[92:95], v1 offset:23552\n"
        "v_cvt_pk_bf16_f32 v51, v26, v27\n"
        "v_mfma_f32_16x16x32_bf16 a[56:59], a[100:103], v[56:59], a[56:59]\n"
        "v_exp_f32 v30, v30\n"
        "v_mfma_f32_16x16x32_bf16 a[60:63], a[100:103], v[60:63], a[60:63]\n"
        "ds_read_b128 a[100:103], v3 offset:7168\n"
        "v_exp_f32 v31, v31\n"
        "v_mfma_f32_16x16x32_bf16 a[64:67], a[104:107], v[56:59], a[64:67]\n"
        "v_cvt_pk_bf16_f32 v54, v28, v29\n"
        "v_mfma_f32_16x16x32_bf16 a[68:71], a[104:107], v[60:63], a[68:71]\n"
        "v_cvt_pk_bf16_f32 v55, v30, v31\n"
        "v_mfma_f32_16x16x32_bf16 v[20:23], v[64:67], v[112:115], v[12:15]\n"
        "ds_read_b128 v[64:67], v1 offset:24576\n"
        "v_mfma_f32_16x16x32_bf16 a[0:3], a[72:75], v[48:51], a[0:3]\n"
        "v_mfma_f32_16x16x32_bf16 a[4:7], a[72:75], v[52:55], a[4:7]\n"
        "ds_read_b128 a[72:75], v3 offset:8192\n"
        "v_exp_f32 v36, v36\n"
        "v_mfma_f32_16x16x32_bf16 v[20:23], v[68:71], v[116:119], v[20:23]\n"
        "ds_read_b128 v[68:71], v1 offset:25600\n"
        "v_mfma_f32_16x16x32_bf16 a[8:11], a[76:79], v[48:51], a[8:11]\n"
        "v_exp_f32 v37, v37\n"
        "v_mfma_f32_16x16x32_bf16 a[12:15], a[76:79], v[52:55], a[12:15]\n"
        "ds_read_b128 a[76:79], v3 offset:9216\n"
        "v_mfma_f32_16x16x32_bf16 v[20:23], v[72:75], v[120:123], v[20:23]\n"
        "ds_read_b128 v[72:75], v1 offset:26624\n"
        "v_exp_f32 v38, v38\n"
        "v_mfma_f32_16x16x32_bf16 a[16:19], a[80:83], v[48:51], a[16:19]\n"
        "v_mfma_f32_16x16x32_bf16 a[20:23], a[80:83], v[52:55], a[20:23]\n"
        "ds_read_b128 a[80:83], v3 offset:10240\n"
        "v_exp_f32 v39, v39\n"
        "v_mfma_f32_16x16x32_bf16 v[20:23], v[76:79], v[124:127], v[20:23]\n"
        "ds_read_b128 v[76:79], v1 offset:27648\n"
        "v_mfma_f32_16x16x32_bf16 a[24:27], a[84:87], v[48:51], a[24:27]\n"
        "v_cvt_pk_bf16_f32 v60, v36, v37\n"
        "v_mfma_f32_16x16x32_bf16 a[28:31], a[84:87], v[52:55], a[28:31]\n"
        "ds_read_b128 a[84:87], v3 offset:11264\n"
        "v_mfma_f32_16x16x32_bf16 v[28:31], v[80:83], v[112:115], v[12:15]\n"
        "v_exp_f32 v44, v44\n"
        "s_waitcnt lgkmcnt(8)\n"
        "s_barrier\n"
        "v_mfma_f32_16x16x32_bf16 a[32:35], a[88:91], v[48:51], a[32:35]\n"
        "v_mfma_f32_16x16x32_bf16 a[36:39], a[88:91], v[52:55], a[36:39]\n"
        "ds_read_b128 a[88:91], v3 offset:12288\n"
        "v_exp_f32 v45, v45\n"
        "v_mfma_f32_16x16x32_bf16 v[28:31], v[84:87], v[116:119], v[28:31]\n"
        "v_mfma_f32_16x16x32_bf16 a[40:43], a[92:95], v[48:51], a[40:43]\n"
        "v_cvt_pk_bf16_f32 v61, v38, v39\n"
        "v_mfma_f32_16x16x32_bf16 a[44:47], a[92:95], v[52:55], a[44:47]\n"
        "ds_read_b128 a[92:95], v3 offset:13312\n"
        "v_mfma_f32_16x16x32_bf16 v[28:31], v[88:91], v[120:123], v[28:31]\n"
        "v_exp_f32 v46, v46\n"
        "v_mfma_f32_16x16x32_bf16 a[48:51], a[96:99], v[48:51], a[48:51]\n"
        "v_mfma_f32_16x16x32_bf16 a[52:55], a[96:99], v[52:55], a[52:55]\n"
        "ds_read_b128 a[96:99], v3 offset:14336\n"
        "v_exp_f32 v47, v47\n"
        "v_mfma_f32_16x16x32_bf16 v[28:31], v[92:95], v[124:127], v[28:31]\n"
        "v_mfma_f32_16x16x32_bf16 a[56:59], a[100:103], v[48:51], a[56:59]\n"
        "v_cvt_pk_bf16_f32 v62, v44, v45\n"
        "v_mfma_f32_16x16x32_bf16 a[60:63], a[100:103], v[52:55], a[60:63]\n"
        "ds_read_b128 a[100:103], v3 offset:15360\n"
        "v_mfma_f32_16x16x32_bf16 a[64:67], a[104:107], v[48:51], a[64:67]\n"
        "v_cvt_pk_bf16_f32 v63, v46, v47\n"
        "v_mfma_f32_16x16x32_bf16 a[68:71], a[104:107], v[52:55], a[68:71]\n"
        "s_waitcnt lgkmcnt(3)\n"
        "v_mfma_f32_16x16x32_bf16 v[36:39], v[64:67], v[112:115], v[12:15]\n"
        "ds_read_b128 v[64:67], v2 offset:0\n"
        "v_mfma_f32_16x16x32_bf16 a[4:7], a[72:75], v[60:63], a[4:7]\n"
        "ds_read_b128 a[72:75], v3 offset:16384\n"
        "v_mfma_f32_16x16x32_bf16 v[36:39], v[68:71], v[116:119], v[36:39]\n"
        "ds_read_b128 v[68:71], v2 offset:1024\n"
        "v_exp_f32 v20, v20\n"
        "v_mfma_f32_16x16x32_bf16 a[12:15], a[76:79], v[60:63], a[12:15]\n"
        "ds_read_b128 a[76:79], v3 offset:17408\n"
        "v_exp_f32 v21, v21\n"
        "v_mfma_f32_16x16x32_bf16 v[36:39], v[72:75], v[120:123], v[36:39]\n"
        "ds_read_b128 v[72:75], v2 offset:2048\n"
        "v_exp_f32 v22, v22\n"
        "v_mfma_f32_16x16x32_bf16 a[20:23], a[80:83], v[60:63], a[20:23]\n"
        "ds_read_b128 a[80:83], v3 offset:18432\n"
        "v_exp_f32 v23, v23\n"
        "v_mfma_f32_16x16x32_bf16 v[36:39], v[76:79], v[124:127], v[36:39]\n"
        "ds_read_b128 v[76:79], v2 offset:3072\n"
        "v_mfma_f32_16x16x32_bf16 a[28:31], a[84:87], v[60:63], a[28:31]\n"
        "ds_read_b128 a[84:87], v3 offset:19456\n"
        "v_cvt_pk_bf16_f32 v52, v20, v21\n"
        "ds_read_b128 v[80:83], v2 offset:4096\n"
        "v_exp_f32 v28, v28\n"
        "v_mfma_f32_16x16x32_bf16 a[36:39], a[88:91], v[60:63], a[36:39]\n"
        "ds_read_b128 a[88:91], v3 offset:20480\n"
        "v_exp_f32 v29, v29\n"
        "ds_read_b128 v[84:87], v2 offset:5120\n"
        "v_cvt_pk_bf16_f32 v53, v22, v23\n"
        "s_waitcnt lgkmcnt(8)\n"
        "v_mfma_f32_16x16x32_bf16 a[44:47], a[92:95], v[60:63], a[44:47]\n"
        "ds_read_b128 a[92:95], v3 offset:21504\n"
        "ds_read_b128 v[88:91], v2 offset:6144\n"
        "v_exp_f32 v30, v30\n"
        "v_mfma_f32_16x16x32_bf16 a[52:55], a[96:99], v[60:63], a[52:55]\n"
        "ds_read_b128 a[96:99], v3 offset:22528\n"
        "v_exp_f32 v31, v31\n"
        "ds_read_b128 v[92:95], v2 offset:7168\n"
        "v_cvt_pk_bf16_f32 v54, v28, v29\n"
        "v_mfma_f32_16x16x32_bf16 a[60:63], a[100:103], v[60:63], a[60:63]\n"
        "ds_read_b128 a[100:103], v3 offset:23552\n"
        "v_cvt_pk_bf16_f32 v55, v30, v31\n"
        "v_mfma_f32_16x16x32_bf16 a[68:71], a[104:107], v[60:63], a[68:71]\n"
        "s_lshl_b32 s52, s36, 13\n"
        "s_add_u32 s52, s52, 131072\n"
        "v_add_u32 v6, s52, v0\n"
        "ds_read_b128 v[96:99], v6 offset:0\n"
        "ds_read_b128 v[100:103], v6 offset:1024\n"
        "ds_read_b128 v[104:107], v6 offset:2048\n"
        "ds_read_b128 v[108:111], v6 offset:3072\n"
        "ds_read_b128 v[112:115], v6 offset:4096\n"
        "ds_read_b128 v[116:119], v6 offset:5120\n"
        "ds_read_b128 v[120:123], v6 offset:6144\n"
        "ds_read_b128 v[124:127], v6 offset:7168\n"
        "v_mfma_f32_16x16x32_bf16 a[4:7], a[72:75], v[52:55], a[4:7]\n"
        "ds_read_b128 a[72:75], v3 offset:24576\n"
        "s_waitcnt lgkmcnt(11)\n"
        "v_mfma_f32_16x16x32_bf16 a[12:15], a[76:79], v[52:55], a[12:15]\n"
        "ds_read_b128 a[76:79], v3 offset:25600\n"
        "v_mfma_f32_16x16x32_bf16 a[20:23], a[80:83], v[52:55], a[20:23]\n"
        "ds_read_b128 a[80:83], v3 offset:26624\n"
        "v_cndmask_b32 v36, v7, v36, s[64:65]\n"
        "v_cndmask_b32 v37, v7, v37, s[66:67]\n"
        "v_mfma_f32_16x16x32_bf16 a[28:31], a[84:87], v[52:55], a[28:31]\n"
        "ds_read_b128 a[84:87], v3 offset:27648\n"
        "v_exp_f32 v36, v36\n"
        "v_exp_f32 v37, v37\n"
        "s_waitcnt lgkmcnt(4)\n"
        "s_barrier\n"
        "v_mfma_f32_16x16x32_bf16 a[36:39], a[88:91], v[52:55], a[36:39]\n"
        "ds_read_b128 a[88:91], v3 offset:28672\n"
        "v_cndmask_b32 v38, v7, v38, s[68:69]\n"
        "v_cndmask_b32 v39, v7, v39, s[70:71]\n"
        "v_mfma_f32_16x16x32_bf16 a[44:47], a[92:95], v[52:55], a[44:47]\n"
        "ds_read_b128 a[92:95], v3 offset:29696\n"
        "v_exp_f32 v38, v38\n"
        "v_mfma_f32_16x16x32_bf16 a[52:55], a[96:99], v[52:55], a[52:55]\n"
        "ds_read_b128 a[96:99], v3 offset:30720\n"
        "v_exp_f32 v39, v39\n"
        "v_cvt_pk_bf16_f32 v60, v36, v37\n"
        "v_mfma_f32_16x16x32_bf16 a[60:63], a[100:103], v[52:55], a[60:63]\n"
        "ds_read_b128 a[100:103], v3 offset:31744\n"
        "v_mov_b32 v62, 0\n"
        "v_mov_b32 v63, 0\n"
        "v_mfma_f32_16x16x32_bf16 a[68:71], a[104:107], v[52:55], a[68:71]\n"
        "v_cvt_pk_bf16_f32 v61, v38, v39\n"
        "v_xor_b32 v1, 0x8000, v1\n"
        "v_xor_b32 v2, 0x8000, v2\n"
        "v_xor_b32 v3, 0x8000, v3\n"
        "s_waitcnt lgkmcnt(4)\n"
        "v_mfma_f32_16x16x32_bf16 a[4:7], a[72:75], v[60:63], a[4:7]\n"
        "v_mfma_f32_16x16x32_bf16 a[12:15], a[76:79], v[60:63], a[12:15]\n"
        "v_mfma_f32_16x16x32_bf16 a[20:23], a[80:83], v[60:63], a[20:23]\n"
        "v_mfma_f32_16x16x32_bf16 a[28:31], a[84:87], v[60:63], a[28:31]\n"
        "s_waitcnt lgkmcnt(3)\n"
        "v_mfma_f32_16x16x32_bf16 a[36:39], a[88:91], v[60:63], a[36:39]\n"
        "s_waitcnt lgkmcnt(2)\n"
        "v_mfma_f32_16x16x32_bf16 a[44:47], a[92:95], v[60:63], a[44:47]\n"
        "s_waitcnt lgkmcnt(1)\n"
        "v_mfma_f32_16x16x32_bf16 a[52:55], a[96:99], v[60:63], a[52:55]\n"
        "s_waitcnt lgkmcnt(0)\n"
        "v_mfma_f32_16x16x32_bf16 a[60:63], a[100:103], v[60:63], a[60:63]\n"
        "v_mfma_f32_16x16x32_bf16 a[68:71], a[104:107], v[60:63], a[68:71]\n"
        "s_branch P3_EPI_%=\n"
        "P3_DM2_%=:\n"
        "v_mfma_f32_16x16x32_bf16 v[32:35], v[64:67], v[96:99], v[8:11]\n"
        "v_mfma_f32_16x16x32_bf16 v[36:39], v[64:67], v[112:115], v[12:15]\n"
        "ds_read_b128 v[64:67], v1 offset:16384\n"
        "v_mfma_f32_16x16x32_bf16 a[0:3], a[72:75], v[56:59], a[0:3]\n"
        "v_exp_f32 v16, v16\n"
        "v_mfma_f32_16x16x32_bf16 a[4:7], a[72:75], v[60:63], a[4:7]\n"
        "ds_read_b128 a[72:75], v3 offset:0\n"
        "v_exp_f32 v17, v17\n"
        "s_waitcnt lgkmcnt(4)\n"
        "v_mfma_f32_16x16x32_bf16 v[32:35], v[68:71], v[100:103], v[32:35]\n"
        "v_exp_f32 v18, v18\n"
        "v_mfma_f32_16x16x32_bf16 v[36:39], v[68:71], v[116:119], v[36:39]\n"
        "ds_read_b128 v[68:71], v1 offset:17408\n"
        "v_exp_f32 v19, v19\n"
        "v_mfma_f32_16x16x32_bf16 a[8:11], a[76:79], v[56:59], a[8:11]\n"
        "v_cvt_pk_bf16_f32 v48, v16, v17\n"
        "v_mfma_f32_16x16x32_bf16 a[12:15], a[76:79], v[60:63], a[12:15]\n"
        "ds_read_b128 a[76:79], v3 offset:1024\n"
        "v_exp_f32 v20, v20\n"
        "v_mfma_f32_16x16x32_bf16 v[32:35], v[72:75], v[104:107], v[32:35]\n"
        "v_mfma_f32_16x16x32_bf16 v[36:39], v[72:75], v[120:123], v[36:39]\n"
        "ds_read_b128 v[72:75], v1 offset:18432\n"
        "v_exp_f32 v21, v21\n"
        "v_mfma_f32_16x16x32_bf16 a[16:19], a[80:83], v[56:59], a[16:19]\n"
        "v_cvt_pk_bf16_f32 v49, v18, v19\n"
        "v_mfma_f32_16x16x32_bf16 a[20:23], a[80:83], v[60:63], a[20:23]\n"
        "ds_read_b128 a[80:83], v3 offset:2048\n"
        "v_exp_f32 v22, v22\n"
        "v_mfma_f32_16x16x32_bf16 v[32:35], v[76:79], v[108:111], v[32:35]\n"
        "v_exp_f32 v23, v23\n"
        "v_mfma_f32_16x16x32_bf16 v[36:39], v[76:79], v[124:127], v[36:39]\n"
        "ds_read_b128 v[76:79], v1 offset:19456\n"
        "v_cvt_pk_bf16_f32 v52, v20, v21\n"
        "v_mfma_f32_16x16x32_bf16 a[24:27], a[84:87], v[56:59], a[24:27]\n"
        "v_exp_f32 v24, v24\n"
        "v_mfma_f32_16x16x32_bf16 a[28:31], a[84:87], v[60:63], a[28:31]\n"
        "ds_read_b128 a[84:87], v3 offset:3072\n"
        "v_mfma_f32_16x16x32_bf16 v[44:47], v[80:83], v[112:115], v[12:15]\n"
        "ds_read_b128 v[80:83], v1 offset:20480\n"
        "v_exp_f32 v25, v25\n"
        "v_mfma_f32_16x16x32_bf16 a[32:35], a[88:91], v[56:59], a[32:35]\n"
        "v_cvt_pk_bf16_f32 v53, v22, v23\n"
        "v_mfma_f32_16x16x32_bf16 a[36:39], a[88:91], v[60:63], a[36:39]\n"
        "ds_read_b128 a[88:91], v3 offset:4096\n"
        "v_exp_f32 v26, v26\n"
        "v_mfma_f32_16x16x32_bf16 v[44:47], v[84:87], v[116:119], v[44:47]\n"
        "ds_read_b128 v[84:87], v1 offset:21504\n"
        "v_exp_f32 v27, v27\n"
        "v_mfma_f32_16x16x32_bf16 a[40:43], a[92:95], v[56:59], a[40:43]\n"
        "v_cvt_pk_bf16_f32 v50, v24, v25\n"
        "v_mfma_f32_16x16x32_bf16 a[44:47], a[92:95], v[60:63], a[44:47]\n"
        "ds_read_b128 a[92:95], v3 offset:5120\n"
        "v_exp_f32 v28, v28\n"
        "v_mfma_f32_16x16x32_bf16 v[44:47], v[88:91], v[120:123], v[44:47]\n"
        "ds_read_b128 v[88:91], v1 offset:22528\n"
        "v_mfma_f32_16x16x32_bf16 a[48:51], a[96:99], v[56:59], a[48:51]\n"
        "v_exp_f32 v29, v29\n"
        "v_mfma_f32_16x16x32_bf16 a[52:55], a[96:99], v[60:63], a[52:55]\n"
        "ds_read_b128 a[96:99], v3 offset:6144\n"
        "v_cvt_pk_bf16_f32 v51, v26, v27\n"
        "s_waitcnt lgkmcnt(5)\n"
        "v_mfma_f32_16x16x32_bf16 v[44:47], v[92:95], v[124:127], v[44:47]\n"
        "ds_read_b128 v[92:95], v1 offset:23552\n"
        "v_exp_f32 v30, v30\n"
        "v_mfma_f32_16x16x32_bf16 a[56:59], a[100:103], v[56:59], a[56:59]\n"
        "v_exp_f32 v31, v31\n"
        "v_mfma_f32_16x16x32_bf16 a[60:63], a[100:103], v[60:63], a[60:63]\n"
        "ds_read_b128 a[100:103], v3 offset:7168\n"
        "v_cvt_pk_bf16_f32 v54, v28, v29\n"
        "v_mfma_f32_16x16x32_bf16 a[64:67], a[104:107], v[56:59], a[64:67]\n"
        "v_cvt_pk_bf16_f32 v55, v30, v31\n"
        "v_mfma_f32_16x16x32_bf16 a[68:71], a[104:107], v[60:63], a[68:71]\n"
        "v_mfma_f32_16x16x32_bf16 v[20:23], v[64:67], v[112:115], v[12:15]\n"
        "v_mfma_f32_16x16x32_bf16 a[0:3], a[72:75], v[48:51], a[0:3]\n"
        "v_mfma_f32_16x16x32_bf16 a[4:7], a[72:75], v[52:55], a[4:7]\n"
        "ds_read_b128 a[72:75], v3 offset:8192\n"
        "v_cndmask_b32 v32, v7, v32, s[64:65]\n"
        "v_mfma_f32_16x16x32_bf16 v[20:23], v[68:71], v[116:119], v[20:23]\n"
        "v_cndmask_b32 v33, v7, v33, s[66:67]\n"
        "v_mfma_f32_16x16x32_bf16 a[8:11], a[76:79], v[48:51], a[8:11]\n"
        "v_exp_f32 v32, v32\n"
        "v_mfma_f32_16x16x32_bf16 a[12:15], a[76:79], v[52:55], a[12:15]\n"
        "ds_read_b128 a[76:79], v3 offset:9216\n"
        "v_exp_f32 v33, v33\n"
        "v_mfma_f32_16x16x32_bf16 v[20:23], v[72:75], v[120:123], v[20:23]\n"
        "v_cndmask_b32 v34, v7, v34, s[68:69]\n"
        "v_mfma_f32_16x16x32_bf16 a[16:19], a[80:83], v[48:51], a[16:19]\n"
        "v_cndmask_b32 v35, v7, v35, s[70:71]\n"
        "v_mfma_f32_16x16x32_bf16 a[20:23], a[80:83], v[52:55], a[20:23]\n"
        "ds_read_b128 a[80:83], v3 offset:10240\n"
        "v_exp_f32 v34, v34\n"
        "v_mfma_f32_16x16x32_bf16 v[20:23], v[76:79], v[124:127], v[20:23]\n"
        "v_exp_f32 v35, v35\n"
        "v_mfma_f32_16x16x32_bf16 a[24:27], a[84:87], v[48:51], a[24:27]\n"
        "v_cvt_pk_bf16_f32 v56, v32, v33\n"
        "v_mfma_f32_16x16x32_bf16 a[28:31], a[84:87], v[52:55], a[28:31]\n"
        "ds_read_b128 a[84:87], v3 offset:11264\n"
        "v_exp_f32 v36, v36\n"
        "v_mfma_f32_16x16x32_bf16 v[28:31], v[80:83], v[112:115], v[12:15]\n"
        "v_exp_f32 v37, v37\n"
        "s_waitcnt lgkmcnt(4)\n"
        "s_barrier\n"
        "v_mfma_f32_16x16x32_bf16 a[32:35], a[88:91], v[48:51], a[32:35]\n"
        "v_cvt_pk_bf16_f32 v57, v34, v35\n"
        "v_mfma_f32_16x16x32_bf16 a[36:39], a[88:91], v[52:55], a[36:39]\n"
        "ds_read_b128 a[88:91], v3 offset:12288\n"
        "v_exp_f32 v38, v38\n"
        "v_mfma_f32_16x16x32_bf16 v[28:31], v[84:87], v[116:119], v[28:31]\n"
        "v_exp_f32 v39, v39\n"
        "v_mfma_f32_16x16x32_bf16 a[40:43], a[92:95], v[48:51], a[40:43]\n"
        "v_cvt_pk_bf16_f32 v60, v36, v37\n"
        "v_mfma_f32_16x16x32_bf16 a[44:47], a[92:95], v[52:55], a[44:47]\n"
        "ds_read_b128 a[92:95], v3 offset:13312\n"
        "v_mov_b32 v58, 0\n"
        "v_mfma_f32_16x16x32_bf16 v[28:31], v[88:91], v[120:123], v[28:31]\n"
        "v_mov_b32 v59, 0\n"
        "v_mfma_f32_16x16x32_bf16 a[48:51], a[96:99], v[48:51], a[48:51]\n"
        "v_exp_f32 v44, v44\n"
        "v_mfma_f32_16x16x32_bf16 a[52:55], a[96:99], v[52:55], a[52:55]\n"
        "ds_read_b128 a[96:99], v3 offset:14336\n"
        "v_exp_f32 v45, v45\n"
        "v_mfma_f32_16x16x32_bf16 v[28:31], v[92:95], v[124:127], v[28:31]\n"
        "v_cvt_pk_bf16_f32 v61, v38, v39\n"
        "v_mfma_f32_16x16x32_bf16 a[56:59], a[100:103], v[48:51], a[56:59]\n"
        "v_exp_f32 v46, v46\n"
        "v_mfma_f32_16x16x32_bf16 a[60:63], a[100:103], v[52:55], a[60:63]\n"
        "ds_read_b128 a[100:103], v3 offset:15360\n"
        "v_exp_f32 v47, v47\n"
        "v_mfma_f32_16x16x32_bf16 a[64:67], a[104:107], v[48:51], a[64:67]\n"
        "v_cvt_pk_bf16_f32 v62, v44, v45\n"
        "v_mfma_f32_16x16x32_bf16 a[68:71], a[104:107], v[52:55], a[68:71]\n"
        "v_cvt_pk_bf16_f32 v63, v46, v47\n"
        "ds_read_b128 v[64:67], v2 offset:0\n"
        "s_waitcnt lgkmcnt(3)\n"
        "v_mfma_f32_16x16x32_bf16 a[0:3], a[72:75], v[56:59], a[0:3]\n"
        "v_mfma_f32_16x16x32_bf16 a[4:7], a[72:75], v[60:63], a[4:7]\n"
        "ds_read_b128 a[72:75], v3 offset:16384\n"
        "v_exp_f32 v20, v20\n"
        "ds_read_b128 v[68:71], v2 offset:1024\n"
        "v_exp_f32 v21, v21\n"
        "v_mfma_f32_16x16x32_bf16 a[8:11], a[76:79], v[56:59], a[8:11]\n"
        "v_mfma_f32_16x16x32_bf16 a[12:15], a[76:79], v[60:63], a[12:15]\n"
        "ds_read_b128 a[76:79], v3 offset:17408\n"
        "v_exp_f32 v22, v22\n"
        "ds_read_b128 v[72:75], v2 offset:2048\n"
        "v_exp_f32 v23, v23\n"
        "v_mfma_f32_16x16x32_bf16 a[16:19], a[80:83], v[56:59], a[16:19]\n"
        "v_mfma_f32_16x16x32_bf16 a[20:23], a[80:83], v[60:63], a[20:23]\n"
        "ds_read_b128 a[80:83], v3 offset:18432\n"
        "v_cvt_pk_bf16_f32 v52, v20, v21\n"
        "ds_read_b128 v[76:79], v2 offset:3072\n"
        "v_cndmask_b32 v28, v7, v28, s[64:65]\n"
        "v_mfma_f32_16x16x32_bf16 a[24:27], a[84:87], v[56:59], a[24:27]\n"
        "v_mfma_f32_16x16x32_bf16 a[28:31], a[84:87], v[60:63], a[28:31]\n"
        "ds_read_b128 a[84:87], v3 offset:19456\n"
        "v_cndmask_b32 v29, v7, v29, s[66:67]\n"
        "ds_read_b128 v[80:83], v2 offset:4096\n"
        "v_exp_f32 v28, v28\n"
        "v_mfma_f32_16x16x32_bf16 a[32:35], a[88:91], v[56:59], a[32:35]\n"
        "v_mfma_f32_16x16x32_bf16 a[36:39], a[88:91], v[60:63], a[36:39]\n"
        "ds_read_b128 a[88:91], v3 offset:20480\n"
        "v_exp_f32 v29, v29\n"
        "ds_read_b128 v[84:87], v2 offset:5120\n"
        "v_cvt_pk_bf16_f32 v53, v22, v23\n"
        "v_mfma_f32_16x16x32_bf16 a[40:43], a[92:95], v[56:59], a[40:43]\n"
        "v_mfma_f32_16x16x32_bf16 a[44:47], a[92:95], v[60:63], a[44:47]\n"
        "ds_read_b128 a[92:95], v3 offset:21504\n"
        "v_cndmask_b32 v30, v7, v30, s[68:69]\n"
        "ds_read_b128 v[88:91], v2 offset:6144\n"
        "v_cndmask_b32 v31, v7, v31, s[70:71]\n"
        "s_waitcnt lgkmcnt(7)\n"
        "v_mfma_f32_16x16x32_bf16 a[48:51], a[96:99], v[56:59], a[48:51]\n"
        "v_mfma_f32_16x16x32_bf16 a[52:55], a[96:99], v[60:63], a[52:55]\n"
        "ds_read_b128 a[96:99], v3 offset:22528\n"
        "v_exp_f32 v30, v30\n"
        "ds_read_b128 v[92:95], v2 offset:7168\n"
        "v_exp_f32 v31, v31\n"
        "v_mfma_f32_16x16x32_bf16 a[56:59], a[100:103], v[56:59], a[56:59]\n"
        "v_mfma_f32_16x16x32_bf16 a[60:63], a[100:103], v[60:63], a[60:63]\n"
        "ds_read_b128 a[100:103], v3 offset:23552\n"
        "v_cvt_pk_bf16_f32 v54, v28, v29\n"
        "v_mfma_f32_16x16x32_bf16 a[64:67], a[104:107], v[56:59], a[64:67]\n"
        "v_cvt_pk_bf16_f32 v55, v30, v31\n"
        "v_mfma_f32_16x16x32_bf16 a[68:71], a[104:107], v[60:63], a[68:71]\n"
        "s_lshl_b32 s52, s36, 13\n"
        "s_add_u32 s52, s52, 131072\n"
        "v_add_u32 v6, s52, v0\n"
        "ds_read_b128 v[96:99], v6 offset:0\n"
        "ds_read_b128 v[100:103], v6 offset:1024\n"
        "ds_read_b128 v[104:107], v6 offset:2048\n"
        "ds_read_b128 v[108:111], v6 offset:3072\n"
        "ds_read_b128 v[112:115], v6 offset:4096\n"
        "ds_read_b128 v[116:119], v6 offset:5120\n"
        "ds_read_b128 v[120:123], v6 offset:6144\n"
        "ds_read_b128 v[124:127], v6 offset:7168\n"
        "v_mfma_f32_16x16x32_bf16 a[4:7], a[72:75], v[52:55], a[4:7]\n"
        "v_mfma_f32_16x16x32_bf16 a[12:15], a[76:79], v[52:55], a[12:15]\n"
        "v_mfma_f32_16x16x32_bf16 a[20:23], a[80:83], v[52:55], a[20:23]\n"
        "s_waitcnt lgkmcnt(9)\n"
        "v_mfma_f32_16x16x32_bf16 a[28:31], a[84:87], v[52:55], a[28:31]\n"
        "s_waitcnt lgkmcnt(0)\n"
        "s_barrier\n"
        "v_mfma_f32_16x16x32_bf16 a[36:39], a[88:91], v[52:55], a[36:39]\n"
        "v_mfma_f32_16x16x32_bf16 a[44:47], a[92:95], v[52:55], a[44:47]\n"
        "v_mfma_f32_16x16x32_bf16 a[52:55], a[96:99], v[52:55], a[52:55]\n"
        "v_mfma_f32_16x16x32_bf16 a[60:63], a[100:103], v[52:55], a[60:63]\n"
        "v_mfma_f32_16x16x32_bf16 a[68:71], a[104:107], v[52:55], a[68:71]\n"
        "v_xor_b32 v1, 0x8000, v1\n"
        "v_xor_b32 v2, 0x8000, v2\n"
        "v_xor_b32 v3, 0x8000, v3\n"
        "s_branch P3_EPI_%=\n"
        "P3_DM3_%=:\n"
        "v_mfma_f32_16x16x32_bf16 v[32:35], v[64:67], v[96:99], v[8:11]\n"
        "v_mfma_f32_16x16x32_bf16 v[36:39], v[64:67], v[112:115], v[12:15]\n"
        "ds_read_b128 v[64:67], v1 offset:16384\n"
        "v_mfma_f32_16x16x32_bf16 a[0:3], a[72:75], v[56:59], a[0:3]\n"
        "v_exp_f32 v16, v16\n"
        "v_mfma_f32_16x16x32_bf16 a[4:7], a[72:75], v[60:63], a[4:7]\n"
        "ds_read_b128 a[72:75], v3 offset:0\n"
        "v_exp_f32 v17, v17\n"
        "s_waitcnt lgkmcnt(4)\n"
        "v_mfma_f32_16x16x32_bf16 v[32:35], v[68:71], v[100:103], v[32:35]\n"
        "v_exp_f32 v18, v18\n"
        "v_mfma_f32_16x16x32_bf16 v[36:39], v[68:71], v[116:119], v[36:39]\n"
        "ds_read_b128 v[68:71], v1 offset:17408\n"
        "v_mfma_f32_16x16x32_bf16 a[8:11], a[76:79], v[56:59], a[8:11]\n"
        "v_exp_f32 v19, v19\n"
        "v_mfma_f32_16x16x32_bf16 a[12:15], a[76:79], v[60:63], a[12:15]\n"
        "ds_read_b128 a[76:79], v3 offset:1024\n"
        "v_cvt_pk_bf16_f32 v48, v16, v17\n"
        "v_mfma_f32_16x16x32_bf16 v[32:35], v[72:75], v[104:107], v[32:35]\n"
        "v_exp_f32 v20, v20\n"
        "v_mfma_f32_16x16x32_bf16 v[36:39], v[72:75], v[120:123], v[36:39]\n"
        "ds_read_b128 v[72:75], v1 offset:18432\n"
        "v_mfma_f32_16x16x32_bf16 a[16:19], a[80:83], v[56:59], a[16:19]\n"
        "v_exp_f32 v21, v21\n"
        "v_mfma_f32_16x16x32_bf16 a[20:23], a[80:83], v[60:63], a[20:23]\n"
        "ds_read_b128 a[80:83], v3 offset:2048\n"
        "v_cvt_pk_bf16_f32 v49, v18, v19\n"
        "v_mfma_f32_16x16x32_bf16 v[32:35], v[76:79], v[108:111], v[32:35]\n"
        "v_exp_f32 v22, v22\n"
        "v_mfma_f32_16x16x32_bf16 v[36:39], v[76:79], v[124:127], v[36:39]\n"
        "ds_read_b128 v[76:79], v1 offset:19456\n"
        "v_mfma_f32_16x16x32_bf16 a[24:27], a[84:87], v[56:59], a[24:27]\n"
        "v_exp_f32 v23, v23\n"
        "v_mfma_f32_16x16x32_bf16 a[28:31], a[84:87], v[60:63], a[28:31]\n"
        "ds_read_b128 a[84:87], v3 offset:3072\n"
        "v_cvt_pk_bf16_f32 v52, v20, v21\n"
        "v_mfma_f32_16x16x32_bf16 v[40:43], v[80:83], v[96:99], v[8:11]\n"
        "v_exp_f32 v24, v24\n"
        "v_mfma_f32_16x16x32_bf16 v[44:47], v[80:83], v[112:115], v[12:15]\n"
        "v_mfma_f32_16x16x32_bf16 a[32:35], a[88:91], v[56:59], a[32:35]\n"
        "v_exp_f32 v25, v25\n"
        "v_mfma_f32_16x16x32_bf16 a[36:39], a[88:91], v[60:63], a[36:39]\n"
        "ds_read_b128 a[88:91], v3 offset:4096\n"
        "v_cvt_pk_bf16_f32 v53, v22, v23\n"
        "v_mfma_f32_16x16x32_bf16 v[40:43], v[84:87], v[100:103], v[40:43]\n"
        "v_exp_f32 v26, v26\n"
        "v_mfma_f32_16x16x32_bf16 v[44:47], v[84:87], v[116:119], v[44:47]\n"
        "v_mfma_f32_16x16x32_bf16 a[40:43], a[92:95], v[56:59], a[40:43]\n"
        "v_exp_f32 v27, v27\n"
        "v_mfma_f32_16x16x32_bf16 a[44:47], a[92:95], v[60:63], a[44:47]\n"
        "ds_read_b128 a[92:95], v3 offset:5120\n"
        "v_cvt_pk_bf16_f32 v50, v24, v25\n"
        "v_mfma_f32_16x16x32_bf16 v[40:43], v[88:91], v[104:107], v[40:43]\n"
        "v_exp_f32 v28, v28\n"
        "v_mfma_f32_16x16x32_bf16 v[44:47], v[88:91], v[120:123], v[44:47]\n"
        "v_mfma_f32_16x16x32_bf16 a[48:51], a[96:99], v[56:59], a[48:51]\n"
        "v_exp_f32 v29, v29\n"
        "v_mfma_f32_16x16x32_bf16 a[52:55], a[96:99], v[60:63], a[52:55]\n"
        "ds_read_b128 a[96:99], v3 offset:6144\n"
        "v_cvt_pk_bf16_f32 v51, v26, v27\n"
        "s_waitcnt lgkmcnt(3)\n"
        "v_mfma_f32_16x16x32_bf16 v[40:43], v[92:95], v[108:111], v[40:43]\n"
        "v_exp_f32 v30, v30\n"
        "v_mfma_f32_16x16x32_bf16 v[44:47], v[92:95], v[124:127], v[44:47]\n"
        "v_mfma_f32_16x16x32_bf16 a[56:59], a[100:103], v[56:59], a[56:59]\n"
        "v_exp_f32 v31, v31\n"
        "v_mfma_f32_16x16x32_bf16 a[60:63], a[100:103], v[60:63], a[60:63]\n"
        "ds_read_b128 a[100:103], v3 offset:7168\n"
        "v_cvt_pk_bf16_f32 v54, v28, v29\n"
        "v_mfma_f32_16x16x32_bf16 a[64:67], a[104:107], v[56:59], a[64:67]\n"
        "v_cvt_pk_bf16_f32 v55, v30, v31\n"
        "v_mfma_f32_16x16x32_bf16 a[68:71], a[104:107], v[60:63], a[68:71]\n"
        "v_mfma_f32_16x16x32_bf16 v[20:23], v[64:67], v[112:115], v[12:15]\n"
        "v_mfma_f32_16x16x32_bf16 a[0:3], a[72:75], v[48:51], a[0:3]\n"
        "v_mfma_f32_16x16x32_bf16 a[4:7], a[72:75], v[52:55], a[4:7]\n"
        "ds_read_b128 a[72:75], v3 offset:8192\n"
        "v_exp_f32 v32, v32\n"
        "v_exp_f32 v33, v33\n"
        "v_mfma_f32_16x16x32_bf16 v[20:23], v[68:71], v[116:119], v[20:23]\n"
        "v_exp_f32 v34, v34\n"
        "v_mfma_f32_16x16x32_bf16 a[8:11], a[76:79], v[48:51], a[8:11]\n"
        "v_exp_f32 v35, v35\n"
        "v_cvt_pk_bf16_f32 v56, v32, v33\n"
        "v_mfma_f32_16x16x32_bf16 a[12:15], a[76:79], v[52:55], a[12:15]\n"
        "ds_read_b128 a[76:79], v3 offset:9216\n"
        "v_exp_f32 v36, v36\n"
        "v_mfma_f32_16x16x32_bf16 v[20:23], v[72:75], v[120:123], v[20:23]\n"
        "v_exp_f32 v37, v37\n"
        "v_mfma_f32_16x16x32_bf16 a[16:19], a[80:83], v[48:51], a[16:19]\n"
        "v_cvt_pk_bf16_f32 v57, v34, v35\n"
        "v_exp_f32 v38, v38\n"
        "v_mfma_f32_16x16x32_bf16 a[20:23], a[80:83], v[52:55], a[20:23]\n"
        "ds_read_b128 a[80:83], v3 offset:10240\n"
        "v_exp_f32 v39, v39\n"
        "v_mfma_f32_16x16x32_bf16 v[20:23], v[76:79], v[124:127], v[20:23]\n"
        "v_cvt_pk_bf16_f32 v60, v36, v37\n"
        "v_cndmask_b32 v40, v7, v40, s[64:65]\n"
        "v_mfma_f32_16x16x32_bf16 a[24:27], a[84:87], v[48:51], a[24:27]\n"
        "v_cndmask_b32 v41, v7, v41, s[66:67]\n"
        "s_waitcnt lgkmcnt(3)\n"
        "s_barrier\n"
        "v_mfma_f32_16x16x32_bf16 a[28:31], a[84:87], v[52:55], a[28:31]\n"
        "ds_read_b128 a[84:87], v3 offset:11264\n"
        "v_exp_f32 v40, v40\n"
        "v_mfma_f32_16x16x32_bf16 a[32:35], a[88:91], v[48:51], a[32:35]\n"
        "v_exp_f32 v41, v41\n"
        "v_cvt_pk_bf16_f32 v61, v38, v39\n"
        "v_mfma_f32_16x16x32_bf16 a[36:39], a[88:91], v[52:55], a[36:39]\n"
        "ds_read_b128 a[88:91], v3 offset:12288\n"
        "v_cndmask_b32 v42, v7, v42, s[68:69]\n"
        "v_mfma_f32_16x16x32_bf16 a[40:43], a[92:95], v[48:51], a[40:43]\n"
        "v_cndmask_b32 v43, v7, v43, s[70:71]\n"
        "v_exp_f32 v42, v42\n"
        "v_mfma_f32_16x16x32_bf16 a[44:47], a[92:95], v[52:55], a[44:47]\n"
        "ds_read_b128 a[92:95], v3 offset:13312\n"
        "v_exp_f32 v43, v43\n"
        "v_mfma_f32_16x16x32_bf16 a[48:51], a[96:99], v[48:51], a[48:51]\n"
        "v_cvt_pk_bf16_f32 v58, v40, v41\n"
        "v_mfma_f32_16x16x32_bf16 a[52:55], a[96:99], v[52:55], a[52:55]\n"
        "ds_read_b128 a[96:99], v3 offset:14336\n"
        "v_exp_f32 v44, v44\n"
        "v_exp_f32 v45, v45\n"
        "v_mfma_f32_16x16x32_bf16 a[56:59], a[100:103], v[48:51], a[56:59]\n"
        "v_cvt_pk_bf16_f32 v59, v42, v43\n"
        "v_mfma_f32_16x16x32_bf16 a[60:63], a[100:103], v[52:55], a[60:63]\n"
        "ds_read_b128 a[100:103], v3 offset:15360\n"
        "v_exp_f32 v46, v46\n"
        "v_exp_f32 v47, v47\n"
        "v_mfma_f32_16x16x32_bf16 a[64:67], a[104:107], v[48:51], a[64:67]\n"
        "v_cvt_pk_bf16_f32 v62, v44, v45\n"
        "v_mfma_f32_16x16x32_bf16 a[68:71], a[104:107], v[52:55], a[68:71]\n"
        "v_cvt_pk_bf16_f32 v63, v46, v47\n"
        "ds_read_b128 v[64:67], v2 offset:0\n"
        "s_waitcnt lgkmcnt(4)\n"
        "v_mfma_f32_16x16x32_bf16 a[0:3], a[72:75], v[56:59], a[0:3]\n"
        "v_mfma_f32_16x16x32_bf16 a[4:7], a[72:75], v[60:63], a[4:7]\n"
        "ds_read_b128 a[72:75], v3 offset:16384\n"
        "v_cndmask_b32 v20, v7, v20, s[64:65]\n"
        "ds_read_b128 v[68:71], v2 offset:1024\n"
        "v_mfma_f32_16x16x32_bf16 a[8:11], a[76:79], v[56:59], a[8:11]\n"
        "v_cndmask_b32 v21, v7, v21, s[66:67]\n"
        "v_mfma_f32_16x16x32_bf16 a[12:15], a[76:79], v[60:63], a[12:15]\n"
        "ds_read_b128 a[76:79], v3 offset:17408\n"
        "ds_read_b128 v[72:75], v2 offset:2048\n"
        "v_exp_f32 v20, v20\n"
        "v_mfma_f32_16x16x32_bf16 a[16:19], a[80:83], v[56:59], a[16:19]\n"
        "v_mfma_f32_16x16x32_bf16 a[20:23], a[80:83], v[60:63], a[20:23]\n"
        "ds_read_b128 a[80:83], v3 offset:18432\n"
        "v_exp_f32 v21, v21\n"
        "ds_read_b128 v[76:79], v2 offset:3072\n"
        "v_mfma_f32_16x16x32_bf16 a[24:27], a[84:87], v[56:59], a[24:27]\n"
        "v_cndmask_b32 v22, v7, v22, s[68:69]\n"
        "v_mfma_f32_16x16x32_bf16 a[28:31], a[84:87], v[60:63], a[28:31]\n"
        "ds_read_b128 a[84:87], v3 offset:19456\n"
        "ds_read_b128 v[80:83], v2 offset:4096\n"
        "v_cndmask_b32 v23, v7, v23, s[70:71]\n"
        "v_mfma_f32_16x16x32_bf16 a[32:35], a[88:91], v[56:59], a[32:35]\n"
        "v_mfma_f32_16x16x32_bf16 a[36:39], a[88:91], v[60:63], a[36:39]\n"
        "ds_read_b128 a[88:91], v3 offset:20480\n"
        "v_exp_f32 v22, v22\n"
        "ds_read_b128 v[84:87], v2 offset:5120\n"
        "s_waitcnt lgkmcnt(8)\n"
        "v_mfma_f32_16x16x32_bf16 a[40:43], a[92:95], v[56:59], a[40:43]\n"
        "v_exp_f32 v23, v23\n"
        "v_mfma_f32_16x16x32_bf16 a[44:47], a[92:95], v[60:63], a[44:47]\n"
        "ds_read_b128 a[92:95], v3 offset:21504\n"
        "ds_read_b128 v[88:91], v2 offset:6144\n"
        "v_cvt_pk_bf16_f32 v52, v20, v21\n"
        "v_mfma_f32_16x16x32_bf16 a[48:51], a[96:99], v[56:59], a[48:51]\n"
        "v_mfma_f32_16x16x32_bf16 a[52:55], a[96:99], v[60:63], a[52:55]\n"
        "ds_read_b128 a[96:99], v3 offset:22528\n"
        "v_mov_b32 v54, 0\n"
        "ds_read_b128 v[92:95], v2 offset:7168\n"
        "v_mfma_f32_16x16x32_bf16 a[56:59], a[100:103], v[56:59], a[56:59]\n"
        "v_mov_b32 v55, 0\n"
        "v_mfma_f32_16x16x32_bf16 a[60:63], a[100:103], v[60:63], a[60:63]\n"
        "ds_read_b128 a[100:103], v3 offset:23552\n"
        "v_mfma_f32_16x16x32_bf16 a[64:67], a[104:107], v[56:59], a[64:67]\n"
        "v_cvt_pk_bf16_f32 v53, v22, v23\n"
        "v_mfma_f32_16x16x32_bf16 a[68:71], a[104:107], v[60:63], a[68:71]\n"
        "s_lshl_b32 s52, s36, 13\n"
        "s_add_u32 s52, s52, 131072\n"
        "v_add_u32 v6, s52, v0\n"
        "ds_read_b128 v[96:99], v6 offset:0\n"
        "ds_read_b128 v[100:103], v6 offset:1024\n"
        "ds_read_b128 v[104:107], v6 offset:2048\n"
        "ds_read_b128 v[108:111], v6 offset:3072\n"
        "ds_read_b128 v[112:115], v6 offset:4096\n"
        "ds_read_b128 v[116:119], v6 offset:5120\n"
        "ds_read_b128 v[120:123], v6 offset:6144\n"
        "ds_read_b128 v[124:127], v6 offset:7168\n"
        "v_mfma_f32_16x16x32_bf16 a[4:7], a[72:75], v[52:55], a[4:7]\n"
        "s_waitcnt lgkmcnt(10)\n"
        "v_mfma_f32_16x16x32_bf16 a[12:15], a[76:79], v[52:55], a[12:15]\n"
        "v_mfma_f32_16x16x32_bf16 a[20:23], a[80:83], v[52:55], a[20:23]\n"
        "v_mfma_f32_16x16x32_bf16 a[28:31], a[84:87], v[52:55], a[28:31]\n"
        "s_waitcnt lgkmcnt(0)\n"
        "s_barrier\n"
        "v_mfma_f32_16x16x32_bf16 a[36:39], a[88:91], v[52:55], a[36:39]\n"
        "v_mfma_f32_16x16x32_bf16 a[44:47], a[92:95], v[52:55], a[44:47]\n"
        "v_mfma_f32_16x16x32_bf16 a[52:55], a[96:99], v[52:55], a[52:55]\n"
        "v_mfma_f32_16x16x32_bf16 a[60:63], a[100:103], v[52:55], a[60:63]\n"
        "v_mfma_f32_16x16x32_bf16 a[68:71], a[104:107], v[52:55], a[68:71]\n"
        "v_xor_b32 v1, 0x8000, v1\n"
        "v_xor_b32 v2, 0x8000, v2\n"
        "v_xor_b32 v3, 0x8000, v3\n"
        "s_branch P3_EPI_%=\n"
        "P3_DF0_%=:\n"
        "s_waitcnt lgkmcnt(0)\n"
        "v_mfma_f32_16x16x32_bf16 v[36:39], v[64:67], v[112:115], v[12:15]\n"
        "ds_read_b128 v[64:67], v1 offset:16384\n"
        "ds_read_b128 a[72:75], v3 offset:0\n"
        "v_mfma_f32_16x16x32_bf16 v[36:39], v[68:71], v[116:119], v[36:39]\n"
        "ds_read_b128 v[68:71], v1 offset:17408\n"
        "v_cndmask_b32 v16, v7, v16, s[64:65]\n"
        "v_cndmask_b32 v17, v7, v17, s[66:67]\n"
        "ds_read_b128 a[76:79], v3 offset:1024\n"
        "v_exp_f32 v16, v16\n"
        "v_exp_f32 v17, v17\n"
        "v_mfma_f32_16x16x32_bf16 v[36:39], v[72:75], v[120:123], v[36:39]\n"
        "ds_read_b128 v[72:75], v1 offset:18432\n"
        "v_cndmask_b32 v18, v7, v18, s[68:69]\n"
        "v_cndmask_b32 v19, v7, v19, s[70:71]\n"
        "ds_read_b128 a[80:83], v3 offset:2048\n"
        "v_exp_f32 v18, v18\n"
        "v_mfma_f32_16x16x32_bf16 v[36:39], v[76:79], v[124:127], v[36:39]\n"
        "ds_read_b128 v[76:79], v1 offset:19456\n"
        "v_exp_f32 v19, v19\n"
        "v_cvt_pk_bf16_f32 v48, v16, v17\n"
        "ds_read_b128 a[84:87], v3 offset:3072\n"
        "v_exp_f32 v20, v20\n"
        "v_exp_f32 v21, v21\n"
        "v_mfma_f32_16x16x32_bf16 v[44:47], v[80:83], v[112:115], v[12:15]\n"
        "ds_read_b128 v[80:83], v1 offset:20480\n"
        "v_cvt_pk_bf16_f32 v49, v18, v19\n"
        "ds_read_b128 a[88:91], v3 offset:4096\n"
        "v_exp_f32 v22, v22\n"
        "v_exp_f32 v23, v23\n"
        "v_mfma_f32_16x16x32_bf16 v[44:47], v[84:87], v[116:119], v[44:47]\n"
        "ds_read_b128 v[84:87], v1 offset:21504\n"
        "v_cvt_pk_bf16_f32 v52, v20, v21\n"
        "v_mov_b32 v50, 0\n"
        "ds_read_b128 a[92:95], v3 offset:5120\n"
        "v_mov_b32 v51, 0\n"
        "v_exp_f32 v28, v28\n"
        "v_mfma_f32_16x16x32_bf16 v[44:47], v[88:91], v[120:123], v[44:47]\n"
        "ds_read_b128 v[88:91], v1 offset:22528\n"
        "v_exp_f32 v29, v29\n"
        "ds_read_b128 a[96:99], v3 offset:6144\n"
        "v_cvt_pk_bf16_f32 v53, v22, v23\n"
        "v_exp_f32 v30, v30\n"
        "v_mfma_f32_16x16x32_bf16 v[44:47], v[92:95], v[124:127], v[44:47]\n"
        "ds_read_b128 v[92:95], v1 offset:23552\n"
        "v_exp_f32 v31, v31\n"
        "v_cvt_pk_bf16_f32 v54, v28, v29\n"
        "ds_read_b128 a[100:103], v3 offset:7168\n"
        "v_cvt_pk_bf16_f32 v55, v30, v31\n"
        "s_waitcnt lgkmcnt(6)\n"
        "v_mfma_f32_16x16x32_bf16 v[20:23], v[64:67], v[112:115], v[12:15]\n"
        "ds_read_b128 v[64:67], v1 offset:24576\n"
        "v_mfma_f32_16x16x32_bf16 a[0:3], a[72:75], v[48:51], 0\n"
        "v_mfma_f32_16x16x32_bf16 a[4:7], a[72:75], v[52:55], 0\n"
        "ds_read_b128 a[72:75], v3 offset:8192\n"
        "v_exp_f32 v36, v36\n"
        "v_mfma_f32_16x16x32_bf16 v[20:23], v[68:71], v[116:119], v[20:23]\n"
        "ds_read_b128 v[68:71], v1 offset:25600\n"
        "v_mfma_f32_16x16x32_bf16 a[8:11], a[76:79], v[48:51], 0\n"
        "v_exp_f32 v37, v37\n"
        "v_mfma_f32_16x16x32_bf16 a[12:15], a[76:79], v[52:55], 0\n"
        "ds_read_b128 a[76:79], v3 offset:9216\n"
        "v_mfma_f32_16x16x32_bf16 v[20:23], v[72:75], v[120:123], v[20:23]\n"
        "ds_read_b128 v[72:75], v1 offset:26624\n"
        "v_exp_f32 v38, v38\n"
        "v_mfma_f32_16x16x32_bf16 a[16:19], a[80:83], v[48:51], 0\n"
        "v_mfma_f32_16x16x32_bf16 a[20:23], a[80:83], v[52:55], 0\n"
        "ds_read_b128 a[80:83], v3 offset:10240\n"
        "v_exp_f32 v39, v39\n"
        "v_mfma_f32_16x16x32_bf16 v[20:23], v[76:79], v[124:127], v[20:23]\n"
        "ds_read_b128 v[76:79], v1 offset:27648\n"
        "v_mfma_f32_16x16x32_bf16 a[24:27], a[84:87], v[48:51], 0\n"
        "v_cvt_pk_bf16_f32 v60, v36, v37\n"
        "v_mfma_f32_16x16x32_bf16 a[28:31], a[84:87], v[52:55], 0\n"
        "ds_read_b128 a[84:87], v3 offset:11264\n"
        "v_mfma_f32_16x16x32_bf16 v[28:31], v[80:83], v[112:115], v[12:15]\n"
        "ds_read_b128 v[80:83], v1 offset:28672\n"
        "v_exp_f32 v44, v44\n"
        "s_waitcnt lgkmcnt(9)\n"
        "s_barrier\n"
        "v_bfe_u32 v5, v0, 4, 2\n"
        "v_lshlrev_b32 v5, 3, v5\n"
        "v_add_u32 v5, 159744, v5\n"
        "ds_read_b32 v4, v5\n"
        "v_mfma_f32_16x16x32_bf16 a[32:35], a[88:91], v[48:51], 0\n"
        "v_mfma_f32_16x16x32_bf16 a[36:39], a[88:91], v[52:55], 0\n"
        "ds_read_b128 a[88:91], v3 offset:12288\n"
        "v_exp_f32 v45, v45\n"
        "v_mfma_f32_16x16x32_bf16 v[28:31], v[84:87], v[116:119], v[28:31]\n"
        "ds_read_b128 v[84:87], v1 offset:29696\n"
        "v_mfma_f32_16x16x32_bf16 a[40:43], a[92:95], v[48:51], 0\n"
        "v_cvt_pk_bf16_f32 v61, v38, v39\n"
        "v_mfma_f32_16x16x32_bf16 a[44:47], a[92:95], v[52:55], 0\n"
        "ds_read_b128 a[92:95], v3 offset:13312\n"
        "v_mfma_f32_16x16x32_bf16 v[28:31], v[88:91], v[120:123], v[28:31]\n"
        "ds_read_b128 v[88:91], v1 offset:30720\n"
        "v_exp_f32 v46, v46\n"
        "v_mfma_f32_16x16x32_bf16 a[48:51], a[96:99], v[48:51], 0\n"
        "v_mfma_f32_16x16x32_bf16 a[52:55], a[96:99], v[52:55], 0\n"
        "ds_read_b128 a[96:99], v3 offset:14336\n"
        "v_exp_f32 v47, v47\n"
        "v_mfma_f32_16x16x32_bf16 v[28:31], v[92:95], v[124:127], v[28:31]\n"
        "ds_read_b128 v[92:95], v1 offset:31744\n"
        "v_mfma_f32_16x16x32_bf16 a[56:59], a[100:103], v[48:51], 0\n"
        "v_cvt_pk_bf16_f32 v62, v44, v45\n"
        "v_mfma_f32_16x16x32_bf16 a[60:63], a[100:103], v[52:55], 0\n"
        "ds_read_b128 a[100:103], v3 offset:15360\n"
        "v_mfma_f32_16x16x32_bf16 a[64:67], a[104:107], v[48:51], 0\n"
        "v_cvt_pk_bf16_f32 v63, v46, v47\n"
        "v_mfma_f32_16x16x32_bf16 a[68:71], a[104:107], v[52:55], 0\n"
        "s_waitcnt lgkmcnt(5)\n"
        "v_mfma_f32_16x16x32_bf16 v[36:39], v[64:67], v[112:115], v[12:15]\n"
        "ds_read_b128 v[64:67], v2 offset:0\n"
        "v_mfma_f32_16x16x32_bf16 a[4:7], a[72:75], v[60:63], a[4:7]\n"
        "ds_read_b128 a[72:75], v3 offset:16384\n"
        "v_mfma_f32_16x16x32_bf16 v[36:39], v[68:71], v[116:119], v[36:39]\n"
        "ds_read_b128 v[68:71], v2 offset:1024\n"
        "v_exp_f32 v20, v20\n"
        "v_mfma_f32_16x16x32_bf16 a[12:15], a[76:79], v[60:63], a[12:15]\n"
        "ds_read_b128 a[76:79], v3 offset:17408\n"
        "v_exp_f32 v21, v21\n"
        "v_mfma_f32_16x16x32_bf16 v[36:39], v[72:75], v[120:123], v[36:39]\n"
        "ds_read_b128 v[72:75], v2 offset:2048\n"
        "v_exp_f32 v22, v22\n"
        "v_mfma_f32_16x16x32_bf16 a[20:23], a[80:83], v[60:63], a[20:23]\n"
        "ds_read_b128 a[80:83], v3 offset:18432\n"
        "v_exp_f32 v23, v23\n"
        "v_mfma_f32_16x16x32_bf16 v[36:39], v[76:79], v[124:127], v[36:39]\n"
        "ds_read_b128 v[76:79], v2 offset:3072\n"
        "v_mfma_f32_16x16x32_bf16 a[28:31], a[84:87], v[60:63], a[28:31]\n"
        "ds_read_b128 a[84:87], v3 offset:19456\n"
        "v_cvt_pk_bf16_f32 v52, v20, v21\n"
        "v_mfma_f32_16x16x32_bf16 v[44:47], v[80:83], v[112:115], v[12:15]\n"
        "ds_read_b128 v[80:83], v2 offset:4096\n"
        "v_exp_f32 v28, v28\n"
        "v_mfma_f32_16x16x32_bf16 a[36:39], a[88:91], v[60:63], a[36:39]\n"
        "ds_read_b128 a[88:91], v3 offset:20480\n"
        "v_exp_f32 v29, v29\n"
        "v_mfma_f32_16x16x32_bf16 v[44:47], v[84:87], v[116:119], v[44:47]\n"
        "ds_read_b128 v[84:87], v2 offset:5120\n"
        "v_cvt_pk_bf16_f32 v53, v22, v23\n"
        "s_waitcnt lgkmcnt(7)\n"
        "v_mfma_f32_16x16x32_bf16 a[44:47], a[92:95], v[60:63], a[44:47]\n"
        "ds_read_b128 a[92:95], v3 offset:21504\n"
        "v_mfma_f32_16x16x32_bf16 v[44:47], v[88:91], v[120:123], v[44:47]\n"
        "ds_read_b128 v[88:91], v2 offset:6144\n"
        "v_exp_f32 v30, v30\n"
        "v_mfma_f32_16x16x32_bf16 a[52:55], a[96:99], v[60:63], a[52:55]\n"
        "ds_read_b128 a[96:99], v3 offset:22528\n"
        "v_exp_f32 v31, v31\n"
        "v_mfma_f32_16x16x32_bf16 v[44:47], v[92:95], v[124:127], v[44:47]\n"
        "ds_read_b128 v[92:95], v2 offset:7168\n"
        "v_cvt_pk_bf16_f32 v54, v28, v29\n"
        "v_mfma_f32_16x16x32_bf16 a[60:63], a[100:103], v[60:63], a[60:63]\n"
        "ds_read_b128 a[100:103], v3 offset:23552\n"
        "v_cvt_pk_bf16_f32 v55, v30, v31\n"
        "v_mfma_f32_16x16x32_bf16 a[68:71], a[104:107], v[60:63], a[68:71]\n"
        "s_lshl_b32 s52, s36, 13\n"
        "s_add_u32 s52, s52, 131072\n"
        "v_add_u32 v6, s52, v0\n"
        "ds_read_b128 v[96:99], v6 offset:0\n"
        "ds_read_b128 v[100:103], v6 offset:1024\n"
        "ds_read_b128 v[104:107], v6 offset:2048\n"
        "ds_read_b128 v[108:111], v6 offset:3072\n"
        "ds_read_b128 v[112:115], v6 offset:4096\n"
        "ds_read_b128 v[116:119], v6 offset:5120\n"
        "ds_read_b128 v[120:123], v6 offset:6144\n"
        "ds_read_b128 v[124:127], v6 offset:7168\n"
        "v_mfma_f32_16x16x32_bf16 a[4:7], a[72:75], v[52:55], a[4:7]\n"
        "ds_read_b128 a[72:75], v3 offset:24576\n"
        "v_mfma_f32_16x16x32_bf16 a[12:15], a[76:79], v[52:55], a[12:15]\n"
        "ds_read_b128 a[76:79], v3 offset:25600\n"
        "s_waitcnt lgkmcnt(11)\n"
        "v_mfma_f32_16x16x32_bf16 a[20:23], a[80:83], v[52:55], a[20:23]\n"
        "ds_read_b128 a[80:83], v3 offset:26624\n"
        "v_exp_f32 v36, v36\n"
        "v_exp_f32 v37, v37\n"
        "v_exp_f32 v38, v38\n"
        "v_mfma_f32_16x16x32_bf16 a[28:31], a[84:87], v[52:55], a[28:31]\n"
        "ds_read_b128 a[84:87], v3 offset:27648\n"
        "v_exp_f32 v39, v39\n"
        "v_cvt_pk_bf16_f32 v60, v36, v37\n"
        "s_waitcnt vmcnt(0)\n"
        "s_waitcnt lgkmcnt(4)\n"
        "s_barrier\n"
        "s_cmp_lg_u32 s60, 1\n"
        "s_cbranch_scc1 KA_NCAD0_%=\n"
        "s_cmp_lg_u32 s36, 0\n"
        "s_cbranch_scc1 KA_NCAD0_%=\n"
        "v_cmp_ne_u32 vcc, 0, v4\n"
        "s_nop 4\n"
        "s_cbranch_vccnz KA_NCAD0_%=\n"
        "s_lshr_b32 s88, s37, 2\n"
        "s_lshl_b32 s88, s88, 3\n"
        "s_add_u32 s88, s88, s38\n"
        "s_lshl_b32 s88, s88, 2\n"
        "s_and_b32 s52, s37, 3\n"
        "s_add_u32 s88, s88, s52\n"
        "s_lshl_b32 s88, s88, 7\n"
        "s_add_u32 s88, s88, 65536\n"
        "s_mov_b64 exec, 1\n"
        "v_mov_b32 v6, s88\n"
        "v_mov_b32 v4, s96\n"
        "v_mov_b32 v5, s97\n"
        "global_store_dwordx2 v6, v[4:5], s[86:87] sc1\n"
        "s_mov_b64 exec, -1\n"
        "KA_NCAD0_%=:\n"
        "s_nop 3\n"
        "v_mfma_f32_16x16x32_bf16 a[36:39], a[88:91], v[52:55], a[36:39]\n"
        "ds_read_b128 a[88:91], v3 offset:28672\n"
        "v_cndmask_b32 v44, v7, v44, s[64:65]\n"
        "v_cndmask_b32 v45, v7, v45, s[66:67]\n"
        "v_mfma_f32_16x16x32_bf16 a[44:47], a[92:95], v[52:55], a[44:47]\n"
        "ds_read_b128 a[92:95], v3 offset:29696\n"
        "v_exp_f32 v44, v44\n"
        "v_exp_f32 v45, v45\n"
        "v_cvt_pk_bf16_f32 v61, v38, v39\n"
        "v_mfma_f32_16x16x32_bf16 a[52:55], a[96:99], v[52:55], a[52:55]\n"
        "ds_read_b128 a[96:99], v3 offset:30720\n"
        "v_cndmask_b32 v46, v7, v46, s[68:69]\n"
        "v_cndmask_b32 v47, v7, v47, s[70:71]\n"
        "v_mfma_f32_16x16x32_bf16 a[60:63], a[100:103], v[52:55], a[60:63]\n"
        "ds_read_b128 a[100:103], v3 offset:31744\n"
        "v_exp_f32 v46, v46\n"
        "v_exp_f32 v47, v47\n"
        "v_mfma_f32_16x16x32_bf16 a[68:71], a[104:107], v[52:55], a[68:71]\n"
        "v_cvt_pk_bf16_f32 v62, v44, v45\n"
        "v_cvt_pk_bf16_f32 v63, v46, v47\n"
        "v_xor_b32 v1, 0x8000, v1\n"
        "v_xor_b32 v2, 0x8000, v2\n"
        "v_xor_b32 v3, 0x8000, v3\n"
        "s_waitcnt lgkmcnt(3)\n"
        "v_mfma_f32_16x16x32_bf16 a[4:7], a[72:75], v[60:63], a[4:7]\n"
        "v_mfma_f32_16x16x32_bf16 a[12:15], a[76:79], v[60:63], a[12:15]\n"
        "v_mfma_f32_16x16x32_bf16 a[20:23], a[80:83], v[60:63], a[20:23]\n"
        "v_mfma_f32_16x16x32_bf16 a[28:31], a[84:87], v[60:63], a[28:31]\n"
        "v_mfma_f32_16x16x32_bf16 a[36:39], a[88:91], v[60:63], a[36:39]\n"
        "s_waitcnt lgkmcnt(2)\n"
        "v_mfma_f32_16x16x32_bf16 a[44:47], a[92:95], v[60:63], a[44:47]\n"
        "s_waitcnt lgkmcnt(1)\n"
        "v_mfma_f32_16x16x32_bf16 a[52:55], a[96:99], v[60:63], a[52:55]\n"
        "s_waitcnt lgkmcnt(0)\n"
        "v_mfma_f32_16x16x32_bf16 a[60:63], a[100:103], v[60:63], a[60:63]\n"
        "v_mfma_f32_16x16x32_bf16 a[68:71], a[104:107], v[60:63], a[68:71]\n"
        "s_branch P3_EPI_%=\n"
        "P3_DF1_%=:\n"
        "s_waitcnt lgkmcnt(0)\n"
        "v_mfma_f32_16x16x32_bf16 v[36:39], v[64:67], v[112:115], v[12:15]\n"
        "ds_read_b128 v[64:67], v1 offset:16384\n"
        "ds_read_b128 a[72:75], v3 offset:0\n"
        "v_mfma_f32_16x16x32_bf16 v[36:39], v[68:71], v[116:119], v[36:39]\n"
        "ds_read_b128 v[68:71], v1 offset:17408\n"
        "v_exp_f32 v16, v16\n"
        "v_exp_f32 v17, v17\n"
        "ds_read_b128 a[76:79], v3 offset:1024\n"
        "v_exp_f32 v18, v18\n"
        "v_exp_f32 v19, v19\n"
        "v_mfma_f32_16x16x32_bf16 v[36:39], v[72:75], v[120:123], v[36:39]\n"
        "ds_read_b128 v[72:75], v1 offset:18432\n"
        "v_cvt_pk_bf16_f32 v48, v16, v17\n"
        "v_exp_f32 v20, v20\n"
        "ds_read_b128 a[80:83], v3 offset:2048\n"
        "v_exp_f32 v21, v21\n"
        "v_cvt_pk_bf16_f32 v49, v18, v19\n"
        "v_mfma_f32_16x16x32_bf16 v[36:39], v[76:79], v[124:127], v[36:39]\n"
        "ds_read_b128 v[76:79], v1 offset:19456\n"
        "v_exp_f32 v22, v22\n"
        "v_exp_f32 v23, v23\n"
        "ds_read_b128 a[84:87], v3 offset:3072\n"
        "v_cvt_pk_bf16_f32 v52, v20, v21\n"
        "v_cndmask_b32 v24, v7, v24, s[64:65]\n"
        "v_mfma_f32_16x16x32_bf16 v[44:47], v[80:83], v[112:115], v[12:15]\n"
        "ds_read_b128 v[80:83], v1 offset:20480\n"
        "v_cndmask_b32 v25, v7, v25, s[66:67]\n"
        "v_exp_f32 v24, v24\n"
        "ds_read_b128 a[88:91], v3 offset:4096\n"
        "v_exp_f32 v25, v25\n"
        "v_cvt_pk_bf16_f32 v53, v22, v23\n"
        "v_mfma_f32_16x16x32_bf16 v[44:47], v[84:87], v[116:119], v[44:47]\n"
        "ds_read_b128 v[84:87], v1 offset:21504\n"
        "v_cndmask_b32 v26, v7, v26, s[68:69]\n"
        "v_cndmask_b32 v27, v7, v27, s[70:71]\n"
        "ds_read_b128 a[92:95], v3 offset:5120\n"
        "v_exp_f32 v26, v26\n"
        "v_exp_f32 v27, v27\n"
        "v_mfma_f32_16x16x32_bf16 v[44:47], v[88:91], v[120:123], v[44:47]\n"
        "ds_read_b128 v[88:91], v1 offset:22528\n"
        "v_cvt_pk_bf16_f32 v50, v24, v25\n"
        "v_exp_f32 v28, v28\n"
        "ds_read_b128 a[96:99], v3 offset:6144\n"
        "v_exp_f32 v29, v29\n"
        "v_cvt_pk_bf16_f32 v51, v26, v27\n"
        "v_mfma_f32_16x16x32_bf16 v[44:47], v[92:95], v[124:127], v[44:47]\n"
        "ds_read_b128 v[92:95], v1 offset:23552\n"
        "v_exp_f32 v30, v30\n"
        "v_exp_f32 v31, v31\n"
        "ds_read_b128 a[100:103], v3 offset:7168\n"
        "v_cvt_pk_bf16_f32 v54, v28, v29\n"
        "v_cvt_pk_bf16_f32 v55, v30, v31\n"
        "s_waitcnt lgkmcnt(6)\n"
        "v_mfma_f32_16x16x32_bf16 v[20:23], v[64:67], v[112:115], v[12:15]\n"
        "ds_read_b128 v[64:67], v1 offset:24576\n"
        "v_mfma_f32_16x16x32_bf16 a[0:3], a[72:75], v[48:51], 0\n"
        "v_mfma_f32_16x16x32_bf16 a[4:7], a[72:75], v[52:55], 0\n"
        "ds_read_b128 a[72:75], v3 offset:8192\n"
        "v_exp_f32 v36, v36\n"
        "v_mfma_f32_16x16x32_bf16 v[20:23], v[68:71], v[116:119], v[20:23]\n"
        "ds_read_b128 v[68:71], v1 offset:25600\n"
        "v_mfma_f32_16x16x32_bf16 a[8:11], a[76:79], v[48:51], 0\n"
        "v_exp_f32 v37, v37\n"
        "v_mfma_f32_16x16x32_bf16 a[12:15], a[76:79], v[52:55], 0\n"
        "ds_read_b128 a[76:79], v3 offset:9216\n"
        "v_mfma_f32_16x16x32_bf16 v[20:23], v[72:75], v[120:123], v[20:23]\n"
        "ds_read_b128 v[72:75], v1 offset:26624\n"
        "v_exp_f32 v38, v38\n"
        "v_mfma_f32_16x16x32_bf16 a[16:19], a[80:83], v[48:51], 0\n"
        "v_mfma_f32_16x16x32_bf16 a[20:23], a[80:83], v[52:55], 0\n"
        "ds_read_b128 a[80:83], v3 offset:10240\n"
        "v_exp_f32 v39, v39\n"
        "v_mfma_f32_16x16x32_bf16 v[20:23], v[76:79], v[124:127], v[20:23]\n"
        "ds_read_b128 v[76:79], v1 offset:27648\n"
        "v_mfma_f32_16x16x32_bf16 a[24:27], a[84:87], v[48:51], 0\n"
        "v_cvt_pk_bf16_f32 v60, v36, v37\n"
        "v_mfma_f32_16x16x32_bf16 a[28:31], a[84:87], v[52:55], 0\n"
        "ds_read_b128 a[84:87], v3 offset:11264\n"
        "v_mfma_f32_16x16x32_bf16 v[28:31], v[80:83], v[112:115], v[12:15]\n"
        "v_exp_f32 v44, v44\n"
        "s_waitcnt lgkmcnt(8)\n"
        "s_barrier\n"
        "v_bfe_u32 v5, v0, 4, 2\n"
        "v_lshlrev_b32 v5, 3, v5\n"
        "v_add_u32 v5, 159744, v5\n"
        "ds_read_b32 v4, v5\n"
        "v_mfma_f32_16x16x32_bf16 a[32:35], a[88:91], v[48:51], 0\n"
        "v_mfma_f32_16x16x32_bf16 a[36:39], a[88:91], v[52:55], 0\n"
        "ds_read_b128 a[88:91], v3 offset:12288\n"
        "v_exp_f32 v45, v45\n"
        "v_mfma_f32_16x16x32_bf16 v[28:31], v[84:87], v[116:119], v[28:31]\n"
        "v_mfma_f32_16x16x32_bf16 a[40:43], a[92:95], v[48:51], 0\n"
        "v_cvt_pk_bf16_f32 v61, v38, v39\n"
        "v_mfma_f32_16x16x32_bf16 a[44:47], a[92:95], v[52:55], 0\n"
        "ds_read_b128 a[92:95], v3 offset:13312\n"
        "v_mfma_f32_16x16x32_bf16 v[28:31], v[88:91], v[120:123], v[28:31]\n"
        "v_exp_f32 v46, v46\n"
        "v_mfma_f32_16x16x32_bf16 a[48:51], a[96:99], v[48:51], 0\n"
        "v_mfma_f32_16x16x32_bf16 a[52:55], a[96:99], v[52:55], 0\n"
        "ds_read_b128 a[96:99], v3 offset:14336\n"
        "v_exp_f32 v47, v47\n"
        "v_mfma_f32_16x16x32_bf16 v[28:31], v[92:95], v[124:127], v[28:31]\n"
        "v_mfma_f32_16x16x32_bf16 a[56:59], a[100:103], v[48:51], 0\n"
        "v_cvt_pk_bf16_f32 v62, v44, v45\n"
        "v_mfma_f32_16x16x32_bf16 a[60:63], a[100:103], v[52:55], 0\n"
        "ds_read_b128 a[100:103], v3 offset:15360\n"
        "v_mfma_f32_16x16x32_bf16 a[64:67], a[104:107], v[48:51], 0\n"
        "v_cvt_pk_bf16_f32 v63, v46, v47\n"
        "v_mfma_f32_16x16x32_bf16 a[68:71], a[104:107], v[52:55], 0\n"
        "s_waitcnt lgkmcnt(3)\n"
        "v_mfma_f32_16x16x32_bf16 v[36:39], v[64:67], v[112:115], v[12:15]\n"
        "ds_read_b128 v[64:67], v2 offset:0\n"
        "v_mfma_f32_16x16x32_bf16 a[4:7], a[72:75], v[60:63], a[4:7]\n"
        "ds_read_b128 a[72:75], v3 offset:16384\n"
        "v_mfma_f32_16x16x32_bf16 v[36:39], v[68:71], v[116:119], v[36:39]\n"
        "ds_read_b128 v[68:71], v2 offset:1024\n"
        "v_exp_f32 v20, v20\n"
        "v_mfma_f32_16x16x32_bf16 a[12:15], a[76:79], v[60:63], a[12:15]\n"
        "ds_read_b128 a[76:79], v3 offset:17408\n"
        "v_exp_f32 v21, v21\n"
        "v_mfma_f32_16x16x32_bf16 v[36:39], v[72:75], v[120:123], v[36:39]\n"
        "ds_read_b128 v[72:75], v2 offset:2048\n"
        "v_exp_f32 v22, v22\n"
        "v_mfma_f32_16x16x32_bf16 a[20:23], a[80:83], v[60:63], a[20:23]\n"
        "ds_read_b128 a[80:83], v3 offset:18432\n"
        "v_exp_f32 v23, v23\n"
        "v_mfma_f32_16x16x32_bf16 v[36:39], v[76:79], v[124:127], v[36:39]\n"
        "ds_read_b128 v[76:79], v2 offset:3072\n"
        "v_mfma_f32_16x16x32_bf16 a[28:31], a[84:87], v[60:63], a[28:31]\n"
        "ds_read_b128 a[84:87], v3 offset:19456\n"
        "v_cvt_pk_bf16_f32 v52, v20, v21\n"
        "ds_read_b128 v[80:83], v2 offset:4096\n"
        "v_exp_f32 v28, v28\n"
        "v_mfma_f32_16x16x32_bf16 a[36:39], a[88:91], v[60:63], a[36:39]\n"
        "ds_read_b128 a[88:91], v3 offset:20480\n"
        "v_exp_f32 v29, v29\n"
        "ds_read_b128 v[84:87], v2 offset:5120\n"
        "v_cvt_pk_bf16_f32 v53, v22, v23\n"
        "s_waitcnt lgkmcnt(8)\n"
        "v_mfma_f32_16x16x32_bf16 a[44:47], a[92:95], v[60:63], a[44:47]\n"
        "ds_read_b128 a[92:95], v3 offset:21504\n"
        "ds_read_b128 v[88:91], v2 offset:6144\n"
        "v_exp_f32 v30, v30\n"
        "v_mfma_f32_16x16x32_bf16 a[52:55], a[96:99], v[60:63], a[52:55]\n"
        "ds_read_b128 a[96:99], v3 offset:22528\n"
        "v_exp_f32 v31, v31\n"
        "ds_read_b128 v[92:95], v2 offset:7168\n"
        "v_cvt_pk_bf16_f32 v54, v28, v29\n"
        "v_mfma_f32_16x16x32_bf16 a[60:63], a[100:103], v[60:63], a[60:63]\n"
        "ds_read_b128 a[100:103], v3 offset:23552\n"
        "v_cvt_pk_bf16_f32 v55, v30, v31\n"
        "v_mfma_f32_16x16x32_bf16 a[68:71], a[104:107], v[60:63], a[68:71]\n"
        "s_lshl_b32 s52, s36, 13\n"
        "s_add_u32 s52, s52, 131072\n"
        "v_add_u32 v6, s52, v0\n"
        "ds_read_b128 v[96:99], v6 offset:0\n"
        "ds_read_b128 v[100:103], v6 offset:1024\n"
        "ds_read_b128 v[104:107], v6 offset:2048\n"
        "ds_read_b128 v[108:111], v6 offset:3072\n"
        "ds_read_b128 v[112:115], v6 offset:4096\n"
        "ds_read_b128 v[116:119], v6 offset:5120\n"
        "ds_read_b128 v[120:123], v6 offset:6144\n"
        "ds_read_b128 v[124:127], v6 offset:7168\n"
        "v_mfma_f32_16x16x32_bf16 a[4:7], a[72:75], v[52:55], a[4:7]\n"
        "ds_read_b128 a[72:75], v3 offset:24576\n"
        "s_waitcnt lgkmcnt(11)\n"
        "v_mfma_f32_16x16x32_bf16 a[12:15], a[76:79], v[52:55], a[12:15]\n"
        "ds_read_b128 a[76:79], v3 offset:25600\n"
        "v_mfma_f32_16x16x32_bf16 a[20:23], a[80:83], v[52:55], a[20:23]\n"
        "ds_read_b128 a[80:83], v3 offset:26624\n"
        "v_cndmask_b32 v36, v7, v36, s[64:65]\n"
        "v_cndmask_b32 v37, v7, v37, s[66:67]\n"
        "v_mfma_f32_16x16x32_bf16 a[28:31], a[84:87], v[52:55], a[28:31]\n"
        "ds_read_b128 a[84:87], v3 offset:27648\n"
        "v_exp_f32 v36, v36\n"
        "v_exp_f32 v37, v37\n"
        "s_waitcnt vmcnt(0)\n"
        "s_waitcnt lgkmcnt(4)\n"
        "s_barrier\n"
        "s_cmp_lg_u32 s60, 1\n"
        "s_cbranch_scc1 KA_NCAD1_%=\n"
        "s_cmp_lg_u32 s36, 0\n"
        "s_cbranch_scc1 KA_NCAD1_%=\n"
        "v_cmp_ne_u32 vcc, 0, v4\n"
        "s_nop 4\n"
        "s_cbranch_vccnz KA_NCAD1_%=\n"
        "s_lshr_b32 s88, s37, 2\n"
        "s_lshl_b32 s88, s88, 3\n"
        "s_add_u32 s88, s88, s38\n"
        "s_lshl_b32 s88, s88, 2\n"
        "s_and_b32 s52, s37, 3\n"
        "s_add_u32 s88, s88, s52\n"
        "s_lshl_b32 s88, s88, 7\n"
        "s_add_u32 s88, s88, 65536\n"
        "s_mov_b64 exec, 1\n"
        "v_mov_b32 v6, s88\n"
        "v_mov_b32 v4, s96\n"
        "v_mov_b32 v5, s97\n"
        "global_store_dwordx2 v6, v[4:5], s[86:87] sc1\n"
        "s_mov_b64 exec, -1\n"
        "KA_NCAD1_%=:\n"
        "s_nop 3\n"
        "v_mfma_f32_16x16x32_bf16 a[36:39], a[88:91], v[52:55], a[36:39]\n"
        "ds_read_b128 a[88:91], v3 offset:28672\n"
        "v_cndmask_b32 v38, v7, v38, s[68:69]\n"
        "v_cndmask_b32 v39, v7, v39, s[70:71]\n"
        "v_mfma_f32_16x16x32_bf16 a[44:47], a[92:95], v[52:55], a[44:47]\n"
        "ds_read_b128 a[92:95], v3 offset:29696\n"
        "v_exp_f32 v38, v38\n"
        "v_mfma_f32_16x16x32_bf16 a[52:55], a[96:99], v[52:55], a[52:55]\n"
        "ds_read_b128 a[96:99], v3 offset:30720\n"
        "v_exp_f32 v39, v39\n"
        "v_cvt_pk_bf16_f32 v60, v36, v37\n"
        "v_mfma_f32_16x16x32_bf16 a[60:63], a[100:103], v[52:55], a[60:63]\n"
        "ds_read_b128 a[100:103], v3 offset:31744\n"
        "v_mov_b32 v62, 0\n"
        "v_mov_b32 v63, 0\n"
        "v_mfma_f32_16x16x32_bf16 a[68:71], a[104:107], v[52:55], a[68:71]\n"
        "v_cvt_pk_bf16_f32 v61, v38, v39\n"
        "v_xor_b32 v1, 0x8000, v1\n"
        "v_xor_b32 v2, 0x8000, v2\n"
        "v_xor_b32 v3, 0x8000, v3\n"
        "s_waitcnt lgkmcnt(4)\n"
        "v_mfma_f32_16x16x32_bf16 a[4:7], a[72:75], v[60:63], a[4:7]\n"
        "v_mfma_f32_16x16x32_bf16 a[12:15], a[76:79], v[60:63], a[12:15]\n"
        "v_mfma_f32_16x16x32_bf16 a[20:23], a[80:83], v[60:63], a[20:23]\n"
        "v_mfma_f32_16x16x32_bf16 a[28:31], a[84:87], v[60:63], a[28:31]\n"
        "s_waitcnt lgkmcnt(3)\n"
        "v_mfma_f32_16x16x32_bf16 a[36:39], a[88:91], v[60:63], a[36:39]\n"
        "s_waitcnt lgkmcnt(2)\n"
        "v_mfma_f32_16x16x32_bf16 a[44:47], a[92:95], v[60:63], a[44:47]\n"
        "s_waitcnt lgkmcnt(1)\n"
        "v_mfma_f32_16x16x32_bf16 a[52:55], a[96:99], v[60:63], a[52:55]\n"
        "s_waitcnt lgkmcnt(0)\n"
        "v_mfma_f32_16x16x32_bf16 a[60:63], a[100:103], v[60:63], a[60:63]\n"
        "v_mfma_f32_16x16x32_bf16 a[68:71], a[104:107], v[60:63], a[68:71]\n"
        "s_branch P3_EPI_%=\n"
        "P3_DF2_%=:\n"
        "s_waitcnt lgkmcnt(0)\n"
        "v_mfma_f32_16x16x32_bf16 v[32:35], v[64:67], v[96:99], v[8:11]\n"
        "v_mfma_f32_16x16x32_bf16 v[36:39], v[64:67], v[112:115], v[12:15]\n"
        "ds_read_b128 v[64:67], v1 offset:16384\n"
        "ds_read_b128 a[72:75], v3 offset:0\n"
        "v_exp_f32 v16, v16\n"
        "v_exp_f32 v17, v17\n"
        "v_mfma_f32_16x16x32_bf16 v[32:35], v[68:71], v[100:103], v[32:35]\n"
        "v_exp_f32 v18, v18\n"
        "v_mfma_f32_16x16x32_bf16 v[36:39], v[68:71], v[116:119], v[36:39]\n"
        "ds_read_b128 v[68:71], v1 offset:17408\n"
        "v_exp_f32 v19, v19\n"
        "ds_read_b128 a[76:79], v3 offset:1024\n"
        "v_cvt_pk_bf16_f32 v48, v16, v17\n"
        "v_exp_f32 v20, v20\n"
        "v_mfma_f32_16x16x32_bf16 v[32:35], v[72:75], v[104:107], v[32:35]\n"
        "v_exp_f32 v21, v21\n"
        "v_mfma_f32_16x16x32_bf16 v[36:39], v[72:75], v[120:123], v[36:39]\n"
        "ds_read_b128 v[72:75], v1 offset:18432\n"
        "v_cvt_pk_bf16_f32 v49, v18, v19\n"
        "ds_read_b128 a[80:83], v3 offset:2048\n"
        "v_exp_f32 v22, v22\n"
        "v_exp_f32 v23, v23\n"
        "v_mfma_f32_16x16x32_bf16 v[32:35], v[76:79], v[108:111], v[32:35]\n"
        "v_cvt_pk_bf16_f32 v52, v20, v21\n"
        "v_mfma_f32_16x16x32_bf16 v[36:39], v[76:79], v[124:127], v[36:39]\n"
        "ds_read_b128 v[76:79], v1 offset:19456\n"
        "v_exp_f32 v24, v24\n"
        "ds_read_b128 a[84:87], v3 offset:3072\n"
        "v_exp_f32 v25, v25\n"
        "v_cvt_pk_bf16_f32 v53, v22, v23\n"
        "v_mfma_f32_16x16x32_bf16 v[44:47], v[80:83], v[112:115], v[12:15]\n"
        "ds_read_b128 v[80:83], v1 offset:20480\n"
        "v_exp_f32 v26, v26\n"
        "ds_read_b128 a[88:91], v3 offset:4096\n"
        "v_exp_f32 v27, v27\n"
        "v_mfma_f32_16x16x32_bf16 v[44:47], v[84:87], v[116:119], v[44:47]\n"
        "ds_read_b128 v[84:87], v1 offset:21504\n"
        "v_cvt_pk_bf16_f32 v50, v24, v25\n"
        "v_exp_f32 v28, v28\n"
        "ds_read_b128 a[92:95], v3 offset:5120\n"
        "v_exp_f32 v29, v29\n"
        "v_mfma_f32_16x16x32_bf16 v[44:47], v[88:91], v[120:123], v[44:47]\n"
        "ds_read_b128 v[88:91], v1 offset:22528\n"
        "v_cvt_pk_bf16_f32 v51, v26, v27\n"
        "ds_read_b128 a[96:99], v3 offset:6144\n"
        "v_exp_f32 v30, v30\n"
        "v_exp_f32 v31, v31\n"
        "v_mfma_f32_16x16x32_bf16 v[44:47], v[92:95], v[124:127], v[44:47]\n"
        "ds_read_b128 v[92:95], v1 offset:23552\n"
        "v_cvt_pk_bf16_f32 v54, v28, v29\n"
        "ds_read_b128 a[100:103], v3 offset:7168\n"
        "v_cvt_pk_bf16_f32 v55, v30, v31\n"
        "s_waitcnt lgkmcnt(7)\n"
        "v_mfma_f32_16x16x32_bf16 v[20:23], v[64:67], v[112:115], v[12:15]\n"
        "v_mfma_f32_16x16x32_bf16 a[0:3], a[72:75], v[48:51], 0\n"
        "v_mfma_f32_16x16x32_bf16 a[4:7], a[72:75], v[52:55], 0\n"
        "ds_read_b128 a[72:75], v3 offset:8192\n"
        "v_cndmask_b32 v32, v7, v32, s[64:65]\n"
        "v_mfma_f32_16x16x32_bf16 v[20:23], v[68:71], v[116:119], v[20:23]\n"
        "v_cndmask_b32 v33, v7, v33, s[66:67]\n"
        "v_mfma_f32_16x16x32_bf16 a[8:11], a[76:79], v[48:51], 0\n"
        "v_exp_f32 v32, v32\n"
        "v_mfma_f32_16x16x32_bf16 a[12:15], a[76:79], v[52:55], 0\n"
        "ds_read_b128 a[76:79], v3 offset:9216\n"
        "v_exp_f32 v33, v33\n"
        "v_mfma_f32_16x16x32_bf16 v[20:23], v[72:75], v[120:123], v[20:23]\n"
        "v_cndmask_b32 v34, v7, v34, s[68:69]\n"
        "v_mfma_f32_16x16x32_bf16 a[16:19], a[80:83], v[48:51], 0\n"
        "v_cndmask_b32 v35, v7, v35, s[70:71]\n"
        "v_mfma_f32_16x16x32_bf16 a[20:23], a[80:83], v[52:55], 0\n"
        "ds_read_b128 a[80:83], v3 offset:10240\n"
        "v_exp_f32 v34, v34\n"
        "v_mfma_f32_16x16x32_bf16 v[20:23], v[76:79], v[124:127], v[20:23]\n"
        "v_exp_f32 v35, v35\n"
        "v_mfma_f32_16x16x32_bf16 a[24:27], a[84:87], v[48:51], 0\n"
        "v_cvt_pk_bf16_f32 v56, v32, v33\n"
        "v_mfma_f32_16x16x32_bf16 a[28:31], a[84:87], v[52:55], 0\n"
        "ds_read_b128 a[84:87], v3 offset:11264\n"
        "v_exp_f32 v36, v36\n"
        "v_mfma_f32_16x16x32_bf16 v[28:31], v[80:83], v[112:115], v[12:15]\n"
        "v_exp_f32 v37, v37\n"
        "s_waitcnt lgkmcnt(4)\n"
        "s_barrier\n"
        "v_bfe_u32 v5, v0, 4, 2\n"
        "v_lshlrev_b32 v5, 3, v5\n"
        "v_add_u32 v5, 159744, v5\n"
        "ds_read_b32 v4, v5\n"
        "v_mfma_f32_16x16x32_bf16 a[32:35], a[88:91], v[48:51], 0\n"
        "v_cvt_pk_bf16_f32 v57, v34, v35\n"
        "v_mfma_f32_16x16x32_bf16 a[36:39], a[88:91], v[52:55], 0\n"
        "ds_read_b128 a[88:91], v3 offset:12288\n"
        "v_exp_f32 v38, v38\n"
        "v_mfma_f32_16x16x32_bf16 v[28:31], v[84:87], v[116:119], v[28:31]\n"
        "v_exp_f32 v39, v39\n"
        "v_mfma_f32_16x16x32_bf16 a[40:43], a[92:95], v[48:51], 0\n"
        "v_cvt_pk_bf16_f32 v60, v36, v37\n"
        "v_mfma_f32_16x16x32_bf16 a[44:47], a[92:95], v[52:55], 0\n"
        "ds_read_b128 a[92:95], v3 offset:13312\n"
        "v_mov_b32 v58, 0\n"
        "v_mfma_f32_16x16x32_bf16 v[28:31], v[88:91], v[120:123], v[28:31]\n"
        "v_mov_b32 v59, 0\n"
        "v_mfma_f32_16x16x32_bf16 a[48:51], a[96:99], v[48:51], 0\n"
        "v_exp_f32 v44, v44\n"
        "v_mfma_f32_16x16x32_bf16 a[52:55], a[96:99], v[52:55], 0\n"
        "ds_read_b128 a[96:99], v3 offset:14336\n"
        "v_exp_f32 v45, v45\n"
        "v_mfma_f32_16x16x32_bf16 v[28:31], v[92:95], v[124:127], v[28:31]\n"
        "v_cvt_pk_bf16_f32 v61, v38, v39\n"
        "v_mfma_f32_16x16x32_bf16 a[56:59], a[100:103], v[48:51], 0\n"
        "v_exp_f32 v46, v46\n"
        "v_mfma_f32_16x16x32_bf16 a[60:63], a[100:103], v[52:55], 0\n"
        "ds_read_b128 a[100:103], v3 offset:15360\n"
        "v_exp_f32 v47, v47\n"
        "v_mfma_f32_16x16x32_bf16 a[64:67], a[104:107], v[48:51], 0\n"
        "v_cvt_pk_bf16_f32 v62, v44, v45\n"
        "v_mfma_f32_16x16x32_bf16 a[68:71], a[104:107], v[52:55], 0\n"
        "v_cvt_pk_bf16_f32 v63, v46, v47\n"
        "ds_read_b128 v[64:67], v2 offset:0\n"
        "s_waitcnt lgkmcnt(3)\n"
        "v_mfma_f32_16x16x32_bf16 a[0:3], a[72:75], v[56:59], a[0:3]\n"
        "v_mfma_f32_16x16x32_bf16 a[4:7], a[72:75], v[60:63], a[4:7]\n"
        "ds_read_b128 a[72:75], v3 offset:16384\n"
        "v_exp_f32 v20, v20\n"
        "ds_read_b128 v[68:71], v2 offset:1024\n"
        "v_exp_f32 v21, v21\n"
        "v_mfma_f32_16x16x32_bf16 a[8:11], a[76:79], v[56:59], a[8:11]\n"
        "v_mfma_f32_16x16x32_bf16 a[12:15], a[76:79], v[60:63], a[12:15]\n"
        "ds_read_b128 a[76:79], v3 offset:17408\n"
        "v_exp_f32 v22, v22\n"
        "ds_read_b128 v[72:75], v2 offset:2048\n"
        "v_exp_f32 v23, v23\n"
        "v_mfma_f32_16x16x32_bf16 a[16:19], a[80:83], v[56:59], a[16:19]\n"
        "v_mfma_f32_16x16x32_bf16 a[20:23], a[80:83], v[60:63], a[20:23]\n"
        "ds_read_b128 a[80:83], v3 offset:18432\n"
        "v_cvt_pk_bf16_f32 v52, v20, v21\n"
        "ds_read_b128 v[76:79], v2 offset:3072\n"
        "v_cndmask_b32 v28, v7, v28, s[64:65]\n"
        "v_mfma_f32_16x16x32_bf16 a[24:27], a[84:87], v[56:59], a[24:27]\n"
        "v_mfma_f32_16x16x32_bf16 a[28:31], a[84:87], v[60:63], a[28:31]\n"
        "ds_read_b128 a[84:87], v3 offset:19456\n"
        "v_cndmask_b32 v29, v7, v29, s[66:67]\n"
        "ds_read_b128 v[80:83], v2 offset:4096\n"
        "v_exp_f32 v28, v28\n"
        "v_mfma_f32_16x16x32_bf16 a[32:35], a[88:91], v[56:59], a[32:35]\n"
        "v_mfma_f32_16x16x32_bf16 a[36:39], a[88:91], v[60:63], a[36:39]\n"
        "ds_read_b128 a[88:91], v3 offset:20480\n"
        "v_exp_f32 v29, v29\n"
        "ds_read_b128 v[84:87], v2 offset:5120\n"
        "v_cvt_pk_bf16_f32 v53, v22, v23\n"
        "v_mfma_f32_16x16x32_bf16 a[40:43], a[92:95], v[56:59], a[40:43]\n"
        "v_mfma_f32_16x16x32_bf16 a[44:47], a[92:95], v[60:63], a[44:47]\n"
        "ds_read_b128 a[92:95], v3 offset:21504\n"
        "v_cndmask_b32 v30, v7, v30, s[68:69]\n"
        "ds_read_b128 v[88:91], v2 offset:6144\n"
        "v_cndmask_b32 v31, v7, v31, s[70:71]\n"
        "s_waitcnt lgkmcnt(7)\n"
        "v_mfma_f32_16x16x32_bf16 a[48:51], a[96:99], v[56:59], a[48:51]\n"
        "v_mfma_f32_16x16x32_bf16 a[52:55], a[96:99], v[60:63], a[52:55]\n"
        "ds_read_b128 a[96:99], v3 offset:22528\n"
        "v_exp_f32 v30, v30\n"
        "ds_read_b128 v[92:95], v2 offset:7168\n"
        "v_exp_f32 v31, v31\n"
        "v_mfma_f32_16x16x32_bf16 a[56:59], a[100:103], v[56:59], a[56:59]\n"
        "v_mfma_f32_16x16x32_bf16 a[60:63], a[100:103], v[60:63], a[60:63]\n"
        "ds_read_b128 a[100:103], v3 offset:23552\n"
        "v_cvt_pk_bf16_f32 v54, v28, v29\n"
        "v_mfma_f32_16x16x32_bf16 a[64:67], a[104:107], v[56:59], a[64:67]\n"
        "v_cvt_pk_bf16_f32 v55, v30, v31\n"
        "v_mfma_f32_16x16x32_bf16 a[68:71], a[104:107], v[60:63], a[68:71]\n"
        "s_lshl_b32 s52, s36, 13\n"
        "s_add_u32 s52, s52, 131072\n"
        "v_add_u32 v6, s52, v0\n"
        "ds_read_b128 v[96:99], v6 offset:0\n"
        "ds_read_b128 v[100:103], v6 offset:1024\n"
        "ds_read_b128 v[104:107], v6 offset:2048\n"
        "ds_read_b128 v[108:111], v6 offset:3072\n"
        "ds_read_b128 v[112:115], v6 offset:4096\n"
        "ds_read_b128 v[116:119], v6 offset:5120\n"
        "ds_read_b128 v[120:123], v6 offset:6144\n"
        "ds_read_b128 v[124:127], v6 offset:7168\n"
        "v_mfma_f32_16x16x32_bf16 a[4:7], a[72:75], v[52:55], a[4:7]\n"
        "v_mfma_f32_16x16x32_bf16 a[12:15], a[76:79], v[52:55], a[12:15]\n"
        "v_mfma_f32_16x16x32_bf16 a[20:23], a[80:83], v[52:55], a[20:23]\n"
        "s_waitcnt lgkmcnt(9)\n"
        "v_mfma_f32_16x16x32_bf16 a[28:31], a[84:87], v[52:55], a[28:31]\n"
        "s_waitcnt vmcnt(0)\n"
        "s_waitcnt lgkmcnt(0)\n"
        "s_barrier\n"
        "s_cmp_lg_u32 s60, 1\n"
        "s_cbranch_scc1 KA_NCAD2_%=\n"
        "s_cmp_lg_u32 s36, 0\n"
        "s_cbranch_scc1 KA_NCAD2_%=\n"
        "v_cmp_ne_u32 vcc, 0, v4\n"
        "s_nop 4\n"
        "s_cbranch_vccnz KA_NCAD2_%=\n"
        "s_lshr_b32 s88, s37, 2\n"
        "s_lshl_b32 s88, s88, 3\n"
        "s_add_u32 s88, s88, s38\n"
        "s_lshl_b32 s88, s88, 2\n"
        "s_and_b32 s52, s37, 3\n"
        "s_add_u32 s88, s88, s52\n"
        "s_lshl_b32 s88, s88, 7\n"
        "s_add_u32 s88, s88, 65536\n"
        "s_mov_b64 exec, 1\n"
        "v_mov_b32 v6, s88\n"
        "v_mov_b32 v4, s96\n"
        "v_mov_b32 v5, s97\n"
        "global_store_dwordx2 v6, v[4:5], s[86:87] sc1\n"
        "s_mov_b64 exec, -1\n"
        "KA_NCAD2_%=:\n"
        "s_nop 3\n"
        "v_mfma_f32_16x16x32_bf16 a[36:39], a[88:91], v[52:55], a[36:39]\n"
        "v_mfma_f32_16x16x32_bf16 a[44:47], a[92:95], v[52:55], a[44:47]\n"
        "v_mfma_f32_16x16x32_bf16 a[52:55], a[96:99], v[52:55], a[52:55]\n"
        "v_mfma_f32_16x16x32_bf16 a[60:63], a[100:103], v[52:55], a[60:63]\n"
        "v_mfma_f32_16x16x32_bf16 a[68:71], a[104:107], v[52:55], a[68:71]\n"
        "v_xor_b32 v1, 0x8000, v1\n"
        "v_xor_b32 v2, 0x8000, v2\n"
        "v_xor_b32 v3, 0x8000, v3\n"
        "s_branch P3_EPI_%=\n"
        "P3_DF3_%=:\n"
        "s_waitcnt lgkmcnt(0)\n"
        "v_mfma_f32_16x16x32_bf16 v[32:35], v[64:67], v[96:99], v[8:11]\n"
        "v_mfma_f32_16x16x32_bf16 v[36:39], v[64:67], v[112:115], v[12:15]\n"
        "ds_read_b128 v[64:67], v1 offset:16384\n"
        "ds_read_b128 a[72:75], v3 offset:0\n"
        "v_exp_f32 v16, v16\n"
        "v_exp_f32 v17, v17\n"
        "v_mfma_f32_16x16x32_bf16 v[32:35], v[68:71], v[100:103], v[32:35]\n"
        "v_exp_f32 v18, v18\n"
        "v_mfma_f32_16x16x32_bf16 v[36:39], v[68:71], v[116:119], v[36:39]\n"
        "ds_read_b128 v[68:71], v1 offset:17408\n"
        "v_exp_f32 v19, v19\n"
        "ds_read_b128 a[76:79], v3 offset:1024\n"
        "v_cvt_pk_bf16_f32 v48, v16, v17\n"
        "v_mfma_f32_16x16x32_bf16 v[32:35], v[72:75], v[104:107], v[32:35]\n"
        "v_exp_f32 v20, v20\n"
        "v_mfma_f32_16x16x32_bf16 v[36:39], v[72:75], v[120:123], v[36:39]\n"
        "ds_read_b128 v[72:75], v1 offset:18432\n"
        "v_exp_f32 v21, v21\n"
        "ds_read_b128 a[80:83], v3 offset:2048\n"
        "v_cvt_pk_bf16_f32 v49, v18, v19\n"
        "v_mfma_f32_16x16x32_bf16 v[32:35], v[76:79], v[108:111], v[32:35]\n"
        "v_exp_f32 v22, v22\n"
        "v_mfma_f32_16x16x32_bf16 v[36:39], v[76:79], v[124:127], v[36:39]\n"
        "ds_read_b128 v[76:79], v1 offset:19456\n"
        "v_exp_f32 v23, v23\n"
        "ds_read_b128 a[84:87], v3 offset:3072\n"
        "v_cvt_pk_bf16_f32 v52, v20, v21\n"
        "v_mfma_f32_16x16x32_bf16 v[40:43], v[80:83], v[96:99], v[8:11]\n"
        "v_exp_f32 v24, v24\n"
        "v_mfma_f32_16x16x32_bf16 v[44:47], v[80:83], v[112:115], v[12:15]\n"
        "v_exp_f32 v25, v25\n"
        "v_cvt_pk_bf16_f32 v53, v22, v23\n"
        "ds_read_b128 a[88:91], v3 offset:4096\n"
        "v_exp_f32 v26, v26\n"
        "v_mfma_f32_16x16x32_bf16 v[40:43], v[84:87], v[100:103], v[40:43]\n"
        "v_exp_f32 v27, v27\n"
        "v_mfma_f32_16x16x32_bf16 v[44:47], v[84:87], v[116:119], v[44:47]\n"
        "v_cvt_pk_bf16_f32 v50, v24, v25\n"
        "ds_read_b128 a[92:95], v3 offset:5120\n"
        "v_exp_f32 v28, v28\n"
        "v_mfma_f32_16x16x32_bf16 v[40:43], v[88:91], v[104:107], v[40:43]\n"
        "v_exp_f32 v29, v29\n"
        "v_mfma_f32_16x16x32_bf16 v[44:47], v[88:91], v[120:123], v[44:47]\n"
        "v_cvt_pk_bf16_f32 v51, v26, v27\n"
        "ds_read_b128 a[96:99], v3 offset:6144\n"
        "v_exp_f32 v30, v30\n"
        "v_mfma_f32_16x16x32_bf16 v[40:43], v[92:95], v[108:111], v[40:43]\n"
        "v_exp_f32 v31, v31\n"
        "v_mfma_f32_16x16x32_bf16 v[44:47], v[92:95], v[124:127], v[44:47]\n"
        "v_cvt_pk_bf16_f32 v54, v28, v29\n"
        "ds_read_b128 a[100:103], v3 offset:7168\n"
        "v_cvt_pk_bf16_f32 v55, v30, v31\n"
        "s_waitcnt lgkmcnt(4)\n"
        "v_mfma_f32_16x16x32_bf16 v[20:23], v[64:67], v[112:115], v[12:15]\n"
        "v_mfma_f32_16x16x32_bf16 a[0:3], a[72:75], v[48:51], 0\n"
        "v_mfma_f32_16x16x32_bf16 a[4:7], a[72:75], v[52:55], 0\n"
        "ds_read_b128 a[72:75], v3 offset:8192\n"
        "v_exp_f32 v32, v32\n"
        "v_exp_f32 v33, v33\n"
        "v_mfma_f32_16x16x32_bf16 v[20:23], v[68:71], v[116:119], v[20:23]\n"
        "v_exp_f32 v34, v34\n"
        "v_mfma_f32_16x16x32_bf16 a[8:11], a[76:79], v[48:51], 0\n"
        "v_exp_f32 v35, v35\n"
        "v_cvt_pk_bf16_f32 v56, v32, v33\n"
        "v_mfma_f32_16x16x32_bf16 a[12:15], a[76:79], v[52:55], 0\n"
        "ds_read_b128 a[76:79], v3 offset:9216\n"
        "v_exp_f32 v36, v36\n"
        "v_mfma_f32_16x16x32_bf16 v[20:23], v[72:75], v[120:123], v[20:23]\n"
        "v_exp_f32 v37, v37\n"
        "v_mfma_f32_16x16x32_bf16 a[16:19], a[80:83], v[48:51], 0\n"
        "v_cvt_pk_bf16_f32 v57, v34, v35\n"
        "v_exp_f32 v38, v38\n"
        "v_mfma_f32_16x16x32_bf16 a[20:23], a[80:83], v[52:55], 0\n"
        "ds_read_b128 a[80:83], v3 offset:10240\n"
        "v_exp_f32 v39, v39\n"
        "v_mfma_f32_16x16x32_bf16 v[20:23], v[76:79], v[124:127], v[20:23]\n"
        "v_cvt_pk_bf16_f32 v60, v36, v37\n"
        "v_cndmask_b32 v40, v7, v40, s[64:65]\n"
        "v_mfma_f32_16x16x32_bf16 a[24:27], a[84:87], v[48:51], 0\n"
        "v_cndmask_b32 v41, v7, v41, s[66:67]\n"
        "s_waitcnt lgkmcnt(3)\n"
        "s_barrier\n"
        "v_bfe_u32 v5, v0, 4, 2\n"
        "v_lshlrev_b32 v5, 3, v5\n"
        "v_add_u32 v5, 159744, v5\n"
        "ds_read_b32 v4, v5\n"
        "v_mfma_f32_16x16x32_bf16 a[28:31], a[84:87], v[52:55], 0\n"
        "ds_read_b128 a[84:87], v3 offset:11264\n"
        "v_exp_f32 v40, v40\n"
        "v_mfma_f32_16x16x32_bf16 a[32:35], a[88:91], v[48:51], 0\n"
        "v_exp_f32 v41, v41\n"
        "v_cvt_pk_bf16_f32 v61, v38, v39\n"
        "v_mfma_f32_16x16x32_bf16 a[36:39], a[88:91], v[52:55], 0\n"
        "ds_read_b128 a[88:91], v3 offset:12288\n"
        "v_cndmask_b32 v42, v7, v42, s[68:69]\n"
        "v_mfma_f32_16x16x32_bf16 a[40:43], a[92:95], v[48:51], 0\n"
        "v_cndmask_b32 v43, v7, v43, s[70:71]\n"
        "v_exp_f32 v42, v42\n"
        "v_mfma_f32_16x16x32_bf16 a[44:47], a[92:95], v[52:55], 0\n"
        "ds_read_b128 a[92:95], v3 offset:13312\n"
        "v_exp_f32 v43, v43\n"
        "v_mfma_f32_16x16x32_bf16 a[48:51], a[96:99], v[48:51], 0\n"
        "v_cvt_pk_bf16_f32 v58, v40, v41\n"
        "v_mfma_f32_16x16x32_bf16 a[52:55], a[96:99], v[52:55], 0\n"
        "ds_read_b128 a[96:99], v3 offset:14336\n"
        "v_exp_f32 v44, v44\n"
        "v_exp_f32 v45, v45\n"
        "v_mfma_f32_16x16x32_bf16 a[56:59], a[100:103], v[48:51], 0\n"
        "v_cvt_pk_bf16_f32 v59, v42, v43\n"
        "v_mfma_f32_16x16x32_bf16 a[60:63], a[100:103], v[52:55], 0\n"
        "ds_read_b128 a[100:103], v3 offset:15360\n"
        "v_exp_f32 v46, v46\n"
        "v_exp_f32 v47, v47\n"
        "v_mfma_f32_16x16x32_bf16 a[64:67], a[104:107], v[48:51], 0\n"
        "v_cvt_pk_bf16_f32 v62, v44, v45\n"
        "v_mfma_f32_16x16x32_bf16 a[68:71], a[104:107], v[52:55], 0\n"
        "v_cvt_pk_bf16_f32 v63, v46, v47\n"
        "ds_read_b128 v[64:67], v2 offset:0\n"
        "s_waitcnt lgkmcnt(4)\n"
        "v_mfma_f32_16x16x32_bf16 a[0:3], a[72:75], v[56:59], a[0:3]\n"
        "v_mfma_f32_16x16x32_bf16 a[4:7], a[72:75], v[60:63], a[4:7]\n"
        "ds_read_b128 a[72:75], v3 offset:16384\n"
        "v_cndmask_b32 v20, v7, v20, s[64:65]\n"
        "ds_read_b128 v[68:71], v2 offset:1024\n"
        "v_mfma_f32_16x16x32_bf16 a[8:11], a[76:79], v[56:59], a[8:11]\n"
        "v_cndmask_b32 v21, v7, v21, s[66:67]\n"
        "v_mfma_f32_16x16x32_bf16 a[12:15], a[76:79], v[60:63], a[12:15]\n"
        "ds_read_b128 a[76:79], v3 offset:17408\n"
        "ds_read_b128 v[72:75], v2 offset:2048\n"
        "v_exp_f32 v20, v20\n"
        "v_mfma_f32_16x16x32_bf16 a[16:19], a[80:83], v[56:59], a[16:19]\n"
        "v_mfma_f32_16x16x32_bf16 a[20:23], a[80:83], v[60:63], a[20:23]\n"
        "ds_read_b128 a[80:83], v3 offset:18432\n"
        "v_exp_f32 v21, v21\n"
        "ds_read_b128 v[76:79], v2 offset:3072\n"
        "v_mfma_f32_16x16x32_bf16 a[24:27], a[84:87], v[56:59], a[24:27]\n"
        "v_cndmask_b32 v22, v7, v22, s[68:69]\n"
        "v_mfma_f32_16x16x32_bf16 a[28:31], a[84:87], v[60:63], a[28:31]\n"
        "ds_read_b128 a[84:87], v3 offset:19456\n"
        "ds_read_b128 v[80:83], v2 offset:4096\n"
        "v_cndmask_b32 v23, v7, v23, s[70:71]\n"
        "v_mfma_f32_16x16x32_bf16 a[32:35], a[88:91], v[56:59], a[32:35]\n"
        "v_mfma_f32_16x16x32_bf16 a[36:39], a[88:91], v[60:63], a[36:39]\n"
        "ds_read_b128 a[88:91], v3 offset:20480\n"
        "v_exp_f32 v22, v22\n"
        "ds_read_b128 v[84:87], v2 offset:5120\n"
        "s_waitcnt lgkmcnt(8)\n"
        "v_mfma_f32_16x16x32_bf16 a[40:43], a[92:95], v[56:59], a[40:43]\n"
        "v_exp_f32 v23, v23\n"
        "v_mfma_f32_16x16x32_bf16 a[44:47], a[92:95], v[60:63], a[44:47]\n"
        "ds_read_b128 a[92:95], v3 offset:21504\n"
        "ds_read_b128 v[88:91], v2 offset:6144\n"
        "v_cvt_pk_bf16_f32 v52, v20, v21\n"
        "v_mfma_f32_16x16x32_bf16 a[48:51], a[96:99], v[56:59], a[48:51]\n"
        "v_mfma_f32_16x16x32_bf16 a[52:55], a[96:99], v[60:63], a[52:55]\n"
        "ds_read_b128 a[96:99], v3 offset:22528\n"
        "v_mov_b32 v54, 0\n"
        "ds_read_b128 v[92:95], v2 offset:7168\n"
        "v_mfma_f32_16x16x32_bf16 a[56:59], a[100:103], v[56:59], a[56:59]\n"
        "v_mov_b32 v55, 0\n"
        "v_mfma_f32_16x16x32_bf16 a[60:63], a[100:103], v[60:63], a[60:63]\n"
        "ds_read_b128 a[100:103], v3 offset:23552\n"
        "v_mfma_f32_16x16x32_bf16 a[64:67], a[104:107], v[56:59], a[64:67]\n"
        "v_cvt_pk_bf16_f32 v53, v22, v23\n"
        "v_mfma_f32_16x16x32_bf16 a[68:71], a[104:107], v[60:63], a[68:71]\n"
        "s_lshl_b32 s52, s36, 13\n"
        "s_add_u32 s52, s52, 131072\n"
        "v_add_u32 v6, s52, v0\n"
        "ds_read_b128 v[96:99], v6 offset:0\n"
        "ds_read_b128 v[100:103], v6 offset:1024\n"
        "ds_read_b128 v[104:107], v6 offset:2048\n"
        "ds_read_b128 v[108:111], v6 offset:3072\n"
        "ds_read_b128 v[112:115], v6 offset:4096\n"
        "ds_read_b128 v[116:119], v6 offset:5120\n"
        "ds_read_b128 v[120:123], v6 offset:6144\n"
        "ds_read_b128 v[124:127], v6 offset:7168\n"
        "v_mfma_f32_16x16x32_bf16 a[4:7], a[72:75], v[52:55], a[4:7]\n"
        "s_waitcnt lgkmcnt(10)\n"
        "v_mfma_f32_16x16x32_bf16 a[12:15], a[76:79], v[52:55], a[12:15]\n"
        "v_mfma_f32_16x16x32_bf16 a[20:23], a[80:83], v[52:55], a[20:23]\n"
        "v_mfma_f32_16x16x32_bf16 a[28:31], a[84:87], v[52:55], a[28:31]\n"
        "s_waitcnt vmcnt(0)\n"
        "s_waitcnt lgkmcnt(0)\n"
        "s_barrier\n"
        "s_cmp_lg_u32 s60, 1\n"
        "s_cbranch_scc1 KA_NCAD3_%=\n"
        "s_cmp_lg_u32 s36, 0\n"
        "s_cbranch_scc1 KA_NCAD3_%=\n"
        "v_cmp_ne_u32 vcc, 0, v4\n"
        "s_nop 4\n"
        "s_cbranch_vccnz KA_NCAD3_%=\n"
        "s_lshr_b32 s88, s37, 2\n"
        "s_lshl_b32 s88, s88, 3\n"
        "s_add_u32 s88, s88, s38\n"
        "s_lshl_b32 s88, s88, 2\n"
        "s_and_b32 s52, s37, 3\n"
        "s_add_u32 s88, s88, s52\n"
        "s_lshl_b32 s88, s88, 7\n"
        "s_add_u32 s88, s88, 65536\n"
        "s_mov_b64 exec, 1\n"
        "v_mov_b32 v6, s88\n"
        "v_mov_b32 v4, s96\n"
        "v_mov_b32 v5, s97\n"
        "global_store_dwordx2 v6, v[4:5], s[86:87] sc1\n"
        "s_mov_b64 exec, -1\n"
        "KA_NCAD3_%=:\n"
        "s_nop 3\n"
        "v_mfma_f32_16x16x32_bf16 a[36:39], a[88:91], v[52:55], a[36:39]\n"
        "v_mfma_f32_16x16x32_bf16 a[44:47], a[92:95], v[52:55], a[44:47]\n"
        "v_mfma_f32_16x16x32_bf16 a[52:55], a[96:99], v[52:55], a[52:55]\n"
        "v_mfma_f32_16x16x32_bf16 a[60:63], a[100:103], v[52:55], a[60:63]\n"
        "v_mfma_f32_16x16x32_bf16 a[68:71], a[104:107], v[52:55], a[68:71]\n"
        "v_xor_b32 v1, 0x8000, v1\n"
        "v_xor_b32 v2, 0x8000, v2\n"
        "v_xor_b32 v3, 0x8000, v3\n"
        "s_branch P3_EPI_%=\n"
        "P3_EPI_%=:\n"
        "s_nop 7\n"
        "s_nop 7\n"
        "v_accvgpr_read_b32 v4, a64\n"
        "v_accvgpr_read_b32 v5, a68\n"
        "v_mov_b32 v6, 0x5f800000\n"
        "v_cmp_nle_f32 vcc, v4, v6\n"
        "s_nop 1\n"
        "s_or_b64 s[80:81], s[80:81], vcc\n"
        "v_cmp_nle_f32 vcc, v5, v6\n"
        "s_nop 1\n"
        "s_or_b64 s[80:81], s[80:81], vcc\n"
        "v_rcp_f32 v4, v4\n"
        "v_rcp_f32 v5, v5\n"
        "v_accvgpr_read_b32 v16, a0\n"
        "v_accvgpr_read_b32 v17, a1\n"
        "v_accvgpr_read_b32 v18, a2\n"
        "v_accvgpr_read_b32 v19, a3\n"
        "v_accvgpr_read_b32 v20, a8\n"
        "v_accvgpr_read_b32 v21, a9\n"
        "v_accvgpr_read_b32 v22, a10\n"
        "v_accvgpr_read_b32 v23, a11\n"
        "v_mul_f32 v16, v16, v4\n"
        "v_mul_f32 v17, v17, v4\n"
        "v_mul_f32 v18, v18, v4\n"
        "v_mul_f32 v19, v19, v4\n"
        "v_mul_f32 v20, v20, v4\n"
        "v_mul_f32 v21, v21, v4\n"
        "v_mul_f32 v22, v22, v4\n"
        "v_mul_f32 v23, v23, v4\n"
        "v_cvt_pk_bf16_f32 v24, v16, v17\n"
        "v_cvt_pk_bf16_f32 v25, v18, v19\n"
        "v_cvt_pk_bf16_f32 v26, v20, v21\n"
        "v_cvt_pk_bf16_f32 v27, v22, v23\n"
        "buffer_store_dwordx4 v[24:27], v0, s[48:51], s61 offen offset:0 sc1\n"
        "v_accvgpr_read_b32 v28, a16\n"
        "v_accvgpr_read_b32 v29, a17\n"
        "v_accvgpr_read_b32 v30, a18\n"
        "v_accvgpr_read_b32 v31, a19\n"
        "v_accvgpr_read_b32 v32, a24\n"
        "v_accvgpr_read_b32 v33, a25\n"
        "v_accvgpr_read_b32 v34, a26\n"
        "v_accvgpr_read_b32 v35, a27\n"
        "v_mul_f32 v28, v28, v4\n"
        "v_mul_f32 v29, v29, v4\n"
        "v_mul_f32 v30, v30, v4\n"
        "v_mul_f32 v31, v31, v4\n"
        "v_mul_f32 v32, v32, v4\n"
        "v_mul_f32 v33, v33, v4\n"
        "v_mul_f32 v34, v34, v4\n"
        "v_mul_f32 v35, v35, v4\n"
        "v_cvt_pk_bf16_f32 v36, v28, v29\n"
        "v_cvt_pk_bf16_f32 v37, v30, v31\n"
        "v_cvt_pk_bf16_f32 v38, v32, v33\n"
        "v_cvt_pk_bf16_f32 v39, v34, v35\n"
        "buffer_store_dwordx4 v[36:39], v0, s[48:51], s61 offen offset:1024 sc1\n"
        "v_accvgpr_read_b32 v40, a32\n"
        "v_accvgpr_read_b32 v41, a33\n"
        "v_accvgpr_read_b32 v42, a34\n"
        "v_accvgpr_read_b32 v43, a35\n"
        "v_accvgpr_read_b32 v44, a40\n"
        "v_accvgpr_read_b32 v45, a41\n"
        "v_accvgpr_read_b32 v46, a42\n"
        "v_accvgpr_read_b32 v47, a43\n"
        "v_mul_f32 v40, v40, v4\n"
        "v_mul_f32 v41, v41, v4\n"
        "v_mul_f32 v42, v42, v4\n"
        "v_mul_f32 v43, v43, v4\n"
        "v_mul_f32 v44, v44, v4\n"
        "v_mul_f32 v45, v45, v4\n"
        "v_mul_f32 v46, v46, v4\n"
        "v_mul_f32 v47, v47, v4\n"
        "v_cvt_pk_bf16_f32 v48, v40, v41\n"
        "v_cvt_pk_bf16_f32 v49, v42, v43\n"
        "v_cvt_pk_bf16_f32 v50, v44, v45\n"
        "v_cvt_pk_bf16_f32 v51, v46, v47\n"
        "buffer_store_dwordx4 v[48:51], v0, s[48:51], s61 offen offset:2048 sc1\n"
        "v_accvgpr_read_b32 v52, a48\n"
        "v_accvgpr_read_b32 v53, a49\n"
        "v_accvgpr_read_b32 v54, a50\n"
        "v_accvgpr_read_b32 v55, a51\n"
        "v_accvgpr_read_b32 v56, a56\n"
        "v_accvgpr_read_b32 v57, a57\n"
        "v_accvgpr_read_b32 v58, a58\n"
        "v_accvgpr_read_b32 v59, a59\n"
        "v_mul_f32 v52, v52, v4\n"
        "v_mul_f32 v53, v53, v4\n"
        "v_mul_f32 v54, v54, v4\n"
        "v_mul_f32 v55, v55, v4\n"
        "v_mul_f32 v56, v56, v4\n"
        "v_mul_f32 v57, v57, v4\n"
        "v_mul_f32 v58, v58, v4\n"
        "v_mul_f32 v59, v59, v4\n"
        "v_cvt_pk_bf16_f32 v60, v52, v53\n"
        "v_cvt_pk_bf16_f32 v61, v54, v55\n"
        "v_cvt_pk_bf16_f32 v62, v56, v57\n"
        "v_cvt_pk_bf16_f32 v63, v58, v59\n"
        "buffer_store_dwordx4 v[60:63], v0, s[48:51], s61 offen offset:3072 sc1\n"
        "v_accvgpr_read_b32 v16, a4\n"
        "v_accvgpr_read_b32 v17, a5\n"
        "v_accvgpr_read_b32 v18, a6\n"
        "v_accvgpr_read_b32 v19, a7\n"
        "v_accvgpr_read_b32 v20, a12\n"
        "v_accvgpr_read_b32 v21, a13\n"
        "v_accvgpr_read_b32 v22, a14\n"
        "v_accvgpr_read_b32 v23, a15\n"
        "v_mul_f32 v16, v16, v5\n"
        "v_mul_f32 v17, v17, v5\n"
        "v_mul_f32 v18, v18, v5\n"
        "v_mul_f32 v19, v19, v5\n"
        "v_mul_f32 v20, v20, v5\n"
        "v_mul_f32 v21, v21, v5\n"
        "v_mul_f32 v22, v22, v5\n"
        "v_mul_f32 v23, v23, v5\n"
        "v_cvt_pk_bf16_f32 v24, v16, v17\n"
        "v_cvt_pk_bf16_f32 v25, v18, v19\n"
        "v_cvt_pk_bf16_f32 v26, v20, v21\n"
        "v_cvt_pk_bf16_f32 v27, v22, v23\n"
        "buffer_store_dwordx4 v[24:27], v0, s[48:51], s62 offen offset:0 sc1\n"
        "v_accvgpr_read_b32 v28, a20\n"
        "v_accvgpr_read_b32 v29, a21\n"
        "v_accvgpr_read_b32 v30, a22\n"
        "v_accvgpr_read_b32 v31, a23\n"
        "v_accvgpr_read_b32 v32, a28\n"
        "v_accvgpr_read_b32 v33, a29\n"
        "v_accvgpr_read_b32 v34, a30\n"
        "v_accvgpr_read_b32 v35, a31\n"
        "v_mul_f32 v28, v28, v5\n"
        "v_mul_f32 v29, v29, v5\n"
        "v_mul_f32 v30, v30, v5\n"
        "v_mul_f32 v31, v31, v5\n"
        "v_mul_f32 v32, v32, v5\n"
        "v_mul_f32 v33, v33, v5\n"
        "v_mul_f32 v34, v34, v5\n"
        "v_mul_f32 v35, v35, v5\n"
        "v_cvt_pk_bf16_f32 v36, v28, v29\n"
        "v_cvt_pk_bf16_f32 v37, v30, v31\n"
        "v_cvt_pk_bf16_f32 v38, v32, v33\n"
        "v_cvt_pk_bf16_f32 v39, v34, v35\n"
        "buffer_store_dwordx4 v[36:39], v0, s[48:51], s62 offen offset:1024 sc1\n"
        "v_accvgpr_read_b32 v40, a36\n"
        "v_accvgpr_read_b32 v41, a37\n"
        "v_accvgpr_read_b32 v42, a38\n"
        "v_accvgpr_read_b32 v43, a39\n"
        "v_accvgpr_read_b32 v44, a44\n"
        "v_accvgpr_read_b32 v45, a45\n"
        "v_accvgpr_read_b32 v46, a46\n"
        "v_accvgpr_read_b32 v47, a47\n"
        "v_mul_f32 v40, v40, v5\n"
        "v_mul_f32 v41, v41, v5\n"
        "v_mul_f32 v42, v42, v5\n"
        "v_mul_f32 v43, v43, v5\n"
        "v_mul_f32 v44, v44, v5\n"
        "v_mul_f32 v45, v45, v5\n"
        "v_mul_f32 v46, v46, v5\n"
        "v_mul_f32 v47, v47, v5\n"
        "v_cvt_pk_bf16_f32 v48, v40, v41\n"
        "v_cvt_pk_bf16_f32 v49, v42, v43\n"
        "v_cvt_pk_bf16_f32 v50, v44, v45\n"
        "v_cvt_pk_bf16_f32 v51, v46, v47\n"
        "buffer_store_dwordx4 v[48:51], v0, s[48:51], s62 offen offset:2048 sc1\n"
        "v_accvgpr_read_b32 v52, a52\n"
        "v_accvgpr_read_b32 v53, a53\n"
        "v_accvgpr_read_b32 v54, a54\n"
        "v_accvgpr_read_b32 v55, a55\n"
        "v_accvgpr_read_b32 v56, a60\n"
        "v_accvgpr_read_b32 v57, a61\n"
        "v_accvgpr_read_b32 v58, a62\n"
        "v_accvgpr_read_b32 v59, a63\n"
        "v_mul_f32 v52, v52, v5\n"
        "v_mul_f32 v53, v53, v5\n"
        "v_mul_f32 v54, v54, v5\n"
        "v_mul_f32 v55, v55, v5\n"
        "v_mul_f32 v56, v56, v5\n"
        "v_mul_f32 v57, v57, v5\n"
        "v_mul_f32 v58, v58, v5\n"
        "v_mul_f32 v59, v59, v5\n"
        "v_cvt_pk_bf16_f32 v60, v52, v53\n"
        "v_cvt_pk_bf16_f32 v61, v54, v55\n"
        "v_cvt_pk_bf16_f32 v62, v56, v57\n"
        "v_cvt_pk_bf16_f32 v63, v58, v59\n"
        "buffer_store_dwordx4 v[60:63], v0, s[48:51], s62 offen offset:3072 sc1\n"
        "s_cmp_lg_u64 s[80:81], 0\n"
        "s_cselect_b32 s52, 1, 0\n"
        "s_mov_b64 s[80:81], 0\n"
        "s_lshl_b32 s53, s36, 3\n"
        "s_lshl_b32 s54, s60, 2\n"
        "s_add_u32 s53, s53, s54\n"
        "s_add_u32 s53, s53, 159744\n"
        "v_mov_b32 v4, s53\n"
        "v_mov_b32 v5, s52\n"
        "ds_write_b32 v4, v5\n"
        "s_nop 1\n"
        "s_cmp_eq_u32 s60, 1\n"
        "s_cbranch_scc1 P3_DONE_%=\n"
        "s_mov_b32 s60, 1\n"
        "s_mov_b32 s83, s38\n"
        "s_lshr_b32 s52, %[blk], 3\n"
        "s_lshl_b32 s52, s52, 4\n"
        "s_lshl_b32 s53, s36, 2\n"
        "s_add_u32 s52, s52, s53\n"
        "s_lshr_b32 s53, s37, 2\n"
        "s_lshl_b32 s53, s53, 9\n"
        "s_add_u32 s52, s52, s53\n"
        "v_lshrrev_b32 v4, 4, v0\n"
        "v_mul_u32_u24 v5, 6554, v4\n"
        "v_lshrrev_b32 v5, 16, v5\n"
        "v_mul_u32_u24 v6, 10, v5\n"
        "v_sub_u32 v6, v4, v6\n"
        "v_add_u32 v5, s52, v5\n"
        "v_mul_u32_u24 v5, 1280, v5\n"
        "v_lshl_add_u32 v5, v6, 7, v5\n"
        "v_cmp_gt_u32 vcc, 40, v4\n"
        "v_mov_b32 v6, 0x7ff00000\n"
        "s_nop 0\n"
        "v_cndmask_b32 v5, v6, v5, vcc\n"
        "s_mov_b32 s92, %[wolo]\n"
        "s_and_b32 s93, %[wohi], 0xffff\n"
        "s_mov_b32 s94, 5242880\n"
        "s_mov_b32 s95, 0x20000\n"
        "buffer_load_dword a112, v5, s[92:95], 0 offen\n"
        "v_lshrrev_b32 v5, 4, v0\n"
        "v_lshlrev_b32 v5, 21, v5\n"
        "s_add_u32 s92, s86, 37748736\n"
        "s_addc_u32 s93, s87, 0\n"
        "s_and_b32 s93, s93, 0xffff\n"
        "s_mov_b32 s94, 40370176\n"
        "buffer_load_dword a113, v5, s[92:95], 0 offen\n"
        "s_mov_b32 s92, %[outlo]\n"
        "s_and_b32 s93, %[outhi], 0xffff\n"
        "s_mov_b32 s94, 2621440\n"
        "buffer_load_dword a114, v5, s[92:95], 0 offen\n"
        "s_getpc_b64 s[92:93]\n"
        "KA_PCREF_%=:\n"
        "s_add_u32 s92, s92, k_block_second_half-KA_PCREF_%=\n"
        "s_addc_u32 s93, s93, 0\n"
        "s_and_b32 s93, s93, 0xffff\n"
        "s_mov_b32 s94, 26624\n"
        "s_mov_b32 s95, 0x20000\n"
        "s_lshl_b32 s52, s36, 13\n"
        "v_lshlrev_b32 v5, 3, v0\n"
        "buffer_load_dword a115, v5, s[92:95], s52 offen\n"
        "s_lshl_b32 s52, s83, 3\n"
        "s_add_u32 s53, s52, s36\n"
        "s_lshl_b32 s61, s53, 17\n"
        "s_add_u32 s53, s52, 7\n"
        "s_sub_u32 s53, s53, s36\n"
        "s_lshl_b32 s62, s53, 17\n"
        "s_mov_b32 s63, s83\n"
        "s_or_b32 s52, s83, s36\n"
        "s_cmp_eq_u32 s52, 0\n"
        "s_cselect_b64 s[72:73], s[64:65], -1\n"
        "s_cselect_b64 s[74:75], s[66:67], -1\n"
        "s_cselect_b64 s[76:77], s[68:69], -1\n"
        "s_cselect_b64 s[78:79], s[70:71], -1\n"
        "s_waitcnt lgkmcnt(0)\n"
        "s_branch P3_BLOCK_%=\n"
        "P3_DONE_%=:\n"
        "s_waitcnt lgkmcnt(0)\n"
        "s_branch KA_END_%=\n"
        "KA_V_%=:\n"
        "s_getpc_b64 s[92:93]\n"
        "KA_PCREFV_%=:\n"
        "s_sub_u32 s92, s92, KA_PCREFV_%=-k_block_first_half\n"
        "s_subb_u32 s93, s93, 0\n"
        "s_and_b32 s93, s93, 0xffff\n"
        "s_mov_b32 s94, k_block_second_half-k_block_first_half\n"
        "s_mov_b32 s95, 0x20000\n"
        "s_lshl_b32 s52, s36, 13\n"
        "v_lshlrev_b32 v119, 3, v0\n"
        "buffer_load_dword v19, v119, s[92:95], s52 offen\n"
        "s_waitcnt vmcnt(13)\n"
        "s_barrier\n"
        "ds_read_b64_tr_b16 v[64:65], v3 offset:0\n"
        "ds_read_b64_tr_b16 v[66:67], v3 offset:1024\n"
        "ds_read_b64_tr_b16 v[68:69], v3 offset:8192\n"
        "ds_read_b64_tr_b16 v[70:71], v3 offset:9216\n"
        "ds_read_b64_tr_b16 v[72:73], v4 offset:0\n"
        "ds_read_b64_tr_b16 v[74:75], v4 offset:1024\n"
        "ds_read_b64_tr_b16 v[76:77], v4 offset:8192\n"
        "ds_read_b64_tr_b16 v[78:79], v4 offset:9216\n"
        "ds_read_b64_tr_b16 v[80:81], v5 offset:0\n"
        "ds_read_b64_tr_b16 v[82:83], v5 offset:1024\n"
        "ds_read_b64_tr_b16 v[84:85], v5 offset:8192\n"
        "ds_read_b64_tr_b16 v[86:87], v5 offset:9216\n"
        "ds_read_b64_tr_b16 v[88:89], v6 offset:0\n"
        "ds_read_b64_tr_b16 v[90:91], v6 offset:1024\n"
        "ds_read_b64_tr_b16 v[92:93], v6 offset:8192\n"
        "ds_read_b64_tr_b16 v[94:95], v6 offset:9216\n"
        "ds_read_b128 v[32:35], v1 offset:0\n"
        "ds_read_b128 v[36:39], v2 offset:0\n"
        "ds_read_b128 v[40:43], v1 offset:2048\n"
        "ds_read_b128 v[44:47], v2 offset:2048\n"
        "ds_read_b128 v[48:51], v1 offset:4096\n"
        "ds_read_b128 v[52:55], v2 offset:4096\n"
        "s_waitcnt lgkmcnt(0)\n"
        "s_setprio 1\n"
        "v_mfma_f32_16x16x32_bf16 a[0:3], v[32:35], v[64:67], 0\n"
        "ds_read_b128 v[56:59], v1 offset:6144\n"
        "v_mfma_f32_16x16x32_bf16 a[0:3], v[36:39], v[68:71], a[0:3]\n"
        "ds_read_b128 v[60:63], v2 offset:6144\n"
        "v_mfma_f32_16x16x32_bf16 a[4:7], v[32:35], v[72:75], 0\n"
        "v_readfirstlane_b32 s96, v126\n"
        "v_mfma_f32_16x16x32_bf16 a[4:7], v[36:39], v[76:79], a[4:7]\n"
        "v_mfma_f32_16x16x32_bf16 a[8:11], v[32:35], v[80:83], 0\n"
        "v_mfma_f32_16x16x32_bf16 a[8:11], v[36:39], v[84:87], a[8:11]\n"
        "v_mfma_f32_16x16x32_bf16 a[12:15], v[32:35], v[88:91], 0\n"
        "v_mfma_f32_16x16x32_bf16 a[12:15], v[36:39], v[92:95], a[12:15]\n"
        "s_setprio 0\n"
        "v_mfma_f32_16x16x32_bf16 a[16:19], v[40:43], v[64:67], 0\n"
        "ds_read_b128 v[32:35], v1 offset:8192\n"
        "v_mfma_f32_16x16x32_bf16 a[16:19], v[44:47], v[68:71], a[16:19]\n"
        "ds_read_b128 v[36:39], v2 offset:8192\n"
        "v_mfma_f32_16x16x32_bf16 a[20:23], v[40:43], v[72:75], 0\n"
        "v_readfirstlane_b32 s97, v127\n"
        "v_mfma_f32_16x16x32_bf16 a[20:23], v[44:47], v[76:79], a[20:23]\n"
        "v_mfma_f32_16x16x32_bf16 a[24:27], v[40:43], v[80:83], 0\n"
        "v_mfma_f32_16x16x32_bf16 a[24:27], v[44:47], v[84:87], a[24:27]\n"
        "v_mfma_f32_16x16x32_bf16 a[28:31], v[40:43], v[88:91], 0\n"
        "v_mfma_f32_16x16x32_bf16 a[28:31], v[44:47], v[92:95], a[28:31]\n"
        "s_setprio 1\n"
        "v_mfma_f32_16x16x32_bf16 a[32:35], v[48:51], v[64:67], 0\n"
        "ds_read_b128 v[40:43], v1 offset:10240\n"
        "v_mfma_f32_16x16x32_bf16 a[32:35], v[52:55], v[68:71], a[32:35]\n"
        "ds_read_b128 v[44:47], v2 offset:10240\n"
        "v_mfma_f32_16x16x32_bf16 a[36:39], v[48:51], v[72:75], 0\n"
        "s_nop 1\n"
        "v_mfma_f32_16x16x32_bf16 a[36:39], v[52:55], v[76:79], a[36:39]\n"
        "v_mfma_f32_16x16x32_bf16 a[40:43], v[48:51], v[80:83], 0\n"
        "v_mfma_f32_16x16x32_bf16 a[40:43], v[52:55], v[84:87], a[40:43]\n"
        "v_mfma_f32_16x16x32_bf16 a[44:47], v[48:51], v[88:91], 0\n"
        "v_mfma_f32_16x16x32_bf16 a[44:47], v[52:55], v[92:95], a[44:47]\n"
        "s_setprio 0\n"
        "s_waitcnt lgkmcnt(2)\n"
        "v_mfma_f32_16x16x32_bf16 a[48:51], v[56:59], v[64:67], 0\n"
        "ds_read_b128 v[48:51], v1 offset:12288\n"
        "v_mfma_f32_16x16x32_bf16 a[48:51], v[60:63], v[68:71], a[48:51]\n"
        "ds_read_b128 v[52:55], v2 offset:12288\n"
        "v_mfma_f32_16x16x32_bf16 a[52:55], v[56:59], v[72:75], 0\n"
        "s_add_u32 s96, s96, 1\n"
        "v_mfma_f32_16x16x32_bf16 a[52:55], v[60:63], v[76:79], a[52:55]\n"
        "v_mfma_f32_16x16x32_bf16 a[56:59], v[56:59], v[80:83], 0\n"
        "v_mfma_f32_16x16x32_bf16 a[56:59], v[60:63], v[84:87], a[56:59]\n"
        "v_mfma_f32_16x16x32_bf16 a[60:63], v[56:59], v[88:91], 0\n"
        "v_mfma_f32_16x16x32_bf16 a[60:63], v[60:63], v[92:95], a[60:63]\n"
        "s_setprio 1\n"
        "v_mfma_f32_16x16x32_bf16 a[64:67], v[32:35], v[64:67], 0\n"
        "ds_read_b128 v[56:59], v1 offset:14336\n"
        "v_mfma_f32_16x16x32_bf16 a[64:67], v[36:39], v[68:71], a[64:67]\n"
        "ds_read_b128 v[60:63], v2 offset:14336\n"
        "v_mfma_f32_16x16x32_bf16 a[68:71], v[32:35], v[72:75], 0\n"
        "s_addc_u32 s97, s97, 0\n"
        "v_mfma_f32_16x16x32_bf16 a[68:71], v[36:39], v[76:79], a[68:71]\n"
        "v_mfma_f32_16x16x32_bf16 a[72:75], v[32:35], v[80:83], 0\n"
        "v_mfma_f32_16x16x32_bf16 a[72:75], v[36:39], v[84:87], a[72:75]\n"
        "v_mfma_f32_16x16x32_bf16 a[76:79], v[32:35], v[88:91], 0\n"
        "v_mfma_f32_16x16x32_bf16 a[76:79], v[36:39], v[92:95], a[76:79]\n"
        "s_waitcnt vmcnt(5) lgkmcnt(0)\n"
        "s_barrier\n"
        "s_mov_b32 s84, s80\n"
        "s_add_u32 s80, s80, 32768\n"
        "s_cmp_eq_u32 s80, 98304\n"
        "s_cselect_b32 s80, 0, s80\n"
        "s_sub_u32 s85, s80, s84\n"
        "v_xor_b32 v1, 32768, v1\n"
        "v_xor_b32 v2, 32768, v2\n"
        "v_add_u32 v3, s85, v3\n"
        "v_add_u32 v4, s85, v4\n"
        "v_add_u32 v5, s85, v5\n"
        "v_add_u32 v6, s85, v6\n"
        "s_setprio 0\n"
        "v_mfma_f32_16x16x32_bf16 a[80:83], v[40:43], v[64:67], 0\n"
        "s_mov_b32 m0, s72\n"
        "s_nop 0\n"
        "buffer_load_dwordx4 v8, s[64:67], s74 offen lds\n"
        "v_mfma_f32_16x16x32_bf16 a[80:83], v[44:47], v[68:71], a[80:83]\n"
        "ds_read_b128 v[32:35], v1 offset:0\n"
        "v_mfma_f32_16x16x32_bf16 a[84:87], v[40:43], v[72:75], 0\n"
        "ds_read_b128 v[36:39], v2 offset:0\n"
        "v_mfma_f32_16x16x32_bf16 a[84:87], v[44:47], v[76:79], a[84:87]\n"
        "ds_read_b64_tr_b16 v[96:97], v3 offset:0\n"
        "v_mfma_f32_16x16x32_bf16 a[88:91], v[40:43], v[80:83], 0\n"
        "ds_read_b64_tr_b16 v[98:99], v3 offset:1024\n"
        "v_mfma_f32_16x16x32_bf16 a[88:91], v[44:47], v[84:87], a[88:91]\n"
        "ds_read_b64_tr_b16 v[100:101], v3 offset:8192\n"
        "v_mfma_f32_16x16x32_bf16 a[92:95], v[40:43], v[88:91], 0\n"
        "ds_read_b64_tr_b16 v[102:103], v3 offset:9216\n"
        "v_mfma_f32_16x16x32_bf16 a[92:95], v[44:47], v[92:95], a[92:95]\n"
        "ds_read_b64_tr_b16 v[104:105], v4 offset:0\n"
        "s_setprio 1\n"
        "v_mfma_f32_16x16x32_bf16 a[96:99], v[48:51], v[64:67], 0\n"
        "buffer_load_dwordx4 v9, s[64:67], s74 offen offset:1024 lds\n"
        "v_mfma_f32_16x16x32_bf16 a[96:99], v[52:55], v[68:71], a[96:99]\n"
        "ds_read_b128 v[40:43], v1 offset:2048\n"
        "v_mfma_f32_16x16x32_bf16 a[100:103], v[48:51], v[72:75], 0\n"
        "ds_read_b128 v[44:47], v2 offset:2048\n"
        "v_mfma_f32_16x16x32_bf16 a[100:103], v[52:55], v[76:79], a[100:103]\n"
        "ds_read_b64_tr_b16 v[106:107], v4 offset:1024\n"
        "v_mfma_f32_16x16x32_bf16 a[104:107], v[48:51], v[80:83], 0\n"
        "ds_read_b64_tr_b16 v[108:109], v4 offset:8192\n"
        "v_mfma_f32_16x16x32_bf16 a[104:107], v[52:55], v[84:87], a[104:107]\n"
        "ds_read_b64_tr_b16 v[110:111], v4 offset:9216\n"
        "v_mfma_f32_16x16x32_bf16 a[108:111], v[48:51], v[88:91], 0\n"
        "ds_read_b64_tr_b16 v[112:113], v5 offset:0\n"
        "v_mfma_f32_16x16x32_bf16 a[108:111], v[52:55], v[92:95], a[108:111]\n"
        "ds_read_b64_tr_b16 v[114:115], v5 offset:1024\n"
        "s_setprio 0\n"
        "v_mfma_f32_16x16x32_bf16 a[112:115], v[56:59], v[64:67], 0\n"
        "buffer_load_dwordx4 v10, s[64:67], s74 offen offset:2048 lds\n"
        "ds_read_b128 v[48:51], v1 offset:4096\n"
        "v_mfma_f32_16x16x32_bf16 a[112:115], v[60:63], v[68:71], a[112:115]\n"
        "ds_read_b128 v[52:55], v2 offset:4096\n"
        "ds_read_b64_tr_b16 v[116:117], v5 offset:8192\n"
        "v_mfma_f32_16x16x32_bf16 a[116:119], v[56:59], v[72:75], 0\n"
        "ds_read_b64_tr_b16 v[118:119], v5 offset:9216\n"
        "buffer_load_dwordx4 v11, s[64:67], s74 offen offset:3072 lds\n"
        "s_xor_b32 s72, s72, 32768\n"
        "s_add_u32 s74, s74, 128\n"
        "v_mfma_f32_16x16x32_bf16 a[116:119], v[60:63], v[76:79], a[116:119]\n"
        "ds_read_b64_tr_b16 v[120:121], v6 offset:0\n"
        "ds_read_b64_tr_b16 v[122:123], v6 offset:1024\n"
        "v_mfma_f32_16x16x32_bf16 a[120:123], v[56:59], v[80:83], 0\n"
        "ds_read_b64_tr_b16 v[124:125], v6 offset:8192\n"
        "ds_read_b64_tr_b16 v[126:127], v6 offset:9216\n"
        "v_mfma_f32_16x16x32_bf16 a[120:123], v[60:63], v[84:87], a[120:123]\n"
        "v_mfma_f32_16x16x32_bf16 a[124:127], v[56:59], v[88:91], 0\n"
        "v_mfma_f32_16x16x32_bf16 a[124:127], v[60:63], v[92:95], a[124:127]\n"
        "s_setprio 1\n"
        "s_waitcnt lgkmcnt(8)\n"
        "v_mfma_f32_16x16x32_bf16 a[0:3], v[32:35], v[96:99], a[0:3]\n"
        "s_add_u32 m0, s73, s84\n"
        "s_nop 0\n"
        "buffer_load_dwordx4 v12, s[68:71], s75 offen lds\n"
        "v_mfma_f32_16x16x32_bf16 a[0:3], v[36:39], v[100:103], a[0:3]\n"
        "ds_read_b128 v[56:59], v1 offset:6144\n"
        "v_mfma_f32_16x16x32_bf16 a[4:7], v[32:35], v[104:107], a[4:7]\n"
        "ds_read_b128 v[60:63], v2 offset:6144\n"
        "v_mfma_f32_16x16x32_bf16 a[4:7], v[36:39], v[108:111], a[4:7]\n"
        "v_mfma_f32_16x16x32_bf16 a[8:11], v[32:35], v[112:115], a[8:11]\n"
        "s_waitcnt lgkmcnt(6)\n"
        "v_mfma_f32_16x16x32_bf16 a[8:11], v[36:39], v[116:119], a[8:11]\n"
        "s_waitcnt lgkmcnt(4)\n"
        "v_mfma_f32_16x16x32_bf16 a[12:15], v[32:35], v[120:123], a[12:15]\n"
        "s_waitcnt lgkmcnt(2)\n"
        "v_mfma_f32_16x16x32_bf16 a[12:15], v[36:39], v[124:127], a[12:15]\n"
        "s_setprio 0\n"
        "v_mfma_f32_16x16x32_bf16 a[16:19], v[40:43], v[96:99], a[16:19]\n"
        "buffer_load_dwordx4 v13, s[68:71], s75 offen offset:1024 lds\n"
        "v_mfma_f32_16x16x32_bf16 a[16:19], v[44:47], v[100:103], a[16:19]\n"
        "ds_read_b128 v[32:35], v1 offset:8192\n"
        "v_mfma_f32_16x16x32_bf16 a[20:23], v[40:43], v[104:107], a[20:23]\n"
        "ds_read_b128 v[36:39], v2 offset:8192\n"
        "v_mfma_f32_16x16x32_bf16 a[20:23], v[44:47], v[108:111], a[20:23]\n"
        "v_mfma_f32_16x16x32_bf16 a[24:27], v[40:43], v[112:115], a[24:27]\n"
        "v_mfma_f32_16x16x32_bf16 a[24:27], v[44:47], v[116:119], a[24:27]\n"
        "v_mfma_f32_16x16x32_bf16 a[28:31], v[40:43], v[120:123], a[28:31]\n"
        "v_mfma_f32_16x16x32_bf16 a[28:31], v[44:47], v[124:127], a[28:31]\n"
        "s_setprio 1\n"
        "v_mfma_f32_16x16x32_bf16 a[32:35], v[48:51], v[96:99], a[32:35]\n"
        "buffer_load_dwordx4 v14, s[68:71], s75 offen offset:2048 lds\n"
        "v_mfma_f32_16x16x32_bf16 a[32:35], v[52:55], v[100:103], a[32:35]\n"
        "ds_read_b128 v[40:43], v1 offset:10240\n"
        "v_mfma_f32_16x16x32_bf16 a[36:39], v[48:51], v[104:107], a[36:39]\n"
        "ds_read_b128 v[44:47], v2 offset:10240\n"
        "v_mfma_f32_16x16x32_bf16 a[36:39], v[52:55], v[108:111], a[36:39]\n"
        "v_mfma_f32_16x16x32_bf16 a[40:43], v[48:51], v[112:115], a[40:43]\n"
        "v_mfma_f32_16x16x32_bf16 a[40:43], v[52:55], v[116:119], a[40:43]\n"
        "v_mfma_f32_16x16x32_bf16 a[44:47], v[48:51], v[120:123], a[44:47]\n"
        "v_mfma_f32_16x16x32_bf16 a[44:47], v[52:55], v[124:127], a[44:47]\n"
        "s_setprio 0\n"
        "s_waitcnt lgkmcnt(3)\n"
        "v_mfma_f32_16x16x32_bf16 a[48:51], v[56:59], v[96:99], a[48:51]\n"
        "buffer_load_dwordx4 v15, s[68:71], s75 offen offset:3072 lds\n"
        "s_add_u32 s75, s75, 0x80000\n"
        "v_mfma_f32_16x16x32_bf16 a[48:51], v[60:63], v[100:103], a[48:51]\n"
        "ds_read_b128 v[48:51], v1 offset:12288\n"
        "v_mfma_f32_16x16x32_bf16 a[52:55], v[56:59], v[104:107], a[52:55]\n"
        "ds_read_b128 v[52:55], v2 offset:12288\n"
        "v_mfma_f32_16x16x32_bf16 a[52:55], v[60:63], v[108:111], a[52:55]\n"
        "v_mfma_f32_16x16x32_bf16 a[56:59], v[56:59], v[112:115], a[56:59]\n"
        "v_mfma_f32_16x16x32_bf16 a[56:59], v[60:63], v[116:119], a[56:59]\n"
        "v_mfma_f32_16x16x32_bf16 a[60:63], v[56:59], v[120:123], a[60:63]\n"
        "v_mfma_f32_16x16x32_bf16 a[60:63], v[60:63], v[124:127], a[60:63]\n"
        "s_setprio 1\n"
        "v_mfma_f32_16x16x32_bf16 a[64:67], v[32:35], v[96:99], a[64:67]\n"
        "ds_read_b128 v[56:59], v1 offset:14336\n"
        "s_waitcnt lgkmcnt(3)\n"
        "v_mfma_f32_16x16x32_bf16 a[64:67], v[36:39], v[100:103], a[64:67]\n"
        "ds_read_b128 v[60:63], v2 offset:14336\n"
        "v_mfma_f32_16x16x32_bf16 a[68:71], v[32:35], v[104:107], a[68:71]\n"
        "v_mfma_f32_16x16x32_bf16 a[68:71], v[36:39], v[108:111], a[68:71]\n"
        "v_mfma_f32_16x16x32_bf16 a[72:75], v[32:35], v[112:115], a[72:75]\n"
        "v_mfma_f32_16x16x32_bf16 a[72:75], v[36:39], v[116:119], a[72:75]\n"
        "v_mfma_f32_16x16x32_bf16 a[76:79], v[32:35], v[120:123], a[76:79]\n"
        "v_mfma_f32_16x16x32_bf16 a[76:79], v[36:39], v[124:127], a[76:79]\n"
        "s_waitcnt vmcnt(4) lgkmcnt(0)\n"
        "s_barrier\n"
        "s_mov_b32 s84, s80\n"
        "s_add_u32 s80, s80, 32768\n"
        "s_cmp_eq_u32 s80, 98304\n"
        "s_cselect_b32 s80, 0, s80\n"
        "s_sub_u32 s85, s80, s84\n"
        "v_xor_b32 v1, 32768, v1\n"
        "v_xor_b32 v2, 32768, v2\n"
        "v_add_u32 v3, s85, v3\n"
        "v_add_u32 v4, s85, v4\n"
        "v_add_u32 v5, s85, v5\n"
        "v_add_u32 v6, s85, v6\n"
        "s_setprio 0\n"
        "v_mfma_f32_16x16x32_bf16 a[80:83], v[40:43], v[96:99], a[80:83]\n"
        "s_mov_b32 m0, s72\n"
        "s_nop 0\n"
        "buffer_load_dwordx4 v8, s[64:67], s74 offen lds\n"
        "v_mfma_f32_16x16x32_bf16 a[80:83], v[44:47], v[100:103], a[80:83]\n"
        "ds_read_b128 v[32:35], v1 offset:0\n"
        "v_mfma_f32_16x16x32_bf16 a[84:87], v[40:43], v[104:107], a[84:87]\n"
        "ds_read_b128 v[36:39], v2 offset:0\n"
        "v_mfma_f32_16x16x32_bf16 a[84:87], v[44:47], v[108:111], a[84:87]\n"
        "ds_read_b64_tr_b16 v[64:65], v3 offset:0\n"
        "v_mfma_f32_16x16x32_bf16 a[88:91], v[40:43], v[112:115], a[88:91]\n"
        "ds_read_b64_tr_b16 v[66:67], v3 offset:1024\n"
        "v_mfma_f32_16x16x32_bf16 a[88:91], v[44:47], v[116:119], a[88:91]\n"
        "ds_read_b64_tr_b16 v[68:69], v3 offset:8192\n"
        "v_mfma_f32_16x16x32_bf16 a[92:95], v[40:43], v[120:123], a[92:95]\n"
        "ds_read_b64_tr_b16 v[70:71], v3 offset:9216\n"
        "v_mfma_f32_16x16x32_bf16 a[92:95], v[44:47], v[124:127], a[92:95]\n"
        "ds_read_b64_tr_b16 v[72:73], v4 offset:0\n"
        "s_setprio 1\n"
        "v_mfma_f32_16x16x32_bf16 a[96:99], v[48:51], v[96:99], a[96:99]\n"
        "buffer_load_dwordx4 v9, s[64:67], s74 offen offset:1024 lds\n"
        "v_mfma_f32_16x16x32_bf16 a[96:99], v[52:55], v[100:103], a[96:99]\n"
        "ds_read_b128 v[40:43], v1 offset:2048\n"
        "v_mfma_f32_16x16x32_bf16 a[100:103], v[48:51], v[104:107], a[100:103]\n"
        "ds_read_b128 v[44:47], v2 offset:2048\n"
        "v_mfma_f32_16x16x32_bf16 a[100:103], v[52:55], v[108:111], a[100:103]\n"
        "ds_read_b64_tr_b16 v[74:75], v4 offset:1024\n"
        "v_mfma_f32_16x16x32_bf16 a[104:107], v[48:51], v[112:115], a[104:107]\n"
        "ds_read_b64_tr_b16 v[76:77], v4 offset:8192\n"
        "v_mfma_f32_16x16x32_bf16 a[104:107], v[52:55], v[116:119], a[104:107]\n"
        "ds_read_b64_tr_b16 v[78:79], v4 offset:9216\n"
        "v_mfma_f32_16x16x32_bf16 a[108:111], v[48:51], v[120:123], a[108:111]\n"
        "ds_read_b64_tr_b16 v[80:81], v5 offset:0\n"
        "v_mfma_f32_16x16x32_bf16 a[108:111], v[52:55], v[124:127], a[108:111]\n"
        "ds_read_b64_tr_b16 v[82:83], v5 offset:1024\n"
        "s_setprio 0\n"
        "v_mfma_f32_16x16x32_bf16 a[112:115], v[56:59], v[96:99], a[112:115]\n"
        "buffer_load_dwordx4 v10, s[64:67], s74 offen offset:2048 lds\n"
        "ds_read_b128 v[48:51], v1 offset:4096\n"
        "v_mfma_f32_16x16x32_bf16 a[112:115], v[60:63], v[100:103], a[112:115]\n"
        "ds_read_b128 v[52:55], v2 offset:4096\n"
        "ds_read_b64_tr_b16 v[84:85], v5 offset:8192\n"
        "v_mfma_f32_16x16x32_bf16 a[116:119], v[56:59], v[104:107], a[116:119]\n"
        "ds_read_b64_tr_b16 v[86:87], v5 offset:9216\n"
        "buffer_load_dwordx4 v11, s[64:67], s74 offen offset:3072 lds\n"
        "s_xor_b32 s72, s72, 32768\n"
        "s_add_u32 s74, s74, 128\n"
        "v_mfma_f32_16x16x32_bf16 a[116:119], v[60:63], v[108:111], a[116:119]\n"
        "ds_read_b64_tr_b16 v[88:89], v6 offset:0\n"
        "ds_read_b64_tr_b16 v[90:91], v6 offset:1024\n"
        "v_mfma_f32_16x16x32_bf16 a[120:123], v[56:59], v[112:115], a[120:123]\n"
        "ds_read_b64_tr_b16 v[92:93], v6 offset:8192\n"
        "ds_read_b64_tr_b16 v[94:95], v6 offset:9216\n"
        "v_mfma_f32_16x16x32_bf16 a[120:123], v[60:63], v[116:119], a[120:123]\n"
        "v_mfma_f32_16x16x32_bf16 a[124:127], v[56:59], v[120:123], a[124:127]\n"
        "v_mfma_f32_16x16x32_bf16 a[124:127], v[60:63], v[124:127], a[124:127]\n"
        "s_mov_b32 s39, 2\n"
        "P1V_LOOP_%=:\n"
        "s_setprio 1\n"
        "s_waitcnt lgkmcnt(8)\n"
        "v_mfma_f32_16x16x32_bf16 a[0:3], v[32:35], v[64:67], a[0:3]\n"
        "s_add_u32 m0, s73, s84\n"
        "s_nop 0\n"
        "buffer_load_dwordx4 v12, s[68:71], s75 offen lds\n"
        "v_mfma_f32_16x16x32_bf16 a[0:3], v[36:39], v[68:71], a[0:3]\n"
        "ds_read_b128 v[56:59], v1 offset:6144\n"
        "v_mfma_f32_16x16x32_bf16 a[4:7], v[32:35], v[72:75], a[4:7]\n"
        "ds_read_b128 v[60:63], v2 offset:6144\n"
        "v_mfma_f32_16x16x32_bf16 a[4:7], v[36:39], v[76:79], a[4:7]\n"
        "v_mfma_f32_16x16x32_bf16 a[8:11], v[32:35], v[80:83], a[8:11]\n"
        "s_waitcnt lgkmcnt(6)\n"
        "v_mfma_f32_16x16x32_bf16 a[8:11], v[36:39], v[84:87], a[8:11]\n"
        "s_waitcnt lgkmcnt(4)\n"
        "v_mfma_f32_16x16x32_bf16 a[12:15], v[32:35], v[88:91], a[12:15]\n"
        "s_waitcnt lgkmcnt(2)\n"
        "v_mfma_f32_16x16x32_bf16 a[12:15], v[36:39], v[92:95], a[12:15]\n"
        "s_setprio 0\n"
        "v_mfma_f32_16x16x32_bf16 a[16:19], v[40:43], v[64:67], a[16:19]\n"
        "buffer_load_dwordx4 v13, s[68:71], s75 offen offset:1024 lds\n"
        "v_mfma_f32_16x16x32_bf16 a[16:19], v[44:47], v[68:71], a[16:19]\n"
        "ds_read_b128 v[32:35], v1 offset:8192\n"
        "v_mfma_f32_16x16x32_bf16 a[20:23], v[40:43], v[72:75], a[20:23]\n"
        "ds_read_b128 v[36:39], v2 offset:8192\n"
        "v_mfma_f32_16x16x32_bf16 a[20:23], v[44:47], v[76:79], a[20:23]\n"
        "v_mfma_f32_16x16x32_bf16 a[24:27], v[40:43], v[80:83], a[24:27]\n"
        "v_mfma_f32_16x16x32_bf16 a[24:27], v[44:47], v[84:87], a[24:27]\n"
        "v_mfma_f32_16x16x32_bf16 a[28:31], v[40:43], v[88:91], a[28:31]\n"
        "v_mfma_f32_16x16x32_bf16 a[28:31], v[44:47], v[92:95], a[28:31]\n"
        "s_setprio 1\n"
        "v_mfma_f32_16x16x32_bf16 a[32:35], v[48:51], v[64:67], a[32:35]\n"
        "buffer_load_dwordx4 v14, s[68:71], s75 offen offset:2048 lds\n"
        "v_mfma_f32_16x16x32_bf16 a[32:35], v[52:55], v[68:71], a[32:35]\n"
        "ds_read_b128 v[40:43], v1 offset:10240\n"
        "v_mfma_f32_16x16x32_bf16 a[36:39], v[48:51], v[72:75], a[36:39]\n"
        "ds_read_b128 v[44:47], v2 offset:10240\n"
        "v_mfma_f32_16x16x32_bf16 a[36:39], v[52:55], v[76:79], a[36:39]\n"
        "v_mfma_f32_16x16x32_bf16 a[40:43], v[48:51], v[80:83], a[40:43]\n"
        "v_mfma_f32_16x16x32_bf16 a[40:43], v[52:55], v[84:87], a[40:43]\n"
        "v_mfma_f32_16x16x32_bf16 a[44:47], v[48:51], v[88:91], a[44:47]\n"
        "v_mfma_f32_16x16x32_bf16 a[44:47], v[52:55], v[92:95], a[44:47]\n"
        "s_setprio 0\n"
        "s_waitcnt lgkmcnt(3)\n"
        "v_mfma_f32_16x16x32_bf16 a[48:51], v[56:59], v[64:67], a[48:51]\n"
        "buffer_load_dwordx4 v15, s[68:71], s75 offen offset:3072 lds\n"
        "s_add_u32 s75, s75, 0x80000\n"
        "v_mfma_f32_16x16x32_bf16 a[48:51], v[60:63], v[68:71], a[48:51]\n"
        "ds_read_b128 v[48:51], v1 offset:12288\n"
        "v_mfma_f32_16x16x32_bf16 a[52:55], v[56:59], v[72:75], a[52:55]\n"
        "ds_read_b128 v[52:55], v2 offset:12288\n"
        "v_mfma_f32_16x16x32_bf16 a[52:55], v[60:63], v[76:79], a[52:55]\n"
        "v_mfma_f32_16x16x32_bf16 a[56:59], v[56:59], v[80:83], a[56:59]\n"
        "v_mfma_f32_16x16x32_bf16 a[56:59], v[60:63], v[84:87], a[56:59]\n"
        "v_mfma_f32_16x16x32_bf16 a[60:63], v[56:59], v[88:91], a[60:63]\n"
        "v_mfma_f32_16x16x32_bf16 a[60:63], v[60:63], v[92:95], a[60:63]\n"
        "s_setprio 1\n"
        "v_mfma_f32_16x16x32_bf16 a[64:67], v[32:35], v[64:67], a[64:67]\n"
        "ds_read_b128 v[56:59], v1 offset:14336\n"
        "s_waitcnt lgkmcnt(3)\n"
        "v_mfma_f32_16x16x32_bf16 a[64:67], v[36:39], v[68:71], a[64:67]\n"
        "ds_read_b128 v[60:63], v2 offset:14336\n"
        "v_mfma_f32_16x16x32_bf16 a[68:71], v[32:35], v[72:75], a[68:71]\n"
        "v_mfma_f32_16x16x32_bf16 a[68:71], v[36:39], v[76:79], a[68:71]\n"
        "v_mfma_f32_16x16x32_bf16 a[72:75], v[32:35], v[80:83], a[72:75]\n"
        "v_mfma_f32_16x16x32_bf16 a[72:75], v[36:39], v[84:87], a[72:75]\n"
        "v_mfma_f32_16x16x32_bf16 a[76:79], v[32:35], v[88:91], a[76:79]\n"
        "v_mfma_f32_16x16x32_bf16 a[76:79], v[36:39], v[92:95], a[76:79]\n"
        "s_waitcnt vmcnt(4) lgkmcnt(0)\n"
        "s_barrier\n"
        "s_mov_b32 s84, s80\n"
        "s_add_u32 s80, s80, 32768\n"
        "s_cmp_eq_u32 s80, 98304\n"
        "s_cselect_b32 s80, 0, s80\n"
        "s_sub_u32 s85, s80, s84\n"
        "v_xor_b32 v1, 32768, v1\n"
        "v_xor_b32 v2, 32768, v2\n"
        "v_add_u32 v3, s85, v3\n"
        "v_add_u32 v4, s85, v4\n"
        "v_add_u32 v5, s85, v5\n"
        "v_add_u32 v6, s85, v6\n"
        "s_setprio 0\n"
        "v_mfma_f32_16x16x32_bf16 a[80:83], v[40:43], v[64:67], a[80:83]\n"
        "s_mov_b32 m0, s72\n"
        "s_nop 0\n"
        "buffer_load_dwordx4 v8, s[64:67], s74 offen lds\n"
        "v_mfma_f32_16x16x32_bf16 a[80:83], v[44:47], v[68:71], a[80:83]\n"
        "ds_read_b128 v[32:35], v1 offset:0\n"
        "v_mfma_f32_16x16x32_bf16 a[84:87], v[40:43], v[72:75], a[84:87]\n"
        "ds_read_b128 v[36:39], v2 offset:0\n"
        "v_mfma_f32_16x16x32_bf16 a[84:87], v[44:47], v[76:79], a[84:87]\n"
        "ds_read_b64_tr_b16 v[96:97], v3 offset:0\n"
        "v_mfma_f32_16x16x32_bf16 a[88:91], v[40:43], v[80:83], a[88:91]\n"
        "ds_read_b64_tr_b16 v[98:99], v3 offset:1024\n"
        "v_mfma_f32_16x16x32_bf16 a[88:91], v[44:47], v[84:87], a[88:91]\n"
        "ds_read_b64_tr_b16 v[100:101], v3 offset:8192\n"
        "v_mfma_f32_16x16x32_bf16 a[92:95], v[40:43], v[88:91], a[92:95]\n"
        "ds_read_b64_tr_b16 v[102:103], v3 offset:9216\n"
        "v_mfma_f32_16x16x32_bf16 a[92:95], v[44:47], v[92:95], a[92:95]\n"
        "ds_read_b64_tr_b16 v[104:105], v4 offset:0\n"
        "s_setprio 1\n"
        "v_mfma_f32_16x16x32_bf16 a[96:99], v[48:51], v[64:67], a[96:99]\n"
        "buffer_load_dwordx4 v9, s[64:67], s74 offen offset:1024 lds\n"
        "v_mfma_f32_16x16x32_bf16 a[96:99], v[52:55], v[68:71], a[96:99]\n"
        "ds_read_b128 v[40:43], v1 offset:2048\n"
        "v_mfma_f32_16x16x32_bf16 a[100:103], v[48:51], v[72:75], a[100:103]\n"
        "ds_read_b128 v[44:47], v2 offset:2048\n"
        "v_mfma_f32_16x16x32_bf16 a[100:103], v[52:55], v[76:79], a[100:103]\n"
        "ds_read_b64_tr_b16 v[106:107], v4 offset:1024\n"
        "v_mfma_f32_16x16x32_bf16 a[104:107], v[48:51], v[80:83], a[104:107]\n"
        "ds_read_b64_tr_b16 v[108:109], v4 offset:8192\n"
        "v_mfma_f32_16x16x32_bf16 a[104:107], v[52:55], v[84:87], a[104:107]\n"
        "ds_read_b64_tr_b16 v[110:111], v4 offset:9216\n"
        "v_mfma_f32_16x16x32_bf16 a[108:111], v[48:51], v[88:91], a[108:111]\n"
        "ds_read_b64_tr_b16 v[112:113], v5 offset:0\n"
        "v_mfma_f32_16x16x32_bf16 a[108:111], v[52:55], v[92:95], a[108:111]\n"
        "ds_read_b64_tr_b16 v[114:115], v5 offset:1024\n"
        "s_setprio 0\n"
        "v_mfma_f32_16x16x32_bf16 a[112:115], v[56:59], v[64:67], a[112:115]\n"
        "buffer_load_dwordx4 v10, s[64:67], s74 offen offset:2048 lds\n"
        "ds_read_b128 v[48:51], v1 offset:4096\n"
        "v_mfma_f32_16x16x32_bf16 a[112:115], v[60:63], v[68:71], a[112:115]\n"
        "ds_read_b128 v[52:55], v2 offset:4096\n"
        "ds_read_b64_tr_b16 v[116:117], v5 offset:8192\n"
        "v_mfma_f32_16x16x32_bf16 a[116:119], v[56:59], v[72:75], a[116:119]\n"
        "ds_read_b64_tr_b16 v[118:119], v5 offset:9216\n"
        "buffer_load_dwordx4 v11, s[64:67], s74 offen offset:3072 lds\n"
        "s_xor_b32 s72, s72, 32768\n"
        "s_add_u32 s74, s74, 128\n"
        "v_mfma_f32_16x16x32_bf16 a[116:119], v[60:63], v[76:79], a[116:119]\n"
        "ds_read_b64_tr_b16 v[120:121], v6 offset:0\n"
        "ds_read_b64_tr_b16 v[122:123], v6 offset:1024\n"
        "v_mfma_f32_16x16x32_bf16 a[120:123], v[56:59], v[80:83], a[120:123]\n"
        "ds_read_b64_tr_b16 v[124:125], v6 offset:8192\n"
        "ds_read_b64_tr_b16 v[126:127], v6 offset:9216\n"
        "v_mfma_f32_16x16x32_bf16 a[120:123], v[60:63], v[84:87], a[120:123]\n"
        "v_mfma_f32_16x16x32_bf16 a[124:127], v[56:59], v[88:91], a[124:127]\n"
        "v_mfma_f32_16x16x32_bf16 a[124:127], v[60:63], v[92:95], a[124:127]\n"
        "s_setprio 1\n"
        "s_waitcnt lgkmcnt(8)\n"
        "v_mfma_f32_16x16x32_bf16 a[0:3], v[32:35], v[96:99], a[0:3]\n"
        "s_add_u32 m0, s73, s84\n"
        "s_nop 0\n"
        "buffer_load_dwordx4 v12, s[68:71], s75 offen lds\n"
        "v_mfma_f32_16x16x32_bf16 a[0:3], v[36:39], v[100:103], a[0:3]\n"
        "ds_read_b128 v[56:59], v1 offset:6144\n"
        "v_mfma_f32_16x16x32_bf16 a[4:7], v[32:35], v[104:107], a[4:7]\n"
        "ds_read_b128 v[60:63], v2 offset:6144\n"
        "v_mfma_f32_16x16x32_bf16 a[4:7], v[36:39], v[108:111], a[4:7]\n"
        "v_mfma_f32_16x16x32_bf16 a[8:11], v[32:35], v[112:115], a[8:11]\n"
        "s_waitcnt lgkmcnt(6)\n"
        "v_mfma_f32_16x16x32_bf16 a[8:11], v[36:39], v[116:119], a[8:11]\n"
        "s_waitcnt lgkmcnt(4)\n"
        "v_mfma_f32_16x16x32_bf16 a[12:15], v[32:35], v[120:123], a[12:15]\n"
        "s_waitcnt lgkmcnt(2)\n"
        "v_mfma_f32_16x16x32_bf16 a[12:15], v[36:39], v[124:127], a[12:15]\n"
        "s_setprio 0\n"
        "v_mfma_f32_16x16x32_bf16 a[16:19], v[40:43], v[96:99], a[16:19]\n"
        "buffer_load_dwordx4 v13, s[68:71], s75 offen offset:1024 lds\n"
        "v_mfma_f32_16x16x32_bf16 a[16:19], v[44:47], v[100:103], a[16:19]\n"
        "ds_read_b128 v[32:35], v1 offset:8192\n"
        "v_mfma_f32_16x16x32_bf16 a[20:23], v[40:43], v[104:107], a[20:23]\n"
        "ds_read_b128 v[36:39], v2 offset:8192\n"
        "v_mfma_f32_16x16x32_bf16 a[20:23], v[44:47], v[108:111], a[20:23]\n"
        "v_mfma_f32_16x16x32_bf16 a[24:27], v[40:43], v[112:115], a[24:27]\n"
        "v_mfma_f32_16x16x32_bf16 a[24:27], v[44:47], v[116:119], a[24:27]\n"
        "v_mfma_f32_16x16x32_bf16 a[28:31], v[40:43], v[120:123], a[28:31]\n"
        "v_mfma_f32_16x16x32_bf16 a[28:31], v[44:47], v[124:127], a[28:31]\n"
        "s_setprio 1\n"
        "v_mfma_f32_16x16x32_bf16 a[32:35], v[48:51], v[96:99], a[32:35]\n"
        "buffer_load_dwordx4 v14, s[68:71], s75 offen offset:2048 lds\n"
        "v_mfma_f32_16x16x32_bf16 a[32:35], v[52:55], v[100:103], a[32:35]\n"
        "ds_read_b128 v[40:43], v1 offset:10240\n"
        "v_mfma_f32_16x16x32_bf16 a[36:39], v[48:51], v[104:107], a[36:39]\n"
        "ds_read_b128 v[44:47], v2 offset:10240\n"
        "v_mfma_f32_16x16x32_bf16 a[36:39], v[52:55], v[108:111], a[36:39]\n"
        "v_mfma_f32_16x16x32_bf16 a[40:43], v[48:51], v[112:115], a[40:43]\n"
        "v_mfma_f32_16x16x32_bf16 a[40:43], v[52:55], v[116:119], a[40:43]\n"
        "v_mfma_f32_16x16x32_bf16 a[44:47], v[48:51], v[120:123], a[44:47]\n"
        "v_mfma_f32_16x16x32_bf16 a[44:47], v[52:55], v[124:127], a[44:47]\n"
        "s_setprio 0\n"
        "s_waitcnt lgkmcnt(3)\n"
        "v_mfma_f32_16x16x32_bf16 a[48:51], v[56:59], v[96:99], a[48:51]\n"
        "buffer_load_dwordx4 v15, s[68:71], s75 offen offset:3072 lds\n"
        "s_add_u32 s75, s75, 0x80000\n"
        "v_mfma_f32_16x16x32_bf16 a[48:51], v[60:63], v[100:103], a[48:51]\n"
        "ds_read_b128 v[48:51], v1 offset:12288\n"
        "v_mfma_f32_16x16x32_bf16 a[52:55], v[56:59], v[104:107], a[52:55]\n"
        "ds_read_b128 v[52:55], v2 offset:12288\n"
        "v_mfma_f32_16x16x32_bf16 a[52:55], v[60:63], v[108:111], a[52:55]\n"
        "v_mfma_f32_16x16x32_bf16 a[56:59], v[56:59], v[112:115], a[56:59]\n"
        "v_mfma_f32_16x16x32_bf16 a[56:59], v[60:63], v[116:119], a[56:59]\n"
        "v_mfma_f32_16x16x32_bf16 a[60:63], v[56:59], v[120:123], a[60:63]\n"
        "v_mfma_f32_16x16x32_bf16 a[60:63], v[60:63], v[124:127], a[60:63]\n"
        "s_setprio 1\n"
        "v_mfma_f32_16x16x32_bf16 a[64:67], v[32:35], v[96:99], a[64:67]\n"
        "ds_read_b128 v[56:59], v1 offset:14336\n"
        "s_waitcnt lgkmcnt(3)\n"
        "v_mfma_f32_16x16x32_bf16 a[64:67], v[36:39], v[100:103], a[64:67]\n"
        "ds_read_b128 v[60:63], v2 offset:14336\n"
        "v_mfma_f32_16x16x32_bf16 a[68:71], v[32:35], v[104:107], a[68:71]\n"
        "v_mfma_f32_16x16x32_bf16 a[68:71], v[36:39], v[108:111], a[68:71]\n"
        "v_mfma_f32_16x16x32_bf16 a[72:75], v[32:35], v[112:115], a[72:75]\n"
        "v_mfma_f32_16x16x32_bf16 a[72:75], v[36:39], v[116:119], a[72:75]\n"
        "v_mfma_f32_16x16x32_bf16 a[76:79], v[32:35], v[120:123], a[76:79]\n"
        "v_mfma_f32_16x16x32_bf16 a[76:79], v[36:39], v[124:127], a[76:79]\n"
        "s_waitcnt vmcnt(4) lgkmcnt(0)\n"
        "s_barrier\n"
        "s_mov_b32 s84, s80\n"
        "s_add_u32 s80, s80, 32768\n"
        "s_cmp_eq_u32 s80, 98304\n"
        "s_cselect_b32 s80, 0, s80\n"
        "s_sub_u32 s85, s80, s84\n"
        "v_xor_b32 v1, 32768, v1\n"
        "v_xor_b32 v2, 32768, v2\n"
        "v_add_u32 v3, s85, v3\n"
        "v_add_u32 v4, s85, v4\n"
        "v_add_u32 v5, s85, v5\n"
        "v_add_u32 v6, s85, v6\n"
        "s_setprio 0\n"
        "v_mfma_f32_16x16x32_bf16 a[80:83], v[40:43], v[96:99], a[80:83]\n"
        "s_mov_b32 m0, s72\n"
        "s_nop 0\n"
        "buffer_load_dwordx4 v8, s[64:67], s74 offen lds\n"
        "v_mfma_f32_16x16x32_bf16 a[80:83], v[44:47], v[100:103], a[80:83]\n"
        "ds_read_b128 v[32:35], v1 offset:0\n"
        "v_mfma_f32_16x16x32_bf16 a[84:87], v[40:43], v[104:107], a[84:87]\n"
        "ds_read_b128 v[36:39], v2 offset:0\n"
        "v_mfma_f32_16x16x32_bf16 a[84:87], v[44:47], v[108:111], a[84:87]\n"
        "ds_read_b64_tr_b16 v[64:65], v3 offset:0\n"
        "v_mfma_f32_16x16x32_bf16 a[88:91], v[40:43], v[112:115], a[88:91]\n"
        "ds_read_b64_tr_b16 v[66:67], v3 offset:1024\n"
        "v_mfma_f32_16x16x32_bf16 a[88:91], v[44:47], v[116:119], a[88:91]\n"
        "ds_read_b64_tr_b16 v[68:69], v3 offset:8192\n"
        "v_mfma_f32_16x16x32_bf16 a[92:95], v[40:43], v[120:123], a[92:95]\n"
        "ds_read_b64_tr_b16 v[70:71], v3 offset:9216\n"
        "v_mfma_f32_16x16x32_bf16 a[92:95], v[44:47], v[124:127], a[92:95]\n"
        "ds_read_b64_tr_b16 v[72:73], v4 offset:0\n"
        "s_setprio 1\n"
        "v_mfma_f32_16x16x32_bf16 a[96:99], v[48:51], v[96:99], a[96:99]\n"
        "buffer_load_dwordx4 v9, s[64:67], s74 offen offset:1024 lds\n"
        "v_mfma_f32_16x16x32_bf16 a[96:99], v[52:55], v[100:103], a[96:99]\n"
        "ds_read_b128 v[40:43], v1 offset:2048\n"
        "v_mfma_f32_16x16x32_bf16 a[100:103], v[48:51], v[104:107], a[100:103]\n"
        "ds_read_b128 v[44:47], v2 offset:2048\n"
        "v_mfma_f32_16x16x32_bf16 a[100:103], v[52:55], v[108:111], a[100:103]\n"
        "ds_read_b64_tr_b16 v[74:75], v4 offset:1024\n"
        "v_mfma_f32_16x16x32_bf16 a[104:107], v[48:51], v[112:115], a[104:107]\n"
        "ds_read_b64_tr_b16 v[76:77], v4 offset:8192\n"
        "v_mfma_f32_16x16x32_bf16 a[104:107], v[52:55], v[116:119], a[104:107]\n"
        "ds_read_b64_tr_b16 v[78:79], v4 offset:9216\n"
        "v_mfma_f32_16x16x32_bf16 a[108:111], v[48:51], v[120:123], a[108:111]\n"
        "ds_read_b64_tr_b16 v[80:81], v5 offset:0\n"
        "v_mfma_f32_16x16x32_bf16 a[108:111], v[52:55], v[124:127], a[108:111]\n"
        "ds_read_b64_tr_b16 v[82:83], v5 offset:1024\n"
        "s_setprio 0\n"
        "v_mfma_f32_16x16x32_bf16 a[112:115], v[56:59], v[96:99], a[112:115]\n"
        "buffer_load_dwordx4 v10, s[64:67], s74 offen offset:2048 lds\n"
        "ds_read_b128 v[48:51], v1 offset:4096\n"
        "v_mfma_f32_16x16x32_bf16 a[112:115], v[60:63], v[100:103], a[112:115]\n"
        "ds_read_b128 v[52:55], v2 offset:4096\n"
        "ds_read_b64_tr_b16 v[84:85], v5 offset:8192\n"
        "v_mfma_f32_16x16x32_bf16 a[116:119], v[56:59], v[104:107], a[116:119]\n"
        "ds_read_b64_tr_b16 v[86:87], v5 offset:9216\n"
        "buffer_load_dwordx4 v11, s[64:67], s74 offen offset:3072 lds\n"
        "s_xor_b32 s72, s72, 32768\n"
        "s_add_u32 s74, s74, 128\n"
        "v_mfma_f32_16x16x32_bf16 a[116:119], v[60:63], v[108:111], a[116:119]\n"
        "ds_read_b64_tr_b16 v[88:89], v6 offset:0\n"
        "ds_read_b64_tr_b16 v[90:91], v6 offset:1024\n"
        "v_mfma_f32_16x16x32_bf16 a[120:123], v[56:59], v[112:115], a[120:123]\n"
        "ds_read_b64_tr_b16 v[92:93], v6 offset:8192\n"
        "ds_read_b64_tr_b16 v[94:95], v6 offset:9216\n"
        "v_mfma_f32_16x16x32_bf16 a[120:123], v[60:63], v[116:119], a[120:123]\n"
        "v_mfma_f32_16x16x32_bf16 a[124:127], v[56:59], v[120:123], a[124:127]\n"
        "v_mfma_f32_16x16x32_bf16 a[124:127], v[60:63], v[124:127], a[124:127]\n"
        "s_sub_u32 s39, s39, 1\n"
        "s_cmp_lg_u32 s39, 0\n"
        "s_cbranch_scc1 P1V_LOOP_%=\n"
        "s_setprio 1\n"
        "s_waitcnt lgkmcnt(8)\n"
        "v_mfma_f32_16x16x32_bf16 a[0:3], v[32:35], v[64:67], a[0:3]\n"
        "ds_read_b128 v[56:59], v1 offset:6144\n"
        "v_mfma_f32_16x16x32_bf16 a[0:3], v[36:39], v[68:71], a[0:3]\n"
        "ds_read_b128 v[60:63], v2 offset:6144\n"
        "v_mfma_f32_16x16x32_bf16 a[4:7], v[32:35], v[72:75], a[4:7]\n"
        "v_mfma_f32_16x16x32_bf16 a[4:7], v[36:39], v[76:79], a[4:7]\n"
        "v_mfma_f32_16x16x32_bf16 a[8:11], v[32:35], v[80:83], a[8:11]\n"
        "s_waitcnt lgkmcnt(6)\n"
        "v_mfma_f32_16x16x32_bf16 a[8:11], v[36:39], v[84:87], a[8:11]\n"
        "s_waitcnt lgkmcnt(4)\n"
        "v_mfma_f32_16x16x32_bf16 a[12:15], v[32:35], v[88:91], a[12:15]\n"
        "s_waitcnt lgkmcnt(2)\n"
        "v_mfma_f32_16x16x32_bf16 a[12:15], v[36:39], v[92:95], a[12:15]\n"
        "s_setprio 0\n"
        "v_mfma_f32_16x16x32_bf16 a[16:19], v[40:43], v[64:67], a[16:19]\n"
        "ds_read_b128 v[32:35], v1 offset:8192\n"
        "v_mfma_f32_16x16x32_bf16 a[16:19], v[44:47], v[68:71], a[16:19]\n"
        "ds_read_b128 v[36:39], v2 offset:8192\n"
        "v_mfma_f32_16x16x32_bf16 a[20:23], v[40:43], v[72:75], a[20:23]\n"
        "v_mfma_f32_16x16x32_bf16 a[20:23], v[44:47], v[76:79], a[20:23]\n"
        "v_mfma_f32_16x16x32_bf16 a[24:27], v[40:43], v[80:83], a[24:27]\n"
        "v_mfma_f32_16x16x32_bf16 a[24:27], v[44:47], v[84:87], a[24:27]\n"
        "v_mfma_f32_16x16x32_bf16 a[28:31], v[40:43], v[88:91], a[28:31]\n"
        "v_mfma_f32_16x16x32_bf16 a[28:31], v[44:47], v[92:95], a[28:31]\n"
        "s_setprio 1\n"
        "v_mfma_f32_16x16x32_bf16 a[32:35], v[48:51], v[64:67], a[32:35]\n"
        "ds_read_b128 v[40:43], v1 offset:10240\n"
        "v_mfma_f32_16x16x32_bf16 a[32:35], v[52:55], v[68:71], a[32:35]\n"
        "ds_read_b128 v[44:47], v2 offset:10240\n"
        "v_mfma_f32_16x16x32_bf16 a[36:39], v[48:51], v[72:75], a[36:39]\n"
        "v_mfma_f32_16x16x32_bf16 a[36:39], v[52:55], v[76:79], a[36:39]\n"
        "v_mfma_f32_16x16x32_bf16 a[40:43], v[48:51], v[80:83], a[40:43]\n"
        "v_mfma_f32_16x16x32_bf16 a[40:43], v[52:55], v[84:87], a[40:43]\n"
        "v_mfma_f32_16x16x32_bf16 a[44:47], v[48:51], v[88:91], a[44:47]\n"
        "v_mfma_f32_16x16x32_bf16 a[44:47], v[52:55], v[92:95], a[44:47]\n"
        "s_setprio 0\n"
        "s_waitcnt lgkmcnt(3)\n"
        "v_mfma_f32_16x16x32_bf16 a[48:51], v[56:59], v[64:67], a[48:51]\n"
        "ds_read_b128 v[48:51], v1 offset:12288\n"
        "v_mfma_f32_16x16x32_bf16 a[48:51], v[60:63], v[68:71], a[48:51]\n"
        "ds_read_b128 v[52:55], v2 offset:12288\n"
        "v_mfma_f32_16x16x32_bf16 a[52:55], v[56:59], v[72:75], a[52:55]\n"
        "v_mfma_f32_16x16x32_bf16 a[52:55], v[60:63], v[76:79], a[52:55]\n"
        "v_mfma_f32_16x16x32_bf16 a[56:59], v[56:59], v[80:83], a[56:59]\n"
        "v_mfma_f32_16x16x32_bf16 a[56:59], v[60:63], v[84:87], a[56:59]\n"
        "v_mfma_f32_16x16x32_bf16 a[60:63], v[56:59], v[88:91], a[60:63]\n"
        "v_mfma_f32_16x16x32_bf16 a[60:63], v[60:63], v[92:95], a[60:63]\n"
        "s_setprio 1\n"
        "v_mfma_f32_16x16x32_bf16 a[64:67], v[32:35], v[64:67], a[64:67]\n"
        "ds_read_b128 v[56:59], v1 offset:14336\n"
        "s_waitcnt lgkmcnt(3)\n"
        "v_mfma_f32_16x16x32_bf16 a[64:67], v[36:39], v[68:71], a[64:67]\n"
        "ds_read_b128 v[60:63], v2 offset:14336\n"
        "v_mfma_f32_16x16x32_bf16 a[68:71], v[32:35], v[72:75], a[68:71]\n"
        "v_mfma_f32_16x16x32_bf16 a[68:71], v[36:39], v[76:79], a[68:71]\n"
        "v_mfma_f32_16x16x32_bf16 a[72:75], v[32:35], v[80:83], a[72:75]\n"
        "v_mfma_f32_16x16x32_bf16 a[72:75], v[36:39], v[84:87], a[72:75]\n"
        "v_mfma_f32_16x16x32_bf16 a[76:79], v[32:35], v[88:91], a[76:79]\n"
        "v_mfma_f32_16x16x32_bf16 a[76:79], v[36:39], v[92:95], a[76:79]\n"
        "s_waitcnt vmcnt(0) lgkmcnt(0)\n"
        "s_barrier\n"
        "s_mov_b32 s84, s80\n"
        "s_add_u32 s80, s80, 32768\n"
        "s_cmp_eq_u32 s80, 98304\n"
        "s_cselect_b32 s80, 0, s80\n"
        "s_sub_u32 s85, s80, s84\n"
        "v_xor_b32 v1, 32768, v1\n"
        "v_xor_b32 v2, 32768, v2\n"
        "v_add_u32 v3, s85, v3\n"
        "v_add_u32 v4, s85, v4\n"
        "v_add_u32 v5, s85, v5\n"
        "v_add_u32 v6, s85, v6\n"
        "s_setprio 0\n"
        "v_mfma_f32_16x16x32_bf16 a[80:83], v[40:43], v[64:67], a[80:83]\n"
        "ds_read_b128 v[32:35], v1 offset:0\n"
        "v_mfma_f32_16x16x32_bf16 a[80:83], v[44:47], v[68:71], a[80:83]\n"
        "ds_read_b128 v[36:39], v2 offset:0\n"
        "v_mfma_f32_16x16x32_bf16 a[84:87], v[40:43], v[72:75], a[84:87]\n"
        "ds_read_b64_tr_b16 v[96:97], v3 offset:0\n"
        "v_mfma_f32_16x16x32_bf16 a[84:87], v[44:47], v[76:79], a[84:87]\n"
        "ds_read_b64_tr_b16 v[98:99], v3 offset:1024\n"
        "v_mfma_f32_16x16x32_bf16 a[88:91], v[40:43], v[80:83], a[88:91]\n"
        "ds_read_b64_tr_b16 v[100:101], v3 offset:8192\n"
        "v_mfma_f32_16x16x32_bf16 a[88:91], v[44:47], v[84:87], a[88:91]\n"
        "ds_read_b64_tr_b16 v[102:103], v3 offset:9216\n"
        "v_mfma_f32_16x16x32_bf16 a[92:95], v[40:43], v[88:91], a[92:95]\n"
        "ds_read_b64_tr_b16 v[104:105], v4 offset:0\n"
        "v_mfma_f32_16x16x32_bf16 a[92:95], v[44:47], v[92:95], a[92:95]\n"
        "s_setprio 1\n"
        "v_mfma_f32_16x16x32_bf16 a[96:99], v[48:51], v[64:67], a[96:99]\n"
        "ds_read_b128 v[40:43], v1 offset:2048\n"
        "v_mfma_f32_16x16x32_bf16 a[96:99], v[52:55], v[68:71], a[96:99]\n"
        "ds_read_b128 v[44:47], v2 offset:2048\n"
        "v_mfma_f32_16x16x32_bf16 a[100:103], v[48:51], v[72:75], a[100:103]\n"
        "ds_read_b64_tr_b16 v[106:107], v4 offset:1024\n"
        "v_mfma_f32_16x16x32_bf16 a[100:103], v[52:55], v[76:79], a[100:103]\n"
        "ds_read_b64_tr_b16 v[108:109], v4 offset:8192\n"
        "v_mfma_f32_16x16x32_bf16 a[104:107], v[48:51], v[80:83], a[104:107]\n"
        "ds_read_b64_tr_b16 v[110:111], v4 offset:9216\n"
        "v_mfma_f32_16x16x32_bf16 a[104:107], v[52:55], v[84:87], a[104:107]\n"
        "ds_read_b64_tr_b16 v[112:113], v5 offset:0\n"
        "v_mfma_f32_16x16x32_bf16 a[108:111], v[48:51], v[88:91], a[108:111]\n"
        "ds_read_b64_tr_b16 v[114:115], v5 offset:1024\n"
        "v_mfma_f32_16x16x32_bf16 a[108:111], v[52:55], v[92:95], a[108:111]\n"
        "s_setprio 0\n"
        "v_mfma_f32_16x16x32_bf16 a[112:115], v[56:59], v[64:67], a[112:115]\n"
        "ds_read_b128 v[48:51], v1 offset:4096\n"
        "v_mfma_f32_16x16x32_bf16 a[112:115], v[60:63], v[68:71], a[112:115]\n"
        "ds_read_b128 v[52:55], v2 offset:4096\n"
        "v_mfma_f32_16x16x32_bf16 a[116:119], v[56:59], v[72:75], a[116:119]\n"
        "ds_read_b64_tr_b16 v[116:117], v5 offset:8192\n"
        "v_mfma_f32_16x16x32_bf16 a[116:119], v[60:63], v[76:79], a[116:119]\n"
        "ds_read_b64_tr_b16 v[118:119], v5 offset:9216\n"
        "v_mfma_f32_16x16x32_bf16 a[120:123], v[56:59], v[80:83], a[120:123]\n"
        "ds_read_b64_tr_b16 v[120:121], v6 offset:0\n"
        "v_mfma_f32_16x16x32_bf16 a[120:123], v[60:63], v[84:87], a[120:123]\n"
        "ds_read_b64_tr_b16 v[122:123], v6 offset:1024\n"
        "v_mfma_f32_16x16x32_bf16 a[124:127], v[56:59], v[88:91], a[124:127]\n"
        "ds_read_b64_tr_b16 v[124:125], v6 offset:8192\n"
        "v_mfma_f32_16x16x32_bf16 a[124:127], v[60:63], v[92:95], a[124:127]\n"
        "ds_read_b64_tr_b16 v[126:127], v6 offset:9216\n"
        "s_setprio 1\n"
        "s_waitcnt lgkmcnt(9)\n"
        "v_mfma_f32_16x16x32_bf16 a[0:3], v[32:35], v[96:99], a[0:3]\n"
        "ds_read_b128 v[56:59], v1 offset:6144\n"
        "v_mfma_f32_16x16x32_bf16 a[0:3], v[36:39], v[100:103], a[0:3]\n"
        "ds_read_b128 v[60:63], v2 offset:6144\n"
        "v_mfma_f32_16x16x32_bf16 a[4:7], v[32:35], v[104:107], a[4:7]\n"
        "v_mfma_f32_16x16x32_bf16 a[4:7], v[36:39], v[108:111], a[4:7]\n"
        "s_waitcnt lgkmcnt(8)\n"
        "v_mfma_f32_16x16x32_bf16 a[8:11], v[32:35], v[112:115], a[8:11]\n"
        "s_waitcnt lgkmcnt(6)\n"
        "v_mfma_f32_16x16x32_bf16 a[8:11], v[36:39], v[116:119], a[8:11]\n"
        "s_waitcnt lgkmcnt(4)\n"
        "v_mfma_f32_16x16x32_bf16 a[12:15], v[32:35], v[120:123], a[12:15]\n"
        "s_waitcnt lgkmcnt(2)\n"
        "v_mfma_f32_16x16x32_bf16 a[12:15], v[36:39], v[124:127], a[12:15]\n"
        "s_setprio 0\n"
        "v_mfma_f32_16x16x32_bf16 a[16:19], v[40:43], v[96:99], a[16:19]\n"
        "ds_read_b128 v[32:35], v1 offset:8192\n"
        "v_mfma_f32_16x16x32_bf16 a[16:19], v[44:47], v[100:103], a[16:19]\n"
        "ds_read_b128 v[36:39], v2 offset:8192\n"
        "v_mfma_f32_16x16x32_bf16 a[20:23], v[40:43], v[104:107], a[20:23]\n"
        "v_mfma_f32_16x16x32_bf16 a[20:23], v[44:47], v[108:111], a[20:23]\n"
        "v_mfma_f32_16x16x32_bf16 a[24:27], v[40:43], v[112:115], a[24:27]\n"
        "v_mfma_f32_16x16x32_bf16 a[24:27], v[44:47], v[116:119], a[24:27]\n"
        "v_mfma_f32_16x16x32_bf16 a[28:31], v[40:43], v[120:123], a[28:31]\n"
        "v_mfma_f32_16x16x32_bf16 a[28:31], v[44:47], v[124:127], a[28:31]\n"
        "s_setprio 1\n"
        "v_mfma_f32_16x16x32_bf16 a[32:35], v[48:51], v[96:99], a[32:35]\n"
        "ds_read_b128 v[40:43], v1 offset:10240\n"
        "v_mfma_f32_16x16x32_bf16 a[32:35], v[52:55], v[100:103], a[32:35]\n"
        "ds_read_b128 v[44:47], v2 offset:10240\n"
        "v_mfma_f32_16x16x32_bf16 a[36:39], v[48:51], v[104:107], a[36:39]\n"
        "v_mfma_f32_16x16x32_bf16 a[36:39], v[52:55], v[108:111], a[36:39]\n"
        "v_mfma_f32_16x16x32_bf16 a[40:43], v[48:51], v[112:115], a[40:43]\n"
        "v_mfma_f32_16x16x32_bf16 a[40:43], v[52:55], v[116:119], a[40:43]\n"
        "v_mfma_f32_16x16x32_bf16 a[44:47], v[48:51], v[120:123], a[44:47]\n"
        "v_mfma_f32_16x16x32_bf16 a[44:47], v[52:55], v[124:127], a[44:47]\n"
        "s_setprio 0\n"
        "s_waitcnt lgkmcnt(3)\n"
        "v_mfma_f32_16x16x32_bf16 a[48:51], v[56:59], v[96:99], a[48:51]\n"
        "ds_read_b128 v[48:51], v1 offset:12288\n"
        "v_mfma_f32_16x16x32_bf16 a[48:51], v[60:63], v[100:103], a[48:51]\n"
        "ds_read_b128 v[52:55], v2 offset:12288\n"
        "v_mfma_f32_16x16x32_bf16 a[52:55], v[56:59], v[104:107], a[52:55]\n"
        "v_mfma_f32_16x16x32_bf16 a[52:55], v[60:63], v[108:111], a[52:55]\n"
        "v_mfma_f32_16x16x32_bf16 a[56:59], v[56:59], v[112:115], a[56:59]\n"
        "v_mfma_f32_16x16x32_bf16 a[56:59], v[60:63], v[116:119], a[56:59]\n"
        "v_mfma_f32_16x16x32_bf16 a[60:63], v[56:59], v[120:123], a[60:63]\n"
        "v_mfma_f32_16x16x32_bf16 a[60:63], v[60:63], v[124:127], a[60:63]\n"
        "s_setprio 1\n"
        "v_mfma_f32_16x16x32_bf16 a[64:67], v[32:35], v[96:99], a[64:67]\n"
        "ds_read_b128 v[56:59], v1 offset:14336\n"
        "s_waitcnt lgkmcnt(3)\n"
        "v_mfma_f32_16x16x32_bf16 a[64:67], v[36:39], v[100:103], a[64:67]\n"
        "ds_read_b128 v[60:63], v2 offset:14336\n"
        "v_mfma_f32_16x16x32_bf16 a[68:71], v[32:35], v[104:107], a[68:71]\n"
        "v_mfma_f32_16x16x32_bf16 a[68:71], v[36:39], v[108:111], a[68:71]\n"
        "v_mfma_f32_16x16x32_bf16 a[72:75], v[32:35], v[112:115], a[72:75]\n"
        "v_mfma_f32_16x16x32_bf16 a[72:75], v[36:39], v[116:119], a[72:75]\n"
        "v_mfma_f32_16x16x32_bf16 a[76:79], v[32:35], v[120:123], a[76:79]\n"
        "v_mfma_f32_16x16x32_bf16 a[76:79], v[36:39], v[124:127], a[76:79]\n"
        "s_waitcnt vmcnt(0) lgkmcnt(0)\n"
        "s_barrier\n"
        "s_setprio 0\n"
        "v_mfma_f32_16x16x32_bf16 a[80:83], v[40:43], v[96:99], a[80:83]\n"
        "s_mov_b32 s52, 262144\n"
        "s_add_u32 s64, s86, s52\n"
        "s_addc_u32 s65, s87, 0\n"
        "s_and_b32 s65, s65, 0xffff\n"
        "s_mov_b32 s66, 2621440\n"
        "s_mov_b32 s67, 0x20000\n"
        "s_lshl_b32 s52, s37, 8\n"
        "s_sub_u32 s53, 5242880, s52\n"
        "s_add_u32 s68, %[wqlo], s52\n"
        "s_addc_u32 s69, %[wqhi], 0\n"
        "s_and_b32 s69, s69, 0xffff\n"
        "s_mov_b32 s70, s53\n"
        "s_mov_b32 s71, 0x20000\n"
        "v_lshrrev_b32 v16, 4, v0\n"
        "v_lshrrev_b32 v17, 4, v16\n"
        "v_mfma_f32_16x16x32_bf16 a[80:83], v[44:47], v[100:103], a[80:83]\n"
        "v_and_b32 v18, 15, v16\n"
        "s_and_b32 s54, s36, 3\n"
        "s_lshl_b32 s54, s54, 4\n"
        "v_mfma_f32_16x16x32_bf16 a[84:87], v[40:43], v[104:107], a[84:87]\n"
        "v_or_b32 v20, 0, v17\n"
        "v_lshlrev_b32 v20, 1, v20\n"
        "v_xor_b32 v20, v18, v20\n"
        "v_mfma_f32_16x16x32_bf16 a[84:87], v[44:47], v[108:111], a[84:87]\n"
        "v_add_u32 v19, 0, v17\n"
        "v_add_u32 v19, s54, v19\n"
        "v_lshlrev_b32 v19, 13, v19\n"
        "v_mfma_f32_16x16x32_bf16 a[88:91], v[40:43], v[112:115], a[88:91]\n"
        "v_lshl_add_u32 v19, v20, 4, v19\n"
        "v_mov_b32 v1, v19\n"
        "v_or_b32 v20, 0, v17\n"
        "v_mfma_f32_16x16x32_bf16 a[88:91], v[44:47], v[116:119], a[88:91]\n"
        "v_lshlrev_b32 v20, 1, v20\n"
        "v_xor_b32 v20, v18, v20\n"
        "v_add_u32 v19, 4, v17\n"
        "v_mfma_f32_16x16x32_bf16 a[92:95], v[40:43], v[120:123], a[92:95]\n"
        "v_mfma_f32_16x16x32_bf16 a[92:95], v[44:47], v[124:127], a[92:95]\n"
        "s_setprio 1\n"
        "v_mfma_f32_16x16x32_bf16 a[96:99], v[48:51], v[96:99], a[96:99]\n"
        "v_add_u32 v19, s54, v19\n"
        "v_lshlrev_b32 v19, 13, v19\n"
        "v_lshl_add_u32 v19, v20, 4, v19\n"
        "v_mfma_f32_16x16x32_bf16 a[96:99], v[52:55], v[100:103], a[96:99]\n"
        "v_subrev_u32 v2, 1024, v19\n"
        "v_or_b32 v20, 4, v17\n"
        "v_lshlrev_b32 v20, 1, v20\n"
        "v_mfma_f32_16x16x32_bf16 a[100:103], v[48:51], v[104:107], a[100:103]\n"
        "v_xor_b32 v20, v18, v20\n"
        "v_add_u32 v19, 8, v17\n"
        "v_add_u32 v19, s54, v19\n"
        "v_mfma_f32_16x16x32_bf16 a[100:103], v[52:55], v[108:111], a[100:103]\n"
        "v_lshlrev_b32 v19, 13, v19\n"
        "v_lshl_add_u32 v19, v20, 4, v19\n"
        "v_subrev_u32 v3, 2048, v19\n"
        "v_mfma_f32_16x16x32_bf16 a[104:107], v[48:51], v[112:115], a[104:107]\n"
        "v_or_b32 v20, 4, v17\n"
        "v_lshlrev_b32 v20, 1, v20\n"
        "v_xor_b32 v20, v18, v20\n"
        "v_mfma_f32_16x16x32_bf16 a[104:107], v[52:55], v[116:119], a[104:107]\n"
        "v_add_u32 v19, 12, v17\n"
        "v_add_u32 v19, s54, v19\n"
        "v_lshlrev_b32 v19, 13, v19\n"
        "v_mfma_f32_16x16x32_bf16 a[108:111], v[48:51], v[120:123], a[108:111]\n"
        "v_mfma_f32_16x16x32_bf16 a[108:111], v[52:55], v[124:127], a[108:111]\n"
        "s_setprio 0\n"
        "v_mfma_f32_16x16x32_bf16 a[112:115], v[56:59], v[96:99], a[112:115]\n"
        "v_lshl_add_u32 v19, v20, 4, v19\n"
        "v_subrev_u32 v4, 3072, v19\n"
        "v_mov_b32 v5, v0\n"
        "v_mfma_f32_16x16x32_bf16 a[112:115], v[60:63], v[100:103], a[112:115]\n"
        "v_add_u32 v6, 18432, v0\n"
        "s_and_b32 s52, s36, 3\n"
        "s_sub_u32 s53, 15, s38\n"
        "v_mfma_f32_16x16x32_bf16 a[116:119], v[56:59], v[104:107], a[116:119]\n"
        "s_cmp_ge_u32 s52, 2\n"
        "s_cselect_b32 s53, s38, s53\n"
        "s_lshl_b32 s53, s53, 3\n"
        "v_mfma_f32_16x16x32_bf16 a[116:119], v[60:63], v[108:111], a[116:119]\n"
        "s_and_b32 s54, s52, 1\n"
        "s_lshl_b32 s54, s54, 2\n"
        "s_add_u32 s53, s53, s54\n"
        "v_mfma_f32_16x16x32_bf16 a[120:123], v[56:59], v[112:115], a[120:123]\n"
        "s_mul_i32 s74, s53, 20480\n"
        "s_add_u32 s75, s74, 40960\n"
        "s_mov_b32 s76, 0\n"
        "v_mfma_f32_16x16x32_bf16 a[120:123], v[60:63], v[116:119], a[120:123]\n"
        "v_lshrrev_b32 v20, 4, v0\n"
        "v_and_b32 v21, 15, v20\n"
        "v_bfe_u32 v20, v20, 4, 1\n"
        "s_sub_u32 s88, 15, s38\n"
        "v_mov_b32 v22, s88\n"
        "v_mov_b32 v23, s38\n"
        "v_cmp_eq_u32 vcc, 0, v20\n"
        "s_nop 1\n"
        "v_cndmask_b32 v20, v23, v22, vcc\n"
        "v_lshl_add_u32 v20, v20, 4, v21\n"
        "v_lshlrev_b32 v20, 3, v20\n"
        "v_add_u32 v20, 4096, v20\n"
        "global_load_dwordx2 v[22:23], v20, s[86:87] sc1\n"
        "s_and_b32 s52, s36, 3\n"
        "s_lshl_b32 s53, s52, 12\n"
        "s_add_u32 m0, s53, 0\n"
        "s_nop 0\n"
        "buffer_load_dwordx4 v1, s[68:71], s76 offen offset:0 lds\n"
        "buffer_load_dwordx4 v2, s[68:71], s76 offen offset:1024 lds\n"
        "buffer_load_dwordx4 v3, s[68:71], s76 offen offset:2048 lds\n"
        "buffer_load_dwordx4 v4, s[68:71], s76 offen offset:3072 lds\n"
        "s_add_u32 s76, s76, 0x80000\n"
        "s_and_b32 s52, s36, 3\n"
        "s_lshl_b32 s53, s52, 12\n"
        "s_add_u32 m0, s53, 49152\n"
        "s_nop 0\n"
        "buffer_load_dwordx4 v1, s[68:71], s76 offen offset:0 lds\n"
        "buffer_load_dwordx4 v2, s[68:71], s76 offen offset:1024 lds\n"
        "buffer_load_dwordx4 v3, s[68:71], s76 offen offset:2048 lds\n"
        "buffer_load_dwordx4 v4, s[68:71], s76 offen offset:3072 lds\n"
        "s_add_u32 s76, s76, 0x80000\n"
        "v_mfma_f32_16x16x32_bf16 a[124:127], v[56:59], v[120:123], a[124:127]\n"
        "v_mfma_f32_16x16x32_bf16 a[124:127], v[60:63], v[124:127], a[124:127]\n"
        "s_setprio 0\n"
        "s_waitcnt vmcnt(8)\n"
        "s_mov_b32 s90, 0\n"
        "LD_X_CHK_%=:\n"
        "v_cmp_eq_u64 vcc, s[96:97], v[22:23]\n"
        "s_nop 1\n"
        "s_cmp_eq_u64 vcc, exec\n"
        "s_cbranch_scc1 LD_X_GOT_%=\n"
        "s_sleep 2\n"
        "global_load_dwordx2 v[22:23], v20, s[86:87] sc1\n"
        "s_waitcnt vmcnt(0)\n"
        "s_add_u32 s90, s90, 1\n"
        "s_cmp_lt_u32 s90, 0x100000\n"
        "s_cbranch_scc1 LD_X_CHK_%=\n"
        "LD_X_GOT_%=:\n"
        "s_and_b32 s52, s36, 3\n"
        "s_lshl_b32 s53, s52, 13\n"
        "s_add_u32 m0, s53, 16384\n"
        "s_nop 0\n"
        "buffer_load_dwordx4 v5, s[64:67], s74 offen sc1 lds\n"
        "buffer_load_dwordx4 v5, s[64:67], s74 offen offset:1024 sc1 lds\n"
        "buffer_load_dwordx4 v6, s[64:67], s74 offen offset:2048 sc1 lds\n"
        "buffer_load_dwordx4 v6, s[64:67], s74 offen offset:3072 sc1 lds\n"
        "s_add_u32 s74, s74, 2048\n"
        "s_add_u32 m0, s53, 20480\n"
        "s_nop 0\n"
        "buffer_load_dwordx4 v5, s[64:67], s75 offen sc1 lds\n"
        "buffer_load_dwordx4 v5, s[64:67], s75 offen offset:1024 sc1 lds\n"
        "buffer_load_dwordx4 v6, s[64:67], s75 offen offset:2048 sc1 lds\n"
        "buffer_load_dwordx4 v6, s[64:67], s75 offen offset:3072 sc1 lds\n"
        "s_add_u32 s75, s75, 2048\n"
        "s_and_b32 s52, s36, 3\n"
        "s_lshl_b32 s53, s52, 13\n"
        "s_add_u32 m0, s53, 65536\n"
        "s_nop 0\n"
        "buffer_load_dwordx4 v5, s[64:67], s74 offen sc1 lds\n"
        "buffer_load_dwordx4 v5, s[64:67], s74 offen offset:1024 sc1 lds\n"
        "buffer_load_dwordx4 v6, s[64:67], s74 offen offset:2048 sc1 lds\n"
        "buffer_load_dwordx4 v6, s[64:67], s74 offen offset:3072 sc1 lds\n"
        "s_add_u32 s74, s74, 2048\n"
        "s_add_u32 m0, s53, 69632\n"
        "s_nop 0\n"
        "buffer_load_dwordx4 v5, s[64:67], s75 offen sc1 lds\n"
        "buffer_load_dwordx4 v5, s[64:67], s75 offen offset:1024 sc1 lds\n"
        "buffer_load_dwordx4 v6, s[64:67], s75 offen offset:2048 sc1 lds\n"
        "buffer_load_dwordx4 v6, s[64:67], s75 offen offset:3072 sc1 lds\n"
        "s_add_u32 s75, s75, 2048\n"
        "s_mov_b32 s48, s40\n"
        "s_mov_b32 s49, s41\n"
        "s_mov_b32 s50, s42\n"
        "s_mov_b32 s51, s43\n"
        "s_mov_b32 s60, s44\n"
        "s_mov_b32 s61, s45\n"
        "s_mov_b32 s62, s46\n"
        "s_mov_b32 s63, s47\n"
        "s_cmp_ge_u32 s38, 2\n"
        "s_cbranch_scc1 LD_HAND_%=\n"
        "s_nop 7\n"
        "s_and_b32 s52, s36, 1\n"
        "s_lshl_b32 s52, s52, 2\n"
        "s_lshl_b32 s53, s38, 3\n"
        "s_add_u32 s52, s52, s53\n"
        "s_lshl_b32 s52, s52, 13\n"
        "s_bfe_u32 s53, s36, 0x10001\n"
        "s_lshl_b32 s54, s53, 11\n"
        "s_add_u32 s84, s52, s54\n"
        "s_lshl_b32 s54, s53, 12\n"
        "s_add_u32 s85, s52, s54\n"
        "s_add_u32 s53, s85, 0\n"
        "v_accvgpr_read_b32 v32, a0\n"
        "v_accvgpr_read_b32 v33, a1\n"
        "v_accvgpr_read_b32 v34, a2\n"
        "v_accvgpr_read_b32 v35, a3\n"
        "v_accvgpr_read_b32 v36, a16\n"
        "v_accvgpr_read_b32 v37, a17\n"
        "v_accvgpr_read_b32 v38, a18\n"
        "v_accvgpr_read_b32 v39, a19\n"
        "v_cvt_pk_bf16_f32 v40, v32, v33\n"
        "v_cvt_pk_bf16_f32 v41, v34, v35\n"
        "v_cvt_pk_bf16_f32 v42, v36, v37\n"
        "v_cvt_pk_bf16_f32 v43, v38, v39\n"
        "buffer_store_dwordx4 v[40:43], v0, s[44:47], s53 offen offset:0 sc1\n"
        "v_accvgpr_read_b32 v44, a4\n"
        "v_accvgpr_read_b32 v45, a5\n"
        "v_accvgpr_read_b32 v46, a6\n"
        "v_accvgpr_read_b32 v47, a7\n"
        "v_accvgpr_read_b32 v48, a20\n"
        "v_accvgpr_read_b32 v49, a21\n"
        "v_accvgpr_read_b32 v50, a22\n"
        "v_accvgpr_read_b32 v51, a23\n"
        "v_cvt_pk_bf16_f32 v52, v44, v45\n"
        "v_cvt_pk_bf16_f32 v53, v46, v47\n"
        "v_cvt_pk_bf16_f32 v54, v48, v49\n"
        "v_cvt_pk_bf16_f32 v55, v50, v51\n"
        "buffer_store_dwordx4 v[52:55], v0, s[44:47], s53 offen offset:1024 sc1\n"
        "v_accvgpr_read_b32 v32, a8\n"
        "v_accvgpr_read_b32 v33, a9\n"
        "v_accvgpr_read_b32 v34, a10\n"
        "v_accvgpr_read_b32 v35, a11\n"
        "v_accvgpr_read_b32 v36, a24\n"
        "v_accvgpr_read_b32 v37, a25\n"
        "v_accvgpr_read_b32 v38, a26\n"
        "v_accvgpr_read_b32 v39, a27\n"
        "v_cvt_pk_bf16_f32 v40, v32, v33\n"
        "v_cvt_pk_bf16_f32 v41, v34, v35\n"
        "v_cvt_pk_bf16_f32 v42, v36, v37\n"
        "v_cvt_pk_bf16_f32 v43, v38, v39\n"
        "buffer_store_dwordx4 v[40:43], v0, s[44:47], s53 offen offset:2048 sc1\n"
        "v_accvgpr_read_b32 v44, a12\n"
        "v_accvgpr_read_b32 v45, a13\n"
        "v_accvgpr_read_b32 v46, a14\n"
        "v_accvgpr_read_b32 v47, a15\n"
        "v_accvgpr_read_b32 v48, a28\n"
        "v_accvgpr_read_b32 v49, a29\n"
        "v_accvgpr_read_b32 v50, a30\n"
        "v_accvgpr_read_b32 v51, a31\n"
        "v_cvt_pk_bf16_f32 v52, v44, v45\n"
        "v_cvt_pk_bf16_f32 v53, v46, v47\n"
        "v_cvt_pk_bf16_f32 v54, v48, v49\n"
        "v_cvt_pk_bf16_f32 v55, v50, v51\n"
        "buffer_store_dwordx4 v[52:55], v0, s[44:47], s53 offen offset:3072 sc1\n"
        "s_add_u32 s53, s85, 8192\n"
        "v_accvgpr_read_b32 v32, a32\n"
        "v_accvgpr_read_b32 v33, a33\n"
        "v_accvgpr_read_b32 v34, a34\n"
        "v_accvgpr_read_b32 v35, a35\n"
        "v_accvgpr_read_b32 v36, a48\n"
        "v_accvgpr_read_b32 v37, a49\n"
        "v_accvgpr_read_b32 v38, a50\n"
        "v_accvgpr_read_b32 v39, a51\n"
        "v_cvt_pk_bf16_f32 v40, v32, v33\n"
        "v_cvt_pk_bf16_f32 v41, v34, v35\n"
        "v_cvt_pk_bf16_f32 v42, v36, v37\n"
        "v_cvt_pk_bf16_f32 v43, v38, v39\n"
        "buffer_store_dwordx4 v[40:43], v0, s[44:47], s53 offen offset:0 sc1\n"
        "v_accvgpr_read_b32 v44, a36\n"
        "v_accvgpr_read_b32 v45, a37\n"
        "v_accvgpr_read_b32 v46, a38\n"
        "v_accvgpr_read_b32 v47, a39\n"
        "v_accvgpr_read_b32 v48, a52\n"
        "v_accvgpr_read_b32 v49, a53\n"
        "v_accvgpr_read_b32 v50, a54\n"
        "v_accvgpr_read_b32 v51, a55\n"
        "v_cvt_pk_bf16_f32 v52, v44, v45\n"
        "v_cvt_pk_bf16_f32 v53, v46, v47\n"
        "v_cvt_pk_bf16_f32 v54, v48, v49\n"
        "v_cvt_pk_bf16_f32 v55, v50, v51\n"
        "buffer_store_dwordx4 v[52:55], v0, s[44:47], s53 offen offset:1024 sc1\n"
        "v_accvgpr_read_b32 v32, a40\n"
        "v_accvgpr_read_b32 v33, a41\n"
        "v_accvgpr_read_b32 v34, a42\n"
        "v_accvgpr_read_b32 v35, a43\n"
        "v_accvgpr_read_b32 v36, a56\n"
        "v_accvgpr_read_b32 v37, a57\n"
        "v_accvgpr_read_b32 v38, a58\n"
        "v_accvgpr_read_b32 v39, a59\n"
        "v_cvt_pk_bf16_f32 v40, v32, v33\n"
        "v_cvt_pk_bf16_f32 v41, v34, v35\n"
        "v_cvt_pk_bf16_f32 v42, v36, v37\n"
        "v_cvt_pk_bf16_f32 v43, v38, v39\n"
        "buffer_store_dwordx4 v[40:43], v0, s[44:47], s53 offen offset:2048 sc1\n"
        "v_accvgpr_read_b32 v44, a44\n"
        "v_accvgpr_read_b32 v45, a45\n"
        "v_accvgpr_read_b32 v46, a46\n"
        "v_accvgpr_read_b32 v47, a47\n"
        "v_accvgpr_read_b32 v48, a60\n"
        "v_accvgpr_read_b32 v49, a61\n"
        "v_accvgpr_read_b32 v50, a62\n"
        "v_accvgpr_read_b32 v51, a63\n"
        "v_cvt_pk_bf16_f32 v52, v44, v45\n"
        "v_cvt_pk_bf16_f32 v53, v46, v47\n"
        "v_cvt_pk_bf16_f32 v54, v48, v49\n"
        "v_cvt_pk_bf16_f32 v55, v50, v51\n"
        "buffer_store_dwordx4 v[52:55], v0, s[44:47], s53 offen offset:3072 sc1\n"
        "s_add_u32 s53, s85, 16384\n"
        "v_accvgpr_read_b32 v32, a64\n"
        "v_accvgpr_read_b32 v33, a65\n"
        "v_accvgpr_read_b32 v34, a66\n"
        "v_accvgpr_read_b32 v35, a67\n"
        "v_accvgpr_read_b32 v36, a80\n"
        "v_accvgpr_read_b32 v37, a81\n"
        "v_accvgpr_read_b32 v38, a82\n"
        "v_accvgpr_read_b32 v39, a83\n"
        "v_cvt_pk_bf16_f32 v40, v32, v33\n"
        "v_cvt_pk_bf16_f32 v41, v34, v35\n"
        "v_cvt_pk_bf16_f32 v42, v36, v37\n"
        "v_cvt_pk_bf16_f32 v43, v38, v39\n"
        "buffer_store_dwordx4 v[40:43], v0, s[44:47], s53 offen offset:0 sc1\n"
        "v_accvgpr_read_b32 v44, a68\n"
        "v_accvgpr_read_b32 v45, a69\n"
        "v_accvgpr_read_b32 v46, a70\n"
        "v_accvgpr_read_b32 v47, a71\n"
        "v_accvgpr_read_b32 v48, a84\n"
        "v_accvgpr_read_b32 v49, a85\n"
        "v_accvgpr_read_b32 v50, a86\n"
        "v_accvgpr_read_b32 v51, a87\n"
        "v_cvt_pk_bf16_f32 v52, v44, v45\n"
        "v_cvt_pk_bf16_f32 v53, v46, v47\n"
        "v_cvt_pk_bf16_f32 v54, v48, v49\n"
        "v_cvt_pk_bf16_f32 v55, v50, v51\n"
        "buffer_store_dwordx4 v[52:55], v0, s[44:47], s53 offen offset:1024 sc1\n"
        "v_accvgpr_read_b32 v32, a72\n"
        "v_accvgpr_read_b32 v33, a73\n"
        "v_accvgpr_read_b32 v34, a74\n"
        "v_accvgpr_read_b32 v35, a75\n"
        "v_accvgpr_read_b32 v36, a88\n"
        "v_accvgpr_read_b32 v37, a89\n"
        "v_accvgpr_read_b32 v38, a90\n"
        "v_accvgpr_read_b32 v39, a91\n"
        "v_cvt_pk_bf16_f32 v40, v32, v33\n"
        "v_cvt_pk_bf16_f32 v41, v34, v35\n"
        "v_cvt_pk_bf16_f32 v42, v36, v37\n"
        "v_cvt_pk_bf16_f32 v43, v38, v39\n"
        "buffer_store_dwordx4 v[40:43], v0, s[44:47], s53 offen offset:2048 sc1\n"
        "v_accvgpr_read_b32 v44, a76\n"
        "v_accvgpr_read_b32 v45, a77\n"
        "v_accvgpr_read_b32 v46, a78\n"
        "v_accvgpr_read_b32 v47, a79\n"
        "v_accvgpr_read_b32 v48, a92\n"
        "v_accvgpr_read_b32 v49, a93\n"
        "v_accvgpr_read_b32 v50, a94\n"
        "v_accvgpr_read_b32 v51, a95\n"
        "v_cvt_pk_bf16_f32 v52, v44, v45\n"
        "v_cvt_pk_bf16_f32 v53, v46, v47\n"
        "v_cvt_pk_bf16_f32 v54, v48, v49\n"
        "v_cvt_pk_bf16_f32 v55, v50, v51\n"
        "buffer_store_dwordx4 v[52:55], v0, s[44:47], s53 offen offset:3072 sc1\n"
        "s_add_u32 s53, s85, 24576\n"
        "v_accvgpr_read_b32 v32, a96\n"
        "v_accvgpr_read_b32 v33, a97\n"
        "v_accvgpr_read_b32 v34, a98\n"
        "v_accvgpr_read_b32 v35, a99\n"
        "v_accvgpr_read_b32 v36, a112\n"
        "v_accvgpr_read_b32 v37, a113\n"
        "v_accvgpr_read_b32 v38, a114\n"
        "v_accvgpr_read_b32 v39, a115\n"
        "v_cvt_pk_bf16_f32 v40, v32, v33\n"
        "v_cvt_pk_bf16_f32 v41, v34, v35\n"
        "v_cvt_pk_bf16_f32 v42, v36, v37\n"
        "v_cvt_pk_bf16_f32 v43, v38, v39\n"
        "buffer_store_dwordx4 v[40:43], v0, s[44:47], s53 offen offset:0 sc1\n"
        "v_accvgpr_read_b32 v44, a100\n"
        "v_accvgpr_read_b32 v45, a101\n"
        "v_accvgpr_read_b32 v46, a102\n"
        "v_accvgpr_read_b32 v47, a103\n"
        "v_accvgpr_read_b32 v48, a116\n"
        "v_accvgpr_read_b32 v49, a117\n"
        "v_accvgpr_read_b32 v50, a118\n"
        "v_accvgpr_read_b32 v51, a119\n"
        "v_cvt_pk_bf16_f32 v52, v44, v45\n"
        "v_cvt_pk_bf16_f32 v53, v46, v47\n"
        "v_cvt_pk_bf16_f32 v54, v48, v49\n"
        "v_cvt_pk_bf16_f32 v55, v50, v51\n"
        "buffer_store_dwordx4 v[52:55], v0, s[44:47], s53 offen offset:1024 sc1\n"
        "v_accvgpr_read_b32 v32, a104\n"
        "v_accvgpr_read_b32 v33, a105\n"
        "v_accvgpr_read_b32 v34, a106\n"
        "v_accvgpr_read_b32 v35, a107\n"
        "v_accvgpr_read_b32 v36, a120\n"
        "v_accvgpr_read_b32 v37, a121\n"
        "v_accvgpr_read_b32 v38, a122\n"
        "v_accvgpr_read_b32 v39, a123\n"
        "v_cvt_pk_bf16_f32 v40, v32, v33\n"
        "v_cvt_pk_bf16_f32 v41, v34, v35\n"
        "v_cvt_pk_bf16_f32 v42, v36, v37\n"
        "v_cvt_pk_bf16_f32 v43, v38, v39\n"
        "buffer_store_dwordx4 v[40:43], v0, s[44:47], s53 offen offset:2048 sc1\n"
        "v_accvgpr_read_b32 v44, a108\n"
        "v_accvgpr_read_b32 v45, a109\n"
        "v_accvgpr_read_b32 v46, a110\n"
        "v_accvgpr_read_b32 v47, a111\n"
        "v_accvgpr_read_b32 v48, a124\n"
        "v_accvgpr_read_b32 v49, a125\n"
        "v_accvgpr_read_b32 v50, a126\n"
        "v_accvgpr_read_b32 v51, a127\n"
        "v_cvt_pk_bf16_f32 v52, v44, v45\n"
        "v_cvt_pk_bf16_f32 v53, v46, v47\n"
        "v_cvt_pk_bf16_f32 v54, v48, v49\n"
        "v_cvt_pk_bf16_f32 v55, v50, v51\n"
        "buffer_store_dwordx4 v[52:55], v0, s[44:47], s53 offen offset:3072 sc1\n"
        "s_mov_b32 s72, 100\n"
        "s_mov_b32 s78, 2\n"
        "s_barrier\n"
        "s_branch LD_H2_%=\n"
        "LD_HAND_%=:\n"
        "s_nop 7\n"
        "v_accvgpr_read_b32 v32, a0\n"
        "v_accvgpr_read_b32 v33, a1\n"
        "v_accvgpr_read_b32 v34, a2\n"
        "v_accvgpr_read_b32 v35, a3\n"
        "v_accvgpr_read_b32 v36, a16\n"
        "v_accvgpr_read_b32 v37, a17\n"
        "v_accvgpr_read_b32 v38, a18\n"
        "v_accvgpr_read_b32 v39, a19\n"
        "v_cvt_pk_bf16_f32 v64, v32, v33\n"
        "v_cvt_pk_bf16_f32 v65, v34, v35\n"
        "v_cvt_pk_bf16_f32 v66, v36, v37\n"
        "v_cvt_pk_bf16_f32 v67, v38, v39\n"
        "v_accvgpr_read_b32 v40, a4\n"
        "v_accvgpr_read_b32 v41, a5\n"
        "v_accvgpr_read_b32 v42, a6\n"
        "v_accvgpr_read_b32 v43, a7\n"
        "v_accvgpr_read_b32 v44, a20\n"
        "v_accvgpr_read_b32 v45, a21\n"
        "v_accvgpr_read_b32 v46, a22\n"
        "v_accvgpr_read_b32 v47, a23\n"
        "v_cvt_pk_bf16_f32 v68, v40, v41\n"
        "v_cvt_pk_bf16_f32 v69, v42, v43\n"
        "v_cvt_pk_bf16_f32 v70, v44, v45\n"
        "v_cvt_pk_bf16_f32 v71, v46, v47\n"
        "v_accvgpr_read_b32 v48, a8\n"
        "v_accvgpr_read_b32 v49, a9\n"
        "v_accvgpr_read_b32 v50, a10\n"
        "v_accvgpr_read_b32 v51, a11\n"
        "v_accvgpr_read_b32 v52, a24\n"
        "v_accvgpr_read_b32 v53, a25\n"
        "v_accvgpr_read_b32 v54, a26\n"
        "v_accvgpr_read_b32 v55, a27\n"
        "v_cvt_pk_bf16_f32 v72, v48, v49\n"
        "v_cvt_pk_bf16_f32 v73, v50, v51\n"
        "v_cvt_pk_bf16_f32 v74, v52, v53\n"
        "v_cvt_pk_bf16_f32 v75, v54, v55\n"
        "v_accvgpr_read_b32 v56, a12\n"
        "v_accvgpr_read_b32 v57, a13\n"
        "v_accvgpr_read_b32 v58, a14\n"
        "v_accvgpr_read_b32 v59, a15\n"
        "v_accvgpr_read_b32 v60, a28\n"
        "v_accvgpr_read_b32 v61, a29\n"
        "v_accvgpr_read_b32 v62, a30\n"
        "v_accvgpr_read_b32 v63, a31\n"
        "v_cvt_pk_bf16_f32 v76, v56, v57\n"
        "v_cvt_pk_bf16_f32 v77, v58, v59\n"
        "v_cvt_pk_bf16_f32 v78, v60, v61\n"
        "v_cvt_pk_bf16_f32 v79, v62, v63\n"
        "v_accvgpr_read_b32 v32, a32\n"
        "v_accvgpr_read_b32 v33, a33\n"
        "v_accvgpr_read_b32 v34, a34\n"
        "v_accvgpr_read_b32 v35, a35\n"
        "v_accvgpr_read_b32 v36, a48\n"
        "v_accvgpr_read_b32 v37, a49\n"
        "v_accvgpr_read_b32 v38, a50\n"
        "v_accvgpr_read_b32 v39, a51\n"
        "v_cvt_pk_bf16_f32 v80, v32, v33\n"
        "v_cvt_pk_bf16_f32 v81, v34, v35\n"
        "v_cvt_pk_bf16_f32 v82, v36, v37\n"
        "v_cvt_pk_bf16_f32 v83, v38, v39\n"
        "v_accvgpr_read_b32 v40, a36\n"
        "v_accvgpr_read_b32 v41, a37\n"
        "v_accvgpr_read_b32 v42, a38\n"
        "v_accvgpr_read_b32 v43, a39\n"
        "v_accvgpr_read_b32 v44, a52\n"
        "v_accvgpr_read_b32 v45, a53\n"
        "v_accvgpr_read_b32 v46, a54\n"
        "v_accvgpr_read_b32 v47, a55\n"
        "v_cvt_pk_bf16_f32 v84, v40, v41\n"
        "v_cvt_pk_bf16_f32 v85, v42, v43\n"
        "v_cvt_pk_bf16_f32 v86, v44, v45\n"
        "v_cvt_pk_bf16_f32 v87, v46, v47\n"
        "v_accvgpr_read_b32 v48, a40\n"
        "v_accvgpr_read_b32 v49, a41\n"
        "v_accvgpr_read_b32 v50, a42\n"
        "v_accvgpr_read_b32 v51, a43\n"
        "v_accvgpr_read_b32 v52, a56\n"
        "v_accvgpr_read_b32 v53, a57\n"
        "v_accvgpr_read_b32 v54, a58\n"
        "v_accvgpr_read_b32 v55, a59\n"
        "v_cvt_pk_bf16_f32 v88, v48, v49\n"
        "v_cvt_pk_bf16_f32 v89, v50, v51\n"
        "v_cvt_pk_bf16_f32 v90, v52, v53\n"
        "v_cvt_pk_bf16_f32 v91, v54, v55\n"
        "v_accvgpr_read_b32 v56, a44\n"
        "v_accvgpr_read_b32 v57, a45\n"
        "v_accvgpr_read_b32 v58, a46\n"
        "v_accvgpr_read_b32 v59, a47\n"
        "v_accvgpr_read_b32 v60, a60\n"
        "v_accvgpr_read_b32 v61, a61\n"
        "v_accvgpr_read_b32 v62, a62\n"
        "v_accvgpr_read_b32 v63, a63\n"
        "v_cvt_pk_bf16_f32 v92, v56, v57\n"
        "v_cvt_pk_bf16_f32 v93, v58, v59\n"
        "v_cvt_pk_bf16_f32 v94, v60, v61\n"
        "v_cvt_pk_bf16_f32 v95, v62, v63\n"
        "v_accvgpr_read_b32 v32, a64\n"
        "v_accvgpr_read_b32 v33, a65\n"
        "v_accvgpr_read_b32 v34, a66\n"
        "v_accvgpr_read_b32 v35, a67\n"
        "v_accvgpr_read_b32 v36, a80\n"
        "v_accvgpr_read_b32 v37, a81\n"
        "v_accvgpr_read_b32 v38, a82\n"
        "v_accvgpr_read_b32 v39, a83\n"
        "v_cvt_pk_bf16_f32 v96, v32, v33\n"
        "v_cvt_pk_bf16_f32 v97, v34, v35\n"
        "v_cvt_pk_bf16_f32 v98, v36, v37\n"
        "v_cvt_pk_bf16_f32 v99, v38, v39\n"
        "v_accvgpr_read_b32 v40, a68\n"
        "v_accvgpr_read_b32 v41, a69\n"
        "v_accvgpr_read_b32 v42, a70\n"
        "v_accvgpr_read_b32 v43, a71\n"
        "v_accvgpr_read_b32 v44, a84\n"
        "v_accvgpr_read_b32 v45, a85\n"
        "v_accvgpr_read_b32 v46, a86\n"
        "v_accvgpr_read_b32 v47, a87\n"
        "v_cvt_pk_bf16_f32 v100, v40, v41\n"
        "v_cvt_pk_bf16_f32 v101, v42, v43\n"
        "v_cvt_pk_bf16_f32 v102, v44, v45\n"
        "v_cvt_pk_bf16_f32 v103, v46, v47\n"
        "v_accvgpr_read_b32 v48, a72\n"
        "v_accvgpr_read_b32 v49, a73\n"
        "v_accvgpr_read_b32 v50, a74\n"
        "v_accvgpr_read_b32 v51, a75\n"
        "v_accvgpr_read_b32 v52, a88\n"
        "v_accvgpr_read_b32 v53, a89\n"
        "v_accvgpr_read_b32 v54, a90\n"
        "v_accvgpr_read_b32 v55, a91\n"
        "v_cvt_pk_bf16_f32 v104, v48, v49\n"
        "v_cvt_pk_bf16_f32 v105, v50, v51\n"
        "v_cvt_pk_bf16_f32 v106, v52, v53\n"
        "v_cvt_pk_bf16_f32 v107, v54, v55\n"
        "v_accvgpr_read_b32 v56, a76\n"
        "v_accvgpr_read_b32 v57, a77\n"
        "v_accvgpr_read_b32 v58, a78\n"
        "v_accvgpr_read_b32 v59, a79\n"
        "v_accvgpr_read_b32 v60, a92\n"
        "v_accvgpr_read_b32 v61, a93\n"
        "v_accvgpr_read_b32 v62, a94\n"
        "v_accvgpr_read_b32 v63, a95\n"
        "v_cvt_pk_bf16_f32 v108, v56, v57\n"
        "v_cvt_pk_bf16_f32 v109, v58, v59\n"
        "v_cvt_pk_bf16_f32 v110, v60, v61\n"
        "v_cvt_pk_bf16_f32 v111, v62, v63\n"
        "v_accvgpr_read_b32 v32, a96\n"
        "v_accvgpr_read_b32 v33, a97\n"
        "v_accvgpr_read_b32 v34, a98\n"
        "v_accvgpr_read_b32 v35, a99\n"
        "v_accvgpr_read_b32 v36, a112\n"
        "v_accvgpr_read_b32 v37, a113\n"
        "v_accvgpr_read_b32 v38, a114\n"
        "v_accvgpr_read_b32 v39, a115\n"
        "v_cvt_pk_bf16_f32 v112, v32, v33\n"
        "v_cvt_pk_bf16_f32 v113, v34, v35\n"
        "v_cvt_pk_bf16_f32 v114, v36, v37\n"
        "v_cvt_pk_bf16_f32 v115, v38, v39\n"
        "v_accvgpr_read_b32 v40, a100\n"
        "v_accvgpr_read_b32 v41, a101\n"
        "v_accvgpr_read_b32 v42, a102\n"
        "v_accvgpr_read_b32 v43, a103\n"
        "v_accvgpr_read_b32 v44, a116\n"
        "v_accvgpr_read_b32 v45, a117\n"
        "v_accvgpr_read_b32 v46, a118\n"
        "v_accvgpr_read_b32 v47, a119\n"
        "v_cvt_pk_bf16_f32 v116, v40, v41\n"
        "v_cvt_pk_bf16_f32 v117, v42, v43\n"
        "v_cvt_pk_bf16_f32 v118, v44, v45\n"
        "v_cvt_pk_bf16_f32 v119, v46, v47\n"
        "v_accvgpr_read_b32 v48, a104\n"
        "v_accvgpr_read_b32 v49, a105\n"
        "v_accvgpr_read_b32 v50, a106\n"
        "v_accvgpr_read_b32 v51, a107\n"
        "v_accvgpr_read_b32 v52, a120\n"
        "v_accvgpr_read_b32 v53, a121\n"
        "v_accvgpr_read_b32 v54, a122\n"
        "v_accvgpr_read_b32 v55, a123\n"
        "v_cvt_pk_bf16_f32 v120, v48, v49\n"
        "v_cvt_pk_bf16_f32 v121, v50, v51\n"
        "v_cvt_pk_bf16_f32 v122, v52, v53\n"
        "v_cvt_pk_bf16_f32 v123, v54, v55\n"
        "v_accvgpr_read_b32 v56, a108\n"
        "v_accvgpr_read_b32 v57, a109\n"
        "v_accvgpr_read_b32 v58, a110\n"
        "v_accvgpr_read_b32 v59, a111\n"
        "v_accvgpr_read_b32 v60, a124\n"
        "v_accvgpr_read_b32 v61, a125\n"
        "v_accvgpr_read_b32 v62, a126\n"
        "v_accvgpr_read_b32 v63, a127\n"
        "v_cvt_pk_bf16_f32 v124, v56, v57\n"
        "v_cvt_pk_bf16_f32 v125, v58, v59\n"
        "v_cvt_pk_bf16_f32 v126, v60, v61\n"
        "v_cvt_pk_bf16_f32 v127, v62, v63\n"
        "s_lshr_b32 s52, s38, 1\n"
        "s_mov_b32 s72, 7\n"
        "s_cmp_eq_u32 s52, 2\n"
        "s_cselect_b32 s72, 11, s72\n"
        "s_cmp_eq_u32 s52, 3\n"
        "s_cselect_b32 s72, 13, s72\n"
        "s_add_u32 s78, s72, 6\n"
        "s_barrier\n"
        "s_and_b32 s52, s36, 3\n"
        "s_lshl_b32 s52, s52, 14\n"
        "s_add_u32 s52, s52, 98304\n"
        "v_add_u32 v7, s52, v0\n"
        "ds_read_b128 a[0:3], v7 offset:0\n"
        "ds_read_b128 a[4:7], v7 offset:1024\n"
        "ds_read_b128 a[8:11], v7 offset:2048\n"
        "ds_read_b128 a[12:15], v7 offset:3072\n"
        "ds_read_b128 a[16:19], v7 offset:4096\n"
        "ds_read_b128 a[20:23], v7 offset:5120\n"
        "ds_read_b128 a[24:27], v7 offset:6144\n"
        "ds_read_b128 a[28:31], v7 offset:7168\n"
        "ds_read_b128 a[32:35], v7 offset:8192\n"
        "ds_read_b128 a[36:39], v7 offset:9216\n"
        "ds_read_b128 a[40:43], v7 offset:10240\n"
        "ds_read_b128 a[44:47], v7 offset:11264\n"
        "ds_read_b128 a[48:51], v7 offset:12288\n"
        "ds_read_b128 a[52:55], v7 offset:13312\n"
        "ds_read_b128 a[56:59], v7 offset:14336\n"
        "ds_read_b128 a[60:63], v7 offset:15360\n"
        "s_waitcnt lgkmcnt(0)\n"
        "LD_H2_%=:\n"
        "s_and_b32 s52, s36, 1\n"
        "s_lshl_b32 s52, s52, 2\n"
        "s_lshl_b32 s53, s38, 3\n"
        "s_add_u32 s52, s52, s53\n"
        "s_lshl_b32 s52, s52, 13\n"
        "s_bfe_u32 s53, s36, 0x10001\n"
        "s_lshl_b32 s54, s53, 11\n"
        "s_add_u32 s73, s52, s54\n"
        "s_lshl_b32 s54, s53, 12\n"
        "s_add_u32 s77, s52, s54\n"
        "s_cmp_ge_u32 s38, 2\n"
        "s_cbranch_scc1 LD_W0H_%=\n"
        "s_waitcnt vmcnt(24)\n"
        "s_branch LD_W0D_%=\n"
        "LD_W0H_%=:\n"
        "s_waitcnt vmcnt(8)\n"
        "LD_W0D_%=:\n"
        "s_barrier\n"
        "s_and_b32 s52, s36, 3\n"
        "s_lshl_b32 s53, s52, 12\n"
        "s_add_u32 m0, s53, 98304\n"
        "s_nop 0\n"
        "buffer_load_dwordx4 v1, s[68:71], s76 offen offset:0 lds\n"
        "buffer_load_dwordx4 v2, s[68:71], s76 offen offset:1024 lds\n"
        "buffer_load_dwordx4 v3, s[68:71], s76 offen offset:2048 lds\n"
        "buffer_load_dwordx4 v4, s[68:71], s76 offen offset:3072 lds\n"
        "s_add_u32 s76, s76, 0x80000\n"
        "s_and_b32 s52, s36, 3\n"
        "s_lshl_b32 s53, s52, 13\n"
        "s_add_u32 m0, s53, 114688\n"
        "s_nop 0\n"
        "buffer_load_dwordx4 v5, s[64:67], s74 offen sc1 lds\n"
        "buffer_load_dwordx4 v5, s[64:67], s74 offen offset:1024 sc1 lds\n"
        "buffer_load_dwordx4 v6, s[64:67], s74 offen offset:2048 sc1 lds\n"
        "buffer_load_dwordx4 v6, s[64:67], s74 offen offset:3072 sc1 lds\n"
        "s_add_u32 s74, s74, 2048\n"
        "s_add_u32 m0, s53, 118784\n"
        "s_nop 0\n"
        "buffer_load_dwordx4 v5, s[64:67], s75 offen sc1 lds\n"
        "buffer_load_dwordx4 v5, s[64:67], s75 offen offset:1024 sc1 lds\n"
        "buffer_load_dwordx4 v6, s[64:67], s75 offen offset:2048 sc1 lds\n"
        "buffer_load_dwordx4 v6, s[64:67], s75 offen offset:3072 sc1 lds\n"
        "s_add_u32 s75, s75, 2048\n"
        "s_cmp_ge_u32 s38, 2\n"
        "s_cbranch_scc1 LD_W1H_%=\n"
        "s_waitcnt vmcnt(28)\n"
        "s_branch LD_W1D_%=\n"
        "LD_W1H_%=:\n"
        "s_waitcnt vmcnt(12)\n"
        "LD_W1D_%=:\n"
        "s_barrier\n"
        "s_and_b32 s52, s36, 3\n"
        "s_lshl_b32 s53, s52, 12\n"
        "s_add_u32 m0, s53, 0\n"
        "s_nop 0\n"
        "buffer_load_dwordx4 v1, s[68:71], s76 offen offset:0 lds\n"
        "buffer_load_dwordx4 v2, s[68:71], s76 offen offset:1024 lds\n"
        "buffer_load_dwordx4 v3, s[68:71], s76 offen offset:2048 lds\n"
        "buffer_load_dwordx4 v4, s[68:71], s76 offen offset:3072 lds\n"
        "s_add_u32 s76, s76, 0x80000\n"
        "s_and_b32 s52, s36, 3\n"
        "s_lshl_b32 s53, s52, 13\n"
        "s_add_u32 m0, s53, 16384\n"
        "s_nop 0\n"
        "buffer_load_dwordx4 v5, s[64:67], s74 offen sc1 lds\n"
        "buffer_load_dwordx4 v5, s[64:67], s74 offen offset:1024 sc1 lds\n"
        "buffer_load_dwordx4 v6, s[64:67], s74 offen offset:2048 sc1 lds\n"
        "buffer_load_dwordx4 v6, s[64:67], s74 offen offset:3072 sc1 lds\n"
        "s_add_u32 s74, s74, 2048\n"
        "s_add_u32 m0, s53, 20480\n"
        "s_nop 0\n"
        "buffer_load_dwordx4 v5, s[64:67], s75 offen sc1 lds\n"
        "buffer_load_dwordx4 v5, s[64:67], s75 offen offset:1024 sc1 lds\n"
        "buffer_load_dwordx4 v6, s[64:67], s75 offen offset:2048 sc1 lds\n"
        "buffer_load_dwordx4 v6, s[64:67], s75 offen offset:3072 sc1 lds\n"
        "s_add_u32 s75, s75, 2048\n"
        "s_waitcnt vmcnt(12)\n"
        "s_barrier\n"
        "s_cmp_lg_u32 s78, 2\n"
        "s_cbranch_scc1 LD_NS2_%=\n"
        "s_cmp_lg_u32 s36, 4\n"
        "s_cbranch_scc1 LD_NS2_%=\n"
        "s_lshl_b32 s88, s37, 3\n"
        "s_add_u32 s88, s88, s38\n"
        "s_lshl_b32 s88, s88, 7\n"
        "s_add_u32 s88, s88, 32768\n"
        "s_mov_b64 exec, 1\n"
        "v_mov_b32 v28, s88\n"
        "v_mov_b32 v30, s96\n"
        "v_mov_b32 v31, s97\n"
        "global_store_dwordx2 v28, v[30:31], s[86:87] sc1\n"
        "s_mov_b64 exec, -1\n"
        "LD_NS2_%=:\n"
        "s_and_b32 s52, s36, 3\n"
        "s_lshl_b32 s53, s52, 12\n"
        "s_add_u32 m0, s53, 49152\n"
        "s_nop 0\n"
        "buffer_load_dwordx4 v1, s[68:71], s76 offen offset:0 lds\n"
        "buffer_load_dwordx4 v2, s[68:71], s76 offen offset:1024 lds\n"
        "buffer_load_dwordx4 v3, s[68:71], s76 offen offset:2048 lds\n"
        "buffer_load_dwordx4 v4, s[68:71], s76 offen offset:3072 lds\n"
        "s_add_u32 s76, s76, 0x80000\n"
        "s_and_b32 s52, s36, 3\n"
        "s_lshl_b32 s53, s52, 13\n"
        "s_add_u32 m0, s53, 65536\n"
        "s_nop 0\n"
        "buffer_load_dwordx4 v5, s[64:67], s74 offen sc1 lds\n"
        "buffer_load_dwordx4 v5, s[64:67], s74 offen offset:1024 sc1 lds\n"
        "buffer_load_dwordx4 v6, s[64:67], s74 offen offset:2048 sc1 lds\n"
        "buffer_load_dwordx4 v6, s[64:67], s74 offen offset:3072 sc1 lds\n"
        "s_add_u32 s74, s74, 2048\n"
        "s_add_u32 m0, s53, 69632\n"
        "s_nop 0\n"
        "buffer_load_dwordx4 v5, s[64:67], s75 offen sc1 lds\n"
        "buffer_load_dwordx4 v5, s[64:67], s75 offen offset:1024 sc1 lds\n"
        "buffer_load_dwordx4 v6, s[64:67], s75 offen offset:2048 sc1 lds\n"
        "buffer_load_dwordx4 v6, s[64:67], s75 offen offset:3072 sc1 lds\n"
        "s_add_u32 s75, s75, 2048\n"
        "s_waitcnt vmcnt(12)\n"
        "s_barrier\n"
        "s_and_b32 s52, s36, 3\n"
        "s_lshl_b32 s53, s52, 12\n"
        "s_add_u32 m0, s53, 98304\n"
        "s_nop 0\n"
        "buffer_load_dwordx4 v1, s[68:71], s76 offen offset:0 lds\n"
        "buffer_load_dwordx4 v2, s[68:71], s76 offen offset:1024 lds\n"
        "buffer_load_dwordx4 v3, s[68:71], s76 offen offset:2048 lds\n"
        "buffer_load_dwordx4 v4, s[68:71], s76 offen offset:3072 lds\n"
        "s_add_u32 s76, s76, 0x80000\n"
        "s_and_b32 s52, s36, 3\n"
        "s_lshl_b32 s53, s52, 13\n"
        "s_add_u32 m0, s53, 114688\n"
        "s_nop 0\n"
        "buffer_load_dwordx4 v5, s[64:67], s74 offen sc1 lds\n"
        "buffer_load_dwordx4 v5, s[64:67], s74 offen offset:1024 sc1 lds\n"
        "buffer_load_dwordx4 v6, s[64:67], s74 offen offset:2048 sc1 lds\n"
        "buffer_load_dwordx4 v6, s[64:67], s74 offen offset:3072 sc1 lds\n"
        "s_add_u32 s74, s74, 2048\n"
        "s_add_u32 m0, s53, 118784\n"
        "s_nop 0\n"
        "buffer_load_dwordx4 v5, s[64:67], s75 offen sc1 lds\n"
        "buffer_load_dwordx4 v5, s[64:67], s75 offen offset:1024 sc1 lds\n"
        "buffer_load_dwordx4 v6, s[64:67], s75 offen offset:2048 sc1 lds\n"
        "buffer_load_dwordx4 v6, s[64:67], s75 offen offset:3072 sc1 lds\n"
        "s_add_u32 s75, s75, 2048\n"
        "s_waitcnt vmcnt(12)\n"
        "s_barrier\n"
        "s_and_b32 s52, s36, 3\n"
        "s_lshl_b32 s53, s52, 12\n"
        "s_add_u32 m0, s53, 0\n"
        "s_nop 0\n"
        "buffer_load_dwordx4 v1, s[68:71], s76 offen offset:0 lds\n"
        "buffer_load_dwordx4 v2, s[68:71], s76 offen offset:1024 lds\n"
        "buffer_load_dwordx4 v3, s[68:71], s76 offen offset:2048 lds\n"
        "buffer_load_dwordx4 v4, s[68:71], s76 offen offset:3072 lds\n"
        "s_add_u32 s76, s76, 0x80000\n"
        "s_and_b32 s52, s36, 3\n"
        "s_lshl_b32 s53, s52, 13\n"
        "s_add_u32 m0, s53, 16384\n"
        "s_nop 0\n"
        "buffer_load_dwordx4 v5, s[64:67], s74 offen sc1 lds\n"
        "buffer_load_dwordx4 v5, s[64:67], s74 offen offset:1024 sc1 lds\n"
        "buffer_load_dwordx4 v6, s[64:67], s74 offen offset:2048 sc1 lds\n"
        "buffer_load_dwordx4 v6, s[64:67], s74 offen offset:3072 sc1 lds\n"
        "s_add_u32 s74, s74, 2048\n"
        "s_add_u32 m0, s53, 20480\n"
        "s_nop 0\n"
        "buffer_load_dwordx4 v5, s[64:67], s75 offen sc1 lds\n"
        "buffer_load_dwordx4 v5, s[64:67], s75 offen offset:1024 sc1 lds\n"
        "buffer_load_dwordx4 v6, s[64:67], s75 offen offset:2048 sc1 lds\n"
        "buffer_load_dwordx4 v6, s[64:67], s75 offen offset:3072 sc1 lds\n"
        "s_add_u32 s75, s75, 2048\n"
        "s_waitcnt vmcnt(12)\n"
        "s_barrier\n"
        "s_and_b32 s52, s36, 3\n"
        "s_lshl_b32 s53, s52, 12\n"
        "s_add_u32 m0, s53, 49152\n"
        "s_nop 0\n"
        "buffer_load_dwordx4 v1, s[68:71], s76 offen offset:0 lds\n"
        "buffer_load_dwordx4 v2, s[68:71], s76 offen offset:1024 lds\n"
        "buffer_load_dwordx4 v3, s[68:71], s76 offen offset:2048 lds\n"
        "buffer_load_dwordx4 v4, s[68:71], s76 offen offset:3072 lds\n"
        "s_add_u32 s76, s76, 0x80000\n"
        "s_and_b32 s52, s36, 3\n"
        "s_lshl_b32 s53, s52, 13\n"
        "s_add_u32 m0, s53, 65536\n"
        "s_nop 0\n"
        "buffer_load_dwordx4 v5, s[64:67], s74 offen sc1 lds\n"
        "buffer_load_dwordx4 v5, s[64:67], s74 offen offset:1024 sc1 lds\n"
        "buffer_load_dwordx4 v6, s[64:67], s74 offen offset:2048 sc1 lds\n"
        "buffer_load_dwordx4 v6, s[64:67], s74 offen offset:3072 sc1 lds\n"
        "s_add_u32 s74, s74, 2048\n"
        "s_add_u32 m0, s53, 69632\n"
        "s_nop 0\n"
        "buffer_load_dwordx4 v5, s[64:67], s75 offen sc1 lds\n"
        "buffer_load_dwordx4 v5, s[64:67], s75 offen offset:1024 sc1 lds\n"
        "buffer_load_dwordx4 v6, s[64:67], s75 offen offset:2048 sc1 lds\n"
        "buffer_load_dwordx4 v6, s[64:67], s75 offen offset:3072 sc1 lds\n"
        "s_add_u32 s75, s75, 2048\n"
        "s_waitcnt vmcnt(12)\n"
        "s_barrier\n"
        "s_and_b32 s52, s36, 3\n"
        "s_lshl_b32 s53, s52, 12\n"
        "s_add_u32 m0, s53, 98304\n"
        "s_nop 0\n"
        "buffer_load_dwordx4 v1, s[68:71], s76 offen offset:0 lds\n"
        "buffer_load_dwordx4 v2, s[68:71], s76 offen offset:1024 lds\n"
        "buffer_load_dwordx4 v3, s[68:71], s76 offen offset:2048 lds\n"
        "buffer_load_dwordx4 v4, s[68:71], s76 offen offset:3072 lds\n"
        "s_add_u32 s76, s76, 0x80000\n"
        "s_and_b32 s52, s36, 3\n"
        "s_lshl_b32 s53, s52, 13\n"
        "s_add_u32 m0, s53, 114688\n"
        "s_nop 0\n"
        "buffer_load_dwordx4 v5, s[64:67], s74 offen sc1 lds\n"
        "buffer_load_dwordx4 v5, s[64:67], s74 offen offset:1024 sc1 lds\n"
        "buffer_load_dwordx4 v6, s[64:67], s74 offen offset:2048 sc1 lds\n"
        "buffer_load_dwordx4 v6, s[64:67], s74 offen offset:3072 sc1 lds\n"
        "s_add_u32 s74, s74, 2048\n"
        "s_add_u32 m0, s53, 118784\n"
        "s_nop 0\n"
        "buffer_load_dwordx4 v5, s[64:67], s75 offen sc1 lds\n"
        "buffer_load_dwordx4 v5, s[64:67], s75 offen offset:1024 sc1 lds\n"
        "buffer_load_dwordx4 v6, s[64:67], s75 offen offset:2048 sc1 lds\n"
        "buffer_load_dwordx4 v6, s[64:67], s75 offen offset:3072 sc1 lds\n"
        "s_add_u32 s75, s75, 2048\n"
        "s_mov_b32 s52, 0\n"
        "s_lshl_b32 s88, s37, 3\n"
        "s_add_u32 s88, s88, s52\n"
        "s_lshl_b32 s88, s88, 7\n"
        "s_add_u32 s88, s88, 32768\n"
        "v_mov_b32 v24, s88\n"
        "global_load_dwordx2 v[26:27], v24, s[86:87] sc1\n"
        "s_waitcnt vmcnt(13)\n"
        "s_barrier\n"
        "s_and_b32 s52, s36, 3\n"
        "s_lshl_b32 s53, s52, 12\n"
        "s_add_u32 m0, s53, 0\n"
        "s_nop 0\n"
        "buffer_load_dwordx4 v1, s[68:71], s76 offen offset:0 lds\n"
        "buffer_load_dwordx4 v2, s[68:71], s76 offen offset:1024 lds\n"
        "buffer_load_dwordx4 v3, s[68:71], s76 offen offset:2048 lds\n"
        "buffer_load_dwordx4 v4, s[68:71], s76 offen offset:3072 lds\n"
        "s_add_u32 s76, s76, 0x80000\n"
        "s_and_b32 s52, s36, 3\n"
        "s_lshl_b32 s53, s52, 13\n"
        "s_add_u32 m0, s53, 16384\n"
        "s_nop 0\n"
        "buffer_load_dwordx4 v5, s[64:67], s74 offen sc1 lds\n"
        "buffer_load_dwordx4 v5, s[64:67], s74 offen offset:1024 sc1 lds\n"
        "buffer_load_dwordx4 v6, s[64:67], s74 offen offset:2048 sc1 lds\n"
        "buffer_load_dwordx4 v6, s[64:67], s74 offen offset:3072 sc1 lds\n"
        "s_add_u32 s74, s74, 2048\n"
        "s_add_u32 m0, s53, 20480\n"
        "s_nop 0\n"
        "buffer_load_dwordx4 v5, s[64:67], s75 offen sc1 lds\n"
        "buffer_load_dwordx4 v5, s[64:67], s75 offen offset:1024 sc1 lds\n"
        "buffer_load_dwordx4 v6, s[64:67], s75 offen offset:2048 sc1 lds\n"
        "buffer_load_dwordx4 v6, s[64:67], s75 offen offset:3072 sc1 lds\n"
        "s_add_u32 s75, s75, 2048\n"
        "s_sub_u32 s52, 7, s72\n"
        "s_cmp_lt_u32 s52, 4\n"
        "s_cselect_b32 s52, 0x80000, 0\n"
        "s_mov_b32 s50, s52\n"
        "s_mov_b32 s62, s52\n"
        "s_add_u32 s53, s77, 0\n"
        "buffer_store_dwordx4 v[64:67], v0, s[60:63], s53 offen offset:0 sc1\n"
        "buffer_store_dwordx4 v[68:71], v0, s[60:63], s53 offen offset:1024 sc1\n"
        "buffer_store_dwordx4 v[72:75], v0, s[60:63], s53 offen offset:2048 sc1\n"
        "buffer_store_dwordx4 v[76:79], v0, s[60:63], s53 offen offset:3072 sc1\n"
        "s_add_u32 s53, s77, 8192\n"
        "buffer_store_dwordx4 v[80:83], v0, s[60:63], s53 offen offset:0 sc1\n"
        "buffer_store_dwordx4 v[84:87], v0, s[60:63], s53 offen offset:1024 sc1\n"
        "buffer_store_dwordx4 v[88:91], v0, s[60:63], s53 offen offset:2048 sc1\n"
        "buffer_store_dwordx4 v[92:95], v0, s[60:63], s53 offen offset:3072 sc1\n"
        "s_waitcnt vmcnt(20)\n"
        "s_mov_b32 s90, 0\n"
        "LD_KV0_CHK_%=:\n"
        "v_cmp_eq_u64 vcc, s[96:97], v[26:27]\n"
        "s_nop 1\n"
        "s_cmp_eq_u64 vcc, exec\n"
        "s_cbranch_scc1 LD_KV0_GOT_%=\n"
        "s_sleep 2\n"
        "global_load_dwordx2 v[26:27], v24, s[86:87] sc1\n"
        "s_waitcnt vmcnt(0)\n"
        "s_add_u32 s90, s90, 1\n"
        "s_cmp_lt_u32 s90, 0x100000\n"
        "s_cbranch_scc1 LD_KV0_CHK_%=\n"
        "LD_KV0_GOT_%=:\n"
        "s_barrier\n"
        "s_sub_u32 s52, s36, 4\n"
        "s_and_b32 s59, s52, 1\n"
        "s_lshl_b32 s59, s59, 12\n"
        "s_lshr_b32 s53, s52, 1\n"
        "s_add_u32 s57, s53, 1\n"
        "s_add_u32 s58, s53, 1\n"
        "s_sub_u32 s56, 16, s38\n"
        "s_lshl_b32 s56, s56, 2\n"
        "s_lshl_b32 s54, s52, 11\n"
        "s_add_u32 m0, s54, 65536\n"
        "s_nop 0\n"
        "buffer_load_dwordx4 v0, s[40:43], s54 offen sc1 lds\n"
        "buffer_load_dwordx4 v0, s[40:43], s54 offen offset:1024 sc1 lds\n"
        "s_cmp_ge_u32 s57, s56\n"
        "s_cselect_b32 s54, s56, 0\n"
        "s_sub_u32 s54, s57, s54\n"
        "s_lshl_b32 s54, s54, 13\n"
        "s_add_u32 s54, s54, s59\n"
        "s_cmp_ge_u32 s57, 68\n"
        "s_cselect_b32 s54, 1073741824, s54\n"
        "s_and_b32 s55, s57, 7\n"
        "s_lshl_b32 s55, s55, 13\n"
        "s_add_u32 s55, s55, s59\n"
        "s_add_u32 m0, s55, 65536\n"
        "s_nop 0\n"
        "buffer_load_dwordx4 v0, s[40:43], s54 offen offset:0 sc1 lds\n"
        "buffer_load_dwordx4 v0, s[40:43], s54 offen offset:1024 sc1 lds\n"
        "buffer_load_dwordx4 v0, s[40:43], s54 offen offset:2048 sc1 lds\n"
        "buffer_load_dwordx4 v0, s[40:43], s54 offen offset:3072 sc1 lds\n"
        "s_add_u32 s57, s57, 2\n"
        "s_mov_b32 s52, 1\n"
        "s_lshl_b32 s88, s37, 3\n"
        "s_add_u32 s88, s88, s52\n"
        "s_lshl_b32 s88, s88, 7\n"
        "s_add_u32 s88, s88, 32768\n"
        "v_mov_b32 v20, s88\n"
        "global_load_dwordx2 v[22:23], v20, s[86:87] sc1\n"
        "s_sub_u32 s52, 8, s72\n"
        "s_cmp_lt_u32 s52, 4\n"
        "s_cselect_b32 s52, 0x80000, 0\n"
        "s_mov_b32 s50, s52\n"
        "s_mov_b32 s62, s52\n"
        "s_add_u32 s53, s77, 16384\n"
        "buffer_store_dwordx4 v[96:99], v0, s[60:63], s53 offen offset:0 sc1\n"
        "buffer_store_dwordx4 v[100:103], v0, s[60:63], s53 offen offset:1024 sc1\n"
        "buffer_store_dwordx4 v[104:107], v0, s[60:63], s53 offen offset:2048 sc1\n"
        "buffer_store_dwordx4 v[108:111], v0, s[60:63], s53 offen offset:3072 sc1\n"
        "s_add_u32 s53, s77, 24576\n"
        "buffer_store_dwordx4 v[112:115], v0, s[60:63], s53 offen offset:0 sc1\n"
        "buffer_store_dwordx4 v[116:119], v0, s[60:63], s53 offen offset:1024 sc1\n"
        "buffer_store_dwordx4 v[120:123], v0, s[60:63], s53 offen offset:2048 sc1\n"
        "buffer_store_dwordx4 v[124:127], v0, s[60:63], s53 offen offset:3072 sc1\n"
        "s_waitcnt vmcnt(23)\n"
        "s_barrier\n"
        "s_cmp_ge_u32 s57, s56\n"
        "s_cselect_b32 s54, s56, 0\n"
        "s_sub_u32 s54, s57, s54\n"
        "s_lshl_b32 s54, s54, 13\n"
        "s_add_u32 s54, s54, s59\n"
        "s_cmp_ge_u32 s57, 68\n"
        "s_cselect_b32 s54, 1073741824, s54\n"
        "s_and_b32 s55, s57, 7\n"
        "s_lshl_b32 s55, s55, 13\n"
        "s_add_u32 s55, s55, s59\n"
        "s_add_u32 m0, s55, 65536\n"
        "s_nop 0\n"
        "buffer_load_dwordx4 v0, s[40:43], s54 offen offset:0 sc1 lds\n"
        "buffer_load_dwordx4 v0, s[40:43], s54 offen offset:1024 sc1 lds\n"
        "buffer_load_dwordx4 v0, s[40:43], s54 offen offset:2048 sc1 lds\n"
        "buffer_load_dwordx4 v0, s[40:43], s54 offen offset:3072 sc1 lds\n"
        "s_add_u32 s57, s57, 2\n"
        "s_barrier\n"
        "s_sub_u32 s52, s36, 4\n"
        "s_lshl_b32 s54, s52, 11\n"
        "s_add_u32 m0, s54, 0\n"
        "s_nop 0\n"
        "buffer_load_dwordx4 v0, s[44:47], s54 offen sc1 lds\n"
        "buffer_load_dwordx4 v0, s[44:47], s54 offen offset:1024 sc1 lds\n"
        "s_cmp_ge_u32 s58, s56\n"
        "s_cselect_b32 s54, s56, 0\n"
        "s_sub_u32 s54, s58, s54\n"
        "s_lshl_b32 s54, s54, 13\n"
        "s_add_u32 s54, s54, s59\n"
        "s_cmp_ge_u32 s58, 68\n"
        "s_cselect_b32 s54, 1073741824, s54\n"
        "s_and_b32 s55, s58, 7\n"
        "s_lshl_b32 s55, s55, 13\n"
        "s_add_u32 s55, s55, s59\n"
        "s_add_u32 m0, s55, 0\n"
        "s_nop 0\n"
        "buffer_load_dwordx4 v0, s[44:47], s54 offen offset:0 sc1 lds\n"
        "buffer_load_dwordx4 v0, s[44:47], s54 offen offset:1024 sc1 lds\n"
        "buffer_load_dwordx4 v0, s[44:47], s54 offen offset:2048 sc1 lds\n"
        "buffer_load_dwordx4 v0, s[44:47], s54 offen offset:3072 sc1 lds\n"
        "s_add_u32 s58, s58, 2\n"
        "s_waitcnt vmcnt(19)\n"
        "s_barrier\n"
        "s_cmp_ge_u32 s57, s56\n"
        "s_cselect_b32 s54, s56, 0\n"
        "s_sub_u32 s54, s57, s54\n"
        "s_lshl_b32 s54, s54, 13\n"
        "s_add_u32 s54, s54, s59\n"
        "s_cmp_ge_u32 s57, 68\n"
        "s_cselect_b32 s54, 1073741824, s54\n"
        "s_and_b32 s55, s57, 7\n"
        "s_lshl_b32 s55, s55, 13\n"
        "s_add_u32 s55, s55, s59\n"
        "s_add_u32 m0, s55, 65536\n"
        "s_nop 0\n"
        "buffer_load_dwordx4 v0, s[40:43], s54 offen offset:0 sc1 lds\n"
        "buffer_load_dwordx4 v0, s[40:43], s54 offen offset:1024 sc1 lds\n"
        "buffer_load_dwordx4 v0, s[40:43], s54 offen offset:2048 sc1 lds\n"
        "buffer_load_dwordx4 v0, s[40:43], s54 offen offset:3072 sc1 lds\n"
        "s_add_u32 s57, s57, 2\n"
        "s_cmp_ge_u32 s58, s56\n"
        "s_cselect_b32 s54, s56, 0\n"
        "s_sub_u32 s54, s58, s54\n"
        "s_lshl_b32 s54, s54, 13\n"
        "s_add_u32 s54, s54, s59\n"
        "s_cmp_ge_u32 s58, 68\n"
        "s_cselect_b32 s54, 1073741824, s54\n"
        "s_and_b32 s55, s58, 7\n"
        "s_lshl_b32 s55, s55, 13\n"
        "s_add_u32 s55, s55, s59\n"
        "s_add_u32 m0, s55, 0\n"
        "s_nop 0\n"
        "buffer_load_dwordx4 v0, s[44:47], s54 offen offset:0 sc1 lds\n"
        "buffer_load_dwordx4 v0, s[44:47], s54 offen offset:1024 sc1 lds\n"
        "buffer_load_dwordx4 v0, s[44:47], s54 offen offset:2048 sc1 lds\n"
        "buffer_load_dwordx4 v0, s[44:47], s54 offen offset:3072 sc1 lds\n"
        "s_add_u32 s58, s58, 2\n"
        "s_add_u32 s52, s57, 2\n"
        "s_cmp_lt_u32 s52, s56\n"
        "s_cselect_b32 s52, s52, 0\n"
        "s_lshr_b32 s52, s52, 3\n"
        "s_lshl_b32 s88, s37, 3\n"
        "s_add_u32 s88, s88, s52\n"
        "s_lshl_b32 s88, s88, 7\n"
        "s_add_u32 s88, s88, 32768\n"
        "v_mov_b32 v24, s88\n"
        "global_load_dwordx2 v[26:27], v24, s[86:87] sc1\n"
        "s_sub_u32 s52, 9, s72\n"
        "s_cmp_lt_u32 s52, 4\n"
        "s_cselect_b32 s52, 0x80000, 0\n"
        "s_mov_b32 s50, s52\n"
        "s_mov_b32 s62, s52\n"
        "s_add_u32 s53, s73, 0\n"
        "buffer_store_dwordx4 a[0:3], v0, s[48:51], s53 offen offset:0 sc1\n"
        "buffer_store_dwordx4 a[4:7], v0, s[48:51], s53 offen offset:1024 sc1\n"
        "s_add_u32 s53, s73, 4096\n"
        "buffer_store_dwordx4 a[8:11], v0, s[48:51], s53 offen offset:0 sc1\n"
        "buffer_store_dwordx4 a[12:15], v0, s[48:51], s53 offen offset:1024 sc1\n"
        "s_add_u32 s53, s73, 8192\n"
        "buffer_store_dwordx4 a[16:19], v0, s[48:51], s53 offen offset:0 sc1\n"
        "buffer_store_dwordx4 a[20:23], v0, s[48:51], s53 offen offset:1024 sc1\n"
        "s_add_u32 s53, s73, 12288\n"
        "buffer_store_dwordx4 a[24:27], v0, s[48:51], s53 offen offset:0 sc1\n"
        "buffer_store_dwordx4 a[28:31], v0, s[48:51], s53 offen offset:1024 sc1\n"
        "s_waitcnt vmcnt(17)\n"
        "s_mov_b32 s90, 0\n"
        "LD_KVQ0_CHK_%=:\n"
        "v_cmp_eq_u64 vcc, s[96:97], v[22:23]\n"
        "s_nop 1\n"
        "s_cmp_eq_u64 vcc, exec\n"
        "s_cbranch_scc1 LD_KVQ0_GOT_%=\n"
        "s_sleep 2\n"
        "global_load_dwordx2 v[22:23], v20, s[86:87] sc1\n"
        "s_waitcnt vmcnt(0)\n"
        "s_add_u32 s90, s90, 1\n"
        "s_cmp_lt_u32 s90, 0x100000\n"
        "s_cbranch_scc1 LD_KVQ0_CHK_%=\n"
        "LD_KVQ0_GOT_%=:\n"
        "s_barrier\n"
        "s_cmp_ge_u32 s57, s56\n"
        "s_cselect_b32 s54, s56, 0\n"
        "s_sub_u32 s54, s57, s54\n"
        "s_lshl_b32 s54, s54, 13\n"
        "s_add_u32 s54, s54, s59\n"
        "s_cmp_ge_u32 s57, 68\n"
        "s_cselect_b32 s54, 1073741824, s54\n"
        "s_and_b32 s55, s57, 7\n"
        "s_lshl_b32 s55, s55, 13\n"
        "s_add_u32 s55, s55, s59\n"
        "s_add_u32 m0, s55, 65536\n"
        "s_nop 0\n"
        "buffer_load_dwordx4 v0, s[40:43], s54 offen offset:0 sc1 lds\n"
        "buffer_load_dwordx4 v0, s[40:43], s54 offen offset:1024 sc1 lds\n"
        "buffer_load_dwordx4 v0, s[40:43], s54 offen offset:2048 sc1 lds\n"
        "buffer_load_dwordx4 v0, s[40:43], s54 offen offset:3072 sc1 lds\n"
        "s_add_u32 s57, s57, 2\n"
        "s_cmp_ge_u32 s58, s56\n"
        "s_cselect_b32 s54, s56, 0\n"
        "s_sub_u32 s54, s58, s54\n"
        "s_lshl_b32 s54, s54, 13\n"
        "s_add_u32 s54, s54, s59\n"
        "s_cmp_ge_u32 s58, 68\n"
        "s_cselect_b32 s54, 1073741824, s54\n"
        "s_and_b32 s55, s58, 7\n"
        "s_lshl_b32 s55, s55, 13\n"
        "s_add_u32 s55, s55, s59\n"
        "s_add_u32 m0, s55, 0\n"
        "s_nop 0\n"
        "buffer_load_dwordx4 v0, s[44:47], s54 offen offset:0 sc1 lds\n"
        "buffer_load_dwordx4 v0, s[44:47], s54 offen offset:1024 sc1 lds\n"
        "buffer_load_dwordx4 v0, s[44:47], s54 offen offset:2048 sc1 lds\n"
        "buffer_load_dwordx4 v0, s[44:47], s54 offen offset:3072 sc1 lds\n"
        "s_add_u32 s58, s58, 2\n"
        "s_add_u32 s52, s57, 2\n"
        "s_cmp_lt_u32 s52, s56\n"
        "s_cselect_b32 s52, s52, 0\n"
        "s_lshr_b32 s52, s52, 3\n"
        "s_lshl_b32 s88, s37, 3\n"
        "s_add_u32 s88, s88, s52\n"
        "s_lshl_b32 s88, s88, 7\n"
        "s_add_u32 s88, s88, 32768\n"
        "v_mov_b32 v20, s88\n"
        "global_load_dwordx2 v[22:23], v20, s[86:87] sc1\n"
        "s_sub_u32 s52, 10, s72\n"
        "s_cmp_lt_u32 s52, 4\n"
        "s_cselect_b32 s52, 0x80000, 0\n"
        "s_mov_b32 s50, s52\n"
        "s_mov_b32 s62, s52\n"
        "s_add_u32 s53, s73, 16384\n"
        "buffer_store_dwordx4 a[32:35], v0, s[48:51], s53 offen offset:0 sc1\n"
        "buffer_store_dwordx4 a[36:39], v0, s[48:51], s53 offen offset:1024 sc1\n"
        "s_add_u32 s53, s73, 20480\n"
        "buffer_store_dwordx4 a[40:43], v0, s[48:51], s53 offen offset:0 sc1\n"
        "buffer_store_dwordx4 a[44:47], v0, s[48:51], s53 offen offset:1024 sc1\n"
        "s_add_u32 s53, s73, 24576\n"
        "buffer_store_dwordx4 a[48:51], v0, s[48:51], s53 offen offset:0 sc1\n"
        "buffer_store_dwordx4 a[52:55], v0, s[48:51], s53 offen offset:1024 sc1\n"
        "s_add_u32 s53, s73, 28672\n"
        "buffer_store_dwordx4 a[56:59], v0, s[48:51], s53 offen offset:0 sc1\n"
        "buffer_store_dwordx4 a[60:63], v0, s[48:51], s53 offen offset:1024 sc1\n"
        "s_waitcnt vmcnt(25)\n"
        "s_mov_b32 s90, 0\n"
        "LD_KVQ1_CHK_%=:\n"
        "v_cmp_eq_u64 vcc, s[96:97], v[26:27]\n"
        "s_nop 1\n"
        "s_cmp_eq_u64 vcc, exec\n"
        "s_cbranch_scc1 LD_KVQ1_GOT_%=\n"
        "s_sleep 2\n"
        "global_load_dwordx2 v[26:27], v24, s[86:87] sc1\n"
        "s_waitcnt vmcnt(0)\n"
        "s_add_u32 s90, s90, 1\n"
        "s_cmp_lt_u32 s90, 0x100000\n"
        "s_cbranch_scc1 LD_KVQ1_CHK_%=\n"
        "LD_KVQ1_GOT_%=:\n"
        "s_barrier\n"
        "s_cmp_ge_u32 s57, s56\n"
        "s_cselect_b32 s54, s56, 0\n"
        "s_sub_u32 s54, s57, s54\n"
        "s_lshl_b32 s54, s54, 13\n"
        "s_add_u32 s54, s54, s59\n"
        "s_cmp_ge_u32 s57, 68\n"
        "s_cselect_b32 s54, 1073741824, s54\n"
        "s_and_b32 s55, s57, 7\n"
        "s_lshl_b32 s55, s55, 13\n"
        "s_add_u32 s55, s55, s59\n"
        "s_add_u32 m0, s55, 65536\n"
        "s_nop 0\n"
        "buffer_load_dwordx4 v0, s[40:43], s54 offen offset:0 sc1 lds\n"
        "buffer_load_dwordx4 v0, s[40:43], s54 offen offset:1024 sc1 lds\n"
        "buffer_load_dwordx4 v0, s[40:43], s54 offen offset:2048 sc1 lds\n"
        "buffer_load_dwordx4 v0, s[40:43], s54 offen offset:3072 sc1 lds\n"
        "s_add_u32 s57, s57, 2\n"
        "s_cmp_ge_u32 s58, s56\n"
        "s_cselect_b32 s54, s56, 0\n"
        "s_sub_u32 s54, s58, s54\n"
        "s_lshl_b32 s54, s54, 13\n"
        "s_add_u32 s54, s54, s59\n"
        "s_cmp_ge_u32 s58, 68\n"
        "s_cselect_b32 s54, 1073741824, s54\n"
        "s_and_b32 s55, s58, 7\n"
        "s_lshl_b32 s55, s55, 13\n"
        "s_add_u32 s55, s55, s59\n"
        "s_add_u32 m0, s55, 0\n"
        "s_nop 0\n"
        "buffer_load_dwordx4 v0, s[44:47], s54 offen offset:0 sc1 lds\n"
        "buffer_load_dwordx4 v0, s[44:47], s54 offen offset:1024 sc1 lds\n"
        "buffer_load_dwordx4 v0, s[44:47], s54 offen offset:2048 sc1 lds\n"
        "buffer_load_dwordx4 v0, s[44:47], s54 offen offset:3072 sc1 lds\n"
        "s_add_u32 s58, s58, 2\n"
        "s_add_u32 s52, s57, 2\n"
        "s_cmp_lt_u32 s52, s56\n"
        "s_cselect_b32 s52, s52, 0\n"
        "s_lshr_b32 s52, s52, 3\n"
        "s_lshl_b32 s88, s37, 3\n"
        "s_add_u32 s88, s88, s52\n"
        "s_lshl_b32 s88, s88, 7\n"
        "s_add_u32 s88, s88, 32768\n"
        "v_mov_b32 v24, s88\n"
        "global_load_dwordx2 v[26:27], v24, s[86:87] sc1\n"
        "s_sub_u32 s52, 11, s72\n"
        "s_cmp_lt_u32 s52, 4\n"
        "s_cselect_b32 s52, 0x80000, 0\n"
        "s_mov_b32 s50, s52\n"
        "s_mov_b32 s62, s52\n"
        "s_add_u32 s53, s77, 0\n"
        "buffer_store_dwordx4 v[64:67], v0, s[60:63], s53 offen offset:0 sc1\n"
        "buffer_store_dwordx4 v[68:71], v0, s[60:63], s53 offen offset:1024 sc1\n"
        "buffer_store_dwordx4 v[72:75], v0, s[60:63], s53 offen offset:2048 sc1\n"
        "buffer_store_dwordx4 v[76:79], v0, s[60:63], s53 offen offset:3072 sc1\n"
        "s_add_u32 s53, s77, 8192\n"
        "buffer_store_dwordx4 v[80:83], v0, s[60:63], s53 offen offset:0 sc1\n"
        "buffer_store_dwordx4 v[84:87], v0, s[60:63], s53 offen offset:1024 sc1\n"
        "buffer_store_dwordx4 v[88:91], v0, s[60:63], s53 offen offset:2048 sc1\n"
        "buffer_store_dwordx4 v[92:95], v0, s[60:63], s53 offen offset:3072 sc1\n"
        "s_waitcnt vmcnt(25)\n"
        "s_mov_b32 s90, 0\n"
        "LD_KVQ2_CHK_%=:\n"
        "v_cmp_eq_u64 vcc, s[96:97], v[22:23]\n"
        "s_nop 1\n"
        "s_cmp_eq_u64 vcc, exec\n"
        "s_cbranch_scc1 LD_KVQ2_GOT_%=\n"
        "s_sleep 2\n"
        "global_load_dwordx2 v[22:23], v20, s[86:87] sc1\n"
        "s_waitcnt vmcnt(0)\n"
        "s_add_u32 s90, s90, 1\n"
        "s_cmp_lt_u32 s90, 0x100000\n"
        "s_cbranch_scc1 LD_KVQ2_CHK_%=\n"
        "LD_KVQ2_GOT_%=:\n"
        "s_barrier\n"
        "s_cmp_ge_u32 s57, s56\n"
        "s_cselect_b32 s54, s56, 0\n"
        "s_sub_u32 s54, s57, s54\n"
        "s_lshl_b32 s54, s54, 13\n"
        "s_add_u32 s54, s54, s59\n"
        "s_cmp_ge_u32 s57, 68\n"
        "s_cselect_b32 s54, 1073741824, s54\n"
        "s_and_b32 s55, s57, 7\n"
        "s_lshl_b32 s55, s55, 13\n"
        "s_add_u32 s55, s55, s59\n"
        "s_add_u32 m0, s55, 65536\n"
        "s_nop 0\n"
        "buffer_load_dwordx4 v0, s[40:43], s54 offen offset:0 sc1 lds\n"
        "buffer_load_dwordx4 v0, s[40:43], s54 offen offset:1024 sc1 lds\n"
        "buffer_load_dwordx4 v0, s[40:43], s54 offen offset:2048 sc1 lds\n"
        "buffer_load_dwordx4 v0, s[40:43], s54 offen offset:3072 sc1 lds\n"
        "s_add_u32 s57, s57, 2\n"
        "s_cmp_ge_u32 s58, s56\n"
        "s_cselect_b32 s54, s56, 0\n"
        "s_sub_u32 s54, s58, s54\n"
        "s_lshl_b32 s54, s54, 13\n"
        "s_add_u32 s54, s54, s59\n"
        "s_cmp_ge_u32 s58, 68\n"
        "s_cselect_b32 s54, 1073741824, s54\n"
        "s_and_b32 s55, s58, 7\n"
        "s_lshl_b32 s55, s55, 13\n"
        "s_add_u32 s55, s55, s59\n"
        "s_add_u32 m0, s55, 0\n"
        "s_nop 0\n"
        "buffer_load_dwordx4 v0, s[44:47], s54 offen offset:0 sc1 lds\n"
        "buffer_load_dwordx4 v0, s[44:47], s54 offen offset:1024 sc1 lds\n"
        "buffer_load_dwordx4 v0, s[44:47], s54 offen offset:2048 sc1 lds\n"
        "buffer_load_dwordx4 v0, s[44:47], s54 offen offset:3072 sc1 lds\n"
        "s_add_u32 s58, s58, 2\n"
        "s_add_u32 s52, s57, 2\n"
        "s_cmp_lt_u32 s52, s56\n"
        "s_cselect_b32 s52, s52, 0\n"
        "s_lshr_b32 s52, s52, 3\n"
        "s_lshl_b32 s88, s37, 3\n"
        "s_add_u32 s88, s88, s52\n"
        "s_lshl_b32 s88, s88, 7\n"
        "s_add_u32 s88, s88, 32768\n"
        "v_mov_b32 v20, s88\n"
        "global_load_dwordx2 v[22:23], v20, s[86:87] sc1\n"
        "s_sub_u32 s52, 12, s72\n"
        "s_cmp_lt_u32 s52, 4\n"
        "s_cselect_b32 s52, 0x80000, 0\n"
        "s_mov_b32 s50, s52\n"
        "s_mov_b32 s62, s52\n"
        "s_add_u32 s53, s77, 16384\n"
        "buffer_store_dwordx4 v[96:99], v0, s[60:63], s53 offen offset:0 sc1\n"
        "buffer_store_dwordx4 v[100:103], v0, s[60:63], s53 offen offset:1024 sc1\n"
        "buffer_store_dwordx4 v[104:107], v0, s[60:63], s53 offen offset:2048 sc1\n"
        "buffer_store_dwordx4 v[108:111], v0, s[60:63], s53 offen offset:3072 sc1\n"
        "s_add_u32 s53, s77, 24576\n"
        "buffer_store_dwordx4 v[112:115], v0, s[60:63], s53 offen offset:0 sc1\n"
        "buffer_store_dwordx4 v[116:119], v0, s[60:63], s53 offen offset:1024 sc1\n"
        "buffer_store_dwordx4 v[120:123], v0, s[60:63], s53 offen offset:2048 sc1\n"
        "buffer_store_dwordx4 v[124:127], v0, s[60:63], s53 offen offset:3072 sc1\n"
        "s_waitcnt vmcnt(25)\n"
        "s_mov_b32 s90, 0\n"
        "LD_KVQ3_CHK_%=:\n"
        "v_cmp_eq_u64 vcc, s[96:97], v[26:27]\n"
        "s_nop 1\n"
        "s_cmp_eq_u64 vcc, exec\n"
        "s_cbranch_scc1 LD_KVQ3_GOT_%=\n"
        "s_sleep 2\n"
        "global_load_dwordx2 v[26:27], v24, s[86:87] sc1\n"
        "s_waitcnt vmcnt(0)\n"
        "s_add_u32 s90, s90, 1\n"
        "s_cmp_lt_u32 s90, 0x100000\n"
        "s_cbranch_scc1 LD_KVQ3_CHK_%=\n"
        "LD_KVQ3_GOT_%=:\n"
        "s_barrier\n"
        "s_cmp_ge_u32 s57, s56\n"
        "s_cselect_b32 s54, s56, 0\n"
        "s_sub_u32 s54, s57, s54\n"
        "s_lshl_b32 s54, s54, 13\n"
        "s_add_u32 s54, s54, s59\n"
        "s_cmp_ge_u32 s57, 68\n"
        "s_cselect_b32 s54, 1073741824, s54\n"
        "s_and_b32 s55, s57, 7\n"
        "s_lshl_b32 s55, s55, 13\n"
        "s_add_u32 s55, s55, s59\n"
        "s_add_u32 m0, s55, 65536\n"
        "s_nop 0\n"
        "buffer_load_dwordx4 v0, s[40:43], s54 offen offset:0 sc1 lds\n"
        "buffer_load_dwordx4 v0, s[40:43], s54 offen offset:1024 sc1 lds\n"
        "buffer_load_dwordx4 v0, s[40:43], s54 offen offset:2048 sc1 lds\n"
        "buffer_load_dwordx4 v0, s[40:43], s54 offen offset:3072 sc1 lds\n"
        "s_add_u32 s57, s57, 2\n"
        "s_cmp_ge_u32 s58, s56\n"
        "s_cselect_b32 s54, s56, 0\n"
        "s_sub_u32 s54, s58, s54\n"
        "s_lshl_b32 s54, s54, 13\n"
        "s_add_u32 s54, s54, s59\n"
        "s_cmp_ge_u32 s58, 68\n"
        "s_cselect_b32 s54, 1073741824, s54\n"
        "s_and_b32 s55, s58, 7\n"
        "s_lshl_b32 s55, s55, 13\n"
        "s_add_u32 s55, s55, s59\n"
        "s_add_u32 m0, s55, 0\n"
        "s_nop 0\n"
        "buffer_load_dwordx4 v0, s[44:47], s54 offen offset:0 sc1 lds\n"
        "buffer_load_dwordx4 v0, s[44:47], s54 offen offset:1024 sc1 lds\n"
        "buffer_load_dwordx4 v0, s[44:47], s54 offen offset:2048 sc1 lds\n"
        "buffer_load_dwordx4 v0, s[44:47], s54 offen offset:3072 sc1 lds\n"
        "s_add_u32 s58, s58, 2\n"
        "s_add_u32 s52, s57, 2\n"
        "s_cmp_lt_u32 s52, s56\n"
        "s_cselect_b32 s52, s52, 0\n"
        "s_lshr_b32 s52, s52, 3\n"
        "s_lshl_b32 s88, s37, 3\n"
        "s_add_u32 s88, s88, s52\n"
        "s_lshl_b32 s88, s88, 7\n"
        "s_add_u32 s88, s88, 32768\n"
        "v_mov_b32 v24, s88\n"
        "global_load_dwordx2 v[26:27], v24, s[86:87] sc1\n"
        "s_cmp_lg_u32 s78, 13\n"
        "s_cbranch_scc1 LD_NS13_%=\n"
        "s_cmp_lg_u32 s36, 4\n"
        "s_cbranch_scc1 LD_NS13_%=\n"
        "s_lshl_b32 s88, s37, 3\n"
        "s_add_u32 s88, s88, s38\n"
        "s_lshl_b32 s88, s88, 7\n"
        "s_add_u32 s88, s88, 32768\n"
        "s_mov_b64 exec, 1\n"
        "v_mov_b32 v28, s88\n"
        "v_mov_b32 v30, s96\n"
        "v_mov_b32 v31, s97\n"
        "global_store_dwordx2 v28, v[30:31], s[86:87] sc1\n"
        "s_mov_b64 exec, -1\n"
        "LD_NS13_%=:\n"
        "s_sub_u32 s52, 13, s72\n"
        "s_cmp_lt_u32 s52, 4\n"
        "s_cselect_b32 s52, 0x80000, 0\n"
        "s_mov_b32 s50, s52\n"
        "s_mov_b32 s62, s52\n"
        "s_add_u32 s53, s73, 0\n"
        "buffer_store_dwordx4 a[0:3], v0, s[48:51], s53 offen offset:0 sc1\n"
        "buffer_store_dwordx4 a[4:7], v0, s[48:51], s53 offen offset:1024 sc1\n"
        "s_add_u32 s53, s73, 4096\n"
        "buffer_store_dwordx4 a[8:11], v0, s[48:51], s53 offen offset:0 sc1\n"
        "buffer_store_dwordx4 a[12:15], v0, s[48:51], s53 offen offset:1024 sc1\n"
        "s_add_u32 s53, s73, 8192\n"
        "buffer_store_dwordx4 a[16:19], v0, s[48:51], s53 offen offset:0 sc1\n"
        "buffer_store_dwordx4 a[20:23], v0, s[48:51], s53 offen offset:1024 sc1\n"
        "s_add_u32 s53, s73, 12288\n"
        "buffer_store_dwordx4 a[24:27], v0, s[48:51], s53 offen offset:0 sc1\n"
        "buffer_store_dwordx4 a[28:31], v0, s[48:51], s53 offen offset:1024 sc1\n"
        "s_waitcnt vmcnt(25)\n"
        "s_mov_b32 s90, 0\n"
        "LD_KVQ4_CHK_%=:\n"
        "v_cmp_eq_u64 vcc, s[96:97], v[22:23]\n"
        "s_nop 1\n"
        "s_cmp_eq_u64 vcc, exec\n"
        "s_cbranch_scc1 LD_KVQ4_GOT_%=\n"
        "s_sleep 2\n"
        "global_load_dwordx2 v[22:23], v20, s[86:87] sc1\n"
        "s_waitcnt vmcnt(0)\n"
        "s_add_u32 s90, s90, 1\n"
        "s_cmp_lt_u32 s90, 0x100000\n"
        "s_cbranch_scc1 LD_KVQ4_CHK_%=\n"
        "LD_KVQ4_GOT_%=:\n"
        "s_barrier\n"
        "s_cmp_ge_u32 s57, s56\n"
        "s_cselect_b32 s54, s56, 0\n"
        "s_sub_u32 s54, s57, s54\n"
        "s_lshl_b32 s54, s54, 13\n"
        "s_add_u32 s54, s54, s59\n"
        "s_cmp_ge_u32 s57, 68\n"
        "s_cselect_b32 s54, 1073741824, s54\n"
        "s_and_b32 s55, s57, 7\n"
        "s_lshl_b32 s55, s55, 13\n"
        "s_add_u32 s55, s55, s59\n"
        "s_add_u32 m0, s55, 65536\n"
        "s_nop 0\n"
        "buffer_load_dwordx4 v0, s[40:43], s54 offen offset:0 sc1 lds\n"
        "buffer_load_dwordx4 v0, s[40:43], s54 offen offset:1024 sc1 lds\n"
        "buffer_load_dwordx4 v0, s[40:43], s54 offen offset:2048 sc1 lds\n"
        "buffer_load_dwordx4 v0, s[40:43], s54 offen offset:3072 sc1 lds\n"
        "s_add_u32 s57, s57, 2\n"
        "s_cmp_ge_u32 s58, s56\n"
        "s_cselect_b32 s54, s56, 0\n"
        "s_sub_u32 s54, s58, s54\n"
        "s_lshl_b32 s54, s54, 13\n"
        "s_add_u32 s54, s54, s59\n"
        "s_cmp_ge_u32 s58, 68\n"
        "s_cselect_b32 s54, 1073741824, s54\n"
        "s_and_b32 s55, s58, 7\n"
        "s_lshl_b32 s55, s55, 13\n"
        "s_add_u32 s55, s55, s59\n"
        "s_add_u32 m0, s55, 0\n"
        "s_nop 0\n"
        "buffer_load_dwordx4 v0, s[44:47], s54 offen offset:0 sc1 lds\n"
        "buffer_load_dwordx4 v0, s[44:47], s54 offen offset:1024 sc1 lds\n"
        "buffer_load_dwordx4 v0, s[44:47], s54 offen offset:2048 sc1 lds\n"
        "buffer_load_dwordx4 v0, s[44:47], s54 offen offset:3072 sc1 lds\n"
        "s_add_u32 s58, s58, 2\n"
        "s_add_u32 s52, s57, 2\n"
        "s_cmp_lt_u32 s52, s56\n"
        "s_cselect_b32 s52, s52, 0\n"
        "s_lshr_b32 s52, s52, 3\n"
        "s_lshl_b32 s88, s37, 3\n"
        "s_add_u32 s88, s88, s52\n"
        "s_lshl_b32 s88, s88, 7\n"
        "s_add_u32 s88, s88, 32768\n"
        "v_mov_b32 v20, s88\n"
        "global_load_dwordx2 v[22:23], v20, s[86:87] sc1\n"
        "s_sub_u32 s52, 14, s72\n"
        "s_cmp_lt_u32 s52, 4\n"
        "s_cselect_b32 s52, 0x80000, 0\n"
        "s_mov_b32 s50, s52\n"
        "s_mov_b32 s62, s52\n"
        "s_add_u32 s53, s73, 16384\n"
        "buffer_store_dwordx4 a[32:35], v0, s[48:51], s53 offen offset:0 sc1\n"
        "buffer_store_dwordx4 a[36:39], v0, s[48:51], s53 offen offset:1024 sc1\n"
        "s_add_u32 s53, s73, 20480\n"
        "buffer_store_dwordx4 a[40:43], v0, s[48:51], s53 offen offset:0 sc1\n"
        "buffer_store_dwordx4 a[44:47], v0, s[48:51], s53 offen offset:1024 sc1\n"
        "s_add_u32 s53, s73, 24576\n"
        "buffer_store_dwordx4 a[48:51], v0, s[48:51], s53 offen offset:0 sc1\n"
        "buffer_store_dwordx4 a[52:55], v0, s[48:51], s53 offen offset:1024 sc1\n"
        "s_add_u32 s53, s73, 28672\n"
        "buffer_store_dwordx4 a[56:59], v0, s[48:51], s53 offen offset:0 sc1\n"
        "buffer_store_dwordx4 a[60:63], v0, s[48:51], s53 offen offset:1024 sc1\n"
        "s_waitcnt vmcnt(25)\n"
        "s_mov_b32 s90, 0\n"
        "LD_KVQ5_CHK_%=:\n"
        "v_cmp_eq_u64 vcc, s[96:97], v[26:27]\n"
        "s_nop 1\n"
        "s_cmp_eq_u64 vcc, exec\n"
        "s_cbranch_scc1 LD_KVQ5_GOT_%=\n"
        "s_sleep 2\n"
        "global_load_dwordx2 v[26:27], v24, s[86:87] sc1\n"
        "s_waitcnt vmcnt(0)\n"
        "s_add_u32 s90, s90, 1\n"
        "s_cmp_lt_u32 s90, 0x100000\n"
        "s_cbranch_scc1 LD_KVQ5_CHK_%=\n"
        "LD_KVQ5_GOT_%=:\n"
        "s_barrier\n"
        "s_cmp_ge_u32 s57, s56\n"
        "s_cselect_b32 s54, s56, 0\n"
        "s_sub_u32 s54, s57, s54\n"
        "s_lshl_b32 s54, s54, 13\n"
        "s_add_u32 s54, s54, s59\n"
        "s_cmp_ge_u32 s57, 68\n"
        "s_cselect_b32 s54, 1073741824, s54\n"
        "s_and_b32 s55, s57, 7\n"
        "s_lshl_b32 s55, s55, 13\n"
        "s_add_u32 s55, s55, s59\n"
        "s_add_u32 m0, s55, 65536\n"
        "s_nop 0\n"
        "buffer_load_dwordx4 v0, s[40:43], s54 offen offset:0 sc1 lds\n"
        "buffer_load_dwordx4 v0, s[40:43], s54 offen offset:1024 sc1 lds\n"
        "buffer_load_dwordx4 v0, s[40:43], s54 offen offset:2048 sc1 lds\n"
        "buffer_load_dwordx4 v0, s[40:43], s54 offen offset:3072 sc1 lds\n"
        "s_add_u32 s57, s57, 2\n"
        "s_cmp_ge_u32 s58, s56\n"
        "s_cselect_b32 s54, s56, 0\n"
        "s_sub_u32 s54, s58, s54\n"
        "s_lshl_b32 s54, s54, 13\n"
        "s_add_u32 s54, s54, s59\n"
        "s_cmp_ge_u32 s58, 68\n"
        "s_cselect_b32 s54, 1073741824, s54\n"
        "s_and_b32 s55, s58, 7\n"
        "s_lshl_b32 s55, s55, 13\n"
        "s_add_u32 s55, s55, s59\n"
        "s_add_u32 m0, s55, 0\n"
        "s_nop 0\n"
        "buffer_load_dwordx4 v0, s[44:47], s54 offen offset:0 sc1 lds\n"
        "buffer_load_dwordx4 v0, s[44:47], s54 offen offset:1024 sc1 lds\n"
        "buffer_load_dwordx4 v0, s[44:47], s54 offen offset:2048 sc1 lds\n"
        "buffer_load_dwordx4 v0, s[44:47], s54 offen offset:3072 sc1 lds\n"
        "s_add_u32 s58, s58, 2\n"
        "s_add_u32 s52, s57, 2\n"
        "s_cmp_lt_u32 s52, s56\n"
        "s_cselect_b32 s52, s52, 0\n"
        "s_lshr_b32 s52, s52, 3\n"
        "s_lshl_b32 s88, s37, 3\n"
        "s_add_u32 s88, s88, s52\n"
        "s_lshl_b32 s88, s88, 7\n"
        "s_add_u32 s88, s88, 32768\n"
        "v_mov_b32 v24, s88\n"
        "global_load_dwordx2 v[26:27], v24, s[86:87] sc1\n"
        "s_sub_u32 s52, 15, s72\n"
        "s_cmp_lt_u32 s52, 4\n"
        "s_cselect_b32 s52, 0x80000, 0\n"
        "s_mov_b32 s50, s52\n"
        "s_mov_b32 s62, s52\n"
        "s_add_u32 s53, s77, 0\n"
        "buffer_store_dwordx4 v[64:67], v0, s[60:63], s53 offen offset:0 sc1\n"
        "buffer_store_dwordx4 v[68:71], v0, s[60:63], s53 offen offset:1024 sc1\n"
        "buffer_store_dwordx4 v[72:75], v0, s[60:63], s53 offen offset:2048 sc1\n"
        "buffer_store_dwordx4 v[76:79], v0, s[60:63], s53 offen offset:3072 sc1\n"
        "s_add_u32 s53, s77, 8192\n"
        "buffer_store_dwordx4 v[80:83], v0, s[60:63], s53 offen offset:0 sc1\n"
        "buffer_store_dwordx4 v[84:87], v0, s[60:63], s53 offen offset:1024 sc1\n"
        "buffer_store_dwordx4 v[88:91], v0, s[60:63], s53 offen offset:2048 sc1\n"
        "buffer_store_dwordx4 v[92:95], v0, s[60:63], s53 offen offset:3072 sc1\n"
        "s_waitcnt vmcnt(25)\n"
        "s_mov_b32 s90, 0\n"
        "LD_KVQ6_CHK_%=:\n"
        "v_cmp_eq_u64 vcc, s[96:97], v[22:23]\n"
        "s_nop 1\n"
        "s_cmp_eq_u64 vcc, exec\n"
        "s_cbranch_scc1 LD_KVQ6_GOT_%=\n"
        "s_sleep 2\n"
        "global_load_dwordx2 v[22:23], v20, s[86:87] sc1\n"
        "s_waitcnt vmcnt(0)\n"
        "s_add_u32 s90, s90, 1\n"
        "s_cmp_lt_u32 s90, 0x100000\n"
        "s_cbranch_scc1 LD_KVQ6_CHK_%=\n"
        "LD_KVQ6_GOT_%=:\n"
        "s_barrier\n"
        "s_cmp_ge_u32 s57, s56\n"
        "s_cselect_b32 s54, s56, 0\n"
        "s_sub_u32 s54, s57, s54\n"
        "s_lshl_b32 s54, s54, 13\n"
        "s_add_u32 s54, s54, s59\n"
        "s_cmp_ge_u32 s57, 68\n"
        "s_cselect_b32 s54, 1073741824, s54\n"
        "s_and_b32 s55, s57, 7\n"
        "s_lshl_b32 s55, s55, 13\n"
        "s_add_u32 s55, s55, s59\n"
        "s_add_u32 m0, s55, 65536\n"
        "s_nop 0\n"
        "buffer_load_dwordx4 v0, s[40:43], s54 offen offset:0 sc1 lds\n"
        "buffer_load_dwordx4 v0, s[40:43], s54 offen offset:1024 sc1 lds\n"
        "buffer_load_dwordx4 v0, s[40:43], s54 offen offset:2048 sc1 lds\n"
        "buffer_load_dwordx4 v0, s[40:43], s54 offen offset:3072 sc1 lds\n"
        "s_add_u32 s57, s57, 2\n"
        "s_cmp_ge_u32 s58, s56\n"
        "s_cselect_b32 s54, s56, 0\n"
        "s_sub_u32 s54, s58, s54\n"
        "s_lshl_b32 s54, s54, 13\n"
        "s_add_u32 s54, s54, s59\n"
        "s_cmp_ge_u32 s58, 68\n"
        "s_cselect_b32 s54, 1073741824, s54\n"
        "s_and_b32 s55, s58, 7\n"
        "s_lshl_b32 s55, s55, 13\n"
        "s_add_u32 s55, s55, s59\n"
        "s_add_u32 m0, s55, 0\n"
        "s_nop 0\n"
        "buffer_load_dwordx4 v0, s[44:47], s54 offen offset:0 sc1 lds\n"
        "buffer_load_dwordx4 v0, s[44:47], s54 offen offset:1024 sc1 lds\n"
        "buffer_load_dwordx4 v0, s[44:47], s54 offen offset:2048 sc1 lds\n"
        "buffer_load_dwordx4 v0, s[44:47], s54 offen offset:3072 sc1 lds\n"
        "s_add_u32 s58, s58, 2\n"
        "s_add_u32 s52, s57, 2\n"
        "s_cmp_lt_u32 s52, s56\n"
        "s_cselect_b32 s52, s52, 0\n"
        "s_lshr_b32 s52, s52, 3\n"
        "s_lshl_b32 s88, s37, 3\n"
        "s_add_u32 s88, s88, s52\n"
        "s_lshl_b32 s88, s88, 7\n"
        "s_add_u32 s88, s88, 32768\n"
        "v_mov_b32 v20, s88\n"
        "global_load_dwordx2 v[22:23], v20, s[86:87] sc1\n"
        "s_sub_u32 s52, 16, s72\n"
        "s_cmp_lt_u32 s52, 4\n"
        "s_cselect_b32 s52, 0x80000, 0\n"
        "s_mov_b32 s50, s52\n"
        "s_mov_b32 s62, s52\n"
        "s_add_u32 s53, s77, 16384\n"
        "buffer_store_dwordx4 v[96:99], v0, s[60:63], s53 offen offset:0 sc1\n"
        "buffer_store_dwordx4 v[100:103], v0, s[60:63], s53 offen offset:1024 sc1\n"
        "buffer_store_dwordx4 v[104:107], v0, s[60:63], s53 offen offset:2048 sc1\n"
        "buffer_store_dwordx4 v[108:111], v0, s[60:63], s53 offen offset:3072 sc1\n"
        "s_add_u32 s53, s77, 24576\n"
        "buffer_store_dwordx4 v[112:115], v0, s[60:63], s53 offen offset:0 sc1\n"
        "buffer_store_dwordx4 v[116:119], v0, s[60:63], s53 offen offset:1024 sc1\n"
        "buffer_store_dwordx4 v[120:123], v0, s[60:63], s53 offen offset:2048 sc1\n"
        "buffer_store_dwordx4 v[124:127], v0, s[60:63], s53 offen offset:3072 sc1\n"
        "s_waitcnt vmcnt(25)\n"
        "s_mov_b32 s90, 0\n"
        "LD_KVQ7_CHK_%=:\n"
        "v_cmp_eq_u64 vcc, s[96:97], v[26:27]\n"
        "s_nop 1\n"
        "s_cmp_eq_u64 vcc, exec\n"
        "s_cbranch_scc1 LD_KVQ7_GOT_%=\n"
        "s_sleep 2\n"
        "global_load_dwordx2 v[26:27], v24, s[86:87] sc1\n"
        "s_waitcnt vmcnt(0)\n"
        "s_add_u32 s90, s90, 1\n"
        "s_cmp_lt_u32 s90, 0x100000\n"
        "s_cbranch_scc1 LD_KVQ7_CHK_%=\n"
        "LD_KVQ7_GOT_%=:\n"
        "s_barrier\n"
        "s_mov_b32 s79, 17\n"
        "s_mov_b32 s39, 13\n"
        "LD_P3LOOP_%=:\n"
        "s_cmp_lg_u32 s79, s78\n"
        "s_cbranch_scc1 LD_NSA_%=\n"
        "s_cmp_lg_u32 s36, 4\n"
        "s_cbranch_scc1 LD_NSA_%=\n"
        "s_lshl_b32 s88, s37, 3\n"
        "s_add_u32 s88, s88, s38\n"
        "s_lshl_b32 s88, s88, 7\n"
        "s_add_u32 s88, s88, 32768\n"
        "s_mov_b64 exec, 1\n"
        "v_mov_b32 v28, s88\n"
        "v_mov_b32 v30, s96\n"
        "v_mov_b32 v31, s97\n"
        "global_store_dwordx2 v28, v[30:31], s[86:87] sc1\n"
        "s_mov_b64 exec, -1\n"
        "LD_NSA_%=:\n"
        "s_add_u32 s79, s79, 1\n"
        "s_cmp_ge_u32 s57, s56\n"
        "s_cselect_b32 s54, s56, 0\n"
        "s_sub_u32 s54, s57, s54\n"
        "s_lshl_b32 s54, s54, 13\n"
        "s_add_u32 s54, s54, s59\n"
        "s_cmp_ge_u32 s57, 68\n"
        "s_cselect_b32 s54, 1073741824, s54\n"
        "s_and_b32 s55, s57, 7\n"
        "s_lshl_b32 s55, s55, 13\n"
        "s_add_u32 s55, s55, s59\n"
        "s_add_u32 m0, s55, 65536\n"
        "s_nop 0\n"
        "buffer_load_dwordx4 v0, s[40:43], s54 offen offset:0 sc1 lds\n"
        "buffer_load_dwordx4 v0, s[40:43], s54 offen offset:1024 sc1 lds\n"
        "buffer_load_dwordx4 v0, s[40:43], s54 offen offset:2048 sc1 lds\n"
        "buffer_load_dwordx4 v0, s[40:43], s54 offen offset:3072 sc1 lds\n"
        "s_add_u32 s57, s57, 2\n"
        "s_cmp_ge_u32 s58, s56\n"
        "s_cselect_b32 s54, s56, 0\n"
        "s_sub_u32 s54, s58, s54\n"
        "s_lshl_b32 s54, s54, 13\n"
        "s_add_u32 s54, s54, s59\n"
        "s_cmp_ge_u32 s58, 68\n"
        "s_cselect_b32 s54, 1073741824, s54\n"
        "s_and_b32 s55, s58, 7\n"
        "s_lshl_b32 s55, s55, 13\n"
        "s_add_u32 s55, s55, s59\n"
        "s_add_u32 m0, s55, 0\n"
        "s_nop 0\n"
        "buffer_load_dwordx4 v0, s[44:47], s54 offen offset:0 sc1 lds\n"
        "buffer_load_dwordx4 v0, s[44:47], s54 offen offset:1024 sc1 lds\n"
        "buffer_load_dwordx4 v0, s[44:47], s54 offen offset:2048 sc1 lds\n"
        "buffer_load_dwordx4 v0, s[44:47], s54 offen offset:3072 sc1 lds\n"
        "s_add_u32 s58, s58, 2\n"
        "s_add_u32 s52, s57, 2\n"
        "s_cmp_lt_u32 s52, s56\n"
        "s_cselect_b32 s52, s52, 0\n"
        "s_lshr_b32 s52, s52, 3\n"
        "s_lshl_b32 s88, s37, 3\n"
        "s_add_u32 s88, s88, s52\n"
        "s_lshl_b32 s88, s88, 7\n"
        "s_add_u32 s88, s88, 32768\n"
        "v_mov_b32 v24, s88\n"
        "global_load_dwordx2 v[26:27], v24, s[86:87] sc1\n"
        "s_waitcnt vmcnt(9)\n"
        "s_mov_b32 s90, 0\n"
        "LD_KVLA_CHK_%=:\n"
        "v_cmp_eq_u64 vcc, s[96:97], v[22:23]\n"
        "s_nop 1\n"
        "s_cmp_eq_u64 vcc, exec\n"
        "s_cbranch_scc1 LD_KVLA_GOT_%=\n"
        "s_sleep 2\n"
        "global_load_dwordx2 v[22:23], v20, s[86:87] sc1\n"
        "s_waitcnt vmcnt(0)\n"
        "s_add_u32 s90, s90, 1\n"
        "s_cmp_lt_u32 s90, 0x100000\n"
        "s_cbranch_scc1 LD_KVLA_CHK_%=\n"
        "LD_KVLA_GOT_%=:\n"
        "s_barrier\n"
        "s_cmp_lg_u32 s79, s78\n"
        "s_cbranch_scc1 LD_NSB_%=\n"
        "s_cmp_lg_u32 s36, 4\n"
        "s_cbranch_scc1 LD_NSB_%=\n"
        "s_lshl_b32 s88, s37, 3\n"
        "s_add_u32 s88, s88, s38\n"
        "s_lshl_b32 s88, s88, 7\n"
        "s_add_u32 s88, s88, 32768\n"
        "s_mov_b64 exec, 1\n"
        "v_mov_b32 v28, s88\n"
        "v_mov_b32 v30, s96\n"
        "v_mov_b32 v31, s97\n"
        "global_store_dwordx2 v28, v[30:31], s[86:87] sc1\n"
        "s_mov_b64 exec, -1\n"
        "LD_NSB_%=:\n"
        "s_add_u32 s79, s79, 1\n"
        "s_cmp_ge_u32 s57, s56\n"
        "s_cselect_b32 s54, s56, 0\n"
        "s_sub_u32 s54, s57, s54\n"
        "s_lshl_b32 s54, s54, 13\n"
        "s_add_u32 s54, s54, s59\n"
        "s_cmp_ge_u32 s57, 68\n"
        "s_cselect_b32 s54, 1073741824, s54\n"
        "s_and_b32 s55, s57, 7\n"
        "s_lshl_b32 s55, s55, 13\n"
        "s_add_u32 s55, s55, s59\n"
        "s_add_u32 m0, s55, 65536\n"
        "s_nop 0\n"
        "buffer_load_dwordx4 v0, s[40:43], s54 offen offset:0 sc1 lds\n"
        "buffer_load_dwordx4 v0, s[40:43], s54 offen offset:1024 sc1 lds\n"
        "buffer_load_dwordx4 v0, s[40:43], s54 offen offset:2048 sc1 lds\n"
        "buffer_load_dwordx4 v0, s[40:43], s54 offen offset:3072 sc1 lds\n"
        "s_add_u32 s57, s57, 2\n"
        "s_cmp_ge_u32 s58, s56\n"
        "s_cselect_b32 s54, s56, 0\n"
        "s_sub_u32 s54, s58, s54\n"
        "s_lshl_b32 s54, s54, 13\n"
        "s_add_u32 s54, s54, s59\n"
        "s_cmp_ge_u32 s58, 68\n"
        "s_cselect_b32 s54, 1073741824, s54\n"
        "s_and_b32 s55, s58, 7\n"
        "s_lshl_b32 s55, s55, 13\n"
        "s_add_u32 s55, s55, s59\n"
        "s_add_u32 m0, s55, 0\n"
        "s_nop 0\n"
        "buffer_load_dwordx4 v0, s[44:47], s54 offen offset:0 sc1 lds\n"
        "buffer_load_dwordx4 v0, s[44:47], s54 offen offset:1024 sc1 lds\n"
        "buffer_load_dwordx4 v0, s[44:47], s54 offen offset:2048 sc1 lds\n"
        "buffer_load_dwordx4 v0, s[44:47], s54 offen offset:3072 sc1 lds\n"
        "s_add_u32 s58, s58, 2\n"
        "s_add_u32 s52, s57, 2\n"
        "s_cmp_lt_u32 s52, s56\n"
        "s_cselect_b32 s52, s52, 0\n"
        "s_lshr_b32 s52, s52, 3\n"
        "s_lshl_b32 s88, s37, 3\n"
        "s_add_u32 s88, s88, s52\n"
        "s_lshl_b32 s88, s88, 7\n"
        "s_add_u32 s88, s88, 32768\n"
        "v_mov_b32 v20, s88\n"
        "global_load_dwordx2 v[22:23], v20, s[86:87] sc1\n"
        "s_waitcnt vmcnt(9)\n"
        "s_mov_b32 s90, 0\n"
        "LD_KVLB_CHK_%=:\n"
        "v_cmp_eq_u64 vcc, s[96:97], v[26:27]\n"
        "s_nop 1\n"
        "s_cmp_eq_u64 vcc, exec\n"
        "s_cbranch_scc1 LD_KVLB_GOT_%=\n"
        "s_sleep 2\n"
        "global_load_dwordx2 v[26:27], v24, s[86:87] sc1\n"
        "s_waitcnt vmcnt(0)\n"
        "s_add_u32 s90, s90, 1\n"
        "s_cmp_lt_u32 s90, 0x100000\n"
        "s_cbranch_scc1 LD_KVLB_CHK_%=\n"
        "LD_KVLB_GOT_%=:\n"
        "s_barrier\n"
        "s_sub_u32 s39, s39, 1\n"
        "s_cmp_lg_u32 s39, 0\n"
        "s_cbranch_scc1 LD_P3LOOP_%=\n"
        "v_bfe_u32 v16, v0, 4, 2\n"
        "v_lshlrev_b32 v16, 3, v16\n"
        "v_add_u32 v16, 159744, v16\n"
        "ds_read_b32 v17, v16\n"
        "s_cmp_ge_u32 s57, s56\n"
        "s_cselect_b32 s54, s56, 0\n"
        "s_sub_u32 s54, s57, s54\n"
        "s_lshl_b32 s54, s54, 13\n"
        "s_add_u32 s54, s54, s59\n"
        "s_cmp_ge_u32 s57, 68\n"
        "s_cselect_b32 s54, 1073741824, s54\n"
        "s_and_b32 s55, s57, 7\n"
        "s_lshl_b32 s55, s55, 13\n"
        "s_add_u32 s55, s55, s59\n"
        "s_add_u32 m0, s55, 65536\n"
        "s_nop 0\n"
        "buffer_load_dwordx4 v0, s[40:43], s54 offen offset:0 sc1 lds\n"
        "buffer_load_dwordx4 v0, s[40:43], s54 offen offset:1024 sc1 lds\n"
        "buffer_load_dwordx4 v0, s[40:43], s54 offen offset:2048 sc1 lds\n"
        "buffer_load_dwordx4 v0, s[40:43], s54 offen offset:3072 sc1 lds\n"
        "s_add_u32 s57, s57, 2\n"
        "s_cmp_ge_u32 s58, s56\n"
        "s_cselect_b32 s54, s56, 0\n"
        "s_sub_u32 s54, s58, s54\n"
        "s_lshl_b32 s54, s54, 13\n"
        "s_add_u32 s54, s54, s59\n"
        "s_cmp_ge_u32 s58, 68\n"
        "s_cselect_b32 s54, 1073741824, s54\n"
        "s_and_b32 s55, s58, 7\n"
        "s_lshl_b32 s55, s55, 13\n"
        "s_add_u32 s55, s55, s59\n"
        "s_add_u32 m0, s55, 0\n"
        "s_nop 0\n"
        "buffer_load_dwordx4 v0, s[44:47], s54 offen offset:0 sc1 lds\n"
        "buffer_load_dwordx4 v0, s[44:47], s54 offen offset:1024 sc1 lds\n"
        "buffer_load_dwordx4 v0, s[44:47], s54 offen offset:2048 sc1 lds\n"
        "buffer_load_dwordx4 v0, s[44:47], s54 offen offset:3072 sc1 lds\n"
        "s_add_u32 s58, s58, 2\n"
        "v_lshlrev_b32 v24, 3, v0\n"
        "v_and_b32 v24, 0x180, v24\n"
        "s_lshr_b32 s88, s37, 2\n"
        "s_lshl_b32 s88, s88, 3\n"
        "s_add_u32 s88, s88, s38\n"
        "s_lshl_b32 s88, s88, 9\n"
        "s_add_u32 s88, s88, 65536\n"
        "v_add_u32 v24, s88, v24\n"
        "global_load_dwordx2 v[26:27], v24, s[86:87] sc1\n"
        "s_waitcnt vmcnt(9)\n"
        "s_mov_b32 s90, 0\n"
        "LD_KVLC_CHK_%=:\n"
        "v_cmp_eq_u64 vcc, s[96:97], v[22:23]\n"
        "s_nop 1\n"
        "s_cmp_eq_u64 vcc, exec\n"
        "s_cbranch_scc1 LD_KVLC_GOT_%=\n"
        "s_sleep 2\n"
        "global_load_dwordx2 v[22:23], v20, s[86:87] sc1\n"
        "s_waitcnt vmcnt(0)\n"
        "s_add_u32 s90, s90, 1\n"
        "s_cmp_lt_u32 s90, 0x100000\n"
        "s_cbranch_scc1 LD_KVLC_CHK_%=\n"
        "LD_KVLC_GOT_%=:\n"
        "s_barrier\n"
        "s_waitcnt vmcnt(0) lgkmcnt(0)\n"
        "v_cmp_ne_u32 vcc, 0, v17\n"
        "s_nop 4\n"
        "s_cbranch_vccnz LD_SKIP4_%=\n"
        "s_mov_b32 s90, 0\n"
        "LD_CA_CHK_%=:\n"
        "v_cmp_eq_u64 vcc, s[96:97], v[26:27]\n"
        "s_nop 1\n"
        "s_cmp_eq_u64 vcc, exec\n"
        "s_cbranch_scc1 LD_CA_GOT_%=\n"
        "s_sleep 2\n"
        "global_load_dwordx2 v[26:27], v24, s[86:87] sc1\n"
        "s_waitcnt vmcnt(0)\n"
        "s_add_u32 s90, s90, 1\n"
        "s_cmp_lt_u32 s90, 0x100000\n"
        "s_cbranch_scc1 LD_CA_CHK_%=\n"
        "LD_CA_GOT_%=:\n"
        "v_lshrrev_b32 v16, 4, v0\n"
        "s_and_b32 s52, s36, 3\n"
        "s_mul_i32 s53, s52, 320\n"
        "v_add_u32 v17, s53, v16\n"
        "v_mul_u32_u24 v18, 3277, v17\n"
        "v_lshrrev_b32 v18, 16, v18\n"
        "v_mul_u32_u24 v19, 20, v18\n"
        "v_sub_u32 v19, v17, v19\n"
        "v_bfe_u32 v20, v18, 2, 1\n"
        "v_lshlrev_b32 v20, 1, v20\n"
        "v_xor_b32 v19, v19, v20\n"
        "v_mul_u32_u24 v18, 1280, v18\n"
        "v_lshl_add_u32 v18, v19, 4, v18\n"
        "v_mov_b32 v1, v18\n"
        "v_add_u32 v17, s53, v16\n"
        "v_add_u32 v17, 64, v17\n"
        "v_mul_u32_u24 v18, 3277, v17\n"
        "v_lshrrev_b32 v18, 16, v18\n"
        "v_mul_u32_u24 v19, 20, v18\n"
        "v_sub_u32 v19, v17, v19\n"
        "v_bfe_u32 v20, v18, 2, 1\n"
        "v_lshlrev_b32 v20, 1, v20\n"
        "v_xor_b32 v19, v19, v20\n"
        "v_mul_u32_u24 v18, 1280, v18\n"
        "v_lshl_add_u32 v18, v19, 4, v18\n"
        "v_subrev_u32 v2, 1024, v18\n"
        "v_add_u32 v17, s53, v16\n"
        "v_add_u32 v17, 128, v17\n"
        "v_mul_u32_u24 v18, 3277, v17\n"
        "v_lshrrev_b32 v18, 16, v18\n"
        "v_mul_u32_u24 v19, 20, v18\n"
        "v_sub_u32 v19, v17, v19\n"
        "v_bfe_u32 v20, v18, 2, 1\n"
        "v_lshlrev_b32 v20, 1, v20\n"
        "v_xor_b32 v19, v19, v20\n"
        "v_mul_u32_u24 v18, 1280, v18\n"
        "v_lshl_add_u32 v18, v19, 4, v18\n"
        "v_subrev_u32 v3, 2048, v18\n"
        "v_add_u32 v17, s53, v16\n"
        "v_add_u32 v17, 192, v17\n"
        "v_mul_u32_u24 v18, 3277, v17\n"
        "v_lshrrev_b32 v18, 16, v18\n"
        "v_mul_u32_u24 v19, 20, v18\n"
        "v_sub_u32 v19, v17, v19\n"
        "v_bfe_u32 v20, v18, 2, 1\n"
        "v_lshlrev_b32 v20, 1, v20\n"
        "v_xor_b32 v19, v19, v20\n"
        "v_mul_u32_u24 v18, 1280, v18\n"
        "v_lshl_add_u32 v18, v19, 4, v18\n"
        "v_subrev_u32 v4, 3072, v18\n"
        "v_add_u32 v17, s53, v16\n"
        "v_add_u32 v17, 256, v17\n"
        "v_mul_u32_u24 v18, 3277, v17\n"
        "v_lshrrev_b32 v18, 16, v18\n"
        "v_mul_u32_u24 v19, 20, v18\n"
        "v_sub_u32 v19, v17, v19\n"
        "v_bfe_u32 v20, v18, 2, 1\n"
        "v_lshlrev_b32 v20, 1, v20\n"
        "v_xor_b32 v19, v19, v20\n"
        "v_mul_u32_u24 v18, 1280, v18\n"
        "v_lshl_add_u32 v18, v19, 4, v18\n"
        "v_mov_b32 v5, v18\n"
        "v_mov_b32 v6, v0\n"
        "v_add_u32 v7, 129024, v0\n"
        "s_lshr_b32 s52, s37, 2\n"
        "s_lshl_b32 s52, s52, 14\n"
        "s_sub_u32 s53, 0x1000000, s52\n"
        "s_add_u32 s52, s52, 37748736\n"
        "s_add_u32 s64, s86, s52\n"
        "s_addc_u32 s65, s87, 0\n"
        "s_and_b32 s65, s65, 0xffff\n"
        "s_mov_b32 s66, s53\n"
        "s_mov_b32 s67, 0x20000\n"
        "s_lshr_b32 s52, s37, 2\n"
        "s_mul_i32 s52, s52, 655360\n"
        "s_and_b32 s53, s37, 3\n"
        "s_mul_i32 s53, s53, 320\n"
        "s_add_u32 s52, s52, s53\n"
        "s_sub_u32 s53, 655360, s53\n"
        "s_add_u32 s68, %[wolo], s52\n"
        "s_addc_u32 s69, %[wohi], 0\n"
        "s_and_b32 s69, s69, 0xffff\n"
        "s_mov_b32 s70, s53\n"
        "s_mov_b32 s71, 0x20000\n"
        "s_sub_u32 s53, 15, s38\n"
        "s_lshl_b32 s53, s53, 3\n"
        "s_and_b32 s54, s36, 3\n"
        "s_lshl_b32 s54, s54, 1\n"
        "s_add_u32 s53, s53, s54\n"
        "s_lshl_b32 s76, s53, 17\n"
        "s_and_b32 s52, s36, 3\n"
        "s_mul_i32 s53, s52, 5120\n"
        "s_add_u32 m0, s53, 98304\n"
        "s_mov_b32 s78, 0\n"
        "buffer_load_dwordx4 v1, s[68:71], s78 offen offset:0 lds\n"
        "buffer_load_dwordx4 v2, s[68:71], s78 offen offset:1024 lds\n"
        "buffer_load_dwordx4 v3, s[68:71], s78 offen offset:2048 lds\n"
        "buffer_load_dwordx4 v4, s[68:71], s78 offen offset:3072 lds\n"
        "s_add_u32 m0, s53, 102400\n"
        "s_nop 0\n"
        "buffer_load_dwordx4 v5, s[68:71], s78 offen lds\n"
        "s_and_b32 s52, s36, 3\n"
        "s_lshl_b32 s53, s52, 12\n"
        "s_add_u32 m0, s53, 32768\n"
        "s_add_u32 s54, s76, 0\n"
        "buffer_load_dwordx4 v6, s[64:67], s54 offen sc1 lds\n"
        "buffer_load_dwordx4 v6, s[64:67], s54 offen offset:1024 sc1 lds\n"
        "buffer_load_dwordx4 v7, s[64:67], s54 offen offset:2048 sc1 lds\n"
        "buffer_load_dwordx4 v7, s[64:67], s54 offen offset:3072 sc1 lds\n"
        "s_and_b32 s52, s36, 3\n"
        "s_mul_i32 s53, s52, 5120\n"
        "s_add_u32 m0, s53, 118784\n"
        "s_mov_b32 s78, 81920\n"
        "buffer_load_dwordx4 v1, s[68:71], s78 offen offset:0 lds\n"
        "buffer_load_dwordx4 v2, s[68:71], s78 offen offset:1024 lds\n"
        "buffer_load_dwordx4 v3, s[68:71], s78 offen offset:2048 lds\n"
        "buffer_load_dwordx4 v4, s[68:71], s78 offen offset:3072 lds\n"
        "s_add_u32 m0, s53, 122880\n"
        "s_nop 0\n"
        "buffer_load_dwordx4 v5, s[68:71], s78 offen lds\n"
        "s_and_b32 s52, s36, 3\n"
        "s_lshl_b32 s53, s52, 12\n"
        "s_add_u32 m0, s53, 49152\n"
        "s_add_u32 s54, s76, 2048\n"
        "buffer_load_dwordx4 v6, s[64:67], s54 offen sc1 lds\n"
        "buffer_load_dwordx4 v6, s[64:67], s54 offen offset:1024 sc1 lds\n"
        "buffer_load_dwordx4 v7, s[64:67], s54 offen offset:2048 sc1 lds\n"
        "buffer_load_dwordx4 v7, s[64:67], s54 offen offset:3072 sc1 lds\n"
        "LD_SKIP4_%=:\n"
        "KA_END_%=:\n"
        "s_mov_b32 s92, %[wolo]\n"
        "s_mov_b32 s93, %[wohi]\n"
        "s_mov_b32 %[wid], s36\n"
        "s_mov_b32 %[taglo], s96\n"
        "s_mov_b32 %[taghi], s97\n"
        "s_mov_b32 %[wo2lo], s92\n"
        "s_mov_b32 %[wo2hi], s93\n"
        "s_mov_b32 %[ws2lo], s86\n"
        "s_mov_b32 %[ws2hi], s87\n"
        : [tid] "+v"(tid), [wid] "=s"(wid), [taglo] "=s"(taglo), [taghi] "=s"(taghi),
          [wo2lo] "=s"(wo2lo), [wo2hi] "=s"(wo2hi), [ws2lo] "=s"(ws2lo), [ws2hi] "=s"(ws2hi)
        : [h] "s"(h), [i] "s"(i), [blk] "s"(b),
          [ckvlo] "s"((unsigned)(size_t)ckv), [ckvhi] "s"((unsigned)((size_t)ckv >> 32)), [wqlo] "s"((unsigned)(size_t)wq), [wqhi] "s"((unsigned)((size_t)wq >> 32)), [wuklo] "s"((unsigned)(size_t)wuk), [wukhi] "s"((unsigned)((size_t)wuk >> 32)), [wuvlo] "s"((unsigned)(size_t)wuv), [wuvhi] "s"((unsigned)((size_t)wuv >> 32)), [wolo] "s"((unsigned)(size_t)wo), [wohi] "s"((unsigned)((size_t)wo >> 32)),
          [wslo] "s"((unsigned)(size_t)ws), [wshi] "s"((unsigned)((size_t)ws >> 32)), [xlo] "s"((unsigned)(size_t)x), [xhi] "s"((unsigned)((size_t)x >> 32)), [outlo] "s"((unsigned)(size_t)out), [outhi] "s"((unsigned)((size_t)out >> 32))
        : "v1", "v2", "v3", "v4", "v5", "v6", "v7", "v8", "v9", "v10", "v11", "v12", "v13", "v14", "v15", "v16", "v17", "v18", "v19", "v20", "v21", "v22", "v23", "v24", "v25", "v26", "v27", "v28", "v29", "v30", "v31", "v32", "v33", "v34", "v35", "v36", "v37", "v38", "v39", "v40", "v41", "v42", "v43", "v44", "v45", "v46", "v47", "v48", "v49", "v50", "v51", "v52", "v53", "v54", "v55", "v56", "v57", "v58", "v59", "v60", "v61", "v62", "v63", "v64", "v65", "v66", "v67", "v68", "v69", "v70", "v71", "v72", "v73", "v74", "v75", "v76", "v77", "v78", "v79", "v80", "v81", "v82", "v83", "v84", "v85", "v86", "v87", "v88", "v89", "v90", "v91", "v92", "v93", "v94", "v95", "v96", "v97", "v98", "v99", "v100", "v101", "v102", "v103", "v104", "v105", "v106", "v107", "v108", "v109", "v110", "v111", "v112", "v113", "v114", "v115", "v116", "v117", "v118", "v119", "v120", "v121", "v122", "v123", "v124", "v125", "v126", "v127", "a0", "a1", "a2", "a3", "a4", "a5", "a6", "a7", "a8", "a9", "a10", "a11", "a12", "a13", "a14", "a15", "a16", "a17", "a18", "a19", "a20", "a21", "a22", "a23", "a24", "a25", "a26", "a27", "a28", "a29", "a30", "a31", "a32", "a33", "a34", "a35", "a36", "a37", "a38", "a39", "a40", "a41", "a42", "a43", "a44", "a45", "a46", "a47", "a48", "a49", "a50", "a51", "a52", "a53", "a54", "a55", "a56", "a57", "a58", "a59", "a60", "a61", "a62", "a63", "a64", "a65", "a66", "a67", "a68", "a69", "a70", "a71", "a72", "a73", "a74", "a75", "a76", "a77", "a78", "a79", "a80", "a81", "a82", "a83", "a84", "a85", "a86", "a87", "a88", "a89", "a90", "a91", "a92", "a93", "a94", "a95", "a96", "a97", "a98", "a99", "a100", "a101", "a102", "a103", "a104", "a105", "a106", "a107", "a108", "a109", "a110", "a111", "a112", "a113", "a114", "a115", "a116", "a117", "a118", "a119", "a120", "a121", "a122", "a123", "a124", "a125", "a126", "a127", "s36", "s37", "s38", "s39", "s40", "s41", "s42", "s43", "s44", "s45", "s46", "s47", "s48", "s49", "s50", "s51", "s52", "s53", "s54", "s55", "s56", "s57", "s58", "s59", "s60", "s61", "s62", "s63", "s64", "s65", "s66", "s67", "s68", "s69", "s70", "s71", "s72", "s73", "s74", "s75", "s76", "s77", "s78", "s79", "s80", "s81", "s82", "s83", "s84", "s85", "s86", "s87", "s88", "s89", "s90", "s91", "s92", "s93", "s94", "s95", "s96", "s97", "s98", "s99", "vcc", "scc", "memory");
    kargs_t ka = (kargs_t)__builtin_amdgcn_kernarg_segment_ptr();
    asm volatile("" : "+s"(ka));
    unsigned b2 = blockIdx.x;
    asm volatile("" : "+s"(b2));
    const unsigned long long tag = ((unsigned long long)taghi << 32) | taglo;
    const unsigned restart = __builtin_amdgcn_readfirstlane(exact_path_if_needed(lds, wid, b2, tag, ka));
    {
        const unsigned h2 = (b2 & 7) * 4 + ((b2 >> 3) & 3), i2 = b2 >> 5;
        unsigned dummy;
        asm volatile(
        "k_block_second_half:\n"
        "v_mbcnt_lo_u32_b32 v0, -1, 0\n"
        "v_mbcnt_hi_u32_b32 v0, -1, v0\n"
        "v_lshlrev_b32 v0, 4, v0\n"
        "s_mov_b32 s36, %[wid]\n"
        "s_mov_b32 s37, %[h]\n"
        "s_mov_b32 s38, %[i]\n"
        "s_mov_b32 s86, %[wslo]\n"
        "s_mov_b32 s87, %[wshi]\n"
        "s_mov_b32 s96, %[taglo]\n"
        "s_mov_b32 s97, %[taghi]\n"
        "s_cmp_ge_u32 s36, 4\n"
        "s_cbranch_scc1 KB_LOADER_%=\n"
        "s_lshr_b32 s52, s37, 2\n"
        "s_mul_i32 s52, s52, 2621440\n"
        "s_and_b32 s53, s37, 3\n"
        "s_mul_i32 s53, s53, 320\n"
        "s_add_u32 s52, s52, s53\n"
        "s_add_u32 s52, s52, 54525952\n"
        "s_sub_u32 s53, 2621440, s53\n"
        "s_add_u32 s72, s86, s52\n"
        "s_addc_u32 s73, s87, 0\n"
        "s_and_b32 s73, s73, 0xffff\n"
        "s_mov_b32 s74, s53\n"
        "s_mov_b32 s75, 0x20000\n"
        "v_lshrrev_b32 v16, 4, v0\n"
        "v_lshrrev_b32 v18, 4, v16\n"
        "v_bfe_u32 v17, v16, 2, 2\n"
        "v_lshl_add_u32 v17, v18, 2, v17\n"
        "v_mul_u32_u24 v19, 320, v17\n"
        "v_bfe_u32 v20, v16, 1, 1\n"
        "v_lshl_add_u32 v19, v20, 4, v19\n"
        "v_and_b32 v20, 1, v16\n"
        "v_lshl_add_u32 v19, v20, 3, v19\n"
        "v_add_u32 v19, 98304, v19\n"
        "v_and_b32 v20, 1, v18\n"
        "v_lshl_add_u32 v17, v20, 5, v19\n"
        "v_xor_b32 v20, 1, v20\n"
        "v_lshl_add_u32 v18, v20, 5, v19\n"
        "s_lshr_b32 s52, s36, 1\n"
        "s_mul_i32 s78, s52, 192\n"
        "v_add_u32 v1, s78, v17\n"
        "v_add_u32 v2, s78, v18\n"
        "v_add_u32 v3, 128, v17\n"
        "v_add_u32 v4, 128, v18\n"
        "s_and_b32 s53, s36, 1\n"
        "s_lshl_b32 s53, s53, 13\n"
        "s_add_u32 s53, s53, 32768\n"
        "s_lshl_b32 s54, s52, 12\n"
        "s_add_u32 s55, s53, s54\n"
        "v_add_u32 v5, s55, v0\n"
        "s_xor_b32 s54, s54, 4096\n"
        "s_add_u32 s55, s53, s54\n"
        "v_add_u32 v6, s55, v0\n"
        "v_and_b32 v19, 15, v16\n"
        "v_mul_u32_u24 v19, 1280, v19\n"
        "v_lshrrev_b32 v20, 4, v16\n"
        "v_lshl_add_u32 v7, v20, 4, v19\n"
        "v_mov_b32 v19, 160000\n"
        "v_mov_b32 v22, 0\n"
        "v_mov_b32 v23, 0\n"
        "ds_write_b64 v19, v[22:23]\n"
        "s_barrier\n"
        "ds_read_b128 v[8:11], v5 offset:0\n"
        "ds_read_b128 v[12:15], v5 offset:1024\n"
        "ds_read_b128 v[16:19], v5 offset:2048\n"
        "ds_read_b128 v[20:23], v5 offset:3072\n"
        "ds_read_b128 v[24:27], v6 offset:0\n"
        "ds_read_b128 v[28:31], v6 offset:1024\n"
        "ds_read_b128 v[32:35], v6 offset:2048\n"
        "ds_read_b128 v[36:39], v6 offset:3072\n"
        "ds_read_b64_tr_b16 v[72:73], v1 offset:0\n"
        "ds_read_b64_tr_b16 v[74:75], v1 offset:5120\n"
        "ds_read_b64_tr_b16 v[76:77], v1 offset:10240\n"
        "ds_read_b64_tr_b16 v[78:79], v1 offset:15360\n"
        "ds_read_b64_tr_b16 v[80:81], v2 offset:0\n"
        "ds_read_b64_tr_b16 v[82:83], v2 offset:5120\n"
        "ds_read_b64_tr_b16 v[84:85], v2 offset:10240\n"
        "ds_read_b64_tr_b16 v[86:87], v2 offset:15360\n"
        "s_waitcnt lgkmcnt(0)\n"
        "v_mfma_f32_16x16x32_bf16 a[0:3], v[72:75], v[8:11], 0\n"
        "ds_read_b64_tr_b16 v[88:89], v1 offset:64\n"
        "v_mfma_f32_16x16x32_bf16 a[0:3], v[76:79], v[12:15], a[0:3]\n"
        "ds_read_b64_tr_b16 v[90:91], v1 offset:5184\n"
        "v_mfma_f32_16x16x32_bf16 a[4:7], v[72:75], v[16:19], 0\n"
        "ds_read_b64_tr_b16 v[92:93], v1 offset:10304\n"
        "v_mfma_f32_16x16x32_bf16 a[4:7], v[76:79], v[20:23], a[4:7]\n"
        "ds_read_b64_tr_b16 v[94:95], v1 offset:15424\n"
        "v_mfma_f32_16x16x32_bf16 a[8:11], v[72:75], v[24:27], 0\n"
        "v_mfma_f32_16x16x32_bf16 a[8:11], v[76:79], v[28:31], a[8:11]\n"
        "v_mfma_f32_16x16x32_bf16 a[12:15], v[72:75], v[32:35], 0\n"
        "v_mfma_f32_16x16x32_bf16 a[12:15], v[76:79], v[36:39], a[12:15]\n"
        "v_mfma_f32_16x16x32_bf16 a[16:19], v[80:83], v[8:11], 0\n"
        "ds_read_b64_tr_b16 v[72:73], v2 offset:64\n"
        "v_mfma_f32_16x16x32_bf16 a[16:19], v[84:87], v[12:15], a[16:19]\n"
        "ds_read_b64_tr_b16 v[74:75], v2 offset:5184\n"
        "v_mfma_f32_16x16x32_bf16 a[20:23], v[80:83], v[16:19], 0\n"
        "ds_read_b64_tr_b16 v[76:77], v2 offset:10304\n"
        "v_mfma_f32_16x16x32_bf16 a[20:23], v[84:87], v[20:23], a[20:23]\n"
        "ds_read_b64_tr_b16 v[78:79], v2 offset:15424\n"
        "v_mfma_f32_16x16x32_bf16 a[24:27], v[80:83], v[24:27], 0\n"
        "v_mfma_f32_16x16x32_bf16 a[24:27], v[84:87], v[28:31], a[24:27]\n"
        "v_mfma_f32_16x16x32_bf16 a[28:31], v[80:83], v[32:35], 0\n"
        "v_mfma_f32_16x16x32_bf16 a[28:31], v[84:87], v[36:39], a[28:31]\n"
        "s_waitcnt lgkmcnt(6)\n"
        "v_mfma_f32_16x16x32_bf16 a[32:35], v[88:91], v[8:11], 0\n"
        "ds_read_b64_tr_b16 v[80:81], v3 offset:0\n"
        "s_waitcnt lgkmcnt(5)\n"
        "v_mfma_f32_16x16x32_bf16 a[32:35], v[92:95], v[12:15], a[32:35]\n"
        "ds_read_b64_tr_b16 v[82:83], v3 offset:5120\n"
        "v_mfma_f32_16x16x32_bf16 a[36:39], v[88:91], v[16:19], 0\n"
        "ds_read_b64_tr_b16 v[84:85], v3 offset:10240\n"
        "v_mfma_f32_16x16x32_bf16 a[36:39], v[92:95], v[20:23], a[36:39]\n"
        "ds_read_b64_tr_b16 v[86:87], v3 offset:15360\n"
        "v_mfma_f32_16x16x32_bf16 a[40:43], v[88:91], v[24:27], 0\n"
        "v_mfma_f32_16x16x32_bf16 a[40:43], v[92:95], v[28:31], a[40:43]\n"
        "v_mfma_f32_16x16x32_bf16 a[44:47], v[88:91], v[32:35], 0\n"
        "v_mfma_f32_16x16x32_bf16 a[44:47], v[92:95], v[36:39], a[44:47]\n"
        "s_waitcnt lgkmcnt(5)\n"
        "v_mfma_f32_16x16x32_bf16 a[48:51], v[72:75], v[8:11], 0\n"
        "ds_read_b64_tr_b16 v[88:89], v4 offset:0\n"
        "s_waitcnt lgkmcnt(5)\n"
        "v_mfma_f32_16x16x32_bf16 a[48:51], v[76:79], v[12:15], a[48:51]\n"
        "ds_read_b64_tr_b16 v[90:91], v4 offset:5120\n"
        "v_mfma_f32_16x16x32_bf16 a[52:55], v[72:75], v[16:19], 0\n"
        "ds_read_b64_tr_b16 v[92:93], v4 offset:10240\n"
        "v_mfma_f32_16x16x32_bf16 a[52:55], v[76:79], v[20:23], a[52:55]\n"
        "ds_read_b64_tr_b16 v[94:95], v4 offset:15360\n"
        "v_mfma_f32_16x16x32_bf16 a[56:59], v[72:75], v[24:27], 0\n"
        "v_mfma_f32_16x16x32_bf16 a[56:59], v[76:79], v[28:31], a[56:59]\n"
        "v_mfma_f32_16x16x32_bf16 a[60:63], v[72:75], v[32:35], 0\n"
        "v_mfma_f32_16x16x32_bf16 a[60:63], v[76:79], v[36:39], a[60:63]\n"
        "s_waitcnt lgkmcnt(0)\n"
        "s_barrier\n"
        "v_add_u32 v1, 20480, v1\n"
        "v_add_u32 v2, 20480, v2\n"
        "v_add_u32 v3, 20480, v3\n"
        "v_add_u32 v4, 20480, v4\n"
        "v_add_u32 v5, 16384, v5\n"
        "v_add_u32 v6, 16384, v6\n"
        "v_mfma_f32_16x16x32_bf16 a[64:67], v[80:83], v[8:11], 0\n"
        "ds_read_b64_tr_b16 v[72:73], v1 offset:0\n"
        "ds_read_b64_tr_b16 v[74:75], v1 offset:5120\n"
        "v_mfma_f32_16x16x32_bf16 a[64:67], v[84:87], v[12:15], a[64:67]\n"
        "ds_read_b64_tr_b16 v[76:77], v1 offset:10240\n"
        "ds_read_b64_tr_b16 v[78:79], v1 offset:15360\n"
        "v_mfma_f32_16x16x32_bf16 a[68:71], v[80:83], v[16:19], 0\n"
        "ds_read_b128 v[40:43], v5 offset:0\n"
        "ds_read_b128 v[44:47], v5 offset:1024\n"
        "v_mfma_f32_16x16x32_bf16 a[68:71], v[84:87], v[20:23], a[68:71]\n"
        "ds_read_b128 v[48:51], v5 offset:2048\n"
        "ds_read_b128 v[52:55], v5 offset:3072\n"
        "v_mfma_f32_16x16x32_bf16 a[72:75], v[88:91], v[8:11], 0\n"
        "ds_read_b64_tr_b16 v[80:81], v2 offset:0\n"
        "ds_read_b64_tr_b16 v[82:83], v2 offset:5120\n"
        "v_mfma_f32_16x16x32_bf16 a[72:75], v[92:95], v[12:15], a[72:75]\n"
        "ds_read_b64_tr_b16 v[84:85], v2 offset:10240\n"
        "ds_read_b64_tr_b16 v[86:87], v2 offset:15360\n"
        "v_mfma_f32_16x16x32_bf16 a[76:79], v[88:91], v[16:19], 0\n"
        "ds_read_b128 v[56:59], v6 offset:0\n"
        "ds_read_b128 v[60:63], v6 offset:1024\n"
        "v_mfma_f32_16x16x32_bf16 a[76:79], v[92:95], v[20:23], a[76:79]\n"
        "ds_read_b128 v[64:67], v6 offset:2048\n"
        "ds_read_b128 v[68:71], v6 offset:3072\n"
        "s_waitcnt lgkmcnt(11)\n"
        "v_mfma_f32_16x16x32_bf16 a[0:3], v[72:75], v[40:43], a[0:3]\n"
        "ds_read_b64_tr_b16 v[88:89], v1 offset:64\n"
        "s_waitcnt lgkmcnt(11)\n"
        "v_mfma_f32_16x16x32_bf16 a[0:3], v[76:79], v[44:47], a[0:3]\n"
        "ds_read_b64_tr_b16 v[90:91], v1 offset:5184\n"
        "s_waitcnt lgkmcnt(11)\n"
        "v_mfma_f32_16x16x32_bf16 a[4:7], v[72:75], v[48:51], a[4:7]\n"
        "ds_read_b64_tr_b16 v[92:93], v1 offset:10304\n"
        "s_waitcnt lgkmcnt(11)\n"
        "v_mfma_f32_16x16x32_bf16 a[4:7], v[76:79], v[52:55], a[4:7]\n"
        "ds_read_b64_tr_b16 v[94:95], v1 offset:15424\n"
        "s_waitcnt lgkmcnt(7)\n"
        "v_mfma_f32_16x16x32_bf16 a[8:11], v[72:75], v[56:59], a[8:11]\n"
        "s_waitcnt lgkmcnt(6)\n"
        "v_mfma_f32_16x16x32_bf16 a[8:11], v[76:79], v[60:63], a[8:11]\n"
        "s_waitcnt lgkmcnt(5)\n"
        "v_mfma_f32_16x16x32_bf16 a[12:15], v[72:75], v[64:67], a[12:15]\n"
        "s_waitcnt lgkmcnt(4)\n"
        "v_mfma_f32_16x16x32_bf16 a[12:15], v[76:79], v[68:71], a[12:15]\n"
        "v_mfma_f32_16x16x32_bf16 a[16:19], v[80:83], v[40:43], a[16:19]\n"
        "ds_read_b64_tr_b16 v[72:73], v2 offset:64\n"
        "v_mfma_f32_16x16x32_bf16 a[16:19], v[84:87], v[44:47], a[16:19]\n"
        "ds_read_b64_tr_b16 v[74:75], v2 offset:5184\n"
        "v_mfma_f32_16x16x32_bf16 a[20:23], v[80:83], v[48:51], a[20:23]\n"
        "ds_read_b64_tr_b16 v[76:77], v2 offset:10304\n"
        "v_mfma_f32_16x16x32_bf16 a[20:23], v[84:87], v[52:55], a[20:23]\n"
        "ds_read_b64_tr_b16 v[78:79], v2 offset:15424\n"
        "v_mfma_f32_16x16x32_bf16 a[24:27], v[80:83], v[56:59], a[24:27]\n"
        "v_mfma_f32_16x16x32_bf16 a[24:27], v[84:87], v[60:63], a[24:27]\n"
        "v_mfma_f32_16x16x32_bf16 a[28:31], v[80:83], v[64:67], a[28:31]\n"
        "v_mfma_f32_16x16x32_bf16 a[28:31], v[84:87], v[68:71], a[28:31]\n"
        "s_waitcnt lgkmcnt(4)\n"
        "v_mfma_f32_16x16x32_bf16 a[32:35], v[88:91], v[40:43], a[32:35]\n"
        "ds_read_b64_tr_b16 v[80:81], v3 offset:0\n"
        "v_mfma_f32_16x16x32_bf16 a[32:35], v[92:95], v[44:47], a[32:35]\n"
        "ds_read_b64_tr_b16 v[82:83], v3 offset:5120\n"
        "v_mfma_f32_16x16x32_bf16 a[36:39], v[88:91], v[48:51], a[36:39]\n"
        "ds_read_b64_tr_b16 v[84:85], v3 offset:10240\n"
        "v_mfma_f32_16x16x32_bf16 a[36:39], v[92:95], v[52:55], a[36:39]\n"
        "ds_read_b64_tr_b16 v[86:87], v3 offset:15360\n"
        "v_mfma_f32_16x16x32_bf16 a[40:43], v[88:91], v[56:59], a[40:43]\n"
        "v_mfma_f32_16x16x32_bf16 a[40:43], v[92:95], v[60:63], a[40:43]\n"
        "v_mfma_f32_16x16x32_bf16 a[44:47], v[88:91], v[64:67], a[44:47]\n"
        "v_mfma_f32_16x16x32_bf16 a[44:47], v[92:95], v[68:71], a[44:47]\n"
        "s_waitcnt lgkmcnt(6)\n"
        "v_mfma_f32_16x16x32_bf16 a[48:51], v[72:75], v[40:43], a[48:51]\n"
        "ds_read_b64_tr_b16 v[88:89], v4 offset:0\n"
        "s_waitcnt lgkmcnt(5)\n"
        "v_mfma_f32_16x16x32_bf16 a[48:51], v[76:79], v[44:47], a[48:51]\n"
        "ds_read_b64_tr_b16 v[90:91], v4 offset:5120\n"
        "v_mfma_f32_16x16x32_bf16 a[52:55], v[72:75], v[48:51], a[52:55]\n"
        "ds_read_b64_tr_b16 v[92:93], v4 offset:10240\n"
        "v_mfma_f32_16x16x32_bf16 a[52:55], v[76:79], v[52:55], a[52:55]\n"
        "ds_read_b64_tr_b16 v[94:95], v4 offset:15360\n"
        "v_mfma_f32_16x16x32_bf16 a[56:59], v[72:75], v[56:59], a[56:59]\n"
        "v_mfma_f32_16x16x32_bf16 a[56:59], v[76:79], v[60:63], a[56:59]\n"
        "v_mfma_f32_16x16x32_bf16 a[60:63], v[72:75], v[64:67], a[60:63]\n"
        "v_mfma_f32_16x16x32_bf16 a[60:63], v[76:79], v[68:71], a[60:63]\n"
        "s_waitcnt vmcnt(0)\n"
        "s_waitcnt lgkmcnt(0)\n"
        "s_barrier\n"
        "s_cmp_lg_u32 s36, 0\n"
        "s_cbranch_scc1 KA_NCB_%=\n"
        "s_lshr_b32 s88, s37, 2\n"
        "s_lshl_b32 s88, s88, 3\n"
        "s_add_u32 s88, s88, s38\n"
        "s_lshl_b32 s88, s88, 2\n"
        "s_and_b32 s52, s37, 3\n"
        "s_add_u32 s88, s88, s52\n"
        "s_lshl_b32 s88, s88, 7\n"
        "s_add_u32 s88, s88, 98304\n"
        "s_mov_b64 exec, 1\n"
        "v_mov_b32 v121, s88\n"
        "v_mov_b32 v122, s96\n"
        "v_mov_b32 v123, s97\n"
        "global_store_dwordx2 v121, v[122:123], s[86:87] sc1\n"
        "s_mov_b64 exec, -1\n"
        "KA_NCB_%=:\n"
        "v_add_u32 v1, 20480, v1\n"
        "v_add_u32 v2, 20480, v2\n"
        "v_add_u32 v3, 20480, v3\n"
        "v_add_u32 v4, 20480, v4\n"
        "v_add_u32 v5, 16384, v5\n"
        "v_add_u32 v6, 16384, v6\n"
        "v_mfma_f32_16x16x32_bf16 a[64:67], v[80:83], v[40:43], a[64:67]\n"
        "ds_read_b64_tr_b16 v[72:73], v1 offset:0\n"
        "ds_read_b64_tr_b16 v[74:75], v1 offset:5120\n"
        "v_mfma_f32_16x16x32_bf16 a[64:67], v[84:87], v[44:47], a[64:67]\n"
        "ds_read_b64_tr_b16 v[76:77], v1 offset:10240\n"
        "ds_read_b64_tr_b16 v[78:79], v1 offset:15360\n"
        "v_mfma_f32_16x16x32_bf16 a[68:71], v[80:83], v[48:51], a[68:71]\n"
        "ds_read_b128 v[8:11], v5 offset:0\n"
        "ds_read_b128 v[12:15], v5 offset:1024\n"
        "v_mfma_f32_16x16x32_bf16 a[68:71], v[84:87], v[52:55], a[68:71]\n"
        "ds_read_b128 v[16:19], v5 offset:2048\n"
        "ds_read_b128 v[20:23], v5 offset:3072\n"
        "v_mfma_f32_16x16x32_bf16 a[72:75], v[88:91], v[40:43], a[72:75]\n"
        "ds_read_b64_tr_b16 v[80:81], v2 offset:0\n"
        "ds_read_b64_tr_b16 v[82:83], v2 offset:5120\n"
        "v_mfma_f32_16x16x32_bf16 a[72:75], v[92:95], v[44:47], a[72:75]\n"
        "ds_read_b64_tr_b16 v[84:85], v2 offset:10240\n"
        "ds_read_b64_tr_b16 v[86:87], v2 offset:15360\n"
        "v_mfma_f32_16x16x32_bf16 a[76:79], v[88:91], v[48:51], a[76:79]\n"
        "ds_read_b128 v[24:27], v6 offset:0\n"
        "ds_read_b128 v[28:31], v6 offset:1024\n"
        "v_mfma_f32_16x16x32_bf16 a[76:79], v[92:95], v[52:55], a[76:79]\n"
        "ds_read_b128 v[32:35], v6 offset:2048\n"
        "ds_read_b128 v[36:39], v6 offset:3072\n"
        "s_waitcnt lgkmcnt(11)\n"
        "v_mfma_f32_16x16x32_bf16 a[0:3], v[72:75], v[8:11], a[0:3]\n"
        "ds_read_b64_tr_b16 v[88:89], v1 offset:64\n"
        "s_waitcnt lgkmcnt(11)\n"
        "v_mfma_f32_16x16x32_bf16 a[0:3], v[76:79], v[12:15], a[0:3]\n"
        "ds_read_b64_tr_b16 v[90:91], v1 offset:5184\n"
        "s_waitcnt lgkmcnt(11)\n"
        "v_mfma_f32_16x16x32_bf16 a[4:7], v[72:75], v[16:19], a[4:7]\n"
        "ds_read_b64_tr_b16 v[92:93], v1 offset:10304\n"
        "s_waitcnt lgkmcnt(11)\n"
        "v_mfma_f32_16x16x32_bf16 a[4:7], v[76:79], v[20:23], a[4:7]\n"
        "ds_read_b64_tr_b16 v[94:95], v1 offset:15424\n"
        "s_waitcnt lgkmcnt(7)\n"
        "v_mfma_f32_16x16x32_bf16 a[8:11], v[72:75], v[24:27], a[8:11]\n"
        "s_waitcnt lgkmcnt(6)\n"
        "v_mfma_f32_16x16x32_bf16 a[8:11], v[76:79], v[28:31], a[8:11]\n"
        "s_waitcnt lgkmcnt(5)\n"
        "v_mfma_f32_16x16x32_bf16 a[12:15], v[72:75], v[32:35], a[12:15]\n"
        "s_waitcnt lgkmcnt(4)\n"
        "v_mfma_f32_16x16x32_bf16 a[12:15], v[76:79], v[36:39], a[12:15]\n"
        "v_mfma_f32_16x16x32_bf16 a[16:19], v[80:83], v[8:11], a[16:19]\n"
        "ds_read_b64_tr_b16 v[72:73], v2 offset:64\n"
        "v_mfma_f32_16x16x32_bf16 a[16:19], v[84:87], v[12:15], a[16:19]\n"
        "ds_read_b64_tr_b16 v[74:75], v2 offset:5184\n"
        "v_mfma_f32_16x16x32_bf16 a[20:23], v[80:83], v[16:19], a[20:23]\n"
        "ds_read_b64_tr_b16 v[76:77], v2 offset:10304\n"
        "v_mfma_f32_16x16x32_bf16 a[20:23], v[84:87], v[20:23], a[20:23]\n"
        "ds_read_b64_tr_b16 v[78:79], v2 offset:15424\n"
        "v_mfma_f32_16x16x32_bf16 a[24:27], v[80:83], v[24:27], a[24:27]\n"
        "v_mfma_f32_16x16x32_bf16 a[24:27], v[84:87], v[28:31], a[24:27]\n"
        "v_mfma_f32_16x16x32_bf16 a[28:31], v[80:83], v[32:35], a[28:31]\n"
        "v_mfma_f32_16x16x32_bf16 a[28:31], v[84:87], v[36:39], a[28:31]\n"
        "s_waitcnt lgkmcnt(4)\n"
        "v_mfma_f32_16x16x32_bf16 a[32:35], v[88:91], v[8:11], a[32:35]\n"
        "ds_read_b64_tr_b16 v[80:81], v3 offset:0\n"
        "v_mfma_f32_16x16x32_bf16 a[32:35], v[92:95], v[12:15], a[32:35]\n"
        "ds_read_b64_tr_b16 v[82:83], v3 offset:5120\n"
        "v_mfma_f32_16x16x32_bf16 a[36:39], v[88:91], v[16:19], a[36:39]\n"
        "ds_read_b64_tr_b16 v[84:85], v3 offset:10240\n"
        "v_mfma_f32_16x16x32_bf16 a[36:39], v[92:95], v[20:23], a[36:39]\n"
        "ds_read_b64_tr_b16 v[86:87], v3 offset:15360\n"
        "v_mfma_f32_16x16x32_bf16 a[40:43], v[88:91], v[24:27], a[40:43]\n"
        "v_mfma_f32_16x16x32_bf16 a[40:43], v[92:95], v[28:31], a[40:43]\n"
        "v_mfma_f32_16x16x32_bf16 a[44:47], v[88:91], v[32:35], a[44:47]\n"
        "v_mfma_f32_16x16x32_bf16 a[44:47], v[92:95], v[36:39], a[44:47]\n"
        "s_waitcnt lgkmcnt(6)\n"
        "v_mfma_f32_16x16x32_bf16 a[48:51], v[72:75], v[8:11], a[48:51]\n"
        "ds_read_b64_tr_b16 v[88:89], v4 offset:0\n"
        "s_waitcnt lgkmcnt(5)\n"
        "v_mfma_f32_16x16x32_bf16 a[48:51], v[76:79], v[12:15], a[48:51]\n"
        "ds_read_b64_tr_b16 v[90:91], v4 offset:5120\n"
        "v_mfma_f32_16x16x32_bf16 a[52:55], v[72:75], v[16:19], a[52:55]\n"
        "ds_read_b64_tr_b16 v[92:93], v4 offset:10240\n"
        "v_mfma_f32_16x16x32_bf16 a[52:55], v[76:79], v[20:23], a[52:55]\n"
        "ds_read_b64_tr_b16 v[94:95], v4 offset:15360\n"
        "v_mfma_f32_16x16x32_bf16 a[56:59], v[72:75], v[24:27], a[56:59]\n"
        "v_mfma_f32_16x16x32_bf16 a[56:59], v[76:79], v[28:31], a[56:59]\n"
        "v_mfma_f32_16x16x32_bf16 a[60:63], v[72:75], v[32:35], a[60:63]\n"
        "v_mfma_f32_16x16x32_bf16 a[60:63], v[76:79], v[36:39], a[60:63]\n"
        "s_waitcnt lgkmcnt(0)\n"
        "s_barrier\n"
        "v_subrev_u32 v1, 40960, v1\n"
        "v_subrev_u32 v2, 40960, v2\n"
        "v_subrev_u32 v3, 40960, v3\n"
        "v_subrev_u32 v4, 40960, v4\n"
        "v_add_u32 v5, 16384, v5\n"
        "v_add_u32 v6, 16384, v6\n"
        "v_mfma_f32_16x16x32_bf16 a[64:67], v[80:83], v[8:11], a[64:67]\n"
        "ds_read_b64_tr_b16 v[72:73], v1 offset:0\n"
        "ds_read_b64_tr_b16 v[74:75], v1 offset:5120\n"
        "v_mfma_f32_16x16x32_bf16 a[64:67], v[84:87], v[12:15], a[64:67]\n"
        "ds_read_b64_tr_b16 v[76:77], v1 offset:10240\n"
        "ds_read_b64_tr_b16 v[78:79], v1 offset:15360\n"
        "v_mfma_f32_16x16x32_bf16 a[68:71], v[80:83], v[16:19], a[68:71]\n"
        "ds_read_b128 v[40:43], v5 offset:0\n"
        "ds_read_b128 v[44:47], v5 offset:1024\n"
        "v_mfma_f32_16x16x32_bf16 a[68:71], v[84:87], v[20:23], a[68:71]\n"
        "ds_read_b128 v[48:51], v5 offset:2048\n"
        "ds_read_b128 v[52:55], v5 offset:3072\n"
        "v_mfma_f32_16x16x32_bf16 a[72:75], v[88:91], v[8:11], a[72:75]\n"
        "ds_read_b64_tr_b16 v[80:81], v2 offset:0\n"
        "ds_read_b64_tr_b16 v[82:83], v2 offset:5120\n"
        "v_mfma_f32_16x16x32_bf16 a[72:75], v[92:95], v[12:15], a[72:75]\n"
        "ds_read_b64_tr_b16 v[84:85], v2 offset:10240\n"
        "ds_read_b64_tr_b16 v[86:87], v2 offset:15360\n"
        "v_mfma_f32_16x16x32_bf16 a[76:79], v[88:91], v[16:19], a[76:79]\n"
        "ds_read_b128 v[56:59], v6 offset:0\n"
        "ds_read_b128 v[60:63], v6 offset:1024\n"
        "v_mfma_f32_16x16x32_bf16 a[76:79], v[92:95], v[20:23], a[76:79]\n"
        "ds_read_b128 v[64:67], v6 offset:2048\n"
        "ds_read_b128 v[68:71], v6 offset:3072\n"
        "s_waitcnt lgkmcnt(11)\n"
        "v_mfma_f32_16x16x32_bf16 a[0:3], v[72:75], v[40:43], a[0:3]\n"
        "ds_read_b64_tr_b16 v[88:89], v1 offset:64\n"
        "s_waitcnt lgkmcnt(11)\n"
        "v_mfma_f32_16x16x32_bf16 a[0:3], v[76:79], v[44:47], a[0:3]\n"
        "ds_read_b64_tr_b16 v[90:91], v1 offset:5184\n"
        "s_waitcnt lgkmcnt(11)\n"
        "v_mfma_f32_16x16x32_bf16 a[4:7], v[72:75], v[48:51], a[4:7]\n"
        "ds_read_b64_tr_b16 v[92:93], v1 offset:10304\n"
        "s_waitcnt lgkmcnt(11)\n"
        "v_mfma_f32_16x16x32_bf16 a[4:7], v[76:79], v[52:55], a[4:7]\n"
        "ds_read_b64_tr_b16 v[94:95], v1 offset:15424\n"
        "s_waitcnt lgkmcnt(7)\n"
        "v_mfma_f32_16x16x32_bf16 a[8:11], v[72:75], v[56:59], a[8:11]\n"
        "s_waitcnt lgkmcnt(6)\n"
        "v_mfma_f32_16x16x32_bf16 a[8:11], v[76:79], v[60:63], a[8:11]\n"
        "s_waitcnt lgkmcnt(5)\n"
        "v_mfma_f32_16x16x32_bf16 a[12:15], v[72:75], v[64:67], a[12:15]\n"
        "s_waitcnt lgkmcnt(4)\n"
        "v_mfma_f32_16x16x32_bf16 a[12:15], v[76:79], v[68:71], a[12:15]\n"
        "v_mfma_f32_16x16x32_bf16 a[16:19], v[80:83], v[40:43], a[16:19]\n"
        "ds_read_b64_tr_b16 v[72:73], v2 offset:64\n"
        "v_mfma_f32_16x16x32_bf16 a[16:19], v[84:87], v[44:47], a[16:19]\n"
        "ds_read_b64_tr_b16 v[74:75], v2 offset:5184\n"
        "v_mfma_f32_16x16x32_bf16 a[20:23], v[80:83], v[48:51], a[20:23]\n"
        "ds_read_b64_tr_b16 v[76:77], v2 offset:10304\n"
        "v_mfma_f32_16x16x32_bf16 a[20:23], v[84:87], v[52:55], a[20:23]\n"
        "ds_read_b64_tr_b16 v[78:79], v2 offset:15424\n"
        "v_mfma_f32_16x16x32_bf16 a[24:27], v[80:83], v[56:59], a[24:27]\n"
        "v_mfma_f32_16x16x32_bf16 a[24:27], v[84:87], v[60:63], a[24:27]\n"
        "v_mfma_f32_16x16x32_bf16 a[28:31], v[80:83], v[64:67], a[28:31]\n"
        "v_mfma_f32_16x16x32_bf16 a[28:31], v[84:87], v[68:71], a[28:31]\n"
        "s_waitcnt lgkmcnt(4)\n"
        "v_mfma_f32_16x16x32_bf16 a[32:35], v[88:91], v[40:43], a[32:35]\n"
        "ds_read_b64_tr_b16 v[80:81], v3 offset:0\n"
        "v_mfma_f32_16x16x32_bf16 a[32:35], v[92:95], v[44:47], a[32:35]\n"
        "ds_read_b64_tr_b16 v[82:83], v3 offset:5120\n"
        "v_mfma_f32_16x16x32_bf16 a[36:39], v[88:91], v[48:51], a[36:39]\n"
        "ds_read_b64_tr_b16 v[84:85], v3 offset:10240\n"
        "v_mfma_f32_16x16x32_bf16 a[36:39], v[92:95], v[52:55], a[36:39]\n"
        "ds_read_b64_tr_b16 v[86:87], v3 offset:15360\n"
        "v_mfma_f32_16x16x32_bf16 a[40:43], v[88:91], v[56:59], a[40:43]\n"
        "v_mfma_f32_16x16x32_bf16 a[40:43], v[92:95], v[60:63], a[40:43]\n"
        "v_mfma_f32_16x16x32_bf16 a[44:47], v[88:91], v[64:67], a[44:47]\n"
        "v_mfma_f32_16x16x32_bf16 a[44:47], v[92:95], v[68:71], a[44:47]\n"
        "s_waitcnt lgkmcnt(6)\n"
        "v_mfma_f32_16x16x32_bf16 a[48:51], v[72:75], v[40:43], a[48:51]\n"
        "ds_read_b64_tr_b16 v[88:89], v4 offset:0\n"
        "s_waitcnt lgkmcnt(5)\n"
        "v_mfma_f32_16x16x32_bf16 a[48:51], v[76:79], v[44:47], a[48:51]\n"
        "ds_read_b64_tr_b16 v[90:91], v4 offset:5120\n"
        "v_mfma_f32_16x16x32_bf16 a[52:55], v[72:75], v[48:51], a[52:55]\n"
        "ds_read_b64_tr_b16 v[92:93], v4 offset:10240\n"
        "v_mfma_f32_16x16x32_bf16 a[52:55], v[76:79], v[52:55], a[52:55]\n"
        "ds_read_b64_tr_b16 v[94:95], v4 offset:15360\n"
        "v_mfma_f32_16x16x32_bf16 a[56:59], v[72:75], v[56:59], a[56:59]\n"
        "v_mfma_f32_16x16x32_bf16 a[56:59], v[76:79], v[60:63], a[56:59]\n"
        "v_mfma_f32_16x16x32_bf16 a[60:63], v[72:75], v[64:67], a[60:63]\n"
        "v_mfma_f32_16x16x32_bf16 a[60:63], v[76:79], v[68:71], a[60:63]\n"
        "s_waitcnt lgkmcnt(0)\n"
        "s_barrier\n"
        "v_add_u32 v1, 20480, v1\n"
        "v_add_u32 v2, 20480, v2\n"
        "v_add_u32 v3, 20480, v3\n"
        "v_add_u32 v4, 20480, v4\n"
        "v_subrev_u32 v5, 81920, v5\n"
        "v_subrev_u32 v6, 81920, v6\n"
        "v_mfma_f32_16x16x32_bf16 a[64:67], v[80:83], v[40:43], a[64:67]\n"
        "ds_read_b64_tr_b16 v[72:73], v1 offset:0\n"
        "ds_read_b64_tr_b16 v[74:75], v1 offset:5120\n"
        "v_mfma_f32_16x16x32_bf16 a[64:67], v[84:87], v[44:47], a[64:67]\n"
        "ds_read_b64_tr_b16 v[76:77], v1 offset:10240\n"
        "ds_read_b64_tr_b16 v[78:79], v1 offset:15360\n"
        "v_mfma_f32_16x16x32_bf16 a[68:71], v[80:83], v[48:51], a[68:71]\n"
        "ds_read_b128 v[8:11], v5 offset:0\n"
        "ds_read_b128 v[12:15], v5 offset:1024\n"
        "v_mfma_f32_16x16x32_bf16 a[68:71], v[84:87], v[52:55], a[68:71]\n"
        "ds_read_b128 v[16:19], v5 offset:2048\n"
        "ds_read_b128 v[20:23], v5 offset:3072\n"
        "v_mfma_f32_16x16x32_bf16 a[72:75], v[88:91], v[40:43], a[72:75]\n"
        "ds_read_b64_tr_b16 v[80:81], v2 offset:0\n"
        "ds_read_b64_tr_b16 v[82:83], v2 offset:5120\n"
        "v_mfma_f32_16x16x32_bf16 a[72:75], v[92:95], v[44:47], a[72:75]\n"
        "ds_read_b64_tr_b16 v[84:85], v2 offset:10240\n"
        "ds_read_b64_tr_b16 v[86:87], v2 offset:15360\n"
        "v_mfma_f32_16x16x32_bf16 a[76:79], v[88:91], v[48:51], a[76:79]\n"
        "ds_read_b128 v[24:27], v6 offset:0\n"
        "ds_read_b128 v[28:31], v6 offset:1024\n"
        "v_mfma_f32_16x16x32_bf16 a[76:79], v[92:95], v[52:55], a[76:79]\n"
        "ds_read_b128 v[32:35], v6 offset:2048\n"
        "ds_read_b128 v[36:39], v6 offset:3072\n"
        "s_waitcnt lgkmcnt(11)\n"
        "v_mfma_f32_16x16x32_bf16 a[0:3], v[72:75], v[8:11], a[0:3]\n"
        "ds_read_b64_tr_b16 v[88:89], v1 offset:64\n"
        "s_waitcnt lgkmcnt(11)\n"
        "v_mfma_f32_16x16x32_bf16 a[0:3], v[76:79], v[12:15], a[0:3]\n"
        "ds_read_b64_tr_b16 v[90:91], v1 offset:5184\n"
        "s_waitcnt lgkmcnt(11)\n"
        "v_mfma_f32_16x16x32_bf16 a[4:7], v[72:75], v[16:19], a[4:7]\n"
        "ds_read_b64_tr_b16 v[92:93], v1 offset:10304\n"
        "s_waitcnt lgkmcnt(11)\n"
        "v_mfma_f32_16x16x32_bf16 a[4:7], v[76:79], v[20:23], a[4:7]\n"
        "ds_read_b64_tr_b16 v[94:95], v1 offset:15424\n"
        "s_waitcnt lgkmcnt(7)\n"
        "v_mfma_f32_16x16x32_bf16 a[8:11], v[72:75], v[24:27], a[8:11]\n"
        "s_waitcnt lgkmcnt(6)\n"
        "v_mfma_f32_16x16x32_bf16 a[8:11], v[76:79], v[28:31], a[8:11]\n"
        "s_waitcnt lgkmcnt(5)\n"
        "v_mfma_f32_16x16x32_bf16 a[12:15], v[72:75], v[32:35], a[12:15]\n"
        "s_waitcnt lgkmcnt(4)\n"
        "v_mfma_f32_16x16x32_bf16 a[12:15], v[76:79], v[36:39], a[12:15]\n"
        "v_mfma_f32_16x16x32_bf16 a[16:19], v[80:83], v[8:11], a[16:19]\n"
        "ds_read_b64_tr_b16 v[72:73], v2 offset:64\n"
        "v_mfma_f32_16x16x32_bf16 a[16:19], v[84:87], v[12:15], a[16:19]\n"
        "ds_read_b64_tr_b16 v[74:75], v2 offset:5184\n"
        "v_mfma_f32_16x16x32_bf16 a[20:23], v[80:83], v[16:19], a[20:23]\n"
        "ds_read_b64_tr_b16 v[76:77], v2 offset:10304\n"
        "v_mfma_f32_16x16x32_bf16 a[20:23], v[84:87], v[20:23], a[20:23]\n"
        "ds_read_b64_tr_b16 v[78:79], v2 offset:15424\n"
        "v_mfma_f32_16x16x32_bf16 a[24:27], v[80:83], v[24:27], a[24:27]\n"
        "v_mfma_f32_16x16x32_bf16 a[24:27], v[84:87], v[28:31], a[24:27]\n"
        "v_mfma_f32_16x16x32_bf16 a[28:31], v[80:83], v[32:35], a[28:31]\n"
        "v_mfma_f32_16x16x32_bf16 a[28:31], v[84:87], v[36:39], a[28:31]\n"
        "s_waitcnt lgkmcnt(4)\n"
        "v_mfma_f32_16x16x32_bf16 a[32:35], v[88:91], v[8:11], a[32:35]\n"
        "ds_read_b64_tr_b16 v[80:81], v3 offset:0\n"
        "v_mfma_f32_16x16x32_bf16 a[32:35], v[92:95], v[12:15], a[32:35]\n"
        "ds_read_b64_tr_b16 v[82:83], v3 offset:5120\n"
        "v_mfma_f32_16x16x32_bf16 a[36:39], v[88:91], v[16:19], a[36:39]\n"
        "ds_read_b64_tr_b16 v[84:85], v3 offset:10240\n"
        "v_mfma_f32_16x16x32_bf16 a[36:39], v[92:95], v[20:23], a[36:39]\n"
        "ds_read_b64_tr_b16 v[86:87], v3 offset:15360\n"
        "v_mfma_f32_16x16x32_bf16 a[40:43], v[88:91], v[24:27], a[40:43]\n"
        "v_mfma_f32_16x16x32_bf16 a[40:43], v[92:95], v[28:31], a[40:43]\n"
        "v_mfma_f32_16x16x32_bf16 a[44:47], v[88:91], v[32:35], a[44:47]\n"
        "v_mfma_f32_16x16x32_bf16 a[44:47], v[92:95], v[36:39], a[44:47]\n"
        "s_waitcnt lgkmcnt(6)\n"
        "v_mfma_f32_16x16x32_bf16 a[48:51], v[72:75], v[8:11], a[48:51]\n"
        "ds_read_b64_tr_b16 v[88:89], v4 offset:0\n"
        "s_waitcnt lgkmcnt(5)\n"
        "v_mfma_f32_16x16x32_bf16 a[48:51], v[76:79], v[12:15], a[48:51]\n"
        "ds_read_b64_tr_b16 v[90:91], v4 offset:5120\n"
        "v_mfma_f32_16x16x32_bf16 a[52:55], v[72:75], v[16:19], a[52:55]\n"
        "ds_read_b64_tr_b16 v[92:93], v4 offset:10240\n"
        "v_mfma_f32_16x16x32_bf16 a[52:55], v[76:79], v[20:23], a[52:55]\n"
        "ds_read_b64_tr_b16 v[94:95], v4 offset:15360\n"
        "v_mfma_f32_16x16x32_bf16 a[56:59], v[72:75], v[24:27], a[56:59]\n"
        "v_mfma_f32_16x16x32_bf16 a[56:59], v[76:79], v[28:31], a[56:59]\n"
        "v_mfma_f32_16x16x32_bf16 a[60:63], v[72:75], v[32:35], a[60:63]\n"
        "v_mfma_f32_16x16x32_bf16 a[60:63], v[76:79], v[36:39], a[60:63]\n"
        "s_waitcnt lgkmcnt(0)\n"
        "s_barrier\n"
        "v_add_u32 v1, 20480, v1\n"
        "v_add_u32 v2, 20480, v2\n"
        "v_add_u32 v3, 20480, v3\n"
        "v_add_u32 v4, 20480, v4\n"
        "v_add_u32 v5, 16384, v5\n"
        "v_add_u32 v6, 16384, v6\n"
        "v_mfma_f32_16x16x32_bf16 a[64:67], v[80:83], v[8:11], a[64:67]\n"
        "ds_read_b64_tr_b16 v[72:73], v1 offset:0\n"
        "ds_read_b64_tr_b16 v[74:75], v1 offset:5120\n"
        "v_mfma_f32_16x16x32_bf16 a[64:67], v[84:87], v[12:15], a[64:67]\n"
        "ds_read_b64_tr_b16 v[76:77], v1 offset:10240\n"
        "ds_read_b64_tr_b16 v[78:79], v1 offset:15360\n"
        "v_mfma_f32_16x16x32_bf16 a[68:71], v[80:83], v[16:19], a[68:71]\n"
        "ds_read_b128 v[40:43], v5 offset:0\n"
        "ds_read_b128 v[44:47], v5 offset:1024\n"
        "v_mfma_f32_16x16x32_bf16 a[68:71], v[84:87], v[20:23], a[68:71]\n"
        "ds_read_b128 v[48:51], v5 offset:2048\n"
        "ds_read_b128 v[52:55], v5 offset:3072\n"
        "v_mfma_f32_16x16x32_bf16 a[72:75], v[88:91], v[8:11], a[72:75]\n"
        "ds_read_b64_tr_b16 v[80:81], v2 offset:0\n"
        "ds_read_b64_tr_b16 v[82:83], v2 offset:5120\n"
        "v_mfma_f32_16x16x32_bf16 a[72:75], v[92:95], v[12:15], a[72:75]\n"
        "ds_read_b64_tr_b16 v[84:85], v2 offset:10240\n"
        "ds_read_b64_tr_b16 v[86:87], v2 offset:15360\n"
        "v_mfma_f32_16x16x32_bf16 a[76:79], v[88:91], v[16:19], a[76:79]\n"
        "ds_read_b128 v[56:59], v6 offset:0\n"
        "ds_read_b128 v[60:63], v6 offset:1024\n"
        "v_mfma_f32_16x16x32_bf16 a[76:79], v[92:95], v[20:23], a[76:79]\n"
        "ds_read_b128 v[64:67], v6 offset:2048\n"
        "ds_read_b128 v[68:71], v6 offset:3072\n"
        "s_waitcnt lgkmcnt(11)\n"
        "v_mfma_f32_16x16x32_bf16 a[0:3], v[72:75], v[40:43], a[0:3]\n"
        "ds_read_b64_tr_b16 v[88:89], v1 offset:64\n"
        "s_waitcnt lgkmcnt(11)\n"
        "v_mfma_f32_16x16x32_bf16 a[0:3], v[76:79], v[44:47], a[0:3]\n"
        "ds_read_b64_tr_b16 v[90:91], v1 offset:5184\n"
        "s_waitcnt lgkmcnt(11)\n"
        "v_mfma_f32_16x16x32_bf16 a[4:7], v[72:75], v[48:51], a[4:7]\n"
        "ds_read_b64_tr_b16 v[92:93], v1 offset:10304\n"
        "s_waitcnt lgkmcnt(11)\n"
        "v_mfma_f32_16x16x32_bf16 a[4:7], v[76:79], v[52:55], a[4:7]\n"
        "ds_read_b64_tr_b16 v[94:95], v1 offset:15424\n"
        "s_waitcnt lgkmcnt(7)\n"
        "v_mfma_f32_16x16x32_bf16 a[8:11], v[72:75], v[56:59], a[8:11]\n"
        "s_waitcnt lgkmcnt(6)\n"
        "v_mfma_f32_16x16x32_bf16 a[8:11], v[76:79], v[60:63], a[8:11]\n"
        "s_waitcnt lgkmcnt(5)\n"
        "v_mfma_f32_16x16x32_bf16 a[12:15], v[72:75], v[64:67], a[12:15]\n"
        "s_waitcnt lgkmcnt(4)\n"
        "v_mfma_f32_16x16x32_bf16 a[12:15], v[76:79], v[68:71], a[12:15]\n"
        "v_mfma_f32_16x16x32_bf16 a[16:19], v[80:83], v[40:43], a[16:19]\n"
        "ds_read_b64_tr_b16 v[72:73], v2 offset:64\n"
        "v_mfma_f32_16x16x32_bf16 a[16:19], v[84:87], v[44:47], a[16:19]\n"
        "ds_read_b64_tr_b16 v[74:75], v2 offset:5184\n"
        "v_mfma_f32_16x16x32_bf16 a[20:23], v[80:83], v[48:51], a[20:23]\n"
        "ds_read_b64_tr_b16 v[76:77], v2 offset:10304\n"
        "v_mfma_f32_16x16x32_bf16 a[20:23], v[84:87], v[52:55], a[20:23]\n"
        "ds_read_b64_tr_b16 v[78:79], v2 offset:15424\n"
        "v_mfma_f32_16x16x32_bf16 a[24:27], v[80:83], v[56:59], a[24:27]\n"
        "v_mfma_f32_16x16x32_bf16 a[24:27], v[84:87], v[60:63], a[24:27]\n"
        "v_mfma_f32_16x16x32_bf16 a[28:31], v[80:83], v[64:67], a[28:31]\n"
        "v_mfma_f32_16x16x32_bf16 a[28:31], v[84:87], v[68:71], a[28:31]\n"
        "s_waitcnt lgkmcnt(4)\n"
        "v_mfma_f32_16x16x32_bf16 a[32:35], v[88:91], v[40:43], a[32:35]\n"
        "ds_read_b64_tr_b16 v[80:81], v3 offset:0\n"
        "v_mfma_f32_16x16x32_bf16 a[32:35], v[92:95], v[44:47], a[32:35]\n"
        "ds_read_b64_tr_b16 v[82:83], v3 offset:5120\n"
        "v_mfma_f32_16x16x32_bf16 a[36:39], v[88:91], v[48:51], a[36:39]\n"
        "ds_read_b64_tr_b16 v[84:85], v3 offset:10240\n"
        "v_mfma_f32_16x16x32_bf16 a[36:39], v[92:95], v[52:55], a[36:39]\n"
        "ds_read_b64_tr_b16 v[86:87], v3 offset:15360\n"
        "v_mfma_f32_16x16x32_bf16 a[40:43], v[88:91], v[56:59], a[40:43]\n"
        "v_mfma_f32_16x16x32_bf16 a[40:43], v[92:95], v[60:63], a[40:43]\n"
        "v_mfma_f32_16x16x32_bf16 a[44:47], v[88:91], v[64:67], a[44:47]\n"
        "v_mfma_f32_16x16x32_bf16 a[44:47], v[92:95], v[68:71], a[44:47]\n"
        "s_waitcnt lgkmcnt(6)\n"
        "v_mfma_f32_16x16x32_bf16 a[48:51], v[72:75], v[40:43], a[48:51]\n"
        "ds_read_b64_tr_b16 v[88:89], v4 offset:0\n"
        "s_waitcnt lgkmcnt(5)\n"
        "v_mfma_f32_16x16x32_bf16 a[48:51], v[76:79], v[44:47], a[48:51]\n"
        "ds_read_b64_tr_b16 v[90:91], v4 offset:5120\n"
        "v_mfma_f32_16x16x32_bf16 a[52:55], v[72:75], v[48:51], a[52:55]\n"
        "ds_read_b64_tr_b16 v[92:93], v4 offset:10240\n"
        "v_mfma_f32_16x16x32_bf16 a[52:55], v[76:79], v[52:55], a[52:55]\n"
        "ds_read_b64_tr_b16 v[94:95], v4 offset:15360\n"
        "v_mfma_f32_16x16x32_bf16 a[56:59], v[72:75], v[56:59], a[56:59]\n"
        "v_mfma_f32_16x16x32_bf16 a[56:59], v[76:79], v[60:63], a[56:59]\n"
        "v_mfma_f32_16x16x32_bf16 a[60:63], v[72:75], v[64:67], a[60:63]\n"
        "v_mfma_f32_16x16x32_bf16 a[60:63], v[76:79], v[68:71], a[60:63]\n"
        "s_waitcnt lgkmcnt(0)\n"
        "s_barrier\n"
        "v_subrev_u32 v1, 40960, v1\n"
        "v_subrev_u32 v2, 40960, v2\n"
        "v_subrev_u32 v3, 40960, v3\n"
        "v_subrev_u32 v4, 40960, v4\n"
        "v_add_u32 v5, 16384, v5\n"
        "v_add_u32 v6, 16384, v6\n"
        "v_mfma_f32_16x16x32_bf16 a[64:67], v[80:83], v[40:43], a[64:67]\n"
        "ds_read_b64_tr_b16 v[72:73], v1 offset:0\n"
        "ds_read_b64_tr_b16 v[74:75], v1 offset:5120\n"
        "v_mfma_f32_16x16x32_bf16 a[64:67], v[84:87], v[44:47], a[64:67]\n"
        "ds_read_b64_tr_b16 v[76:77], v1 offset:10240\n"
        "ds_read_b64_tr_b16 v[78:79], v1 offset:15360\n"
        "v_mfma_f32_16x16x32_bf16 a[68:71], v[80:83], v[48:51], a[68:71]\n"
        "ds_read_b128 v[8:11], v5 offset:0\n"
        "ds_read_b128 v[12:15], v5 offset:1024\n"
        "v_mfma_f32_16x16x32_bf16 a[68:71], v[84:87], v[52:55], a[68:71]\n"
        "ds_read_b128 v[16:19], v5 offset:2048\n"
        "ds_read_b128 v[20:23], v5 offset:3072\n"
        "v_mfma_f32_16x16x32_bf16 a[72:75], v[88:91], v[40:43], a[72:75]\n"
        "ds_read_b64_tr_b16 v[80:81], v2 offset:0\n"
        "ds_read_b64_tr_b16 v[82:83], v2 offset:5120\n"
        "v_mfma_f32_16x16x32_bf16 a[72:75], v[92:95], v[44:47], a[72:75]\n"
        "ds_read_b64_tr_b16 v[84:85], v2 offset:10240\n"
        "ds_read_b64_tr_b16 v[86:87], v2 offset:15360\n"
        "v_mfma_f32_16x16x32_bf16 a[76:79], v[88:91], v[48:51], a[76:79]\n"
        "ds_read_b128 v[24:27], v6 offset:0\n"
        "ds_read_b128 v[28:31], v6 offset:1024\n"
        "v_mfma_f32_16x16x32_bf16 a[76:79], v[92:95], v[52:55], a[76:79]\n"
        "ds_read_b128 v[32:35], v6 offset:2048\n"
        "ds_read_b128 v[36:39], v6 offset:3072\n"
        "s_waitcnt lgkmcnt(11)\n"
        "v_mfma_f32_16x16x32_bf16 a[0:3], v[72:75], v[8:11], a[0:3]\n"
        "ds_read_b64_tr_b16 v[88:89], v1 offset:64\n"
        "s_waitcnt lgkmcnt(11)\n"
        "v_mfma_f32_16x16x32_bf16 a[0:3], v[76:79], v[12:15], a[0:3]\n"
        "ds_read_b64_tr_b16 v[90:91], v1 offset:5184\n"
        "s_waitcnt lgkmcnt(11)\n"
        "v_mfma_f32_16x16x32_bf16 a[4:7], v[72:75], v[16:19], a[4:7]\n"
        "ds_read_b64_tr_b16 v[92:93], v1 offset:10304\n"
        "s_waitcnt lgkmcnt(11)\n"
        "v_mfma_f32_16x16x32_bf16 a[4:7], v[76:79], v[20:23], a[4:7]\n"
        "ds_read_b64_tr_b16 v[94:95], v1 offset:15424\n"
        "s_waitcnt lgkmcnt(7)\n"
        "v_mfma_f32_16x16x32_bf16 a[8:11], v[72:75], v[24:27], a[8:11]\n"
        "s_waitcnt lgkmcnt(6)\n"
        "v_mfma_f32_16x16x32_bf16 a[8:11], v[76:79], v[28:31], a[8:11]\n"
        "s_waitcnt lgkmcnt(5)\n"
        "v_mfma_f32_16x16x32_bf16 a[12:15], v[72:75], v[32:35], a[12:15]\n"
        "s_waitcnt lgkmcnt(4)\n"
        "v_mfma_f32_16x16x32_bf16 a[12:15], v[76:79], v[36:39], a[12:15]\n"
        "v_mfma_f32_16x16x32_bf16 a[16:19], v[80:83], v[8:11], a[16:19]\n"
        "ds_read_b64_tr_b16 v[72:73], v2 offset:64\n"
        "v_mfma_f32_16x16x32_bf16 a[16:19], v[84:87], v[12:15], a[16:19]\n"
        "ds_read_b64_tr_b16 v[74:75], v2 offset:5184\n"
        "v_mfma_f32_16x16x32_bf16 a[20:23], v[80:83], v[16:19], a[20:23]\n"
        "ds_read_b64_tr_b16 v[76:77], v2 offset:10304\n"
        "v_mfma_f32_16x16x32_bf16 a[20:23], v[84:87], v[20:23], a[20:23]\n"
        "ds_read_b64_tr_b16 v[78:79], v2 offset:15424\n"
        "v_mfma_f32_16x16x32_bf16 a[24:27], v[80:83], v[24:27], a[24:27]\n"
        "v_mfma_f32_16x16x32_bf16 a[24:27], v[84:87], v[28:31], a[24:27]\n"
        "v_mfma_f32_16x16x32_bf16 a[28:31], v[80:83], v[32:35], a[28:31]\n"
        "v_mfma_f32_16x16x32_bf16 a[28:31], v[84:87], v[36:39], a[28:31]\n"
        "s_waitcnt lgkmcnt(4)\n"
        "v_mfma_f32_16x16x32_bf16 a[32:35], v[88:91], v[8:11], a[32:35]\n"
        "ds_read_b64_tr_b16 v[80:81], v3 offset:0\n"
        "v_mfma_f32_16x16x32_bf16 a[32:35], v[92:95], v[12:15], a[32:35]\n"
        "ds_read_b64_tr_b16 v[82:83], v3 offset:5120\n"
        "v_mfma_f32_16x16x32_bf16 a[36:39], v[88:91], v[16:19], a[36:39]\n"
        "ds_read_b64_tr_b16 v[84:85], v3 offset:10240\n"
        "v_mfma_f32_16x16x32_bf16 a[36:39], v[92:95], v[20:23], a[36:39]\n"
        "ds_read_b64_tr_b16 v[86:87], v3 offset:15360\n"
        "v_mfma_f32_16x16x32_bf16 a[40:43], v[88:91], v[24:27], a[40:43]\n"
        "v_mfma_f32_16x16x32_bf16 a[40:43], v[92:95], v[28:31], a[40:43]\n"
        "v_mfma_f32_16x16x32_bf16 a[44:47], v[88:91], v[32:35], a[44:47]\n"
        "v_mfma_f32_16x16x32_bf16 a[44:47], v[92:95], v[36:39], a[44:47]\n"
        "s_waitcnt lgkmcnt(6)\n"
        "v_mfma_f32_16x16x32_bf16 a[48:51], v[72:75], v[8:11], a[48:51]\n"
        "ds_read_b64_tr_b16 v[88:89], v4 offset:0\n"
        "s_waitcnt lgkmcnt(5)\n"
        "v_mfma_f32_16x16x32_bf16 a[48:51], v[76:79], v[12:15], a[48:51]\n"
        "ds_read_b64_tr_b16 v[90:91], v4 offset:5120\n"
        "v_mfma_f32_16x16x32_bf16 a[52:55], v[72:75], v[16:19], a[52:55]\n"
        "ds_read_b64_tr_b16 v[92:93], v4 offset:10240\n"
        "v_mfma_f32_16x16x32_bf16 a[52:55], v[76:79], v[20:23], a[52:55]\n"
        "ds_read_b64_tr_b16 v[94:95], v4 offset:15360\n"
        "v_mfma_f32_16x16x32_bf16 a[56:59], v[72:75], v[24:27], a[56:59]\n"
        "v_mfma_f32_16x16x32_bf16 a[56:59], v[76:79], v[28:31], a[56:59]\n"
        "v_mfma_f32_16x16x32_bf16 a[60:63], v[72:75], v[32:35], a[60:63]\n"
        "v_mfma_f32_16x16x32_bf16 a[60:63], v[76:79], v[36:39], a[60:63]\n"
        "s_waitcnt lgkmcnt(0)\n"
        "s_barrier\n"
        "v_add_u32 v1, 20480, v1\n"
        "v_add_u32 v2, 20480, v2\n"
        "v_add_u32 v3, 20480, v3\n"
        "v_add_u32 v4, 20480, v4\n"
        "v_add_u32 v5, 16384, v5\n"
        "v_add_u32 v6, 16384, v6\n"
        "v_mfma_f32_16x16x32_bf16 a[64:67], v[80:83], v[8:11], a[64:67]\n"
        "ds_read_b64_tr_b16 v[72:73], v1 offset:0\n"
        "ds_read_b64_tr_b16 v[74:75], v1 offset:5120\n"
        "v_mfma_f32_16x16x32_bf16 a[64:67], v[84:87], v[12:15], a[64:67]\n"
        "ds_read_b64_tr_b16 v[76:77], v1 offset:10240\n"
        "ds_read_b64_tr_b16 v[78:79], v1 offset:15360\n"
        "v_mfma_f32_16x16x32_bf16 a[68:71], v[80:83], v[16:19], a[68:71]\n"
        "ds_read_b128 v[40:43], v5 offset:0\n"
        "ds_read_b128 v[44:47], v5 offset:1024\n"
        "v_mfma_f32_16x16x32_bf16 a[68:71], v[84:87], v[20:23], a[68:71]\n"
        "ds_read_b128 v[48:51], v5 offset:2048\n"
        "ds_read_b128 v[52:55], v5 offset:3072\n"
        "v_mfma_f32_16x16x32_bf16 a[72:75], v[88:91], v[8:11], a[72:75]\n"
        "ds_read_b64_tr_b16 v[80:81], v2 offset:0\n"
        "ds_read_b64_tr_b16 v[82:83], v2 offset:5120\n"
        "v_mfma_f32_16x16x32_bf16 a[72:75], v[92:95], v[12:15], a[72:75]\n"
        "ds_read_b64_tr_b16 v[84:85], v2 offset:10240\n"
        "ds_read_b64_tr_b16 v[86:87], v2 offset:15360\n"
        "v_mfma_f32_16x16x32_bf16 a[76:79], v[88:91], v[16:19], a[76:79]\n"
        "ds_read_b128 v[56:59], v6 offset:0\n"
        "ds_read_b128 v[60:63], v6 offset:1024\n"
        "v_mfma_f32_16x16x32_bf16 a[76:79], v[92:95], v[20:23], a[76:79]\n"
        "ds_read_b128 v[64:67], v6 offset:2048\n"
        "ds_read_b128 v[68:71], v6 offset:3072\n"
        "s_waitcnt lgkmcnt(11)\n"
        "v_mfma_f32_16x16x32_bf16 a[0:3], v[72:75], v[40:43], a[0:3]\n"
        "ds_read_b64_tr_b16 v[88:89], v1 offset:64\n"
        "s_waitcnt lgkmcnt(11)\n"
        "v_mfma_f32_16x16x32_bf16 a[0:3], v[76:79], v[44:47], a[0:3]\n"
        "ds_read_b64_tr_b16 v[90:91], v1 offset:5184\n"
        "s_waitcnt lgkmcnt(11)\n"
        "v_mfma_f32_16x16x32_bf16 a[4:7], v[72:75], v[48:51], a[4:7]\n"
        "ds_read_b64_tr_b16 v[92:93], v1 offset:10304\n"
        "s_waitcnt lgkmcnt(11)\n"
        "v_mfma_f32_16x16x32_bf16 a[4:7], v[76:79], v[52:55], a[4:7]\n"
        "ds_read_b64_tr_b16 v[94:95], v1 offset:15424\n"
        "s_waitcnt lgkmcnt(7)\n"
        "v_mfma_f32_16x16x32_bf16 a[8:11], v[72:75], v[56:59], a[8:11]\n"
        "s_waitcnt lgkmcnt(6)\n"
        "v_mfma_f32_16x16x32_bf16 a[8:11], v[76:79], v[60:63], a[8:11]\n"
        "s_waitcnt lgkmcnt(5)\n"
        "v_mfma_f32_16x16x32_bf16 a[12:15], v[72:75], v[64:67], a[12:15]\n"
        "s_waitcnt lgkmcnt(4)\n"
        "v_mfma_f32_16x16x32_bf16 a[12:15], v[76:79], v[68:71], a[12:15]\n"
        "v_mfma_f32_16x16x32_bf16 a[16:19], v[80:83], v[40:43], a[16:19]\n"
        "ds_read_b64_tr_b16 v[72:73], v2 offset:64\n"
        "v_mfma_f32_16x16x32_bf16 a[16:19], v[84:87], v[44:47], a[16:19]\n"
        "ds_read_b64_tr_b16 v[74:75], v2 offset:5184\n"
        "v_mfma_f32_16x16x32_bf16 a[20:23], v[80:83], v[48:51], a[20:23]\n"
        "ds_read_b64_tr_b16 v[76:77], v2 offset:10304\n"
        "v_mfma_f32_16x16x32_bf16 a[20:23], v[84:87], v[52:55], a[20:23]\n"
        "ds_read_b64_tr_b16 v[78:79], v2 offset:15424\n"
        "v_mfma_f32_16x16x32_bf16 a[24:27], v[80:83], v[56:59], a[24:27]\n"
        "v_mfma_f32_16x16x32_bf16 a[24:27], v[84:87], v[60:63], a[24:27]\n"
        "v_mfma_f32_16x16x32_bf16 a[28:31], v[80:83], v[64:67], a[28:31]\n"
        "v_mfma_f32_16x16x32_bf16 a[28:31], v[84:87], v[68:71], a[28:31]\n"
        "s_waitcnt lgkmcnt(4)\n"
        "v_mfma_f32_16x16x32_bf16 a[32:35], v[88:91], v[40:43], a[32:35]\n"
        "ds_read_b64_tr_b16 v[80:81], v3 offset:0\n"
        "v_mfma_f32_16x16x32_bf16 a[32:35], v[92:95], v[44:47], a[32:35]\n"
        "ds_read_b64_tr_b16 v[82:83], v3 offset:5120\n"
        "v_mfma_f32_16x16x32_bf16 a[36:39], v[88:91], v[48:51], a[36:39]\n"
        "ds_read_b64_tr_b16 v[84:85], v3 offset:10240\n"
        "v_mfma_f32_16x16x32_bf16 a[36:39], v[92:95], v[52:55], a[36:39]\n"
        "ds_read_b64_tr_b16 v[86:87], v3 offset:15360\n"
        "v_mfma_f32_16x16x32_bf16 a[40:43], v[88:91], v[56:59], a[40:43]\n"
        "v_mfma_f32_16x16x32_bf16 a[40:43], v[92:95], v[60:63], a[40:43]\n"
        "v_mfma_f32_16x16x32_bf16 a[44:47], v[88:91], v[64:67], a[44:47]\n"
        "v_mfma_f32_16x16x32_bf16 a[44:47], v[92:95], v[68:71], a[44:47]\n"
        "s_waitcnt lgkmcnt(6)\n"
        "v_mfma_f32_16x16x32_bf16 a[48:51], v[72:75], v[40:43], a[48:51]\n"
        "ds_read_b64_tr_b16 v[88:89], v4 offset:0\n"
        "s_waitcnt lgkmcnt(5)\n"
        "v_mfma_f32_16x16x32_bf16 a[48:51], v[76:79], v[44:47], a[48:51]\n"
        "ds_read_b64_tr_b16 v[90:91], v4 offset:5120\n"
        "v_mfma_f32_16x16x32_bf16 a[52:55], v[72:75], v[48:51], a[52:55]\n"
        "ds_read_b64_tr_b16 v[92:93], v4 offset:10240\n"
        "v_mfma_f32_16x16x32_bf16 a[52:55], v[76:79], v[52:55], a[52:55]\n"
        "ds_read_b64_tr_b16 v[94:95], v4 offset:15360\n"
        "v_mfma_f32_16x16x32_bf16 a[56:59], v[72:75], v[56:59], a[56:59]\n"
        "v_mfma_f32_16x16x32_bf16 a[56:59], v[76:79], v[60:63], a[56:59]\n"
        "v_mfma_f32_16x16x32_bf16 a[60:63], v[72:75], v[64:67], a[60:63]\n"
        "v_mfma_f32_16x16x32_bf16 a[60:63], v[76:79], v[68:71], a[60:63]\n"
        "s_waitcnt lgkmcnt(0)\n"
        "s_barrier\n"
        "v_add_u32 v1, 20480, v1\n"
        "v_add_u32 v2, 20480, v2\n"
        "v_add_u32 v3, 20480, v3\n"
        "v_add_u32 v4, 20480, v4\n"
        "v_add_u32 v5, 16384, v5\n"
        "v_add_u32 v6, 16384, v6\n"
        "v_mfma_f32_16x16x32_bf16 a[64:67], v[80:83], v[40:43], a[64:67]\n"
        "ds_read_b64_tr_b16 v[72:73], v1 offset:0\n"
        "ds_read_b64_tr_b16 v[74:75], v1 offset:5120\n"
        "v_mfma_f32_16x16x32_bf16 a[64:67], v[84:87], v[44:47], a[64:67]\n"
        "ds_read_b64_tr_b16 v[76:77], v1 offset:10240\n"
        "ds_read_b64_tr_b16 v[78:79], v1 offset:15360\n"
        "v_mfma_f32_16x16x32_bf16 a[68:71], v[80:83], v[48:51], a[68:71]\n"
        "ds_read_b128 v[8:11], v5 offset:0\n"
        "ds_read_b128 v[12:15], v5 offset:1024\n"
        "v_mfma_f32_16x16x32_bf16 a[68:71], v[84:87], v[52:55], a[68:71]\n"
        "ds_read_b128 v[16:19], v5 offset:2048\n"
        "ds_read_b128 v[20:23], v5 offset:3072\n"
        "v_mfma_f32_16x16x32_bf16 a[72:75], v[88:91], v[40:43], a[72:75]\n"
        "ds_read_b64_tr_b16 v[80:81], v2 offset:0\n"
        "ds_read_b64_tr_b16 v[82:83], v2 offset:5120\n"
        "v_mfma_f32_16x16x32_bf16 a[72:75], v[92:95], v[44:47], a[72:75]\n"
        "ds_read_b64_tr_b16 v[84:85], v2 offset:10240\n"
        "ds_read_b64_tr_b16 v[86:87], v2 offset:15360\n"
        "v_mfma_f32_16x16x32_bf16 a[76:79], v[88:91], v[48:51], a[76:79]\n"
        "ds_read_b128 v[24:27], v6 offset:0\n"
        "ds_read_b128 v[28:31], v6 offset:1024\n"
        "v_mfma_f32_16x16x32_bf16 a[76:79], v[92:95], v[52:55], a[76:79]\n"
        "ds_read_b128 v[32:35], v6 offset:2048\n"
        "ds_read_b128 v[36:39], v6 offset:3072\n"
        "s_nop 7\n"
        "s_sub_u32 s52, 15, s38\n"
        "s_lshl_b32 s52, s52, 3\n"
        "s_and_b32 s53, s36, 1\n"
        "s_lshl_b32 s53, s53, 2\n"
        "s_add_u32 s52, s52, s53\n"
        "s_and_b32 s53, s36, 2\n"
        "s_add_u32 s54, s52, s53\n"
        "s_mul_i32 s76, s54, 20480\n"
        "s_xor_b32 s53, s53, 2\n"
        "s_add_u32 s54, s52, s53\n"
        "s_mul_i32 s77, s54, 20480\n"
        "v_accvgpr_read_b32 v96, a0\n"
        "v_accvgpr_read_b32 v97, a1\n"
        "v_accvgpr_read_b32 v98, a2\n"
        "v_accvgpr_read_b32 v99, a3\n"
        "v_accvgpr_read_b32 v100, a16\n"
        "v_accvgpr_read_b32 v101, a17\n"
        "v_accvgpr_read_b32 v102, a18\n"
        "v_accvgpr_read_b32 v103, a19\n"
        "v_cvt_pk_bf16_f32 v104, v96, v97\n"
        "v_cvt_pk_bf16_f32 v105, v98, v99\n"
        "v_cvt_pk_bf16_f32 v106, v100, v101\n"
        "v_cvt_pk_bf16_f32 v107, v102, v103\n"
        "v_accvgpr_write_b32 a80, v104\n"
        "v_accvgpr_write_b32 a81, v105\n"
        "v_accvgpr_write_b32 a82, v106\n"
        "v_accvgpr_write_b32 a83, v107\n"
        "v_accvgpr_read_b32 v108, a32\n"
        "v_accvgpr_read_b32 v109, a33\n"
        "v_accvgpr_read_b32 v110, a34\n"
        "v_accvgpr_read_b32 v111, a35\n"
        "v_accvgpr_read_b32 v112, a48\n"
        "v_accvgpr_read_b32 v113, a49\n"
        "v_accvgpr_read_b32 v114, a50\n"
        "v_accvgpr_read_b32 v115, a51\n"
        "v_cvt_pk_bf16_f32 v116, v108, v109\n"
        "v_cvt_pk_bf16_f32 v117, v110, v111\n"
        "v_cvt_pk_bf16_f32 v118, v112, v113\n"
        "v_cvt_pk_bf16_f32 v119, v114, v115\n"
        "v_accvgpr_write_b32 a84, v116\n"
        "v_accvgpr_write_b32 a85, v117\n"
        "v_accvgpr_write_b32 a86, v118\n"
        "v_accvgpr_write_b32 a87, v119\n"
        "v_accvgpr_read_b32 v96, a64\n"
        "v_accvgpr_read_b32 v97, a65\n"
        "v_accvgpr_read_b32 v98, a66\n"
        "v_accvgpr_read_b32 v99, a67\n"
        "v_accvgpr_read_b32 v100, a72\n"
        "v_accvgpr_read_b32 v101, a73\n"
        "v_accvgpr_read_b32 v102, a74\n"
        "v_accvgpr_read_b32 v103, a75\n"
        "v_cvt_pk_bf16_f32 v104, v96, v97\n"
        "v_cvt_pk_bf16_f32 v105, v98, v99\n"
        "v_cvt_pk_bf16_f32 v106, v100, v101\n"
        "v_cvt_pk_bf16_f32 v107, v102, v103\n"
        "v_accvgpr_write_b32 a88, v104\n"
        "v_accvgpr_write_b32 a89, v105\n"
        "v_accvgpr_write_b32 a90, v106\n"
        "v_accvgpr_write_b32 a91, v107\n"
        "v_accvgpr_read_b32 v108, a4\n"
        "v_accvgpr_read_b32 v109, a5\n"
        "v_accvgpr_read_b32 v110, a6\n"
        "v_accvgpr_read_b32 v111, a7\n"
        "v_accvgpr_read_b32 v112, a20\n"
        "v_accvgpr_read_b32 v113, a21\n"
        "v_accvgpr_read_b32 v114, a22\n"
        "v_accvgpr_read_b32 v115, a23\n"
        "v_cvt_pk_bf16_f32 v116, v108, v109\n"
        "v_cvt_pk_bf16_f32 v117, v110, v111\n"
        "v_cvt_pk_bf16_f32 v118, v112, v113\n"
        "v_cvt_pk_bf16_f32 v119, v114, v115\n"
        "v_accvgpr_write_b32 a92, v116\n"
        "v_accvgpr_write_b32 a93, v117\n"
        "v_accvgpr_write_b32 a94, v118\n"
        "v_accvgpr_write_b32 a95, v119\n"
        "v_accvgpr_read_b32 v96, a36\n"
        "v_accvgpr_read_b32 v97, a37\n"
        "v_accvgpr_read_b32 v98, a38\n"
        "v_accvgpr_read_b32 v99, a39\n"
        "v_accvgpr_read_b32 v100, a52\n"
        "v_accvgpr_read_b32 v101, a53\n"
        "v_accvgpr_read_b32 v102, a54\n"
        "v_accvgpr_read_b32 v103, a55\n"
        "v_cvt_pk_bf16_f32 v104, v96, v97\n"
        "v_cvt_pk_bf16_f32 v105, v98, v99\n"
        "v_cvt_pk_bf16_f32 v106, v100, v101\n"
        "v_cvt_pk_bf16_f32 v107, v102, v103\n"
        "v_accvgpr_write_b32 a96, v104\n"
        "v_accvgpr_write_b32 a97, v105\n"
        "v_accvgpr_write_b32 a98, v106\n"
        "v_accvgpr_write_b32 a99, v107\n"
        "v_accvgpr_read_b32 v108, a68\n"
        "v_accvgpr_read_b32 v109, a69\n"
        "v_accvgpr_read_b32 v110, a70\n"
        "v_accvgpr_read_b32 v111, a71\n"
        "v_accvgpr_read_b32 v112, a76\n"
        "v_accvgpr_read_b32 v113, a77\n"
        "v_accvgpr_read_b32 v114, a78\n"
        "v_accvgpr_read_b32 v115, a79\n"
        "v_cvt_pk_bf16_f32 v116, v108, v109\n"
        "v_cvt_pk_bf16_f32 v117, v110, v111\n"
        "v_cvt_pk_bf16_f32 v118, v112, v113\n"
        "v_cvt_pk_bf16_f32 v119, v114, v115\n"
        "v_accvgpr_write_b32 a100, v116\n"
        "v_accvgpr_write_b32 a101, v117\n"
        "v_accvgpr_write_b32 a102, v118\n"
        "v_accvgpr_write_b32 a103, v119\n"
        "v_accvgpr_read_b32 v96, a8\n"
        "v_accvgpr_read_b32 v97, a9\n"
        "v_accvgpr_read_b32 v98, a10\n"
        "v_accvgpr_read_b32 v99, a11\n"
        "v_accvgpr_read_b32 v100, a24\n"
        "v_accvgpr_read_b32 v101, a25\n"
        "v_accvgpr_read_b32 v102, a26\n"
        "v_accvgpr_read_b32 v103, a27\n"
        "v_cvt_pk_bf16_f32 v104, v96, v97\n"
        "v_cvt_pk_bf16_f32 v105, v98, v99\n"
        "v_cvt_pk_bf16_f32 v106, v100, v101\n"
        "v_cvt_pk_bf16_f32 v107, v102, v103\n"
        "v_accvgpr_write_b32 a104, v104\n"
        "v_accvgpr_write_b32 a105, v105\n"
        "v_accvgpr_write_b32 a106, v106\n"
        "v_accvgpr_write_b32 a107, v107\n"
        "v_accvgpr_read_b32 v108, a40\n"
        "v_accvgpr_read_b32 v109, a41\n"
        "v_accvgpr_read_b32 v110, a42\n"
        "v_accvgpr_read_b32 v111, a43\n"
        "v_accvgpr_read_b32 v112, a56\n"
        "v_accvgpr_read_b32 v113, a57\n"
        "v_accvgpr_read_b32 v114, a58\n"
        "v_accvgpr_read_b32 v115, a59\n"
        "v_cvt_pk_bf16_f32 v116, v108, v109\n"
        "v_cvt_pk_bf16_f32 v117, v110, v111\n"
        "v_cvt_pk_bf16_f32 v118, v112, v113\n"
        "v_cvt_pk_bf16_f32 v119, v114, v115\n"
        "v_accvgpr_write_b32 a108, v116\n"
        "v_accvgpr_write_b32 a109, v117\n"
        "v_accvgpr_write_b32 a110, v118\n"
        "v_accvgpr_write_b32 a111, v119\n"
        "v_accvgpr_read_b32 v96, a12\n"
        "v_accvgpr_read_b32 v97, a13\n"
        "v_accvgpr_read_b32 v98, a14\n"
        "v_accvgpr_read_b32 v99, a15\n"
        "v_accvgpr_read_b32 v100, a28\n"
        "v_accvgpr_read_b32 v101, a29\n"
        "v_accvgpr_read_b32 v102, a30\n"
        "v_accvgpr_read_b32 v103, a31\n"
        "v_cvt_pk_bf16_f32 v104, v96, v97\n"
        "v_cvt_pk_bf16_f32 v105, v98, v99\n"
        "v_cvt_pk_bf16_f32 v106, v100, v101\n"
        "v_cvt_pk_bf16_f32 v107, v102, v103\n"
        "v_accvgpr_write_b32 a112, v104\n"
        "v_accvgpr_write_b32 a113, v105\n"
        "v_accvgpr_write_b32 a114, v106\n"
        "v_accvgpr_write_b32 a115, v107\n"
        "v_accvgpr_read_b32 v108, a44\n"
        "v_accvgpr_read_b32 v109, a45\n"
        "v_accvgpr_read_b32 v110, a46\n"
        "v_accvgpr_read_b32 v111, a47\n"
        "v_accvgpr_read_b32 v112, a60\n"
        "v_accvgpr_read_b32 v113, a61\n"
        "v_accvgpr_read_b32 v114, a62\n"
        "v_accvgpr_read_b32 v115, a63\n"
        "v_cvt_pk_bf16_f32 v116, v108, v109\n"
        "v_cvt_pk_bf16_f32 v117, v110, v111\n"
        "v_cvt_pk_bf16_f32 v118, v112, v113\n"
        "v_cvt_pk_bf16_f32 v119, v114, v115\n"
        "v_accvgpr_write_b32 a116, v116\n"
        "v_accvgpr_write_b32 a117, v117\n"
        "v_accvgpr_write_b32 a118, v118\n"
        "v_accvgpr_write_b32 a119, v119\n"
        "s_waitcnt lgkmcnt(0)\n"
        "v_mfma_f32_16x16x32_bf16 a[0:3], v[72:75], v[8:11], 0\n"
        "ds_read_b64_tr_b16 v[88:89], v1 offset:64\n"
        "v_mfma_f32_16x16x32_bf16 a[0:3], v[76:79], v[12:15], a[0:3]\n"
        "ds_read_b64_tr_b16 v[90:91], v1 offset:5184\n"
        "v_mfma_f32_16x16x32_bf16 a[4:7], v[72:75], v[16:19], 0\n"
        "ds_read_b64_tr_b16 v[92:93], v1 offset:10304\n"
        "v_mfma_f32_16x16x32_bf16 a[4:7], v[76:79], v[20:23], a[4:7]\n"
        "ds_read_b64_tr_b16 v[94:95], v1 offset:15424\n"
        "v_mfma_f32_16x16x32_bf16 a[8:11], v[72:75], v[24:27], 0\n"
        "v_mfma_f32_16x16x32_bf16 a[8:11], v[76:79], v[28:31], a[8:11]\n"
        "v_mfma_f32_16x16x32_bf16 a[12:15], v[72:75], v[32:35], 0\n"
        "v_mfma_f32_16x16x32_bf16 a[12:15], v[76:79], v[36:39], a[12:15]\n"
        "s_add_u32 s53, s76, 0\n"
        "s_add_u32 s53, s53, s78\n"
        "buffer_store_dwordx4 a[80:83], v7, s[72:75], s53 offen sc1\n"
        "v_mfma_f32_16x16x32_bf16 a[16:19], v[80:83], v[8:11], 0\n"
        "ds_read_b64_tr_b16 v[72:73], v2 offset:64\n"
        "v_mfma_f32_16x16x32_bf16 a[16:19], v[84:87], v[12:15], a[16:19]\n"
        "ds_read_b64_tr_b16 v[74:75], v2 offset:5184\n"
        "v_mfma_f32_16x16x32_bf16 a[20:23], v[80:83], v[16:19], 0\n"
        "ds_read_b64_tr_b16 v[76:77], v2 offset:10304\n"
        "v_mfma_f32_16x16x32_bf16 a[20:23], v[84:87], v[20:23], a[20:23]\n"
        "ds_read_b64_tr_b16 v[78:79], v2 offset:15424\n"
        "v_mfma_f32_16x16x32_bf16 a[24:27], v[80:83], v[24:27], 0\n"
        "v_mfma_f32_16x16x32_bf16 a[24:27], v[84:87], v[28:31], a[24:27]\n"
        "v_mfma_f32_16x16x32_bf16 a[28:31], v[80:83], v[32:35], 0\n"
        "v_mfma_f32_16x16x32_bf16 a[28:31], v[84:87], v[36:39], a[28:31]\n"
        "s_add_u32 s53, s76, 64\n"
        "s_add_u32 s53, s53, s78\n"
        "buffer_store_dwordx4 a[84:87], v7, s[72:75], s53 offen sc1\n"
        "s_waitcnt lgkmcnt(4)\n"
        "v_mfma_f32_16x16x32_bf16 a[32:35], v[88:91], v[8:11], 0\n"
        "ds_read_b64_tr_b16 v[80:81], v3 offset:0\n"
        "v_mfma_f32_16x16x32_bf16 a[32:35], v[92:95], v[12:15], a[32:35]\n"
        "ds_read_b64_tr_b16 v[82:83], v3 offset:5120\n"
        "v_mfma_f32_16x16x32_bf16 a[36:39], v[88:91], v[16:19], 0\n"
        "ds_read_b64_tr_b16 v[84:85], v3 offset:10240\n"
        "v_mfma_f32_16x16x32_bf16 a[36:39], v[92:95], v[20:23], a[36:39]\n"
        "ds_read_b64_tr_b16 v[86:87], v3 offset:15360\n"
        "v_mfma_f32_16x16x32_bf16 a[40:43], v[88:91], v[24:27], 0\n"
        "v_mfma_f32_16x16x32_bf16 a[40:43], v[92:95], v[28:31], a[40:43]\n"
        "v_mfma_f32_16x16x32_bf16 a[44:47], v[88:91], v[32:35], 0\n"
        "v_mfma_f32_16x16x32_bf16 a[44:47], v[92:95], v[36:39], a[44:47]\n"
        "s_waitcnt lgkmcnt(4)\n"
        "v_mfma_f32_16x16x32_bf16 a[48:51], v[72:75], v[8:11], 0\n"
        "ds_read_b64_tr_b16 v[88:89], v4 offset:0\n"
        "v_mfma_f32_16x16x32_bf16 a[48:51], v[76:79], v[12:15], a[48:51]\n"
        "ds_read_b64_tr_b16 v[90:91], v4 offset:5120\n"
        "v_mfma_f32_16x16x32_bf16 a[52:55], v[72:75], v[16:19], 0\n"
        "ds_read_b64_tr_b16 v[92:93], v4 offset:10240\n"
        "v_mfma_f32_16x16x32_bf16 a[52:55], v[76:79], v[20:23], a[52:55]\n"
        "ds_read_b64_tr_b16 v[94:95], v4 offset:15360\n"
        "v_mfma_f32_16x16x32_bf16 a[56:59], v[72:75], v[24:27], 0\n"
        "v_mfma_f32_16x16x32_bf16 a[56:59], v[76:79], v[28:31], a[56:59]\n"
        "v_mfma_f32_16x16x32_bf16 a[60:63], v[72:75], v[32:35], 0\n"
        "v_mfma_f32_16x16x32_bf16 a[60:63], v[76:79], v[36:39], a[60:63]\n"
        "s_waitcnt lgkmcnt(0)\n"
        "s_barrier\n"
        "v_subrev_u32 v1, 40960, v1\n"
        "v_subrev_u32 v2, 40960, v2\n"
        "v_subrev_u32 v3, 40960, v3\n"
        "v_subrev_u32 v4, 40960, v4\n"
        "v_add_u32 v5, 16384, v5\n"
        "v_add_u32 v6, 16384, v6\n"
        "v_mfma_f32_16x16x32_bf16 a[64:67], v[80:83], v[8:11], 0\n"
        "ds_read_b64_tr_b16 v[72:73], v1 offset:0\n"
        "ds_read_b64_tr_b16 v[74:75], v1 offset:5120\n"
        "v_mfma_f32_16x16x32_bf16 a[64:67], v[84:87], v[12:15], a[64:67]\n"
        "ds_read_b64_tr_b16 v[76:77], v1 offset:10240\n"
        "ds_read_b64_tr_b16 v[78:79], v1 offset:15360\n"
        "v_mfma_f32_16x16x32_bf16 a[68:71], v[80:83], v[16:19], 0\n"
        "ds_read_b128 v[40:43], v5 offset:0\n"
        "ds_read_b128 v[44:47], v5 offset:1024\n"
        "v_mfma_f32_16x16x32_bf16 a[68:71], v[84:87], v[20:23], a[68:71]\n"
        "ds_read_b128 v[48:51], v5 offset:2048\n"
        "ds_read_b128 v[52:55], v5 offset:3072\n"
        "v_mfma_f32_16x16x32_bf16 a[72:75], v[88:91], v[8:11], 0\n"
        "ds_read_b64_tr_b16 v[80:81], v2 offset:0\n"
        "ds_read_b64_tr_b16 v[82:83], v2 offset:5120\n"
        "v_mfma_f32_16x16x32_bf16 a[72:75], v[92:95], v[12:15], a[72:75]\n"
        "ds_read_b64_tr_b16 v[84:85], v2 offset:10240\n"
        "ds_read_b64_tr_b16 v[86:87], v2 offset:15360\n"
        "v_mfma_f32_16x16x32_bf16 a[76:79], v[88:91], v[16:19], 0\n"
        "ds_read_b128 v[56:59], v6 offset:0\n"
        "ds_read_b128 v[60:63], v6 offset:1024\n"
        "v_mfma_f32_16x16x32_bf16 a[76:79], v[92:95], v[20:23], a[76:79]\n"
        "ds_read_b128 v[64:67], v6 offset:2048\n"
        "ds_read_b128 v[68:71], v6 offset:3072\n"
        "s_waitcnt lgkmcnt(11)\n"
        "v_mfma_f32_16x16x32_bf16 a[0:3], v[72:75], v[40:43], a[0:3]\n"
        "ds_read_b64_tr_b16 v[88:89], v1 offset:64\n"
        "s_waitcnt lgkmcnt(11)\n"
        "v_mfma_f32_16x16x32_bf16 a[0:3], v[76:79], v[44:47], a[0:3]\n"
        "ds_read_b64_tr_b16 v[90:91], v1 offset:5184\n"
        "s_waitcnt lgkmcnt(11)\n"
        "v_mfma_f32_16x16x32_bf16 a[4:7], v[72:75], v[48:51], a[4:7]\n"
        "ds_read_b64_tr_b16 v[92:93], v1 offset:10304\n"
        "s_waitcnt lgkmcnt(11)\n"
        "v_mfma_f32_16x16x32_bf16 a[4:7], v[76:79], v[52:55], a[4:7]\n"
        "ds_read_b64_tr_b16 v[94:95], v1 offset:15424\n"
        "s_waitcnt lgkmcnt(7)\n"
        "v_mfma_f32_16x16x32_bf16 a[8:11], v[72:75], v[56:59], a[8:11]\n"
        "s_waitcnt lgkmcnt(6)\n"
        "v_mfma_f32_16x16x32_bf16 a[8:11], v[76:79], v[60:63], a[8:11]\n"
        "s_waitcnt lgkmcnt(5)\n"
        "v_mfma_f32_16x16x32_bf16 a[12:15], v[72:75], v[64:67], a[12:15]\n"
        "s_waitcnt lgkmcnt(4)\n"
        "v_mfma_f32_16x16x32_bf16 a[12:15], v[76:79], v[68:71], a[12:15]\n"
        "s_add_u32 s53, s76, 128\n"
        "buffer_store_dwordx4 a[88:91], v7, s[72:75], s53 offen sc1\n"
        "v_mfma_f32_16x16x32_bf16 a[16:19], v[80:83], v[40:43], a[16:19]\n"
        "ds_read_b64_tr_b16 v[72:73], v2 offset:64\n"
        "v_mfma_f32_16x16x32_bf16 a[16:19], v[84:87], v[44:47], a[16:19]\n"
        "ds_read_b64_tr_b16 v[74:75], v2 offset:5184\n"
        "v_mfma_f32_16x16x32_bf16 a[20:23], v[80:83], v[48:51], a[20:23]\n"
        "ds_read_b64_tr_b16 v[76:77], v2 offset:10304\n"
        "v_mfma_f32_16x16x32_bf16 a[20:23], v[84:87], v[52:55], a[20:23]\n"
        "ds_read_b64_tr_b16 v[78:79], v2 offset:15424\n"
        "v_mfma_f32_16x16x32_bf16 a[24:27], v[80:83], v[56:59], a[24:27]\n"
        "v_mfma_f32_16x16x32_bf16 a[24:27], v[84:87], v[60:63], a[24:27]\n"
        "v_mfma_f32_16x16x32_bf16 a[28:31], v[80:83], v[64:67], a[28:31]\n"
        "v_mfma_f32_16x16x32_bf16 a[28:31], v[84:87], v[68:71], a[28:31]\n"
        "s_add_u32 s53, s76, 20480\n"
        "s_add_u32 s53, s53, s78\n"
        "buffer_store_dwordx4 a[92:95], v7, s[72:75], s53 offen sc1\n"
        "s_waitcnt lgkmcnt(4)\n"
        "v_mfma_f32_16x16x32_bf16 a[32:35], v[88:91], v[40:43], a[32:35]\n"
        "ds_read_b64_tr_b16 v[80:81], v3 offset:0\n"
        "v_mfma_f32_16x16x32_bf16 a[32:35], v[92:95], v[44:47], a[32:35]\n"
        "ds_read_b64_tr_b16 v[82:83], v3 offset:5120\n"
        "v_mfma_f32_16x16x32_bf16 a[36:39], v[88:91], v[48:51], a[36:39]\n"
        "ds_read_b64_tr_b16 v[84:85], v3 offset:10240\n"
        "v_mfma_f32_16x16x32_bf16 a[36:39], v[92:95], v[52:55], a[36:39]\n"
        "ds_read_b64_tr_b16 v[86:87], v3 offset:15360\n"
        "v_mfma_f32_16x16x32_bf16 a[40:43], v[88:91], v[56:59], a[40:43]\n"
        "v_mfma_f32_16x16x32_bf16 a[40:43], v[92:95], v[60:63], a[40:43]\n"
        "v_mfma_f32_16x16x32_bf16 a[44:47], v[88:91], v[64:67], a[44:47]\n"
        "v_mfma_f32_16x16x32_bf16 a[44:47], v[92:95], v[68:71], a[44:47]\n"
        "s_waitcnt lgkmcnt(4)\n"
        "v_mfma_f32_16x16x32_bf16 a[48:51], v[72:75], v[40:43], a[48:51]\n"
        "ds_read_b64_tr_b16 v[88:89], v4 offset:0\n"
        "v_mfma_f32_16x16x32_bf16 a[48:51], v[76:79], v[44:47], a[48:51]\n"
        "ds_read_b64_tr_b16 v[90:91], v4 offset:5120\n"
        "v_mfma_f32_16x16x32_bf16 a[52:55], v[72:75], v[48:51], a[52:55]\n"
        "ds_read_b64_tr_b16 v[92:93], v4 offset:10240\n"
        "v_mfma_f32_16x16x32_bf16 a[52:55], v[76:79], v[52:55], a[52:55]\n"
        "ds_read_b64_tr_b16 v[94:95], v4 offset:15360\n"
        "v_mfma_f32_16x16x32_bf16 a[56:59], v[72:75], v[56:59], a[56:59]\n"
        "v_mfma_f32_16x16x32_bf16 a[56:59], v[76:79], v[60:63], a[56:59]\n"
        "v_mfma_f32_16x16x32_bf16 a[60:63], v[72:75], v[64:67], a[60:63]\n"
        "v_mfma_f32_16x16x32_bf16 a[60:63], v[76:79], v[68:71], a[60:63]\n"
        "s_waitcnt lgkmcnt(0)\n"
        "s_barrier\n"
        "v_add_u32 v1, 20480, v1\n"
        "v_add_u32 v2, 20480, v2\n"
        "v_add_u32 v3, 20480, v3\n"
        "v_add_u32 v4, 20480, v4\n"
        "v_subrev_u32 v5, 81920, v5\n"
        "v_subrev_u32 v6, 81920, v6\n"
        "v_mfma_f32_16x16x32_bf16 a[64:67], v[80:83], v[40:43], a[64:67]\n"
        "ds_read_b64_tr_b16 v[72:73], v1 offset:0\n"
        "ds_read_b64_tr_b16 v[74:75], v1 offset:5120\n"
        "v_mfma_f32_16x16x32_bf16 a[64:67], v[84:87], v[44:47], a[64:67]\n"
        "ds_read_b64_tr_b16 v[76:77], v1 offset:10240\n"
        "ds_read_b64_tr_b16 v[78:79], v1 offset:15360\n"
        "v_mfma_f32_16x16x32_bf16 a[68:71], v[80:83], v[48:51], a[68:71]\n"
        "ds_read_b128 v[8:11], v5 offset:0\n"
        "ds_read_b128 v[12:15], v5 offset:1024\n"
        "v_mfma_f32_16x16x32_bf16 a[68:71], v[84:87], v[52:55], a[68:71]\n"
        "ds_read_b128 v[16:19], v5 offset:2048\n"
        "ds_read_b128 v[20:23], v5 offset:3072\n"
        "v_mfma_f32_16x16x32_bf16 a[72:75], v[88:91], v[40:43], a[72:75]\n"
        "ds_read_b64_tr_b16 v[80:81], v2 offset:0\n"
        "ds_read_b64_tr_b16 v[82:83], v2 offset:5120\n"
        "v_mfma_f32_16x16x32_bf16 a[72:75], v[92:95], v[44:47], a[72:75]\n"
        "ds_read_b64_tr_b16 v[84:85], v2 offset:10240\n"
        "ds_read_b64_tr_b16 v[86:87], v2 offset:15360\n"
        "v_mfma_f32_16x16x32_bf16 a[76:79], v[88:91], v[48:51], a[76:79]\n"
        "ds_read_b128 v[24:27], v6 offset:0\n"
        "ds_read_b128 v[28:31], v6 offset:1024\n"
        "v_mfma_f32_16x16x32_bf16 a[76:79], v[92:95], v[52:55], a[76:79]\n"
        "ds_read_b128 v[32:35], v6 offset:2048\n"
        "ds_read_b128 v[36:39], v6 offset:3072\n"
        "s_waitcnt lgkmcnt(11)\n"
        "v_mfma_f32_16x16x32_bf16 a[0:3], v[72:75], v[8:11], a[0:3]\n"
        "ds_read_b64_tr_b16 v[88:89], v1 offset:64\n"
        "s_waitcnt lgkmcnt(11)\n"
        "v_mfma_f32_16x16x32_bf16 a[0:3], v[76:79], v[12:15], a[0:3]\n"
        "ds_read_b64_tr_b16 v[90:91], v1 offset:5184\n"
        "s_waitcnt lgkmcnt(11)\n"
        "v_mfma_f32_16x16x32_bf16 a[4:7], v[72:75], v[16:19], a[4:7]\n"
        "ds_read_b64_tr_b16 v[92:93], v1 offset:10304\n"
        "s_waitcnt lgkmcnt(11)\n"
        "v_mfma_f32_16x16x32_bf16 a[4:7], v[76:79], v[20:23], a[4:7]\n"
        "ds_read_b64_tr_b16 v[94:95], v1 offset:15424\n"
        "s_waitcnt lgkmcnt(7)\n"
        "v_mfma_f32_16x16x32_bf16 a[8:11], v[72:75], v[24:27], a[8:11]\n"
        "s_waitcnt lgkmcnt(6)\n"
        "v_mfma_f32_16x16x32_bf16 a[8:11], v[76:79], v[28:31], a[8:11]\n"
        "s_waitcnt lgkmcnt(5)\n"
        "v_mfma_f32_16x16x32_bf16 a[12:15], v[72:75], v[32:35], a[12:15]\n"
        "s_waitcnt lgkmcnt(4)\n"
        "v_mfma_f32_16x16x32_bf16 a[12:15], v[76:79], v[36:39], a[12:15]\n"
        "s_add_u32 s53, s76, 20544\n"
        "s_add_u32 s53, s53, s78\n"
        "buffer_store_dwordx4 a[96:99], v7, s[72:75], s53 offen sc1\n"
        "v_mfma_f32_16x16x32_bf16 a[16:19], v[80:83], v[8:11], a[16:19]\n"
        "ds_read_b64_tr_b16 v[72:73], v2 offset:64\n"
        "v_mfma_f32_16x16x32_bf16 a[16:19], v[84:87], v[12:15], a[16:19]\n"
        "ds_read_b64_tr_b16 v[74:75], v2 offset:5184\n"
        "v_mfma_f32_16x16x32_bf16 a[20:23], v[80:83], v[16:19], a[20:23]\n"
        "ds_read_b64_tr_b16 v[76:77], v2 offset:10304\n"
        "v_mfma_f32_16x16x32_bf16 a[20:23], v[84:87], v[20:23], a[20:23]\n"
        "ds_read_b64_tr_b16 v[78:79], v2 offset:15424\n"
        "v_mfma_f32_16x16x32_bf16 a[24:27], v[80:83], v[24:27], a[24:27]\n"
        "v_mfma_f32_16x16x32_bf16 a[24:27], v[84:87], v[28:31], a[24:27]\n"
        "v_mfma_f32_16x16x32_bf16 a[28:31], v[80:83], v[32:35], a[28:31]\n"
        "v_mfma_f32_16x16x32_bf16 a[28:31], v[84:87], v[36:39], a[28:31]\n"
        "s_add_u32 s53, s76, 20608\n"
        "buffer_store_dwordx4 a[100:103], v7, s[72:75], s53 offen sc1\n"
        "s_waitcnt lgkmcnt(4)\n"
        "v_mfma_f32_16x16x32_bf16 a[32:35], v[88:91], v[8:11], a[32:35]\n"
        "ds_read_b64_tr_b16 v[80:81], v3 offset:0\n"
        "v_mfma_f32_16x16x32_bf16 a[32:35], v[92:95], v[12:15], a[32:35]\n"
        "ds_read_b64_tr_b16 v[82:83], v3 offset:5120\n"
        "v_mfma_f32_16x16x32_bf16 a[36:39], v[88:91], v[16:19], a[36:39]\n"
        "ds_read_b64_tr_b16 v[84:85], v3 offset:10240\n"
        "v_mfma_f32_16x16x32_bf16 a[36:39], v[92:95], v[20:23], a[36:39]\n"
        "ds_read_b64_tr_b16 v[86:87], v3 offset:15360\n"
        "v_mfma_f32_16x16x32_bf16 a[40:43], v[88:91], v[24:27], a[40:43]\n"
        "v_mfma_f32_16x16x32_bf16 a[40:43], v[92:95], v[28:31], a[40:43]\n"
        "v_mfma_f32_16x16x32_bf16 a[44:47], v[88:91], v[32:35], a[44:47]\n"
        "v_mfma_f32_16x16x32_bf16 a[44:47], v[92:95], v[36:39], a[44:47]\n"
        "s_waitcnt lgkmcnt(5)\n"
        "v_mfma_f32_16x16x32_bf16 a[48:51], v[72:75], v[8:11], a[48:51]\n"
        "ds_read_b64_tr_b16 v[88:89], v4 offset:0\n"
        "s_waitcnt lgkmcnt(5)\n"
        "v_mfma_f32_16x16x32_bf16 a[48:51], v[76:79], v[12:15], a[48:51]\n"
        "ds_read_b64_tr_b16 v[90:91], v4 offset:5120\n"
        "v_mfma_f32_16x16x32_bf16 a[52:55], v[72:75], v[16:19], a[52:55]\n"
        "ds_read_b64_tr_b16 v[92:93], v4 offset:10240\n"
        "v_mfma_f32_16x16x32_bf16 a[52:55], v[76:79], v[20:23], a[52:55]\n"
        "ds_read_b64_tr_b16 v[94:95], v4 offset:15360\n"
        "v_mfma_f32_16x16x32_bf16 a[56:59], v[72:75], v[24:27], a[56:59]\n"
        "v_mfma_f32_16x16x32_bf16 a[56:59], v[76:79], v[28:31], a[56:59]\n"
        "v_mfma_f32_16x16x32_bf16 a[60:63], v[72:75], v[32:35], a[60:63]\n"
        "v_mfma_f32_16x16x32_bf16 a[60:63], v[76:79], v[36:39], a[60:63]\n"
        "s_waitcnt lgkmcnt(0)\n"
        "s_barrier\n"
        "v_add_u32 v1, 20480, v1\n"
        "v_add_u32 v2, 20480, v2\n"
        "v_add_u32 v3, 20480, v3\n"
        "v_add_u32 v4, 20480, v4\n"
        "v_add_u32 v5, 16384, v5\n"
        "v_add_u32 v6, 16384, v6\n"
        "v_mfma_f32_16x16x32_bf16 a[64:67], v[80:83], v[8:11], a[64:67]\n"
        "ds_read_b64_tr_b16 v[72:73], v1 offset:0\n"
        "ds_read_b64_tr_b16 v[74:75], v1 offset:5120\n"
        "v_mfma_f32_16x16x32_bf16 a[64:67], v[84:87], v[12:15], a[64:67]\n"
        "ds_read_b64_tr_b16 v[76:77], v1 offset:10240\n"
        "ds_read_b64_tr_b16 v[78:79], v1 offset:15360\n"
        "v_mfma_f32_16x16x32_bf16 a[68:71], v[80:83], v[16:19], a[68:71]\n"
        "ds_read_b128 v[40:43], v5 offset:0\n"
        "ds_read_b128 v[44:47], v5 offset:1024\n"
        "v_mfma_f32_16x16x32_bf16 a[68:71], v[84:87], v[20:23], a[68:71]\n"
        "ds_read_b128 v[48:51], v5 offset:2048\n"
        "ds_read_b128 v[52:55], v5 offset:3072\n"
        "v_mfma_f32_16x16x32_bf16 a[72:75], v[88:91], v[8:11], a[72:75]\n"
        "ds_read_b64_tr_b16 v[80:81], v2 offset:0\n"
        "ds_read_b64_tr_b16 v[82:83], v2 offset:5120\n"
        "v_mfma_f32_16x16x32_bf16 a[72:75], v[92:95], v[12:15], a[72:75]\n"
        "ds_read_b64_tr_b16 v[84:85], v2 offset:10240\n"
        "ds_read_b64_tr_b16 v[86:87], v2 offset:15360\n"
        "v_mfma_f32_16x16x32_bf16 a[76:79], v[88:91], v[16:19], a[76:79]\n"
        "ds_read_b128 v[56:59], v6 offset:0\n"
        "ds_read_b128 v[60:63], v6 offset:1024\n"
        "v_mfma_f32_16x16x32_bf16 a[76:79], v[92:95], v[20:23], a[76:79]\n"
        "ds_read_b128 v[64:67], v6 offset:2048\n"
        "ds_read_b128 v[68:71], v6 offset:3072\n"
        "s_waitcnt lgkmcnt(11)\n"
        "v_mfma_f32_16x16x32_bf16 a[0:3], v[72:75], v[40:43], a[0:3]\n"
        "ds_read_b64_tr_b16 v[88:89], v1 offset:64\n"
        "s_waitcnt lgkmcnt(11)\n"
        "v_mfma_f32_16x16x32_bf16 a[0:3], v[76:79], v[44:47], a[0:3]\n"
        "ds_read_b64_tr_b16 v[90:91], v1 offset:5184\n"
        "s_waitcnt lgkmcnt(11)\n"
        "v_mfma_f32_16x16x32_bf16 a[4:7], v[72:75], v[48:51], a[4:7]\n"
        "ds_read_b64_tr_b16 v[92:93], v1 offset:10304\n"
        "s_waitcnt lgkmcnt(11)\n"
        "v_mfma_f32_16x16x32_bf16 a[4:7], v[76:79], v[52:55], a[4:7]\n"
        "ds_read_b64_tr_b16 v[94:95], v1 offset:15424\n"
        "s_waitcnt lgkmcnt(7)\n"
        "v_mfma_f32_16x16x32_bf16 a[8:11], v[72:75], v[56:59], a[8:11]\n"
        "s_waitcnt lgkmcnt(6)\n"
        "v_mfma_f32_16x16x32_bf16 a[8:11], v[76:79], v[60:63], a[8:11]\n"
        "s_waitcnt lgkmcnt(5)\n"
        "v_mfma_f32_16x16x32_bf16 a[12:15], v[72:75], v[64:67], a[12:15]\n"
        "s_waitcnt lgkmcnt(4)\n"
        "v_mfma_f32_16x16x32_bf16 a[12:15], v[76:79], v[68:71], a[12:15]\n"
        "s_add_u32 s53, s77, 0\n"
        "s_add_u32 s53, s53, s78\n"
        "buffer_store_dwordx4 a[104:107], v7, s[72:75], s53 offen sc1\n"
        "v_mfma_f32_16x16x32_bf16 a[16:19], v[80:83], v[40:43], a[16:19]\n"
        "ds_read_b64_tr_b16 v[72:73], v2 offset:64\n"
        "v_mfma_f32_16x16x32_bf16 a[16:19], v[84:87], v[44:47], a[16:19]\n"
        "ds_read_b64_tr_b16 v[74:75], v2 offset:5184\n"
        "v_mfma_f32_16x16x32_bf16 a[20:23], v[80:83], v[48:51], a[20:23]\n"
        "ds_read_b64_tr_b16 v[76:77], v2 offset:10304\n"
        "v_mfma_f32_16x16x32_bf16 a[20:23], v[84:87], v[52:55], a[20:23]\n"
        "ds_read_b64_tr_b16 v[78:79], v2 offset:15424\n"
        "v_mfma_f32_16x16x32_bf16 a[24:27], v[80:83], v[56:59], a[24:27]\n"
        "v_mfma_f32_16x16x32_bf16 a[24:27], v[84:87], v[60:63], a[24:27]\n"
        "v_mfma_f32_16x16x32_bf16 a[28:31], v[80:83], v[64:67], a[28:31]\n"
        "v_mfma_f32_16x16x32_bf16 a[28:31], v[84:87], v[68:71], a[28:31]\n"
        "s_add_u32 s53, s77, 64\n"
        "s_add_u32 s53, s53, s78\n"
        "buffer_store_dwordx4 a[108:111], v7, s[72:75], s53 offen sc1\n"
        "s_waitcnt lgkmcnt(4)\n"
        "v_mfma_f32_16x16x32_bf16 a[32:35], v[88:91], v[40:43], a[32:35]\n"
        "ds_read_b64_tr_b16 v[80:81], v3 offset:0\n"
        "v_mfma_f32_16x16x32_bf16 a[32:35], v[92:95], v[44:47], a[32:35]\n"
        "ds_read_b64_tr_b16 v[82:83], v3 offset:5120\n"
        "v_mfma_f32_16x16x32_bf16 a[36:39], v[88:91], v[48:51], a[36:39]\n"
        "ds_read_b64_tr_b16 v[84:85], v3 offset:10240\n"
        "v_mfma_f32_16x16x32_bf16 a[36:39], v[92:95], v[52:55], a[36:39]\n"
        "ds_read_b64_tr_b16 v[86:87], v3 offset:15360\n"
        "v_mfma_f32_16x16x32_bf16 a[40:43], v[88:91], v[56:59], a[40:43]\n"
        "v_mfma_f32_16x16x32_bf16 a[40:43], v[92:95], v[60:63], a[40:43]\n"
        "v_mfma_f32_16x16x32_bf16 a[44:47], v[88:91], v[64:67], a[44:47]\n"
        "v_mfma_f32_16x16x32_bf16 a[44:47], v[92:95], v[68:71], a[44:47]\n"
        "s_waitcnt lgkmcnt(4)\n"
        "v_mfma_f32_16x16x32_bf16 a[48:51], v[72:75], v[40:43], a[48:51]\n"
        "ds_read_b64_tr_b16 v[88:89], v4 offset:0\n"
        "v_mfma_f32_16x16x32_bf16 a[48:51], v[76:79], v[44:47], a[48:51]\n"
        "ds_read_b64_tr_b16 v[90:91], v4 offset:5120\n"
        "v_mfma_f32_16x16x32_bf16 a[52:55], v[72:75], v[48:51], a[52:55]\n"
        "ds_read_b64_tr_b16 v[92:93], v4 offset:10240\n"
        "v_mfma_f32_16x16x32_bf16 a[52:55], v[76:79], v[52:55], a[52:55]\n"
        "ds_read_b64_tr_b16 v[94:95], v4 offset:15360\n"
        "v_mfma_f32_16x16x32_bf16 a[56:59], v[72:75], v[56:59], a[56:59]\n"
        "v_mfma_f32_16x16x32_bf16 a[56:59], v[76:79], v[60:63], a[56:59]\n"
        "v_mfma_f32_16x16x32_bf16 a[60:63], v[72:75], v[64:67], a[60:63]\n"
        "v_mfma_f32_16x16x32_bf16 a[60:63], v[76:79], v[68:71], a[60:63]\n"
        "s_waitcnt lgkmcnt(0)\n"
        "s_barrier\n"
        "v_subrev_u32 v1, 40960, v1\n"
        "v_subrev_u32 v2, 40960, v2\n"
        "v_subrev_u32 v3, 40960, v3\n"
        "v_subrev_u32 v4, 40960, v4\n"
        "v_add_u32 v5, 16384, v5\n"
        "v_add_u32 v6, 16384, v6\n"
        "v_mfma_f32_16x16x32_bf16 a[64:67], v[80:83], v[40:43], a[64:67]\n"
        "ds_read_b64_tr_b16 v[72:73], v1 offset:0\n"
        "ds_read_b64_tr_b16 v[74:75], v1 offset:5120\n"
        "v_mfma_f32_16x16x32_bf16 a[64:67], v[84:87], v[44:47], a[64:67]\n"
        "ds_read_b64_tr_b16 v[76:77], v1 offset:10240\n"
        "ds_read_b64_tr_b16 v[78:79], v1 offset:15360\n"
        "v_mfma_f32_16x16x32_bf16 a[68:71], v[80:83], v[48:51], a[68:71]\n"
        "ds_read_b128 v[8:11], v5 offset:0\n"
        "ds_read_b128 v[12:15], v5 offset:1024\n"
        "v_mfma_f32_16x16x32_bf16 a[68:71], v[84:87], v[52:55], a[68:71]\n"
        "ds_read_b128 v[16:19], v5 offset:2048\n"
        "ds_read_b128 v[20:23], v5 offset:3072\n"
        "v_mfma_f32_16x16x32_bf16 a[72:75], v[88:91], v[40:43], a[72:75]\n"
        "ds_read_b64_tr_b16 v[80:81], v2 offset:0\n"
        "ds_read_b64_tr_b16 v[82:83], v2 offset:5120\n"
        "v_mfma_f32_16x16x32_bf16 a[72:75], v[92:95], v[44:47], a[72:75]\n"
        "ds_read_b64_tr_b16 v[84:85], v2 offset:10240\n"
        "ds_read_b64_tr_b16 v[86:87], v2 offset:15360\n"
        "v_mfma_f32_16x16x32_bf16 a[76:79], v[88:91], v[48:51], a[76:79]\n"
        "ds_read_b128 v[24:27], v6 offset:0\n"
        "ds_read_b128 v[28:31], v6 offset:1024\n"
        "v_mfma_f32_16x16x32_bf16 a[76:79], v[92:95], v[52:55], a[76:79]\n"
        "ds_read_b128 v[32:35], v6 offset:2048\n"
        "ds_read_b128 v[36:39], v6 offset:3072\n"
        "s_waitcnt lgkmcnt(11)\n"
        "v_mfma_f32_16x16x32_bf16 a[0:3], v[72:75], v[8:11], a[0:3]\n"
        "ds_read_b64_tr_b16 v[88:89], v1 offset:64\n"
        "s_waitcnt lgkmcnt(11)\n"
        "v_mfma_f32_16x16x32_bf16 a[0:3], v[76:79], v[12:15], a[0:3]\n"
        "ds_read_b64_tr_b16 v[90:91], v1 offset:5184\n"
        "s_waitcnt lgkmcnt(11)\n"
        "v_mfma_f32_16x16x32_bf16 a[4:7], v[72:75], v[16:19], a[4:7]\n"
        "ds_read_b64_tr_b16 v[92:93], v1 offset:10304\n"
        "s_waitcnt lgkmcnt(11)\n"
        "v_mfma_f32_16x16x32_bf16 a[4:7], v[76:79], v[20:23], a[4:7]\n"
        "ds_read_b64_tr_b16 v[94:95], v1 offset:15424\n"
        "s_waitcnt lgkmcnt(7)\n"
        "v_mfma_f32_16x16x32_bf16 a[8:11], v[72:75], v[24:27], a[8:11]\n"
        "s_waitcnt lgkmcnt(6)\n"
        "v_mfma_f32_16x16x32_bf16 a[8:11], v[76:79], v[28:31], a[8:11]\n"
        "s_waitcnt lgkmcnt(5)\n"
        "v_mfma_f32_16x16x32_bf16 a[12:15], v[72:75], v[32:35], a[12:15]\n"
        "s_waitcnt lgkmcnt(4)\n"
        "v_mfma_f32_16x16x32_bf16 a[12:15], v[76:79], v[36:39], a[12:15]\n"
        "s_add_u32 s53, s77, 20480\n"
        "s_add_u32 s53, s53, s78\n"
        "buffer_store_dwordx4 a[112:115], v7, s[72:75], s53 offen sc1\n"
        "v_mfma_f32_16x16x32_bf16 a[16:19], v[80:83], v[8:11], a[16:19]\n"
        "ds_read_b64_tr_b16 v[72:73], v2 offset:64\n"
        "v_mfma_f32_16x16x32_bf16 a[16:19], v[84:87], v[12:15], a[16:19]\n"
        "ds_read_b64_tr_b16 v[74:75], v2 offset:5184\n"
        "v_mfma_f32_16x16x32_bf16 a[20:23], v[80:83], v[16:19], a[20:23]\n"
        "ds_read_b64_tr_b16 v[76:77], v2 offset:10304\n"
        "v_mfma_f32_16x16x32_bf16 a[20:23], v[84:87], v[20:23], a[20:23]\n"
        "ds_read_b64_tr_b16 v[78:79], v2 offset:15424\n"
        "v_mfma_f32_16x16x32_bf16 a[24:27], v[80:83], v[24:27], a[24:27]\n"
        "v_mfma_f32_16x16x32_bf16 a[24:27], v[84:87], v[28:31], a[24:27]\n"
        "v_mfma_f32_16x16x32_bf16 a[28:31], v[80:83], v[32:35], a[28:31]\n"
        "v_mfma_f32_16x16x32_bf16 a[28:31], v[84:87], v[36:39], a[28:31]\n"
        "s_add_u32 s53, s77, 20544\n"
        "s_add_u32 s53, s53, s78\n"
        "buffer_store_dwordx4 a[116:119], v7, s[72:75], s53 offen sc1\n"
        "s_waitcnt lgkmcnt(4)\n"
        "v_mfma_f32_16x16x32_bf16 a[32:35], v[88:91], v[8:11], a[32:35]\n"
        "ds_read_b64_tr_b16 v[80:81], v3 offset:0\n"
        "v_mfma_f32_16x16x32_bf16 a[32:35], v[92:95], v[12:15], a[32:35]\n"
        "ds_read_b64_tr_b16 v[82:83], v3 offset:5120\n"
        "v_mfma_f32_16x16x32_bf16 a[36:39], v[88:91], v[16:19], a[36:39]\n"
        "ds_read_b64_tr_b16 v[84:85], v3 offset:10240\n"
        "v_mfma_f32_16x16x32_bf16 a[36:39], v[92:95], v[20:23], a[36:39]\n"
        "ds_read_b64_tr_b16 v[86:87], v3 offset:15360\n"
        "v_mfma_f32_16x16x32_bf16 a[40:43], v[88:91], v[24:27], a[40:43]\n"
        "v_mfma_f32_16x16x32_bf16 a[40:43], v[92:95], v[28:31], a[40:43]\n"
        "v_mfma_f32_16x16x32_bf16 a[44:47], v[88:91], v[32:35], a[44:47]\n"
        "v_mfma_f32_16x16x32_bf16 a[44:47], v[92:95], v[36:39], a[44:47]\n"
        "s_waitcnt lgkmcnt(4)\n"
        "v_mfma_f32_16x16x32_bf16 a[48:51], v[72:75], v[8:11], a[48:51]\n"
        "ds_read_b64_tr_b16 v[88:89], v4 offset:0\n"
        "v_mfma_f32_16x16x32_bf16 a[48:51], v[76:79], v[12:15], a[48:51]\n"
        "ds_read_b64_tr_b16 v[90:91], v4 offset:5120\n"
        "v_mfma_f32_16x16x32_bf16 a[52:55], v[72:75], v[16:19], a[52:55]\n"
        "ds_read_b64_tr_b16 v[92:93], v4 offset:10240\n"
        "v_mfma_f32_16x16x32_bf16 a[52:55], v[76:79], v[20:23], a[52:55]\n"
        "ds_read_b64_tr_b16 v[94:95], v4 offset:15360\n"
        "v_mfma_f32_16x16x32_bf16 a[56:59], v[72:75], v[24:27], a[56:59]\n"
        "v_mfma_f32_16x16x32_bf16 a[56:59], v[76:79], v[28:31], a[56:59]\n"
        "v_mfma_f32_16x16x32_bf16 a[60:63], v[72:75], v[32:35], a[60:63]\n"
        "v_mfma_f32_16x16x32_bf16 a[60:63], v[76:79], v[36:39], a[60:63]\n"
        "s_waitcnt lgkmcnt(0)\n"
        "s_barrier\n"
        "v_add_u32 v1, 20480, v1\n"
        "v_add_u32 v2, 20480, v2\n"
        "v_add_u32 v3, 20480, v3\n"
        "v_add_u32 v4, 20480, v4\n"
        "v_add_u32 v5, 16384, v5\n"
        "v_add_u32 v6, 16384, v6\n"
        "v_mfma_f32_16x16x32_bf16 a[64:67], v[80:83], v[8:11], a[64:67]\n"
        "ds_read_b64_tr_b16 v[72:73], v1 offset:0\n"
        "ds_read_b64_tr_b16 v[74:75], v1 offset:5120\n"
        "v_mfma_f32_16x16x32_bf16 a[64:67], v[84:87], v[12:15], a[64:67]\n"
        "ds_read_b64_tr_b16 v[76:77], v1 offset:10240\n"
        "ds_read_b64_tr_b16 v[78:79], v1 offset:15360\n"
        "v_mfma_f32_16x16x32_bf16 a[68:71], v[80:83], v[16:19], a[68:71]\n"
        "ds_read_b128 v[40:43], v5 offset:0\n"
        "ds_read_b128 v[44:47], v5 offset:1024\n"
        "v_mfma_f32_16x16x32_bf16 a[68:71], v[84:87], v[20:23], a[68:71]\n"
        "ds_read_b128 v[48:51], v5 offset:2048\n"
        "ds_read_b128 v[52:55], v5 offset:3072\n"
        "v_mfma_f32_16x16x32_bf16 a[72:75], v[88:91], v[8:11], a[72:75]\n"
        "ds_read_b64_tr_b16 v[80:81], v2 offset:0\n"
        "ds_read_b64_tr_b16 v[82:83], v2 offset:5120\n"
        "v_mfma_f32_16x16x32_bf16 a[72:75], v[92:95], v[12:15], a[72:75]\n"
        "ds_read_b64_tr_b16 v[84:85], v2 offset:10240\n"
        "ds_read_b64_tr_b16 v[86:87], v2 offset:15360\n"
        "v_mfma_f32_16x16x32_bf16 a[76:79], v[88:91], v[16:19], a[76:79]\n"
        "ds_read_b128 v[56:59], v6 offset:0\n"
        "ds_read_b128 v[60:63], v6 offset:1024\n"
        "v_mfma_f32_16x16x32_bf16 a[76:79], v[92:95], v[20:23], a[76:79]\n"
        "ds_read_b128 v[64:67], v6 offset:2048\n"
        "ds_read_b128 v[68:71], v6 offset:3072\n"
        "s_waitcnt lgkmcnt(11)\n"
        "v_mfma_f32_16x16x32_bf16 a[0:3], v[72:75], v[40:43], a[0:3]\n"
        "ds_read_b64_tr_b16 v[88:89], v1 offset:64\n"
        "s_waitcnt lgkmcnt(11)\n"
        "v_mfma_f32_16x16x32_bf16 a[0:3], v[76:79], v[44:47], a[0:3]\n"
        "ds_read_b64_tr_b16 v[90:91], v1 offset:5184\n"
        "s_waitcnt lgkmcnt(11)\n"
        "v_mfma_f32_16x16x32_bf16 a[4:7], v[72:75], v[48:51], a[4:7]\n"
        "ds_read_b64_tr_b16 v[92:93], v1 offset:10304\n"
        "s_waitcnt lgkmcnt(11)\n"
        "v_mfma_f32_16x16x32_bf16 a[4:7], v[76:79], v[52:55], a[4:7]\n"
        "ds_read_b64_tr_b16 v[94:95], v1 offset:15424\n"
        "s_waitcnt lgkmcnt(7)\n"
        "v_mfma_f32_16x16x32_bf16 a[8:11], v[72:75], v[56:59], a[8:11]\n"
        "s_waitcnt lgkmcnt(6)\n"
        "v_mfma_f32_16x16x32_bf16 a[8:11], v[76:79], v[60:63], a[8:11]\n"
        "s_waitcnt lgkmcnt(5)\n"
        "v_mfma_f32_16x16x32_bf16 a[12:15], v[72:75], v[64:67], a[12:15]\n"
        "s_waitcnt lgkmcnt(4)\n"
        "v_mfma_f32_16x16x32_bf16 a[12:15], v[76:79], v[68:71], a[12:15]\n"
        "v_mfma_f32_16x16x32_bf16 a[16:19], v[80:83], v[40:43], a[16:19]\n"
        "ds_read_b64_tr_b16 v[72:73], v2 offset:64\n"
        "v_mfma_f32_16x16x32_bf16 a[16:19], v[84:87], v[44:47], a[16:19]\n"
        "ds_read_b64_tr_b16 v[74:75], v2 offset:5184\n"
        "v_mfma_f32_16x16x32_bf16 a[20:23], v[80:83], v[48:51], a[20:23]\n"
        "ds_read_b64_tr_b16 v[76:77], v2 offset:10304\n"
        "v_mfma_f32_16x16x32_bf16 a[20:23], v[84:87], v[52:55], a[20:23]\n"
        "ds_read_b64_tr_b16 v[78:79], v2 offset:15424\n"
        "v_mfma_f32_16x16x32_bf16 a[24:27], v[80:83], v[56:59], a[24:27]\n"
        "v_mfma_f32_16x16x32_bf16 a[24:27], v[84:87], v[60:63], a[24:27]\n"
        "v_mfma_f32_16x16x32_bf16 a[28:31], v[80:83], v[64:67], a[28:31]\n"
        "v_mfma_f32_16x16x32_bf16 a[28:31], v[84:87], v[68:71], a[28:31]\n"
        "s_waitcnt lgkmcnt(4)\n"
        "v_mfma_f32_16x16x32_bf16 a[32:35], v[88:91], v[40:43], a[32:35]\n"
        "ds_read_b64_tr_b16 v[80:81], v3 offset:0\n"
        "v_mfma_f32_16x16x32_bf16 a[32:35], v[92:95], v[44:47], a[32:35]\n"
        "ds_read_b64_tr_b16 v[82:83], v3 offset:5120\n"
        "v_mfma_f32_16x16x32_bf16 a[36:39], v[88:91], v[48:51], a[36:39]\n"
        "ds_read_b64_tr_b16 v[84:85], v3 offset:10240\n"
        "v_mfma_f32_16x16x32_bf16 a[36:39], v[92:95], v[52:55], a[36:39]\n"
        "ds_read_b64_tr_b16 v[86:87], v3 offset:15360\n"
        "v_mfma_f32_16x16x32_bf16 a[40:43], v[88:91], v[56:59], a[40:43]\n"
        "v_mfma_f32_16x16x32_bf16 a[40:43], v[92:95], v[60:63], a[40:43]\n"
        "v_mfma_f32_16x16x32_bf16 a[44:47], v[88:91], v[64:67], a[44:47]\n"
        "v_mfma_f32_16x16x32_bf16 a[44:47], v[92:95], v[68:71], a[44:47]\n"
        "s_waitcnt lgkmcnt(6)\n"
        "v_mfma_f32_16x16x32_bf16 a[48:51], v[72:75], v[40:43], a[48:51]\n"
        "ds_read_b64_tr_b16 v[88:89], v4 offset:0\n"
        "s_waitcnt lgkmcnt(5)\n"
        "v_mfma_f32_16x16x32_bf16 a[48:51], v[76:79], v[44:47], a[48:51]\n"
        "ds_read_b64_tr_b16 v[90:91], v4 offset:5120\n"
        "v_mfma_f32_16x16x32_bf16 a[52:55], v[72:75], v[48:51], a[52:55]\n"
        "ds_read_b64_tr_b16 v[92:93], v4 offset:10240\n"
        "v_mfma_f32_16x16x32_bf16 a[52:55], v[76:79], v[52:55], a[52:55]\n"
        "ds_read_b64_tr_b16 v[94:95], v4 offset:15360\n"
        "v_mfma_f32_16x16x32_bf16 a[56:59], v[72:75], v[56:59], a[56:59]\n"
        "v_mfma_f32_16x16x32_bf16 a[56:59], v[76:79], v[60:63], a[56:59]\n"
        "v_mfma_f32_16x16x32_bf16 a[60:63], v[72:75], v[64:67], a[60:63]\n"
        "v_mfma_f32_16x16x32_bf16 a[60:63], v[76:79], v[68:71], a[60:63]\n"
        "s_waitcnt lgkmcnt(0)\n"
        "s_barrier\n"
        "v_add_u32 v1, 20480, v1\n"
        "v_add_u32 v2, 20480, v2\n"
        "v_add_u32 v3, 20480, v3\n"
        "v_add_u32 v4, 20480, v4\n"
        "v_add_u32 v5, 16384, v5\n"
        "v_add_u32 v6, 16384, v6\n"
        "v_mfma_f32_16x16x32_bf16 a[64:67], v[80:83], v[40:43], a[64:67]\n"
        "ds_read_b64_tr_b16 v[72:73], v1 offset:0\n"
        "ds_read_b64_tr_b16 v[74:75], v1 offset:5120\n"
        "v_mfma_f32_16x16x32_bf16 a[64:67], v[84:87], v[44:47], a[64:67]\n"
        "ds_read_b64_tr_b16 v[76:77], v1 offset:10240\n"
        "ds_read_b64_tr_b16 v[78:79], v1 offset:15360\n"
        "v_mfma_f32_16x16x32_bf16 a[68:71], v[80:83], v[48:51], a[68:71]\n"
        "ds_read_b128 v[8:11], v5 offset:0\n"
        "ds_read_b128 v[12:15], v5 offset:1024\n"
        "v_mfma_f32_16x16x32_bf16 a[68:71], v[84:87], v[52:55], a[68:71]\n"
        "ds_read_b128 v[16:19], v5 offset:2048\n"
        "ds_read_b128 v[20:23], v5 offset:3072\n"
        "v_mfma_f32_16x16x32_bf16 a[72:75], v[88:91], v[40:43], a[72:75]\n"
        "ds_read_b64_tr_b16 v[80:81], v2 offset:0\n"
        "ds_read_b64_tr_b16 v[82:83], v2 offset:5120\n"
        "v_mfma_f32_16x16x32_bf16 a[72:75], v[92:95], v[44:47], a[72:75]\n"
        "ds_read_b64_tr_b16 v[84:85], v2 offset:10240\n"
        "ds_read_b64_tr_b16 v[86:87], v2 offset:15360\n"
        "v_mfma_f32_16x16x32_bf16 a[76:79], v[88:91], v[48:51], a[76:79]\n"
        "ds_read_b128 v[24:27], v6 offset:0\n"
        "ds_read_b128 v[28:31], v6 offset:1024\n"
        "v_mfma_f32_16x16x32_bf16 a[76:79], v[92:95], v[52:55], a[76:79]\n"
        "ds_read_b128 v[32:35], v6 offset:2048\n"
        "ds_read_b128 v[36:39], v6 offset:3072\n"
        "s_waitcnt lgkmcnt(11)\n"
        "v_mfma_f32_16x16x32_bf16 a[0:3], v[72:75], v[8:11], a[0:3]\n"
        "ds_read_b64_tr_b16 v[88:89], v1 offset:64\n"
        "s_waitcnt lgkmcnt(11)\n"
        "v_mfma_f32_16x16x32_bf16 a[0:3], v[76:79], v[12:15], a[0:3]\n"
        "ds_read_b64_tr_b16 v[90:91], v1 offset:5184\n"
        "s_waitcnt lgkmcnt(11)\n"
        "v_mfma_f32_16x16x32_bf16 a[4:7], v[72:75], v[16:19], a[4:7]\n"
        "ds_read_b64_tr_b16 v[92:93], v1 offset:10304\n"
        "s_waitcnt lgkmcnt(11)\n"
        "v_mfma_f32_16x16x32_bf16 a[4:7], v[76:79], v[20:23], a[4:7]\n"
        "ds_read_b64_tr_b16 v[94:95], v1 offset:15424\n"
        "s_waitcnt lgkmcnt(7)\n"
        "v_mfma_f32_16x16x32_bf16 a[8:11], v[72:75], v[24:27], a[8:11]\n"
        "s_waitcnt lgkmcnt(6)\n"
        "v_mfma_f32_16x16x32_bf16 a[8:11], v[76:79], v[28:31], a[8:11]\n"
        "s_waitcnt lgkmcnt(5)\n"
        "v_mfma_f32_16x16x32_bf16 a[12:15], v[72:75], v[32:35], a[12:15]\n"
        "s_waitcnt lgkmcnt(4)\n"
        "v_mfma_f32_16x16x32_bf16 a[12:15], v[76:79], v[36:39], a[12:15]\n"
        "v_mfma_f32_16x16x32_bf16 a[16:19], v[80:83], v[8:11], a[16:19]\n"
        "ds_read_b64_tr_b16 v[72:73], v2 offset:64\n"
        "v_mfma_f32_16x16x32_bf16 a[16:19], v[84:87], v[12:15], a[16:19]\n"
        "ds_read_b64_tr_b16 v[74:75], v2 offset:5184\n"
        "v_mfma_f32_16x16x32_bf16 a[20:23], v[80:83], v[16:19], a[20:23]\n"
        "ds_read_b64_tr_b16 v[76:77], v2 offset:10304\n"
        "v_mfma_f32_16x16x32_bf16 a[20:23], v[84:87], v[20:23], a[20:23]\n"
        "ds_read_b64_tr_b16 v[78:79], v2 offset:15424\n"
        "v_mfma_f32_16x16x32_bf16 a[24:27], v[80:83], v[24:27], a[24:27]\n"
        "v_mfma_f32_16x16x32_bf16 a[24:27], v[84:87], v[28:31], a[24:27]\n"
        "v_mfma_f32_16x16x32_bf16 a[28:31], v[80:83], v[32:35], a[28:31]\n"
        "v_mfma_f32_16x16x32_bf16 a[28:31], v[84:87], v[36:39], a[28:31]\n"
        "s_waitcnt lgkmcnt(4)\n"
        "v_mfma_f32_16x16x32_bf16 a[32:35], v[88:91], v[8:11], a[32:35]\n"
        "ds_read_b64_tr_b16 v[80:81], v3 offset:0\n"
        "v_mfma_f32_16x16x32_bf16 a[32:35], v[92:95], v[12:15], a[32:35]\n"
        "ds_read_b64_tr_b16 v[82:83], v3 offset:5120\n"
        "v_mfma_f32_16x16x32_bf16 a[36:39], v[88:91], v[16:19], a[36:39]\n"
        "ds_read_b64_tr_b16 v[84:85], v3 offset:10240\n"
        "v_mfma_f32_16x16x32_bf16 a[36:39], v[92:95], v[20:23], a[36:39]\n"
        "ds_read_b64_tr_b16 v[86:87], v3 offset:15360\n"
        "v_mfma_f32_16x16x32_bf16 a[40:43], v[88:91], v[24:27], a[40:43]\n"
        "v_mfma_f32_16x16x32_bf16 a[40:43], v[92:95], v[28:31], a[40:43]\n"
        "v_mfma_f32_16x16x32_bf16 a[44:47], v[88:91], v[32:35], a[44:47]\n"
        "v_mfma_f32_16x16x32_bf16 a[44:47], v[92:95], v[36:39], a[44:47]\n"
        "s_waitcnt lgkmcnt(6)\n"
        "v_mfma_f32_16x16x32_bf16 a[48:51], v[72:75], v[8:11], a[48:51]\n"
        "ds_read_b64_tr_b16 v[88:89], v4 offset:0\n"
        "s_waitcnt lgkmcnt(5)\n"
        "v_mfma_f32_16x16x32_bf16 a[48:51], v[76:79], v[12:15], a[48:51]\n"
        "ds_read_b64_tr_b16 v[90:91], v4 offset:5120\n"
        "v_mfma_f32_16x16x32_bf16 a[52:55], v[72:75], v[16:19], a[52:55]\n"
        "ds_read_b64_tr_b16 v[92:93], v4 offset:10240\n"
        "v_mfma_f32_16x16x32_bf16 a[52:55], v[76:79], v[20:23], a[52:55]\n"
        "ds_read_b64_tr_b16 v[94:95], v4 offset:15360\n"
        "v_mfma_f32_16x16x32_bf16 a[56:59], v[72:75], v[24:27], a[56:59]\n"
        "v_mfma_f32_16x16x32_bf16 a[56:59], v[76:79], v[28:31], a[56:59]\n"
        "v_mfma_f32_16x16x32_bf16 a[60:63], v[72:75], v[32:35], a[60:63]\n"
        "v_mfma_f32_16x16x32_bf16 a[60:63], v[76:79], v[36:39], a[60:63]\n"
        "s_waitcnt vmcnt(0)\n"
        "s_waitcnt lgkmcnt(0)\n"
        "s_barrier\n"
        "s_cmp_lg_u32 s36, 0\n"
        "s_cbranch_scc1 KA_NPA_%=\n"
        "s_lshl_b32 s88, s38, 5\n"
        "s_add_u32 s88, s88, s37\n"
        "s_lshl_b32 s88, s88, 7\n"
        "s_add_u32 s88, s88, 131072\n"
        "s_mov_b64 exec, 1\n"
        "v_mov_b32 v121, s88\n"
        "v_mov_b32 v122, s96\n"
        "v_mov_b32 v123, s97\n"
        "global_store_dwordx2 v121, v[122:123], s[86:87] sc1\n"
        "s_mov_b64 exec, -1\n"
        "KA_NPA_%=:\n"
        "v_subrev_u32 v1, 40960, v1\n"
        "v_subrev_u32 v2, 40960, v2\n"
        "v_subrev_u32 v3, 40960, v3\n"
        "v_subrev_u32 v4, 40960, v4\n"
        "v_add_u32 v5, 16384, v5\n"
        "v_add_u32 v6, 16384, v6\n"
        "v_mfma_f32_16x16x32_bf16 a[64:67], v[80:83], v[8:11], a[64:67]\n"
        "ds_read_b64_tr_b16 v[72:73], v1 offset:0\n"
        "ds_read_b64_tr_b16 v[74:75], v1 offset:5120\n"
        "v_mfma_f32_16x16x32_bf16 a[64:67], v[84:87], v[12:15], a[64:67]\n"
        "ds_read_b64_tr_b16 v[76:77], v1 offset:10240\n"
        "ds_read_b64_tr_b16 v[78:79], v1 offset:15360\n"
        "v_mfma_f32_16x16x32_bf16 a[68:71], v[80:83], v[16:19], a[68:71]\n"
        "ds_read_b128 v[40:43], v5 offset:0\n"
        "ds_read_b128 v[44:47], v5 offset:1024\n"
        "v_mfma_f32_16x16x32_bf16 a[68:71], v[84:87], v[20:23], a[68:71]\n"
        "ds_read_b128 v[48:51], v5 offset:2048\n"
        "ds_read_b128 v[52:55], v5 offset:3072\n"
        "v_mfma_f32_16x16x32_bf16 a[72:75], v[88:91], v[8:11], a[72:75]\n"
        "ds_read_b64_tr_b16 v[80:81], v2 offset:0\n"
        "ds_read_b64_tr_b16 v[82:83], v2 offset:5120\n"
        "v_mfma_f32_16x16x32_bf16 a[72:75], v[92:95], v[12:15], a[72:75]\n"
        "ds_read_b64_tr_b16 v[84:85], v2 offset:10240\n"
        "ds_read_b64_tr_b16 v[86:87], v2 offset:15360\n"
        "v_mfma_f32_16x16x32_bf16 a[76:79], v[88:91], v[16:19], a[76:79]\n"
        "ds_read_b128 v[56:59], v6 offset:0\n"
        "ds_read_b128 v[60:63], v6 offset:1024\n"
        "v_mfma_f32_16x16x32_bf16 a[76:79], v[92:95], v[20:23], a[76:79]\n"
        "ds_read_b128 v[64:67], v6 offset:2048\n"
        "ds_read_b128 v[68:71], v6 offset:3072\n"
        "s_waitcnt lgkmcnt(11)\n"
        "v_mfma_f32_16x16x32_bf16 a[0:3], v[72:75], v[40:43], a[0:3]\n"
        "ds_read_b64_tr_b16 v[88:89], v1 offset:64\n"
        "s_waitcnt lgkmcnt(11)\n"
        "v_mfma_f32_16x16x32_bf16 a[0:3], v[76:79], v[44:47], a[0:3]\n"
        "ds_read_b64_tr_b16 v[90:91], v1 offset:5184\n"
        "s_waitcnt lgkmcnt(11)\n"
        "v_mfma_f32_16x16x32_bf16 a[4:7], v[72:75], v[48:51], a[4:7]\n"
        "ds_read_b64_tr_b16 v[92:93], v1 offset:10304\n"
        "s_waitcnt lgkmcnt(11)\n"
        "v_mfma_f32_16x16x32_bf16 a[4:7], v[76:79], v[52:55], a[4:7]\n"
        "ds_read_b64_tr_b16 v[94:95], v1 offset:15424\n"
        "s_waitcnt lgkmcnt(7)\n"
        "v_mfma_f32_16x16x32_bf16 a[8:11], v[72:75], v[56:59], a[8:11]\n"
        "s_waitcnt lgkmcnt(6)\n"
        "v_mfma_f32_16x16x32_bf16 a[8:11], v[76:79], v[60:63], a[8:11]\n"
        "s_waitcnt lgkmcnt(5)\n"
        "v_mfma_f32_16x16x32_bf16 a[12:15], v[72:75], v[64:67], a[12:15]\n"
        "s_waitcnt lgkmcnt(4)\n"
        "v_mfma_f32_16x16x32_bf16 a[12:15], v[76:79], v[68:71], a[12:15]\n"
        "v_mfma_f32_16x16x32_bf16 a[16:19], v[80:83], v[40:43], a[16:19]\n"
        "ds_read_b64_tr_b16 v[72:73], v2 offset:64\n"
        "v_mfma_f32_16x16x32_bf16 a[16:19], v[84:87], v[44:47], a[16:19]\n"
        "ds_read_b64_tr_b16 v[74:75], v2 offset:5184\n"
        "v_mfma_f32_16x16x32_bf16 a[20:23], v[80:83], v[48:51], a[20:23]\n"
        "ds_read_b64_tr_b16 v[76:77], v2 offset:10304\n"
        "v_mfma_f32_16x16x32_bf16 a[20:23], v[84:87], v[52:55], a[20:23]\n"
        "ds_read_b64_tr_b16 v[78:79], v2 offset:15424\n"
        "v_mfma_f32_16x16x32_bf16 a[24:27], v[80:83], v[56:59], a[24:27]\n"
        "v_mfma_f32_16x16x32_bf16 a[24:27], v[84:87], v[60:63], a[24:27]\n"
        "v_mfma_f32_16x16x32_bf16 a[28:31], v[80:83], v[64:67], a[28:31]\n"
        "v_mfma_f32_16x16x32_bf16 a[28:31], v[84:87], v[68:71], a[28:31]\n"
        "s_waitcnt lgkmcnt(4)\n"
        "v_mfma_f32_16x16x32_bf16 a[32:35], v[88:91], v[40:43], a[32:35]\n"
        "ds_read_b64_tr_b16 v[80:81], v3 offset:0\n"
        "v_mfma_f32_16x16x32_bf16 a[32:35], v[92:95], v[44:47], a[32:35]\n"
        "ds_read_b64_tr_b16 v[82:83], v3 offset:5120\n"
        "v_mfma_f32_16x16x32_bf16 a[36:39], v[88:91], v[48:51], a[36:39]\n"
        "ds_read_b64_tr_b16 v[84:85], v3 offset:10240\n"
        "v_mfma_f32_16x16x32_bf16 a[36:39], v[92:95], v[52:55], a[36:39]\n"
        "ds_read_b64_tr_b16 v[86:87], v3 offset:15360\n"
        "v_mfma_f32_16x16x32_bf16 a[40:43], v[88:91], v[56:59], a[40:43]\n"
        "v_mfma_f32_16x16x32_bf16 a[40:43], v[92:95], v[60:63], a[40:43]\n"
        "v_mfma_f32_16x16x32_bf16 a[44:47], v[88:91], v[64:67], a[44:47]\n"
        "v_mfma_f32_16x16x32_bf16 a[44:47], v[92:95], v[68:71], a[44:47]\n"
        "s_waitcnt lgkmcnt(6)\n"
        "v_mfma_f32_16x16x32_bf16 a[48:51], v[72:75], v[40:43], a[48:51]\n"
        "ds_read_b64_tr_b16 v[88:89], v4 offset:0\n"
        "s_waitcnt lgkmcnt(5)\n"
        "v_mfma_f32_16x16x32_bf16 a[48:51], v[76:79], v[44:47], a[48:51]\n"
        "ds_read_b64_tr_b16 v[90:91], v4 offset:5120\n"
        "v_mfma_f32_16x16x32_bf16 a[52:55], v[72:75], v[48:51], a[52:55]\n"
        "ds_read_b64_tr_b16 v[92:93], v4 offset:10240\n"
        "v_mfma_f32_16x16x32_bf16 a[52:55], v[76:79], v[52:55], a[52:55]\n"
        "ds_read_b64_tr_b16 v[94:95], v4 offset:15360\n"
        "v_mfma_f32_16x16x32_bf16 a[56:59], v[72:75], v[56:59], a[56:59]\n"
        "v_mfma_f32_16x16x32_bf16 a[56:59], v[76:79], v[60:63], a[56:59]\n"
        "v_mfma_f32_16x16x32_bf16 a[60:63], v[72:75], v[64:67], a[60:63]\n"
        "v_mfma_f32_16x16x32_bf16 a[60:63], v[76:79], v[68:71], a[60:63]\n"
        "s_waitcnt lgkmcnt(5)\n"
        "v_mfma_f32_16x16x32_bf16 a[64:67], v[80:83], v[40:43], a[64:67]\n"
        "s_waitcnt lgkmcnt(4)\n"
        "v_mfma_f32_16x16x32_bf16 a[64:67], v[84:87], v[44:47], a[64:67]\n"
        "v_mfma_f32_16x16x32_bf16 a[68:71], v[80:83], v[48:51], a[68:71]\n"
        "v_mfma_f32_16x16x32_bf16 a[68:71], v[84:87], v[52:55], a[68:71]\n"
        "s_waitcnt lgkmcnt(2)\n"
        "v_mfma_f32_16x16x32_bf16 a[72:75], v[88:91], v[40:43], a[72:75]\n"
        "s_waitcnt lgkmcnt(0)\n"
        "v_mfma_f32_16x16x32_bf16 a[72:75], v[92:95], v[44:47], a[72:75]\n"
        "v_mfma_f32_16x16x32_bf16 a[76:79], v[88:91], v[48:51], a[76:79]\n"
        "v_mfma_f32_16x16x32_bf16 a[76:79], v[92:95], v[52:55], a[76:79]\n"
        "s_nop 7\n"
        "s_mov_b32 s52, s38\n"
        "s_lshl_b32 s52, s52, 3\n"
        "s_and_b32 s53, s36, 1\n"
        "s_lshl_b32 s53, s53, 2\n"
        "s_add_u32 s52, s52, s53\n"
        "s_and_b32 s53, s36, 2\n"
        "s_add_u32 s54, s52, s53\n"
        "s_mul_i32 s76, s54, 20480\n"
        "s_xor_b32 s53, s53, 2\n"
        "s_add_u32 s54, s52, s53\n"
        "s_mul_i32 s77, s54, 20480\n"
        "v_accvgpr_read_b32 v96, a0\n"
        "v_accvgpr_read_b32 v97, a1\n"
        "v_accvgpr_read_b32 v98, a2\n"
        "v_accvgpr_read_b32 v99, a3\n"
        "v_accvgpr_read_b32 v100, a16\n"
        "v_accvgpr_read_b32 v101, a17\n"
        "v_accvgpr_read_b32 v102, a18\n"
        "v_accvgpr_read_b32 v103, a19\n"
        "v_cvt_pk_bf16_f32 v104, v96, v97\n"
        "v_cvt_pk_bf16_f32 v105, v98, v99\n"
        "v_cvt_pk_bf16_f32 v106, v100, v101\n"
        "v_cvt_pk_bf16_f32 v107, v102, v103\n"
        "s_add_u32 s53, s76, 0\n"
        "s_add_u32 s53, s53, s78\n"
        "buffer_store_dwordx4 v[104:107], v7, s[72:75], s53 offen sc1\n"
        "v_accvgpr_read_b32 v108, a32\n"
        "v_accvgpr_read_b32 v109, a33\n"
        "v_accvgpr_read_b32 v110, a34\n"
        "v_accvgpr_read_b32 v111, a35\n"
        "v_accvgpr_read_b32 v112, a48\n"
        "v_accvgpr_read_b32 v113, a49\n"
        "v_accvgpr_read_b32 v114, a50\n"
        "v_accvgpr_read_b32 v115, a51\n"
        "v_cvt_pk_bf16_f32 v116, v108, v109\n"
        "v_cvt_pk_bf16_f32 v117, v110, v111\n"
        "v_cvt_pk_bf16_f32 v118, v112, v113\n"
        "v_cvt_pk_bf16_f32 v119, v114, v115\n"
        "s_add_u32 s53, s76, 64\n"
        "s_add_u32 s53, s53, s78\n"
        "buffer_store_dwordx4 v[116:119], v7, s[72:75], s53 offen sc1\n"
        "v_accvgpr_read_b32 v96, a64\n"
        "v_accvgpr_read_b32 v97, a65\n"
        "v_accvgpr_read_b32 v98, a66\n"
        "v_accvgpr_read_b32 v99, a67\n"
        "v_accvgpr_read_b32 v100, a72\n"
        "v_accvgpr_read_b32 v101, a73\n"
        "v_accvgpr_read_b32 v102, a74\n"
        "v_accvgpr_read_b32 v103, a75\n"
        "v_cvt_pk_bf16_f32 v104, v96, v97\n"
        "v_cvt_pk_bf16_f32 v105, v98, v99\n"
        "v_cvt_pk_bf16_f32 v106, v100, v101\n"
        "v_cvt_pk_bf16_f32 v107, v102, v103\n"
        "s_add_u32 s53, s76, 128\n"
        "buffer_store_dwordx4 v[104:107], v7, s[72:75], s53 offen sc1\n"
        "v_accvgpr_read_b32 v108, a4\n"
        "v_accvgpr_read_b32 v109, a5\n"
        "v_accvgpr_read_b32 v110, a6\n"
        "v_accvgpr_read_b32 v111, a7\n"
        "v_accvgpr_read_b32 v112, a20\n"
        "v_accvgpr_read_b32 v113, a21\n"
        "v_accvgpr_read_b32 v114, a22\n"
        "v_accvgpr_read_b32 v115, a23\n"
        "v_cvt_pk_bf16_f32 v116, v108, v109\n"
        "v_cvt_pk_bf16_f32 v117, v110, v111\n"
        "v_cvt_pk_bf16_f32 v118, v112, v113\n"
        "v_cvt_pk_bf16_f32 v119, v114, v115\n"
        "s_add_u32 s53, s76, 20480\n"
        "s_add_u32 s53, s53, s78\n"
        "buffer_store_dwordx4 v[116:119], v7, s[72:75], s53 offen sc1\n"
        "v_accvgpr_read_b32 v96, a36\n"
        "v_accvgpr_read_b32 v97, a37\n"
        "v_accvgpr_read_b32 v98, a38\n"
        "v_accvgpr_read_b32 v99, a39\n"
        "v_accvgpr_read_b32 v100, a52\n"
        "v_accvgpr_read_b32 v101, a53\n"
        "v_accvgpr_read_b32 v102, a54\n"
        "v_accvgpr_read_b32 v103, a55\n"
        "v_cvt_pk_bf16_f32 v104, v96, v97\n"
        "v_cvt_pk_bf16_f32 v105, v98, v99\n"
        "v_cvt_pk_bf16_f32 v106, v100, v101\n"
        "v_cvt_pk_bf16_f32 v107, v102, v103\n"
        "s_add_u32 s53, s76, 20544\n"
        "s_add_u32 s53, s53, s78\n"
        "buffer_store_dwordx4 v[104:107], v7, s[72:75], s53 offen sc1\n"
        "v_accvgpr_read_b32 v108, a68\n"
        "v_accvgpr_read_b32 v109, a69\n"
        "v_accvgpr_read_b32 v110, a70\n"
        "v_accvgpr_read_b32 v111, a71\n"
        "v_accvgpr_read_b32 v112, a76\n"
        "v_accvgpr_read_b32 v113, a77\n"
        "v_accvgpr_read_b32 v114, a78\n"
        "v_accvgpr_read_b32 v115, a79\n"
        "v_cvt_pk_bf16_f32 v116, v108, v109\n"
        "v_cvt_pk_bf16_f32 v117, v110, v111\n"
        "v_cvt_pk_bf16_f32 v118, v112, v113\n"
        "v_cvt_pk_bf16_f32 v119, v114, v115\n"
        "s_add_u32 s53, s76, 20608\n"
        "buffer_store_dwordx4 v[116:119], v7, s[72:75], s53 offen sc1\n"
        "v_accvgpr_read_b32 v96, a8\n"
        "v_accvgpr_read_b32 v97, a9\n"
        "v_accvgpr_read_b32 v98, a10\n"
        "v_accvgpr_read_b32 v99, a11\n"
        "v_accvgpr_read_b32 v100, a24\n"
        "v_accvgpr_read_b32 v101, a25\n"
        "v_accvgpr_read_b32 v102, a26\n"
        "v_accvgpr_read_b32 v103, a27\n"
        "v_cvt_pk_bf16_f32 v104, v96, v97\n"
        "v_cvt_pk_bf16_f32 v105, v98, v99\n"
        "v_cvt_pk_bf16_f32 v106, v100, v101\n"
        "v_cvt_pk_bf16_f32 v107, v102, v103\n"
        "s_add_u32 s53, s77, 0\n"
        "s_add_u32 s53, s53, s78\n"
        "buffer_store_dwordx4 v[104:107], v7, s[72:75], s53 offen sc1\n"
        "v_accvgpr_read_b32 v108, a40\n"
        "v_accvgpr_read_b32 v109, a41\n"
        "v_accvgpr_read_b32 v110, a42\n"
        "v_accvgpr_read_b32 v111, a43\n"
        "v_accvgpr_read_b32 v112, a56\n"
        "v_accvgpr_read_b32 v113, a57\n"
        "v_accvgpr_read_b32 v114, a58\n"
        "v_accvgpr_read_b32 v115, a59\n"
        "v_cvt_pk_bf16_f32 v116, v108, v109\n"
        "v_cvt_pk_bf16_f32 v117, v110, v111\n"
        "v_cvt_pk_bf16_f32 v118, v112, v113\n"
        "v_cvt_pk_bf16_f32 v119, v114, v115\n"
        "s_add_u32 s53, s77, 64\n"
        "s_add_u32 s53, s53, s78\n"
        "buffer_store_dwordx4 v[116:119], v7, s[72:75], s53 offen sc1\n"
        "v_accvgpr_read_b32 v96, a12\n"
        "v_accvgpr_read_b32 v97, a13\n"
        "v_accvgpr_read_b32 v98, a14\n"
        "v_accvgpr_read_b32 v99, a15\n"
        "v_accvgpr_read_b32 v100, a28\n"
        "v_accvgpr_read_b32 v101, a29\n"
        "v_accvgpr_read_b32 v102, a30\n"
        "v_accvgpr_read_b32 v103, a31\n"
        "v_cvt_pk_bf16_f32 v104, v96, v97\n"
        "v_cvt_pk_bf16_f32 v105, v98, v99\n"
        "v_cvt_pk_bf16_f32 v106, v100, v101\n"
        "v_cvt_pk_bf16_f32 v107, v102, v103\n"
        "s_add_u32 s53, s77, 20480\n"
        "s_add_u32 s53, s53, s78\n"
        "buffer_store_dwordx4 v[104:107], v7, s[72:75], s53 offen sc1\n"
        "v_accvgpr_read_b32 v108, a44\n"
        "v_accvgpr_read_b32 v109, a45\n"
        "v_accvgpr_read_b32 v110, a46\n"
        "v_accvgpr_read_b32 v111, a47\n"
        "v_accvgpr_read_b32 v112, a60\n"
        "v_accvgpr_read_b32 v113, a61\n"
        "v_accvgpr_read_b32 v114, a62\n"
        "v_accvgpr_read_b32 v115, a63\n"
        "v_cvt_pk_bf16_f32 v116, v108, v109\n"
        "v_cvt_pk_bf16_f32 v117, v110, v111\n"
        "v_cvt_pk_bf16_f32 v118, v112, v113\n"
        "v_cvt_pk_bf16_f32 v119, v114, v115\n"
        "s_add_u32 s53, s77, 20544\n"
        "s_add_u32 s53, s53, s78\n"
        "buffer_store_dwordx4 v[116:119], v7, s[72:75], s53 offen sc1\n"
        "s_waitcnt vmcnt(0)\n"
        "s_branch KB_END_%=\n"
        "KB_LOADER_%=:\n"
        "s_cmp_eq_u32 %[restart], 0\n"
        "s_cbranch_scc1 LD_GO4_%=\n"
        "v_lshlrev_b32 v24, 3, v0\n"
        "v_and_b32 v24, 0x180, v24\n"
        "s_lshr_b32 s88, s37, 2\n"
        "s_lshl_b32 s88, s88, 3\n"
        "s_add_u32 s88, s88, s38\n"
        "s_lshl_b32 s88, s88, 9\n"
        "s_add_u32 s88, s88, 65536\n"
        "v_add_u32 v24, s88, v24\n"
        "global_load_dwordx2 v[26:27], v24, s[86:87] sc1\n"
        "s_waitcnt vmcnt(0)\n"
        "s_mov_b32 s90, 0\n"
        "LD_CA2_CHK_%=:\n"
        "v_cmp_eq_u64 vcc, s[96:97], v[26:27]\n"
        "s_nop 1\n"
        "s_cmp_eq_u64 vcc, exec\n"
        "s_cbranch_scc1 LD_CA2_GOT_%=\n"
        "s_sleep 2\n"
        "global_load_dwordx2 v[26:27], v24, s[86:87] sc1\n"
        "s_waitcnt vmcnt(0)\n"
        "s_add_u32 s90, s90, 1\n"
        "s_cmp_lt_u32 s90, 0x100000\n"
        "s_cbranch_scc1 LD_CA2_CHK_%=\n"
        "LD_CA2_GOT_%=:\n"
        "v_lshrrev_b32 v16, 4, v0\n"
        "s_and_b32 s52, s36, 3\n"
        "s_mul_i32 s53, s52, 320\n"
        "v_add_u32 v17, s53, v16\n"
        "v_mul_u32_u24 v18, 3277, v17\n"
        "v_lshrrev_b32 v18, 16, v18\n"
        "v_mul_u32_u24 v19, 20, v18\n"
        "v_sub_u32 v19, v17, v19\n"
        "v_bfe_u32 v20, v18, 2, 1\n"
        "v_lshlrev_b32 v20, 1, v20\n"
        "v_xor_b32 v19, v19, v20\n"
        "v_mul_u32_u24 v18, 1280, v18\n"
        "v_lshl_add_u32 v18, v19, 4, v18\n"
        "v_mov_b32 v1, v18\n"
        "v_add_u32 v17, s53, v16\n"
        "v_add_u32 v17, 64, v17\n"
        "v_mul_u32_u24 v18, 3277, v17\n"
        "v_lshrrev_b32 v18, 16, v18\n"
        "v_mul_u32_u24 v19, 20, v18\n"
        "v_sub_u32 v19, v17, v19\n"
        "v_bfe_u32 v20, v18, 2, 1\n"
        "v_lshlrev_b32 v20, 1, v20\n"
        "v_xor_b32 v19, v19, v20\n"
        "v_mul_u32_u24 v18, 1280, v18\n"
        "v_lshl_add_u32 v18, v19, 4, v18\n"
        "v_subrev_u32 v2, 1024, v18\n"
        "v_add_u32 v17, s53, v16\n"
        "v_add_u32 v17, 128, v17\n"
        "v_mul_u32_u24 v18, 3277, v17\n"
        "v_lshrrev_b32 v18, 16, v18\n"
        "v_mul_u32_u24 v19, 20, v18\n"
        "v_sub_u32 v19, v17, v19\n"
        "v_bfe_u32 v20, v18, 2, 1\n"
        "v_lshlrev_b32 v20, 1, v20\n"
        "v_xor_b32 v19, v19, v20\n"
        "v_mul_u32_u24 v18, 1280, v18\n"
        "v_lshl_add_u32 v18, v19, 4, v18\n"
        "v_subrev_u32 v3, 2048, v18\n"
        "v_add_u32 v17, s53, v16\n"
        "v_add_u32 v17, 192, v17\n"
        "v_mul_u32_u24 v18, 3277, v17\n"
        "v_lshrrev_b32 v18, 16, v18\n"
        "v_mul_u32_u24 v19, 20, v18\n"
        "v_sub_u32 v19, v17, v19\n"
        "v_bfe_u32 v20, v18, 2, 1\n"
        "v_lshlrev_b32 v20, 1, v20\n"
        "v_xor_b32 v19, v19, v20\n"
        "v_mul_u32_u24 v18, 1280, v18\n"
        "v_lshl_add_u32 v18, v19, 4, v18\n"
        "v_subrev_u32 v4, 3072, v18\n"
        "v_add_u32 v17, s53, v16\n"
        "v_add_u32 v17, 256, v17\n"
        "v_mul_u32_u24 v18, 3277, v17\n"
        "v_lshrrev_b32 v18, 16, v18\n"
        "v_mul_u32_u24 v19, 20, v18\n"
        "v_sub_u32 v19, v17, v19\n"
        "v_bfe_u32 v20, v18, 2, 1\n"
        "v_lshlrev_b32 v20, 1, v20\n"
        "v_xor_b32 v19, v19, v20\n"
        "v_mul_u32_u24 v18, 1280, v18\n"
        "v_lshl_add_u32 v18, v19, 4, v18\n"
        "v_mov_b32 v5, v18\n"
        "v_mov_b32 v6, v0\n"
        "v_add_u32 v7, 129024, v0\n"
        "s_lshr_b32 s52, s37, 2\n"
        "s_lshl_b32 s52, s52, 14\n"
        "s_sub_u32 s53, 0x1000000, s52\n"
        "s_add_u32 s52, s52, 37748736\n"
        "s_add_u32 s64, s86, s52\n"
        "s_addc_u32 s65, s87, 0\n"
        "s_and_b32 s65, s65, 0xffff\n"
        "s_mov_b32 s66, s53\n"
        "s_mov_b32 s67, 0x20000\n"
        "s_lshr_b32 s52, s37, 2\n"
        "s_mul_i32 s52, s52, 655360\n"
        "s_and_b32 s53, s37, 3\n"
        "s_mul_i32 s53, s53, 320\n"
        "s_add_u32 s52, s52, s53\n"
        "s_sub_u32 s53, 655360, s53\n"
        "s_add_u32 s68, %[wolo], s52\n"
        "s_addc_u32 s69, %[wohi], 0\n"
        "s_and_b32 s69, s69, 0xffff\n"
        "s_mov_b32 s70, s53\n"
        "s_mov_b32 s71, 0x20000\n"
        "s_sub_u32 s53, 15, s38\n"
        "s_lshl_b32 s53, s53, 3\n"
        "s_and_b32 s54, s36, 3\n"
        "s_lshl_b32 s54, s54, 1\n"
        "s_add_u32 s53, s53, s54\n"
        "s_lshl_b32 s76, s53, 17\n"
        "s_and_b32 s52, s36, 3\n"
        "s_mul_i32 s53, s52, 5120\n"
        "s_add_u32 m0, s53, 98304\n"
        "s_mov_b32 s78, 0\n"
        "buffer_load_dwordx4 v1, s[68:71], s78 offen offset:0 lds\n"
        "buffer_load_dwordx4 v2, s[68:71], s78 offen offset:1024 lds\n"
        "buffer_load_dwordx4 v3, s[68:71], s78 offen offset:2048 lds\n"
        "buffer_load_dwordx4 v4, s[68:71], s78 offen offset:3072 lds\n"
        "s_add_u32 m0, s53, 102400\n"
        "s_nop 0\n"
        "buffer_load_dwordx4 v5, s[68:71], s78 offen lds\n"
        "s_and_b32 s52, s36, 3\n"
        "s_lshl_b32 s53, s52, 12\n"
        "s_add_u32 m0, s53, 32768\n"
        "s_add_u32 s54, s76, 0\n"
        "buffer_load_dwordx4 v6, s[64:67], s54 offen sc1 lds\n"
        "buffer_load_dwordx4 v6, s[64:67], s54 offen offset:1024 sc1 lds\n"
        "buffer_load_dwordx4 v7, s[64:67], s54 offen offset:2048 sc1 lds\n"
        "buffer_load_dwordx4 v7, s[64:67], s54 offen offset:3072 sc1 lds\n"
        "s_and_b32 s52, s36, 3\n"
        "s_mul_i32 s53, s52, 5120\n"
        "s_add_u32 m0, s53, 118784\n"
        "s_mov_b32 s78, 81920\n"
        "buffer_load_dwordx4 v1, s[68:71], s78 offen offset:0 lds\n"
        "buffer_load_dwordx4 v2, s[68:71], s78 offen offset:1024 lds\n"
        "buffer_load_dwordx4 v3, s[68:71], s78 offen offset:2048 lds\n"
        "buffer_load_dwordx4 v4, s[68:71], s78 offen offset:3072 lds\n"
        "s_add_u32 m0, s53, 122880\n"
        "s_nop 0\n"
        "buffer_load_dwordx4 v5, s[68:71], s78 offen lds\n"
        "s_and_b32 s52, s36, 3\n"
        "s_lshl_b32 s53, s52, 12\n"
        "s_add_u32 m0, s53, 49152\n"
        "s_add_u32 s54, s76, 2048\n"
        "buffer_load_dwordx4 v6, s[64:67], s54 offen sc1 lds\n"
        "buffer_load_dwordx4 v6, s[64:67], s54 offen offset:1024 sc1 lds\n"
        "buffer_load_dwordx4 v7, s[64:67], s54 offen offset:2048 sc1 lds\n"
        "buffer_load_dwordx4 v7, s[64:67], s54 offen offset:3072 sc1 lds\n"
        "LD_GO4_%=:\n"
        "s_waitcnt vmcnt(9)\n"
        "s_barrier\n"
        "v_lshrrev_b32 v16, 4, v0\n"
        "s_and_b32 s52, s36, 3\n"
        "s_mul_i32 s53, s52, 320\n"
        "v_add_u32 v17, s53, v16\n"
        "v_mul_u32_u24 v18, 3277, v17\n"
        "v_lshrrev_b32 v18, 16, v18\n"
        "v_mul_u32_u24 v19, 20, v18\n"
        "v_sub_u32 v19, v17, v19\n"
        "v_bfe_u32 v20, v18, 2, 1\n"
        "v_lshlrev_b32 v20, 1, v20\n"
        "v_xor_b32 v19, v19, v20\n"
        "v_mul_u32_u24 v18, 1280, v18\n"
        "v_lshl_add_u32 v18, v19, 4, v18\n"
        "v_mov_b32 v1, v18\n"
        "v_add_u32 v17, s53, v16\n"
        "v_add_u32 v17, 64, v17\n"
        "v_mul_u32_u24 v18, 3277, v17\n"
        "v_lshrrev_b32 v18, 16, v18\n"
        "v_mul_u32_u24 v19, 20, v18\n"
        "v_sub_u32 v19, v17, v19\n"
        "v_bfe_u32 v20, v18, 2, 1\n"
        "v_lshlrev_b32 v20, 1, v20\n"
        "v_xor_b32 v19, v19, v20\n"
        "v_mul_u32_u24 v18, 1280, v18\n"
        "v_lshl_add_u32 v18, v19, 4, v18\n"
        "v_subrev_u32 v2, 1024, v18\n"
        "v_add_u32 v17, s53, v16\n"
        "v_add_u32 v17, 128, v17\n"
        "v_mul_u32_u24 v18, 3277, v17\n"
        "v_lshrrev_b32 v18, 16, v18\n"
        "v_mul_u32_u24 v19, 20, v18\n"
        "v_sub_u32 v19, v17, v19\n"
        "v_bfe_u32 v20, v18, 2, 1\n"
        "v_lshlrev_b32 v20, 1, v20\n"
        "v_xor_b32 v19, v19, v20\n"
        "v_mul_u32_u24 v18, 1280, v18\n"
        "v_lshl_add_u32 v18, v19, 4, v18\n"
        "v_subrev_u32 v3, 2048, v18\n"
        "v_add_u32 v17, s53, v16\n"
        "v_add_u32 v17, 192, v17\n"
        "v_mul_u32_u24 v18, 3277, v17\n"
        "v_lshrrev_b32 v18, 16, v18\n"
        "v_mul_u32_u24 v19, 20, v18\n"
        "v_sub_u32 v19, v17, v19\n"
        "v_bfe_u32 v20, v18, 2, 1\n"
        "v_lshlrev_b32 v20, 1, v20\n"
        "v_xor_b32 v19, v19, v20\n"
        "v_mul_u32_u24 v18, 1280, v18\n"
        "v_lshl_add_u32 v18, v19, 4, v18\n"
        "v_subrev_u32 v4, 3072, v18\n"
        "v_add_u32 v17, s53, v16\n"
        "v_add_u32 v17, 256, v17\n"
        "v_mul_u32_u24 v18, 3277, v17\n"
        "v_lshrrev_b32 v18, 16, v18\n"
        "v_mul_u32_u24 v19, 20, v18\n"
        "v_sub_u32 v19, v17, v19\n"
        "v_bfe_u32 v20, v18, 2, 1\n"
        "v_lshlrev_b32 v20, 1, v20\n"
        "v_xor_b32 v19, v19, v20\n"
        "v_mul_u32_u24 v18, 1280, v18\n"
        "v_lshl_add_u32 v18, v19, 4, v18\n"
        "v_mov_b32 v5, v18\n"
        "v_mov_b32 v6, v0\n"
        "v_add_u32 v7, 129024, v0\n"
        "s_lshr_b32 s52, s37, 2\n"
        "s_lshl_b32 s52, s52, 14\n"
        "s_sub_u32 s53, 0x1000000, s52\n"
        "s_add_u32 s52, s52, 37748736\n"
        "s_add_u32 s64, s86, s52\n"
        "s_addc_u32 s65, s87, 0\n"
        "s_and_b32 s65, s65, 0xffff\n"
        "s_mov_b32 s66, s53\n"
        "s_mov_b32 s67, 0x20000\n"
        "s_lshr_b32 s52, s37, 2\n"
        "s_mul_i32 s52, s52, 655360\n"
        "s_and_b32 s53, s37, 3\n"
        "s_mul_i32 s53, s53, 320\n"
        "s_add_u32 s52, s52, s53\n"
        "s_sub_u32 s53, 655360, s53\n"
        "s_add_u32 s68, %[wolo], s52\n"
        "s_addc_u32 s69, %[wohi], 0\n"
        "s_and_b32 s69, s69, 0xffff\n"
        "s_mov_b32 s70, s53\n"
        "s_mov_b32 s71, 0x20000\n"
        "s_sub_u32 s53, 15, s38\n"
        "s_lshl_b32 s53, s53, 3\n"
        "s_and_b32 s54, s36, 3\n"
        "s_lshl_b32 s54, s54, 1\n"
        "s_add_u32 s53, s53, s54\n"
        "s_lshl_b32 s76, s53, 17\n"
        "s_and_b32 s52, s36, 3\n"
        "s_mul_i32 s53, s52, 5120\n"
        "s_add_u32 m0, s53, 139264\n"
        "s_mov_b32 s78, 163840\n"
        "buffer_load_dwordx4 v1, s[68:71], s78 offen offset:0 lds\n"
        "buffer_load_dwordx4 v2, s[68:71], s78 offen offset:1024 lds\n"
        "buffer_load_dwordx4 v3, s[68:71], s78 offen offset:2048 lds\n"
        "buffer_load_dwordx4 v4, s[68:71], s78 offen offset:3072 lds\n"
        "s_add_u32 m0, s53, 143360\n"
        "s_nop 0\n"
        "buffer_load_dwordx4 v5, s[68:71], s78 offen lds\n"
        "s_and_b32 s52, s36, 3\n"
        "s_lshl_b32 s53, s52, 12\n"
        "s_add_u32 m0, s53, 65536\n"
        "s_add_u32 s54, s76, 4096\n"
        "buffer_load_dwordx4 v6, s[64:67], s54 offen sc1 lds\n"
        "buffer_load_dwordx4 v6, s[64:67], s54 offen offset:1024 sc1 lds\n"
        "buffer_load_dwordx4 v7, s[64:67], s54 offen offset:2048 sc1 lds\n"
        "buffer_load_dwordx4 v7, s[64:67], s54 offen offset:3072 sc1 lds\n"
        "s_and_b32 s52, s36, 3\n"
        "s_lshl_b32 s53, s52, 12\n"
        "s_add_u32 m0, s53, 81920\n"
        "s_add_u32 s54, s76, 6144\n"
        "buffer_load_dwordx4 v6, s[64:67], s54 offen sc1 lds\n"
        "buffer_load_dwordx4 v6, s[64:67], s54 offen offset:1024 sc1 lds\n"
        "buffer_load_dwordx4 v7, s[64:67], s54 offen offset:2048 sc1 lds\n"
        "buffer_load_dwordx4 v7, s[64:67], s54 offen offset:3072 sc1 lds\n"
        "s_and_b32 s52, s36, 3\n"
        "s_lshl_b32 s53, s52, 12\n"
        "s_add_u32 m0, s53, 0\n"
        "s_add_u32 s54, s76, 8192\n"
        "buffer_load_dwordx4 v6, s[64:67], s54 offen sc1 lds\n"
        "buffer_load_dwordx4 v6, s[64:67], s54 offen offset:1024 sc1 lds\n"
        "buffer_load_dwordx4 v7, s[64:67], s54 offen offset:2048 sc1 lds\n"
        "buffer_load_dwordx4 v7, s[64:67], s54 offen offset:3072 sc1 lds\n"
        "s_and_b32 s52, s36, 3\n"
        "s_lshl_b32 s53, s52, 12\n"
        "s_add_u32 m0, s53, 16384\n"
        "s_add_u32 s54, s76, 10240\n"
        "buffer_load_dwordx4 v6, s[64:67], s54 offen sc1 lds\n"
        "buffer_load_dwordx4 v6, s[64:67], s54 offen offset:1024 sc1 lds\n"
        "buffer_load_dwordx4 v7, s[64:67], s54 offen offset:2048 sc1 lds\n"
        "buffer_load_dwordx4 v7, s[64:67], s54 offen offset:3072 sc1 lds\n"
        "s_waitcnt vmcnt(21)\n"
        "s_barrier\n"
        "s_and_b32 s52, s36, 3\n"
        "s_mul_i32 s53, s52, 5120\n"
        "s_add_u32 m0, s53, 98304\n"
        "s_mov_b32 s78, 245760\n"
        "buffer_load_dwordx4 v1, s[68:71], s78 offen offset:0 lds\n"
        "buffer_load_dwordx4 v2, s[68:71], s78 offen offset:1024 lds\n"
        "buffer_load_dwordx4 v3, s[68:71], s78 offen offset:2048 lds\n"
        "buffer_load_dwordx4 v4, s[68:71], s78 offen offset:3072 lds\n"
        "s_add_u32 m0, s53, 102400\n"
        "s_nop 0\n"
        "buffer_load_dwordx4 v5, s[68:71], s78 offen lds\n"
        "s_and_b32 s52, s36, 3\n"
        "s_lshl_b32 s53, s52, 12\n"
        "s_add_u32 m0, s53, 32768\n"
        "s_add_u32 s54, s76, 12288\n"
        "buffer_load_dwordx4 v6, s[64:67], s54 offen sc1 lds\n"
        "buffer_load_dwordx4 v6, s[64:67], s54 offen offset:1024 sc1 lds\n"
        "buffer_load_dwordx4 v7, s[64:67], s54 offen offset:2048 sc1 lds\n"
        "buffer_load_dwordx4 v7, s[64:67], s54 offen offset:3072 sc1 lds\n"
        "s_waitcnt vmcnt(21)\n"
        "s_barrier\n"
        "s_and_b32 s52, s36, 3\n"
        "s_mul_i32 s53, s52, 5120\n"
        "s_add_u32 m0, s53, 118784\n"
        "s_mov_b32 s78, 327680\n"
        "buffer_load_dwordx4 v1, s[68:71], s78 offen offset:0 lds\n"
        "buffer_load_dwordx4 v2, s[68:71], s78 offen offset:1024 lds\n"
        "buffer_load_dwordx4 v3, s[68:71], s78 offen offset:2048 lds\n"
        "buffer_load_dwordx4 v4, s[68:71], s78 offen offset:3072 lds\n"
        "s_add_u32 m0, s53, 122880\n"
        "s_nop 0\n"
        "buffer_load_dwordx4 v5, s[68:71], s78 offen lds\n"
        "s_and_b32 s52, s36, 3\n"
        "s_lshl_b32 s53, s52, 12\n"
        "s_add_u32 m0, s53, 49152\n"
        "s_add_u32 s54, s76, 14336\n"
        "buffer_load_dwordx4 v6, s[64:67], s54 offen sc1 lds\n"
        "buffer_load_dwordx4 v6, s[64:67], s54 offen offset:1024 sc1 lds\n"
        "buffer_load_dwordx4 v7, s[64:67], s54 offen offset:2048 sc1 lds\n"
        "buffer_load_dwordx4 v7, s[64:67], s54 offen offset:3072 sc1 lds\n"
        "s_waitcnt vmcnt(13)\n"
        "s_barrier\n"
        "s_and_b32 s52, s36, 3\n"
        "s_mul_i32 s53, s52, 5120\n"
        "s_add_u32 m0, s53, 139264\n"
        "s_mov_b32 s78, 409600\n"
        "buffer_load_dwordx4 v1, s[68:71], s78 offen offset:0 lds\n"
        "buffer_load_dwordx4 v2, s[68:71], s78 offen offset:1024 lds\n"
        "buffer_load_dwordx4 v3, s[68:71], s78 offen offset:2048 lds\n"
        "buffer_load_dwordx4 v4, s[68:71], s78 offen offset:3072 lds\n"
        "s_add_u32 m0, s53, 143360\n"
        "s_nop 0\n"
        "buffer_load_dwordx4 v5, s[68:71], s78 offen lds\n"
        "s_waitcnt vmcnt(9)\n"
        "s_barrier\n"
        "s_and_b32 s52, s36, 3\n"
        "s_mul_i32 s53, s52, 5120\n"
        "s_add_u32 m0, s53, 98304\n"
        "s_mov_b32 s78, 491520\n"
        "buffer_load_dwordx4 v1, s[68:71], s78 offen offset:0 lds\n"
        "buffer_load_dwordx4 v2, s[68:71], s78 offen offset:1024 lds\n"
        "buffer_load_dwordx4 v3, s[68:71], s78 offen offset:2048 lds\n"
        "buffer_load_dwordx4 v4, s[68:71], s78 offen offset:3072 lds\n"
        "s_add_u32 m0, s53, 102400\n"
        "s_nop 0\n"
        "buffer_load_dwordx4 v5, s[68:71], s78 offen lds\n"
        "s_waitcnt vmcnt(5)\n"
        "s_barrier\n"
        "s_and_b32 s52, s36, 3\n"
        "s_mul_i32 s53, s52, 5120\n"
        "s_add_u32 m0, s53, 118784\n"
        "s_mov_b32 s78, 573440\n"
        "buffer_load_dwordx4 v1, s[68:71], s78 offen offset:0 lds\n"
        "buffer_load_dwordx4 v2, s[68:71], s78 offen offset:1024 lds\n"
        "buffer_load_dwordx4 v3, s[68:71], s78 offen offset:2048 lds\n"
        "buffer_load_dwordx4 v4, s[68:71], s78 offen offset:3072 lds\n"
        "s_add_u32 m0, s53, 122880\n"
        "s_nop 0\n"
        "buffer_load_dwordx4 v5, s[68:71], s78 offen lds\n"
        "v_lshlrev_b32 v20, 3, v0\n"
        "v_and_b32 v20, 0x180, v20\n"
        "s_lshr_b32 s88, s37, 2\n"
        "s_lshl_b32 s88, s88, 3\n"
        "s_add_u32 s88, s88, s38\n"
        "s_lshl_b32 s88, s88, 9\n"
        "s_add_u32 s88, s88, 98304\n"
        "v_add_u32 v20, s88, v20\n"
        "global_load_dwordx2 v[22:23], v20, s[86:87] sc1\n"
        "s_waitcnt vmcnt(0)\n"
        "s_mov_b32 s90, 0\n"
        "LD_CB_CHK_%=:\n"
        "v_cmp_eq_u64 vcc, s[96:97], v[22:23]\n"
        "s_nop 1\n"
        "s_cmp_eq_u64 vcc, exec\n"
        "s_cbranch_scc1 LD_CB_GOT_%=\n"
        "s_sleep 2\n"
        "global_load_dwordx2 v[22:23], v20, s[86:87] sc1\n"
        "s_waitcnt vmcnt(0)\n"
        "s_add_u32 s90, s90, 1\n"
        "s_cmp_lt_u32 s90, 0x100000\n"
        "s_cbranch_scc1 LD_CB_CHK_%=\n"
        "LD_CB_GOT_%=:\n"
        "s_mov_b32 s53, s38\n"
        "s_lshl_b32 s53, s53, 3\n"
        "s_and_b32 s54, s36, 3\n"
        "s_lshl_b32 s54, s54, 1\n"
        "s_add_u32 s53, s53, s54\n"
        "s_lshl_b32 s76, s53, 17\n"
        "s_and_b32 s52, s36, 3\n"
        "s_lshl_b32 s53, s52, 12\n"
        "s_add_u32 m0, s53, 65536\n"
        "s_add_u32 s54, s76, 0\n"
        "buffer_load_dwordx4 v6, s[64:67], s54 offen sc1 lds\n"
        "buffer_load_dwordx4 v6, s[64:67], s54 offen offset:1024 sc1 lds\n"
        "buffer_load_dwordx4 v7, s[64:67], s54 offen offset:2048 sc1 lds\n"
        "buffer_load_dwordx4 v7, s[64:67], s54 offen offset:3072 sc1 lds\n"
        "s_and_b32 s52, s36, 3\n"
        "s_lshl_b32 s53, s52, 12\n"
        "s_add_u32 m0, s53, 81920\n"
        "s_add_u32 s54, s76, 2048\n"
        "buffer_load_dwordx4 v6, s[64:67], s54 offen sc1 lds\n"
        "buffer_load_dwordx4 v6, s[64:67], s54 offen offset:1024 sc1 lds\n"
        "buffer_load_dwordx4 v7, s[64:67], s54 offen offset:2048 sc1 lds\n"
        "buffer_load_dwordx4 v7, s[64:67], s54 offen offset:3072 sc1 lds\n"
        "s_and_b32 s52, s36, 3\n"
        "s_lshl_b32 s53, s52, 12\n"
        "s_add_u32 m0, s53, 0\n"
        "s_add_u32 s54, s76, 4096\n"
        "buffer_load_dwordx4 v6, s[64:67], s54 offen sc1 lds\n"
        "buffer_load_dwordx4 v6, s[64:67], s54 offen offset:1024 sc1 lds\n"
        "buffer_load_dwordx4 v7, s[64:67], s54 offen offset:2048 sc1 lds\n"
        "buffer_load_dwordx4 v7, s[64:67], s54 offen offset:3072 sc1 lds\n"
        "s_waitcnt vmcnt(18)\n"
        "s_barrier\n"
        "s_and_b32 s52, s36, 3\n"
        "s_mul_i32 s53, s52, 5120\n"
        "s_add_u32 m0, s53, 139264\n"
        "s_mov_b32 s78, 0\n"
        "buffer_load_dwordx4 v1, s[68:71], s78 offen offset:0 lds\n"
        "buffer_load_dwordx4 v2, s[68:71], s78 offen offset:1024 lds\n"
        "buffer_load_dwordx4 v3, s[68:71], s78 offen offset:2048 lds\n"
        "buffer_load_dwordx4 v4, s[68:71], s78 offen offset:3072 lds\n"
        "s_add_u32 m0, s53, 143360\n"
        "s_nop 0\n"
        "buffer_load_dwordx4 v5, s[68:71], s78 offen lds\n"
        "s_and_b32 s52, s36, 3\n"
        "s_lshl_b32 s53, s52, 12\n"
        "s_add_u32 m0, s53, 16384\n"
        "s_add_u32 s54, s76, 6144\n"
        "buffer_load_dwordx4 v6, s[64:67], s54 offen sc1 lds\n"
        "buffer_load_dwordx4 v6, s[64:67], s54 offen offset:1024 sc1 lds\n"
        "buffer_load_dwordx4 v7, s[64:67], s54 offen offset:2048 sc1 lds\n"
        "buffer_load_dwordx4 v7, s[64:67], s54 offen offset:3072 sc1 lds\n"
        "s_waitcnt vmcnt(22)\n"
        "s_barrier\n"
        "s_and_b32 s52, s36, 3\n"
        "s_mul_i32 s53, s52, 5120\n"
        "s_add_u32 m0, s53, 98304\n"
        "s_mov_b32 s78, 81920\n"
        "buffer_load_dwordx4 v1, s[68:71], s78 offen offset:0 lds\n"
        "buffer_load_dwordx4 v2, s[68:71], s78 offen offset:1024 lds\n"
        "buffer_load_dwordx4 v3, s[68:71], s78 offen offset:2048 lds\n"
        "buffer_load_dwordx4 v4, s[68:71], s78 offen offset:3072 lds\n"
        "s_add_u32 m0, s53, 102400\n"
        "s_nop 0\n"
        "buffer_load_dwordx4 v5, s[68:71], s78 offen lds\n"
        "s_and_b32 s52, s36, 3\n"
        "s_lshl_b32 s53, s52, 12\n"
        "s_add_u32 m0, s53, 32768\n"
        "s_add_u32 s54, s76, 8192\n"
        "buffer_load_dwordx4 v6, s[64:67], s54 offen sc1 lds\n"
        "buffer_load_dwordx4 v6, s[64:67], s54 offen offset:1024 sc1 lds\n"
        "buffer_load_dwordx4 v7, s[64:67], s54 offen offset:2048 sc1 lds\n"
        "buffer_load_dwordx4 v7, s[64:67], s54 offen offset:3072 sc1 lds\n"
        "s_waitcnt vmcnt(13)\n"
        "s_barrier\n"
        "s_and_b32 s52, s36, 3\n"
        "s_mul_i32 s53, s52, 5120\n"
        "s_add_u32 m0, s53, 118784\n"
        "s_mov_b32 s78, 163840\n"
        "buffer_load_dwordx4 v1, s[68:71], s78 offen offset:0 lds\n"
        "buffer_load_dwordx4 v2, s[68:71], s78 offen offset:1024 lds\n"
        "buffer_load_dwordx4 v3, s[68:71], s78 offen offset:2048 lds\n"
        "buffer_load_dwordx4 v4, s[68:71], s78 offen offset:3072 lds\n"
        "s_add_u32 m0, s53, 122880\n"
        "s_nop 0\n"
        "buffer_load_dwordx4 v5, s[68:71], s78 offen lds\n"
        "s_and_b32 s52, s36, 3\n"
        "s_lshl_b32 s53, s52, 12\n"
        "s_add_u32 m0, s53, 49152\n"
        "s_add_u32 s54, s76, 10240\n"
        "buffer_load_dwordx4 v6, s[64:67], s54 offen sc1 lds\n"
        "buffer_load_dwordx4 v6, s[64:67], s54 offen offset:1024 sc1 lds\n"
        "buffer_load_dwordx4 v7, s[64:67], s54 offen offset:2048 sc1 lds\n"
        "buffer_load_dwordx4 v7, s[64:67], s54 offen offset:3072 sc1 lds\n"
        "s_waitcnt vmcnt(13)\n"
        "s_barrier\n"
        "s_and_b32 s52, s36, 3\n"
        "s_mul_i32 s53, s52, 5120\n"
        "s_add_u32 m0, s53, 139264\n"
        "s_mov_b32 s78, 245760\n"
        "buffer_load_dwordx4 v1, s[68:71], s78 offen offset:0 lds\n"
        "buffer_load_dwordx4 v2, s[68:71], s78 offen offset:1024 lds\n"
        "buffer_load_dwordx4 v3, s[68:71], s78 offen offset:2048 lds\n"
        "buffer_load_dwordx4 v4, s[68:71], s78 offen offset:3072 lds\n"
        "s_add_u32 m0, s53, 143360\n"
        "s_nop 0\n"
        "buffer_load_dwordx4 v5, s[68:71], s78 offen lds\n"
        "s_and_b32 s52, s36, 3\n"
        "s_lshl_b32 s53, s52, 12\n"
        "s_add_u32 m0, s53, 65536\n"
        "s_add_u32 s54, s76, 12288\n"
        "buffer_load_dwordx4 v6, s[64:67], s54 offen sc1 lds\n"
        "buffer_load_dwordx4 v6, s[64:67], s54 offen offset:1024 sc1 lds\n"
        "buffer_load_dwordx4 v7, s[64:67], s54 offen offset:2048 sc1 lds\n"
        "buffer_load_dwordx4 v7, s[64:67], s54 offen offset:3072 sc1 lds\n"
        "s_waitcnt vmcnt(13)\n"
        "s_barrier\n"
        "s_and_b32 s52, s36, 3\n"
        "s_mul_i32 s53, s52, 5120\n"
        "s_add_u32 m0, s53, 98304\n"
        "s_mov_b32 s78, 327680\n"
        "buffer_load_dwordx4 v1, s[68:71], s78 offen offset:0 lds\n"
        "buffer_load_dwordx4 v2, s[68:71], s78 offen offset:1024 lds\n"
        "buffer_load_dwordx4 v3, s[68:71], s78 offen offset:2048 lds\n"
        "buffer_load_dwordx4 v4, s[68:71], s78 offen offset:3072 lds\n"
        "s_add_u32 m0, s53, 102400\n"
        "s_nop 0\n"
        "buffer_load_dwordx4 v5, s[68:71], s78 offen lds\n"
        "s_and_b32 s52, s36, 3\n"
        "s_lshl_b32 s53, s52, 12\n"
        "s_add_u32 m0, s53, 81920\n"
        "s_add_u32 s54, s76, 14336\n"
        "buffer_load_dwordx4 v6, s[64:67], s54 offen sc1 lds\n"
        "buffer_load_dwordx4 v6, s[64:67], s54 offen offset:1024 sc1 lds\n"
        "buffer_load_dwordx4 v7, s[64:67], s54 offen offset:2048 sc1 lds\n"
        "buffer_load_dwordx4 v7, s[64:67], s54 offen offset:3072 sc1 lds\n"
        "s_waitcnt vmcnt(13)\n"
        "s_barrier\n"
        "s_and_b32 s52, s36, 3\n"
        "s_mul_i32 s53, s52, 5120\n"
        "s_add_u32 m0, s53, 118784\n"
        "s_mov_b32 s78, 409600\n"
        "buffer_load_dwordx4 v1, s[68:71], s78 offen offset:0 lds\n"
        "buffer_load_dwordx4 v2, s[68:71], s78 offen offset:1024 lds\n"
        "buffer_load_dwordx4 v3, s[68:71], s78 offen offset:2048 lds\n"
        "buffer_load_dwordx4 v4, s[68:71], s78 offen offset:3072 lds\n"
        "s_add_u32 m0, s53, 122880\n"
        "s_nop 0\n"
        "buffer_load_dwordx4 v5, s[68:71], s78 offen lds\n"
        "s_waitcnt vmcnt(9)\n"
        "s_barrier\n"
        "s_and_b32 s52, s36, 3\n"
        "s_mul_i32 s53, s52, 5120\n"
        "s_add_u32 m0, s53, 139264\n"
        "s_mov_b32 s78, 491520\n"
        "buffer_load_dwordx4 v1, s[68:71], s78 offen offset:0 lds\n"
        "buffer_load_dwordx4 v2, s[68:71], s78 offen offset:1024 lds\n"
        "buffer_load_dwordx4 v3, s[68:71], s78 offen offset:2048 lds\n"
        "buffer_load_dwordx4 v4, s[68:71], s78 offen offset:3072 lds\n"
        "s_add_u32 m0, s53, 143360\n"
        "s_nop 0\n"
        "buffer_load_dwordx4 v5, s[68:71], s78 offen lds\n"
        "s_waitcnt vmcnt(5)\n"
        "s_barrier\n"
        "s_and_b32 s52, s36, 3\n"
        "s_mul_i32 s53, s52, 5120\n"
        "s_add_u32 m0, s53, 98304\n"
        "s_mov_b32 s78, 573440\n"
        "buffer_load_dwordx4 v1, s[68:71], s78 offen offset:0 lds\n"
        "buffer_load_dwordx4 v2, s[68:71], s78 offen offset:1024 lds\n"
        "buffer_load_dwordx4 v3, s[68:71], s78 offen offset:2048 lds\n"
        "buffer_load_dwordx4 v4, s[68:71], s78 offen offset:3072 lds\n"
        "s_add_u32 m0, s53, 102400\n"
        "s_nop 0\n"
        "buffer_load_dwordx4 v5, s[68:71], s78 offen lds\n"
        "s_waitcnt vmcnt(5)\n"
        "s_barrier\n"
        "s_waitcnt vmcnt(0)\n"
        "s_barrier\n"
        "KB_END_%=:\n"
        : [dummy] "=v"(dummy)
        : [h] "s"(h2), [i] "s"(i2), [blk] "s"(b2), [wid] "s"(wid), [taglo] "s"(taglo), [taghi] "s"(taghi), [restart] "s"(restart),
          [wolo] "s"(wo2lo), [wohi] "s"(wo2hi), [wslo] "s"(ws2lo), [wshi] "s"(ws2hi)
        : "v0", "v2", "v3", "v4", "v5", "v6", "v7", "v8", "v9", "v10", "v11", "v12", "v13", "v14", "v15", "v16", "v17", "v18", "v19", "v20", "v21", "v22", "v23", "v24", "v25", "v26", "v27", "v28", "v29", "v30", "v31", "v32", "v33", "v34", "v35", "v36", "v37", "v38", "v39", "v40", "v41", "v42", "v43", "v44", "v45", "v46", "v47", "v48", "v49", "v50", "v51", "v52", "v53", "v54", "v55", "v56", "v57", "v58", "v59", "v60", "v61", "v62", "v63", "v64", "v65", "v66", "v67", "v68", "v69", "v70", "v71", "v72", "v73", "v74", "v75", "v76", "v77", "v78", "v79", "v80", "v81", "v82", "v83", "v84", "v85", "v86", "v87", "v88", "v89", "v90", "v91", "v92", "v93", "v94", "v95", "v96", "v97", "v98", "v99", "v100", "v101", "v102", "v103", "v104", "v105", "v106", "v107", "v108", "v109", "v110", "v111", "v112", "v113", "v114", "v115", "v116", "v117", "v118", "v119", "v120", "v121", "v122", "v123", "v124", "v125", "v126", "v127", "a0", "a1", "a2", "a3", "a4", "a5", "a6", "a7", "a8", "a9", "a10", "a11", "a12", "a13", "a14", "a15", "a16", "a17", "a18", "a19", "a20", "a21", "a22", "a23", "a24", "a25", "a26", "a27", "a28", "a29", "a30", "a31", "a32", "a33", "a34", "a35", "a36", "a37", "a38", "a39", "a40", "a41", "a42", "a43", "a44", "a45", "a46", "a47", "a48", "a49", "a50", "a51", "a52", "a53", "a54", "a55", "a56", "a57", "a58", "a59", "a60", "a61", "a62", "a63", "a64", "a65", "a66", "a67", "a68", "a69", "a70", "a71", "a72", "a73", "a74", "a75", "a76", "a77", "a78", "a79", "a80", "a81", "a82", "a83", "a84", "a85", "a86", "a87", "a88", "a89", "a90", "a91", "a92", "a93", "a94", "a95", "a96", "a97", "a98", "a99", "a100", "a101", "a102", "a103", "a104", "a105", "a106", "a107", "a108", "a109", "a110", "a111", "a112", "a113", "a114", "a115", "a116", "a117", "a118", "a119", "a120", "a121", "a122", "a123", "a124", "a125", "a126", "a127", "s36", "s37", "s38", "s39", "s40", "s41", "s42", "s43", "s44", "s45", "s46", "s47", "s48", "s49", "s50", "s51", "s52", "s53", "s54", "s55", "s56", "s57", "s58", "s59", "s60", "s61", "s62", "s63", "s64", "s65", "s66", "s67", "s68", "s69", "s70", "s71", "s72", "s73", "s74", "s75", "s76", "s77", "s78", "s79", "s80", "s81", "s82", "s83", "s84", "s85", "s86", "s87", "s88", "s89", "s90", "s91", "s92", "s93", "s94", "s95", "s96", "s97", "s98", "s99", "vcc", "scc", "memory");
    }
    asm volatile("" : "+s"(ka));
    asm volatile("" : "+s"(b2));
    asm volatile("" : "+s"(wid));
    block_tail(lds, wid, b2, (b2 & 7) * 4 + ((b2 >> 3) & 3), b2 >> 5, tag, (const unsigned short*)ka[8], (const unsigned short*)ka[9],
               ka[5], ka[6], ka[7], (char*)ka[10], (unsigned short*)ka[11]);
}

extern "C" void kernel_launch(void* const* d_in, const int* in_sizes, int n_in, void* d_out, int out_size, void* d_ws, size_t ws_size, hipStream_t stream) {
    (void)in_sizes; (void)n_in; (void)out_size; (void)ws_size;
    const unsigned short* x = (const unsigned short*)d_in[0];
    char* ws = (char*)d_ws;
    k_block<<<256, 512, 0, stream>>>((const char*)d_in[1], (const char*)d_in[2], (const char*)d_in[3], (const char*)d_in[4], (const char*)d_in[5],
                                     (const char*)d_in[8], (const char*)d_in[9], (const char*)d_in[10], x, (const unsigned short*)d_in[7], ws, (unsigned short*)d_out);
}
```
